```python
import torch

def _softmax(x, axis=-1):
    return torch.softmax(x, dim=axis)
def _gelu(x):
    return torch.nn.functional.gelu(x, approximate="tanh")
def _silu(x):
    return torch.nn.functional.silu(x)
def _sigmoid(x):
    return torch.sigmoid(x)
def _layernorm(x, axis=-1, eps=1e-5):
    m = x.mean(dim=axis, keepdim=True)
    v = x.var(dim=axis, keepdim=True, unbiased=False)
    return (x - m) / torch.sqrt(v + eps)
def _scatter_add(tgt, idx, src, axis=0):
    return tgt.clone().index_add_(axis, idx, src)
def _segment_sum(src, seg, n):
    out = torch.zeros((n,) + src.shape[1:], dtype=src.dtype)
    return out.index_add_(0, seg, src)
def _pad(x, pw):
    flat = tuple(v for pair in reversed(pw) for v in pair)
    return torch.nn.functional.pad(x, flat)

kernel_name = 'concat_heads__gqa_block_cached__compressed_kv_cla__decode'

def setup_inputs():

    _w = (lambda a: (lambda t: t.to(torch.bfloat16) if t.is_floating_point() else t)(torch.as_tensor(a)))
    def _A_setup():
        B, Sq, D = 4, 1, 576
        Hq, Hkv, Sk, Dh = 32, 16, 4096, 128
        assert Hq % Hkv == 0, (Hq, Hkv)
        return {
            "x": _w((lambda *s: torch.randn(*s, dtype=torch.bfloat16) * 1.6)(B, Sq, D)),
            "k_cache": _w((lambda *s: torch.randn(*s, dtype=torch.bfloat16) * 1.6)(B, Hkv, Sk, Dh)),
            "v_cache": _w((lambda *s: torch.randn(*s, dtype=torch.bfloat16) * 0.05)(B, Hkv, Sk, Dh)),
            "wq": _w((lambda *s: torch.randn(*s, dtype=torch.bfloat16) * 1.6)(D, Hq * Dh) * (D ** -0.5)),
            "wo": _w((lambda *s: torch.randn(*s, dtype=torch.bfloat16) * 0.05)(Hq * Dh, D) * ((Hq * Dh) ** -0.5)),
        }
    _A_in = _A_setup()

    def _B_setup():
        B, H, Sq, Sk, D, M = 4, 32, 1, 4096, 128, 576
        Dc = 4 * D
        return {
            "x": _w((lambda *s: torch.randn(*s, dtype=torch.bfloat16) * 3.2)(B, Sq, M)),
            "c_kv": _w((lambda *s: torch.randn(*s, dtype=torch.bfloat16) * 0.1)(B, Sk, Dc)),
            "w_q": _w((lambda *s: torch.randn(*s, dtype=torch.bfloat16) * 6.4)(M, H, D) * (M ** -0.5)),
            "w_uk": _w((lambda *s: torch.randn(*s, dtype=torch.bfloat16) * 6.4)(Dc, H, D) * (Dc ** -0.5)),
            "w_uv": _w((lambda *s: torch.randn(*s, dtype=torch.bfloat16) * 0.1)(Dc, H, D) * (Dc ** -0.5)),
            "w_o": _w((lambda *s: torch.randn(*s, dtype=torch.bfloat16) * 0.1)(H, D, M) * ((H * D) ** -0.5)),
        }
    _B_in = _B_setup()

    return {
        "A_x": _A_in["x"],
        "A_k_cache": _A_in["k_cache"],
        "A_v_cache": _A_in["v_cache"],
        "A_wq": _A_in["wq"],
        "A_wo": _A_in["wo"],
        "B_x": _B_in["x"],
        "B_c_kv": _B_in["c_kv"],
        "B_w_q": _B_in["w_q"],
        "B_w_uk": _B_in["w_uk"],
        "B_w_uv": _B_in["w_uv"],
        "B_w_o": _B_in["w_o"],
    }

def reference(A_x, A_k_cache, A_v_cache, A_wq, A_wo, B_x, B_c_kv, B_w_q, B_w_uk, B_w_uv, B_w_o):
    def _A_ref(x, k_cache, v_cache, wq, wo):
        B, Sq, D = x.shape
        Hq, Dh = 32, 128
        Hkv = k_cache.shape[1]
        rep = Hq // Hkv
        q = (lambda x, *d: x.permute(*d))((x @ wq).reshape(B, Sq, Hq, Dh), 0, 2, 1, 3)
        k = (lambda x, n, axis: torch.repeat_interleave(x, n, dim=axis))(k_cache, rep, axis=1)
        v = (lambda x, n, axis: torch.repeat_interleave(x, n, dim=axis))(v_cache, rep, axis=1)
        scores = torch.einsum("bhqd,bhkd->bhqk", q, k) * (Dh ** -0.5)
        p = _softmax(scores, axis=-1)
        o = torch.einsum("bhqk,bhkd->bhqd", p, v)
        return (lambda x, *d: x.permute(*d))(o, 0, 2, 1, 3).reshape(B, Sq, Hq * Dh) @ wo

    def _B_ref(x, c_kv, w_q, w_uk, w_uv, w_o):
        B, Sq, M = x.shape[0], x.shape[1], x.shape[2]
        Sk = c_kv.shape[1]
        H, D = w_q.shape[1], w_q.shape[2]
        q = torch.einsum("bqm,mhd->bhqd", x, w_q)
        k = torch.einsum("bkc,chd->bhkd", c_kv, w_uk)
        v = torch.einsum("bkc,chd->bhkd", c_kv, w_uv)
        scores = torch.einsum("bhqd,bhkd->bhqk", q, k) * (D ** -0.5)
        off = Sk - Sq
        qi = torch.arange(Sq)[:, None]
        kj = torch.arange(Sk)[None, :]
        cmask = kj <= (qi + off)
        scores = torch.where(cmask[None, None], scores, -1e9)
        p = _softmax(scores, axis=-1)
        ctx = torch.einsum("bhqk,bhkd->bhqd", p, v)
        return torch.einsum("bhqd,hdm->bqm", ctx, w_o)

    a = _A_ref(A_x, A_k_cache, A_v_cache, A_wq, A_wo)
    b = _B_ref(B_x, B_c_kv, B_w_q, B_w_uk, B_w_uv, B_w_o)
    return torch.concatenate([a, b], axis=-1)
```

```cpp
#include <hip/hip_runtime.h>
#include <hip/hip_bf16.h>
#include <stdint.h>

typedef unsigned short u16;
typedef unsigned u32x4 __attribute__((ext_vector_type(4)));
__device__ __forceinline__ float bf2f(u16 v) { return __uint_as_float(((unsigned)v) << 16); }
__device__ __forceinline__ float bflo(unsigned v) { return __uint_as_float(v << 16); }
__device__ __forceinline__ float bfhi(unsigned v) { return __uint_as_float(v & 0xffff0000u); }
__device__ __forceinline__ float rbf(float f) { return __bfloat162float(__float2bfloat16(f)); }

__global__ void k_qproj(const u16* xa, const u16* wqa, const u16* xb, const u16* wqb, float* qa, float* qb) {
  const u16* x = blockIdx.y ? xb : xa; const u16* w = blockIdx.y ? wqb : wqa; float* q = blockIdx.y ? qb : qa;
  __shared__ float xs[4 * 576];
  for (int i = threadIdx.x; i < 4 * 576; i += blockDim.x) xs[i] = bf2f(x[i]);
  __syncthreads();
  int n = blockIdx.x * blockDim.x + threadIdx.x;
  float a0 = 0, a1 = 0, a2 = 0, a3 = 0;
  for (int k = 0; k < 576; ++k) { float wv = bf2f(w[k * 4096 + n]); a0 += xs[k] * wv; a1 += xs[576 + k] * wv; a2 += xs[1152 + k] * wv; a3 += xs[1728 + k] * wv; }
  q[n] = rbf(a0); q[4096 + n] = rbf(a1); q[8192 + n] = rbf(a2); q[12288 + n] = rbf(a3);
}
__global__ void k_qabs(const float* qb, const u16* wuk, float* qabs) {
  int h = blockIdx.x, c = threadIdx.x;
  __shared__ float qs[4 * 128];
  qs[threadIdx.x] = qb[(threadIdx.x >> 7) * 4096 + h * 128 + (threadIdx.x & 127)];
  __syncthreads();
  float a[4] = {0, 0, 0, 0};
  for (int d = 0; d < 128; ++d) { float wv = bf2f(wuk[(c * 32 + h) * 128 + d]); for (int b = 0; b < 4; ++b) a[b] += qs[b * 128 + d] * wv; }
  for (int b = 0; b < 4; ++b) qabs[(b * 32 + h) * 512 + c] = a[b];
}
__global__ void k_scoreA(const float* qa, const u16* K, float* S) {
  int pair = blockIdx.y, b = pair >> 4, g = pair & 15;
  __shared__ float qs[256];
  qs[threadIdx.x] = qa[b * 4096 + g * 256 + threadIdx.x];
  __syncthreads();
  int key = blockIdx.x * 256 + threadIdx.x;
  const u32x4* kr = (const u32x4*)(K + ((size_t)pair * 4096 + key) * 128);
  float s0 = 0, s1 = 0;
  for (int c = 0; c < 16; ++c) { u32x4 v = kr[c]; for (int j = 0; j < 4; ++j) { float lo = bflo(v[j]), hi = bfhi(v[j]); int d = c * 8 + j * 2; s0 += lo * qs[d] + hi * qs[d + 1]; s1 += lo * qs[128 + d] + hi * qs[128 + d + 1]; } }
  S[(size_t)(pair * 2) * 4096 + key] = s0 * 0.08838834764831845f;
  S[(size_t)(pair * 2 + 1) * 4096 + key] = s1 * 0.08838834764831845f;
}
__global__ void k_scoreB(const float* qabs, const u16* C, float* S) {
  int b = blockIdx.y, h = blockIdx.z;
  __shared__ float qs[512];
  qs[threadIdx.x] = qabs[(b * 32 + h) * 512 + threadIdx.x]; qs[256 + threadIdx.x] = qabs[(b * 32 + h) * 512 + 256 + threadIdx.x];
  __syncthreads();
  int key = blockIdx.x * 256 + threadIdx.x;
  const u32x4* kr = (const u32x4*)(C + ((size_t)b * 4096 + key) * 512);
  float s0 = 0;
  for (int c = 0; c < 64; ++c) { u32x4 v = kr[c]; for (int j = 0; j < 4; ++j) { int d = c * 8 + j * 2; s0 += bflo(v[j]) * qs[d] + bfhi(v[j]) * qs[d + 1]; } }
  S[(size_t)(b * 32 + h) * 4096 + key] = s0 * 0.08838834764831845f;
}
__global__ void k_softmax(float* S) {
  float* r = S + (size_t)blockIdx.x * 4096;
  __shared__ float red[256];
  float m = -INFINITY;
  for (int i = threadIdx.x; i < 4096; i += 256) m = fmaxf(m, r[i]);
  red[threadIdx.x] = m; __syncthreads();
  for (int s = 128; s > 0; s >>= 1) { if (threadIdx.x < s) red[threadIdx.x] = fmaxf(red[threadIdx.x], red[threadIdx.x + s]); __syncthreads(); }
  m = red[0]; __syncthreads();
  float l = 0;
  for (int i = threadIdx.x; i < 4096; i += 256) { float e = __expf(r[i] - m); r[i] = e; l += e; }
  red[threadIdx.x] = l; __syncthreads();
  for (int s = 128; s > 0; s >>= 1) { if (threadIdx.x < s) red[threadIdx.x] += red[threadIdx.x + s]; __syncthreads(); }
  float inv = 1.f / red[0];
  for (int i = threadIdx.x; i < 4096; i += 256) r[i] *= inv;
}
__global__ void k_pvA(const float* P, const u16* V, float* o) {
  int row = blockIdx.x, pair = row >> 1; int d = threadIdx.x & 127, grp = threadIdx.x >> 7;
  const float* p = P + (size_t)row * 4096; const u16* v = V + (size_t)pair * 4096 * 128;
  float a = 0;
  for (int k = grp; k < 4096; k += 8) a += p[k] * bf2f(v[(size_t)k * 128 + d]);
  __shared__ float red[1024];
  red[threadIdx.x] = a; __syncthreads();
  if (grp == 0) { float s = 0; for (int i = 0; i < 8; ++i) s += red[i * 128 + d]; o[row * 128 + d] = rbf(s); }
}
__global__ void k_pvB(const float* P, const u16* C, const u16* wuv, float* o) {
  int row = blockIdx.x, b = row >> 5, h = row & 31; int c = threadIdx.x & 511, grp = threadIdx.x >> 9;
  const float* p = P + (size_t)row * 4096; const u16* v = C + (size_t)b * 4096 * 512;
  float a = 0;
  for (int k = grp; k < 4096; k += 2) a += p[k] * bf2f(v[(size_t)k * 512 + c]);
  __shared__ float red[1024];
  red[threadIdx.x] = a; __syncthreads();
  if (threadIdx.x < 512) red[threadIdx.x] += red[threadIdx.x + 512];
  __syncthreads();
  if (threadIdx.x < 128) { float s = 0; for (int cc = 0; cc < 512; ++cc) s += red[cc] * bf2f(wuv[(cc * 32 + h) * 128 + threadIdx.x]); o[row * 128 + threadIdx.x] = rbf(s); }
}
__global__ void k_out(const float* oa, const float* ob, const u16* woa, const u16* wob, __hip_bfloat16* out) {
  int br = blockIdx.y, b = blockIdx.z; const float* o = (br ? ob : oa) + b * 4096; const u16* w = br ? wob : woa;
  int n = blockIdx.x * 64 + (threadIdx.x & 63), grp = threadIdx.x >> 6;
  float a = 0;
  for (int k = grp; k < 4096; k += 16) a += o[k] * bf2f(w[(size_t)k * 576 + n]);
  __shared__ float red[1024];
  red[threadIdx.x] = a; __syncthreads();
  if (grp == 0) { float s = 0; for (int i = 0; i < 16; ++i) s += red[i * 64 + threadIdx.x]; out[b * 1152 + br * 576 + n] = __float2bfloat16(s); }
}

extern "C" void kernel_launch(void* const* d_in, const int* in_sizes, int n_in, void* d_out, int out_size, void* d_ws, size_t ws_size, hipStream_t stream) {
  const u16* Ax = (const u16*)d_in[0]; const u16* Ak = (const u16*)d_in[1]; const u16* Av = (const u16*)d_in[2]; const u16* Awq = (const u16*)d_in[3]; const u16* Awo = (const u16*)d_in[4];
  const u16* Bx = (const u16*)d_in[5]; const u16* Bc = (const u16*)d_in[6]; const u16* Bwq = (const u16*)d_in[7]; const u16* Bwuk = (const u16*)d_in[8]; const u16* Bwuv = (const u16*)d_in[9]; const u16* Bwo = (const u16*)d_in[10];
  float* ws = (float*)d_ws;
  float* qa = ws; float* qb = qa + 16384; float* qabs = qb + 16384; float* S = qabs + 65536; float* oa = S + 256 * 4096; float* ob = oa + 16384;
  k_qproj<<<dim3(16, 2), 256, 0, stream>>>(Ax, Awq, Bx, Bwq, qa, qb);
  k_qabs<<<32, 512, 0, stream>>>(qb, Bwuk, qabs);
  k_scoreA<<<dim3(16, 64), 256, 0, stream>>>(qa, Ak, S);
  k_scoreB<<<dim3(16, 4, 32), 256, 0, stream>>>(qabs, Bc, S + 128 * 4096);
  k_softmax<<<256, 256, 0, stream>>>(S);
  k_pvA<<<128, 1024, 0, stream>>>(S, Av, oa);
  k_pvB<<<128, 1024, 0, stream>>>(S + 128 * 4096, Bc, Bwuv, ob);
  k_out<<<dim3(9, 2, 4), 1024, 0, stream>>>(oa, ob, Awo, Bwo, (__hip_bfloat16*)d_out);
}
```

```cpp
#include <hip/hip_runtime.h>
#include <stdint.h>

#define LAS __attribute__((address_space(3)))
typedef unsigned u32;
typedef u32 u32x2 __attribute__((ext_vector_type(2)));
typedef u32 u32x4 __attribute__((ext_vector_type(4)));
typedef float f32x2 __attribute__((ext_vector_type(2)));
typedef float f32x4 __attribute__((ext_vector_type(4)));
typedef short s16x4 __attribute__((ext_vector_type(4)));
typedef __bf16 bf16x2 __attribute__((ext_vector_type(2)));
typedef __bf16 bf16x8 __attribute__((ext_vector_type(8)));
typedef __amdgpu_buffer_rsrc_t rsrc_t;

#define N0 40
#define N1 40
#define N2 (128 - N0 - N1)
#define POST 4
#define LW_A 28
#define LW_B 28
#define HOLD 12
#define LW_U 44
#define SWAPQ 1
#define HOLD2 40
#define PAUSE_C 1
#define HOLD_ST 1
#define ABL 0
#define POLL_A 6
#define POLL_H 8
#define POLL_B 5
#define AUX_KV 2
#define AUX_W 2

constexpr int SLOT = 18432, NS = 8, RING = SLOT * NS;
constexpr int CTL = RING;
constexpr int W_FULL = CTL, W_QS = CTL + 4, W_QA = CTL + 8, W_MRG = CTL + 12, W_HINT = CTL + 16, W_SGF = CTL + 20, W_OB = CTL + 24, W_BP = CTL + 28, W_FREE = CTL + 32, W_S = CTL + 64;
constexpr int QA_OFF = CTL + 256;
constexpr int OB_OFF = QA_OFF + 512, OB_STR = 576;
constexpr int SB_OFF = OB_OFF + 4 * OB_STR;
constexpr int MG_OFF = SB_OFF + 8192;
constexpr int MS_OFF = MG_OFF + 4096;
constexpr int LDS_BYTES = MS_OFF + 64;
static_assert(LDS_BYTES <= 163840 && (SB_OFF & 15) == 0 && (MG_OFF & 15) == 0, "lds");

constexpr u32 WS_EPOCH = 0, WS_HINT = 1024;
constexpr u32 WS_QG = 4096;
constexpr u32 WS_QAG = WS_QG + 2 * 4 * 2048 * 8;
constexpr u32 WS_AG = WS_QAG + 4 * 32 * 256 * 8;
constexpr u32 AG_STR = 1152;
constexpr u32 WS_SG = WS_AG + 256 * AG_STR;
constexpr u32 WS_CG = WS_SG + 4 * 16 * 32 * 16;
constexpr u32 WS_YG = WS_CG + 16 * 4 * 4 * 256 * 8;
constexpr u32 WS_PB = WS_YG + 2 * 4 * 16 * 4 * 144 * 8;
constexpr u32 WS_END = WS_PB + 4 * 16 * 4 * 16 * 512;
constexpr u32 WS_DIAG = 8u << 20;
static_assert(WS_END <= WS_DIAG && (WS_PB & 127) == 0, "ws");

constexpr float CS = 0.08838834764831845f * 1.4426950408889634f;
constexpr int SC1 = 16;
constexpr u32 SPIN_BUDGET = 1u << 19;

struct Args { const void* in[11]; void* out; void* ws; };

__device__ __forceinline__ rsrc_t mkrs(const void* p, u32 n) { return __builtin_amdgcn_make_buffer_rsrc((void*)p, 0, n, 0x00020000); }
__device__ __forceinline__ u32 swz(u32 r) { return (((r & 7u) ^ ((r >> 1) & 4u)) << 1) | ((r >> 3) & 1u); }
__device__ __forceinline__ f32x4 mfma(u32x4 a, u32x4 b, f32x4 c) {
  return __builtin_amdgcn_mfma_f32_16x16x32_bf16(__builtin_bit_cast(bf16x8, a), __builtin_bit_cast(bf16x8, b), c, 0, 0, 0);
}
__device__ __forceinline__ u32 pk(float lo, float hi) { f32x2 v = {lo, hi}; return __builtin_bit_cast(u32, __builtin_convertvector(v, bf16x2)); }
__device__ __forceinline__ float bflo(u32 v) { return __builtin_bit_cast(float, v << 16); }
__device__ __forceinline__ float bfhi(u32 v) { return __builtin_bit_cast(float, v & 0xffff0000u); }
__device__ __forceinline__ float asf(u32 v) { return __builtin_bit_cast(float, v); }
__device__ __forceinline__ u32 asu(float v) { return __builtin_bit_cast(u32, v); }
__device__ __forceinline__ float ex2(float x) { return __builtin_amdgcn_exp2f(x); }
__device__ __forceinline__ u32x4 ld128(LAS unsigned char* L, u32 a) { return *(LAS u32x4*)(L + a); }
__device__ __forceinline__ u32x4 ldtr2(LAS unsigned char* L, u32 a0, u32 a1) {
  const u32x2 x = __builtin_bit_cast(u32x2, __builtin_amdgcn_ds_read_tr16_b64_v4i16((LAS s16x4*)(L + a0)));
  const u32x2 y = __builtin_bit_cast(u32x2, __builtin_amdgcn_ds_read_tr16_b64_v4i16((LAS s16x4*)(L + a1)));
  return u32x4{x[0], x[1], y[0], y[1]};
}
__device__ __forceinline__ float xg_max(float x) {
  auto s = __builtin_amdgcn_permlane16_swap(asu(x), asu(x), false, false);
  x = fmaxf(asf(s[0]), asf(s[1]));
  auto t = __builtin_amdgcn_permlane32_swap(asu(x), asu(x), false, false);
  return fmaxf(asf(t[0]), asf(t[1]));
}
__device__ __forceinline__ float xg_sum(float x) {
  auto s = __builtin_amdgcn_permlane16_swap(asu(x), asu(x), false, false);
  x = asf(s[0]) + asf(s[1]);
  auto t = __builtin_amdgcn_permlane32_swap(asu(x), asu(x), false, false);
  return asf(t[0]) + asf(t[1]);
}
template <int CTRL> __device__ __forceinline__ float dpp(float x) { return asf(__builtin_amdgcn_mov_dpp(asu(x), CTRL, 0xf, 0xf, true)); }
__device__ __forceinline__ float row_sum(float x) { x += dpp<0x128>(x); x += dpp<0x124>(x); x += dpp<0x122>(x); x += dpp<0x121>(x); return x; }
__device__ __forceinline__ float row_max(float x) { x = fmaxf(x, dpp<0x128>(x)); x = fmaxf(x, dpp<0x124>(x)); x = fmaxf(x, dpp<0x122>(x)); x = fmaxf(x, dpp<0x121>(x)); return x; }

__device__ __forceinline__ u32 cw_ld(u32 a) { u32 v; asm volatile("ds_read_b32 %0, %1\n\ts_waitcnt lgkmcnt(0)" : "=v"(v) : "v"(a) : "memory"); return __builtin_amdgcn_readfirstlane(v); }
__device__ __forceinline__ void cw_st(u32 a, u32 v) { asm volatile("ds_write_b32 %0, %1" ::"v"(a), "v"(v) : "memory"); }
__device__ __forceinline__ void cw_add(u32 a, u32 v, int lane) { if (lane == 0) asm volatile("ds_add_u32 %0, %1" ::"v"(a), "v"(v) : "memory"); asm volatile("" ::: "memory"); }
__device__ __forceinline__ u32 cw_add_rtn(u32 a, u32 v, int lane) {
  u32 r = 0;
  if (lane == 0) asm volatile("ds_add_rtn_u32 %0, %1, %2\n\ts_waitcnt lgkmcnt(0)" : "=v"(r) : "v"(a), "v"(v) : "memory");
  asm volatile("" ::: "memory");
  return __builtin_amdgcn_readfirstlane(r);
}
__device__ __forceinline__ void cw_wait_(u32 a, u32 want, u32& bud) {
  u32 t;
  asm volatile("1:\n\tds_read_b32 v255, %2\n\ts_waitcnt lgkmcnt(0)\n\tv_readfirstlane_b32 %0, v255\n\ts_cmp_ge_u32 %0, %3\n\ts_cbranch_scc1 2f\n\t"
               "s_sleep 1\n\ts_sub_u32 %1, %1, 1\n\ts_cbranch_scc0 1b\n\ts_mov_b32 %1, 0\n2:"
               : "=&s"(t), "+s"(bud) : "v"(a), "s"(want) : "memory", "scc", "v255");
}
#define cw_wait(a, want) cw_wait_((a), (want), bud)
__device__ __forceinline__ bool glb_retry_(u32& bud) {
  asm volatile("s_sleep 2\n\ts_sub_u32 %0, %0, 16\n\ts_cselect_b32 %0, 0, %0" : "+s"(bud) :: "memory", "scc");
  return bud != 0u;
}
#define glb_retry() glb_retry_(bud)
__device__ __forceinline__ u32x4 gld(rsrc_t r, u32 voff, u32 soff) { return __builtin_amdgcn_raw_buffer_load_b128(r, voff, soff, SC1); }
__device__ __forceinline__ void gst(rsrc_t r, u32x4 v, u32 voff, u32 soff) { __builtin_amdgcn_raw_buffer_store_b128(v, r, voff + soff, 0, SC1); }
__device__ __forceinline__ u32 tagx(u32x4 v, u32 tag) { return (v[1] ^ tag) | (v[3] ^ tag); }
__device__ __forceinline__ bool allz(u32 x) { return __builtin_amdgcn_ballot_w64(x != 0u) == 0ull; }
template <int N> __device__ __forceinline__ void fetch(LAS unsigned char* L, rsrc_t r, u32 voff, u32 ss, u32 tag, u32 dst, u32 ds, u32 flag, u32& bud) {
  u32x4 v[N];
  for (;;) {
    u32 bad = 0;
#pragma unroll
    for (int i = 0; i < N; ++i) v[i] = gld(r, voff + i * ss, 0);
#pragma unroll
    for (int i = 0; i < N; ++i) bad |= tagx(v[i], tag);
    if (allz(bad) || !glb_retry()) break;
  }
#pragma unroll
  for (int i = 0; i < N; ++i) *(LAS u32x2*)(L + dst + i * ds) = u32x2{v[i][0], v[i][2]};
  cw_st(flag, 1u);
}
#define DMA(r, ldsoff, voff, soff, imm, aux) __builtin_amdgcn_raw_ptr_buffer_load_lds((r), (LAS void*)(L + (ldsoff)), 16, (voff), (soff), (imm), (aux))

#define STAMP(i) do {} while (0)
#define STAMP_BEGIN() do {} while (0)
#define STAMP_END() do {} while (0)

#define VMW(n) asm volatile("s_waitcnt vmcnt(" #n ")" ::: "memory")
#define ZERO4 f32x4{0.f, 0.f, 0.f, 0.f}
#define MAX8(a, b) fmaxf(fmaxf(fmaxf(a[0], a[1]), fmaxf(a[2], a[3])), fmaxf(fmaxf(b[0], b[1]), fmaxf(b[2], b[3])))
constexpr float LAZY = 192.f;
template <int NA> __device__ __forceinline__ u32x4 soft(const f32x4 s0, const f32x4 s1, float& m, float& l, f32x4* acc) {
  const float mx = MAX8(s0, s1);
  if (__builtin_amdgcn_ballot_w64(mx > m + LAZY) != 0ull) {
    const float mn = fmaxf(m, xg_max(mx)), al = ex2((m - mn) * CS);
    m = mn; l *= al;
#pragma unroll
    for (int i = 0; i < NA; ++i) acc[i] *= al;
  }
  const float mc = -m * CS;
  float pr[8];
#pragma unroll
  for (int i = 0; i < 4; ++i) { pr[i] = ex2(fmaf(s0[i], CS, mc)); pr[4 + i] = ex2(fmaf(s1[i], CS, mc)); }
  l += ((pr[0] + pr[1]) + (pr[2] + pr[3])) + ((pr[4] + pr[5]) + (pr[6] + pr[7]));
  return u32x4{pk(pr[0], pr[1]), pk(pr[2], pr[3]), pk(pr[4], pr[5]), pk(pr[6], pr[7])};
}
__device__ __forceinline__ u32x4 pick2(const f32x4 d0, const f32x4 d1, int q, u32 tag) {
  const float x0 = (q & 2) ? d1[0] : d0[0], x1 = (q & 2) ? d1[1] : d0[1], x2 = (q & 2) ? d1[2] : d0[2], x3 = (q & 2) ? d1[3] : d0[3];
  return u32x4{asu((q & 1) ? x2 : x0), tag, asu((q & 1) ? x3 : x1), tag};
}

constexpr int IT_UK = 2, IT_C = 6, IT_UV = 22, IT_END = 26;
template <int N> __device__ __forceinline__ void vmw() { asm volatile("s_waitcnt vmcnt(%0)" ::"i"(N) : "memory"); }

__global__ void __launch_bounds__(384, 2) mk_kernel(Args a) {
  __shared__ __attribute__((aligned(16))) unsigned char smem[LDS_BYTES];
  LAS unsigned char* L = (LAS unsigned char*)smem;
  const int lane = threadIdx.x & 63, wave = __builtin_amdgcn_readfirstlane(threadIdx.x >> 6);
  const int g = blockIdx.x, gi = g >> 3, hkv = 2 * (g & 7) + (gi >> 4), j = gi & 15, b = j >> 2, s = j & 3;
  const int nkv = s == 0 ? N0 : s == 1 ? N1 : N2;
  constexpr int it0 = 2;
  const int itw = s < 2 ? 2 + nkv - POST : 1 << 20, nit = s < 2 ? nkv + 6 : s == 2 ? nkv + 2 : IT_END;
  const int br0 = (s >> 1) ^ SWAPQ, sl = 2 * b + (s & 1);
  const int bq = (g & 7) >> 1, sp = (g & 1) * 8 + (gi >> 2);
  u32 bud = SPIN_BUDGET;
  STAMP_BEGIN();

  if (__builtin_expect(wave == 0, 1)) {
    __builtin_amdgcn_s_setprio(3);
    {
      const rsrc_t rw = mkrs(a.in[br0 ? 7 : 3], 576u * 8192u), rx = mkrs(a.in[br0 ? 5 : 0], 4u * 576u * 2u);
      const u32 vo = (lane >> 2) * 8192 + (lane & 3) * 16, so = (hkv * 256 + sl * 32) * 2;
#pragma unroll
      for (int i = 0; i < 36; ++i) DMA(rw, i * 1024, vo, so + i * (16 * 8192), 0, AUX_W);
#pragma unroll
      for (int i = 0; i < 5; ++i) DMA(rx, SB_OFF + i * 1024, lane * 16, i * 1024, 0, 0);
      cw_st(CTL + lane * 4, 0u); asm volatile("s_waitcnt lgkmcnt(0)" ::: "memory"); __builtin_amdgcn_s_barrier();
    }
    u32 vq[4];
#pragma unroll
    for (int q = 0; q < 4; ++q) vq[q] = (lane >> 4) * 256 + (((lane & 15) ^ swz(4 * q + (lane >> 4))) << 4);
    int pub = 0, it = it0;
#define SLOT_WAIT() do { if (it >= NS) { const u32 need = 4u * (it >> 3), wa = W_FREE + (it & 7) * 4; \
      if (cw_ld(wa) < need) { VMW(16); if (it - 1 > pub) cw_st(W_FULL, it - 1); VMW(0); pub = it; cw_st(W_FULL, pub); cw_wait(wa, need); } } } while (0)
#define LSTAMP() do { if (it == 4) STAMP(2); if (it == 8) STAMP(3); if (it == 12) STAMP(4); if (it == 16) STAMP(5); if (it == 20) STAMP(6); if (it == 24) STAMP(7); if (it == 28) STAMP(8); if (it == 32) STAMP(9); if (it == 36) STAMP(10); if (it == 40) STAMP(11); if (it == 44) STAMP(12); } while (0)
#define PACE(LW, k) do { vmw<LW>(); if ((k) == ((LW / 4) & 3) && it - LW / 16 > pub) { pub = it - LW / 16; cw_st(W_FULL, pub); } } while (0)
    if (s < 3) {
      const rsrc_t rk = mkrs(a.in[1], 1u << 26), rv = mkrs(a.in[2], 1u << 26);
      u32 so = ((u32)(b * 16 + hkv) * 4096u + (s == 0 ? 0 : s == 1 ? N0 : N0 + N1) * 32u) * 256u;
      asm volatile("" : "+s"(it));
      if (br0 && HOLD_ST) { VMW(0); pub = 2; cw_st(W_FULL, 2); cw_wait(W_QS, 2); }
      else if (br0) vmw<HOLD>(); else vmw<HOLD2>();
#pragma unroll 1
      for (; it < nit; ++it) {
        const u32 sb = (it & 7) * SLOT;
        SLOT_WAIT();
        LSTAMP();
        if ((u32)(it - itw) >= 4u) {
#pragma unroll
          for (int k = 0; k < 4; ++k) {
            const rsrc_t r = k < 2 ? rk : rv;
            PACE(LW_A, k);
            DMA(r, sb + k * 4096, vq[0], so + (k & 1) * 4096, 0, AUX_KV);
            DMA(r, sb + k * 4096, vq[1], so + (k & 1) * 4096, 1024, AUX_KV);
            DMA(r, sb + k * 4096, vq[2], so + (k & 1) * 4096, 2048, AUX_KV);
            DMA(r, sb + k * 4096, vq[3], so + (k & 1) * 4096, 3072, AUX_KV);
          }
          so += 8192u;
        } else {
          const rsrc_t r = mkrs(a.in[s ? 10 : 4], 4096u * 1152u);
          const u32 sw = ((u32)hkv * 256u + (it - itw) * 64u) * 1152u + b * 288u;
          int ln = lane; asm volatile("" : "+v"(ln));
#pragma unroll
          for (int i = 0; i < 18; ++i) { if (i < 16 && (i & 3) == 0) PACE(LW_A, i >> 2); const u32 ci = i * 64 + ln; DMA(r, sb + i * 1024, ci * 16 + ((ci * 3641u) >> 16) * 864u, sw, 0, AUX_W); }
        }
        if (it == 2 && br0 && !HOLD_ST) { VMW(16); pub = 2; cw_st(W_FULL, 2); }
      }
    } else {
      asm volatile("" : "+s"(it));
#pragma unroll 1
      for (; it < nit; ++it) {
        const u32 sb = (it & 7) * SLOT;
        if (PAUSE_C && it == IT_C) { VMW(32); cw_st(W_FULL, it - 2); VMW(16); cw_st(W_FULL, it - 1); VMW(0); pub = it; cw_st(W_FULL, it); cw_wait(W_QS, 7); }
        SLOT_WAIT();
        LSTAMP();
        if ((u32)(it - IT_C) < 16u) {
          const rsrc_t r = mkrs(a.in[6], 1u << 24);
          const u32 so = ((u32)bq * 4096u + sp * 256u + (it - IT_C) * 16u) * 1024u;
#pragma unroll
          for (int k = 0; k < 4; ++k) {
            PACE(LW_B, k);
            DMA(r, sb + k * 4096, (lane ^ swz(4 * k + 0)) << 4, so + k * 4096, 0, AUX_KV);
            DMA(r, sb + k * 4096, (lane ^ swz(4 * k + 1)) << 4, so + k * 4096, 1024, AUX_KV);
            DMA(r, sb + k * 4096, (lane ^ swz(4 * k + 2)) << 4, so + k * 4096, 2048, AUX_KV);
            DMA(r, sb + k * 4096, (lane ^ swz(4 * k + 3)) << 4, so + k * 4096, 3072, AUX_KV);
          }
        } else {
          const rsrc_t r = mkrs(a.in[it < IT_C ? 8 : 9], 512u * 8192u);
          const u32 so = ((u32)b * 128u + ((it - IT_UK) & 3) * 32u) * 8192u + hkv * 512u;
          int ln = lane; asm volatile("" : "+v"(ln));
#pragma unroll
          for (int i = 0; i < 16; ++i) { if ((i & 3) == 0) PACE(LW_B, i >> 2); DMA(r, sb + i * 1024, (ln >> 5) * 8192 + (((ln & 31) ^ swz((2 * i + (ln >> 5)) & 15)) << 4), so + i * (2 * 8192), 0, AUX_W); }
        }
      }
    }
    VMW(32); if (nit - 2 > pub) cw_st(W_FULL, nit - 2);
    VMW(16); if (nit - 1 > pub) cw_st(W_FULL, nit - 1);
    VMW(0); cw_st(W_FULL, nit);
    STAMP(1);
    STAMP_END();
    return;
  }

  const int c = wave - 1, g4 = lane >> 4, n = lane & 15, q = n >> 2, p = lane & 3;
  const rsrc_t rws = mkrs(a.ws, WS_END);
  const u32 tag = *(const __attribute__((address_space(4))) u32*)((const char*)a.ws + WS_EPOCH) + 1u;

  __builtin_amdgcn_s_barrier();
  if (c == 4) {
    cw_wait(W_FULL, s == 3 ? POLL_B : s == 2 ? POLL_A + 2 : POLL_A);
    cw_wait(W_FREE, 4);
    STAMP(1);
    if (s < 3) fetch<1>(L, rws, lane * 16 + WS_QG + (b * 2048 + hkv * 128) * 8, 0, tag, QA_OFF + lane * 8, 0, W_QA, bud);
    else {
      fetch<4>(L, rws, lane * 16 + WS_QG + (8192 + hkv * 128) * 8, 16384, tag, OB_OFF + lane * 8, OB_STR, W_QA, bud);
      STAMP(3);
      cw_wait(W_FULL, POLL_H);
      fetch<1>(L, rws, lane * 16 + WS_HINT, 0, tag, MG_OFF + lane * 8, 0, W_HINT, bud);
      STAMP(4);
      cw_wait(W_BP, 4);
      fetch<4>(L, rws, n * 32 + (g4 & 1) * 16 + WS_SG + hkv * 2048, 512, tag, MG_OFF + lane * 8, 512, W_SGF, bud);
    }
    STAMP(2);
    STAMP_END();
    return;
  }
  if (c < 2) {
    const u32 ab = (8 * g4 + q) * 64 + 32 * c + 8 * p, ax = SB_OFF + p * 1152 + g4 * 16;
    const u32 vo = (n * 2048 + 2 * g4) * 8, so = WS_QG + (br0 * 8192 + hkv * 128 + sl * 16 + 8 * c) * 8;
    cw_wait(W_FULL, 2);
    STAMP(1);
    u32x4 xr[18], wf[18];
#pragma unroll
    for (int ks = 0; ks < 18; ++ks) { wf[ks] = ldtr2(L, ab + ks * 2048, ab + ks * 2048 + 256); xr[ks] = ld128(L, ax + ks * 64); }
    if (lane < 2) asm volatile("ds_add_u32 %0, %1" ::"v"(W_FREE + lane * 4), "v"(2u) : "memory");
    __builtin_amdgcn_sched_barrier(0);
    f32x4 a0 = ZERO4, a1 = ZERO4;
#pragma unroll
    for (int ks = 0; ks < 18; ks += 2) { a0 = mfma(wf[ks], xr[ks], a0); a1 = mfma(wf[ks + 1], xr[ks + 1], a1); }
    a0 += a1;
    if (n < 4) gst(rws, u32x4{pk(a0[0], a0[1]), tag, pk(a0[2], a0[3]), tag}, vo, so);
    cw_add(W_QS, 1, lane);
    STAMP(2);
  }

  if (s < 3) {
    float m = -1e30f, l = 0.f;
    f32x4 acc[8];
#pragma unroll
    for (int i = 0; i < 8; ++i) acc[i] = ZERO4;
    u32x4 qf[4];
    const u32 lpK = n * 256 + ((g4 ^ swz(n)) << 4);
    const u32 lpV = (4 * g4 + q) * 256 + (((p >> 1) ^ swz(4 * g4 + q)) << 4) + 8 * (p & 1);
    bool first = true;
#pragma unroll 1
    for (int it = it0 + ((c + 2) & 3); it < nit; it += 4) {
      if ((u32)(it - itw) < 4u) continue;
      const u32 sb = (it & 7) * SLOT;
      cw_wait(W_FULL, it + 1);
      u32x4 kf[8], vf[8];
#pragma unroll
      for (int kk = 0; kk < 4; ++kk) { const u32 ak = (sb + lpK) ^ (kk << 6); kf[kk] = ld128(L, ak); kf[4 + kk] = ld128(L, ak + 4096); }
#pragma unroll
      for (int ct = 0; ct < 8; ++ct) { const u32 av = (sb + lpV) ^ (ct << 5); vf[ct] = ldtr2(L, av + 8192, av + 12288); }
      cw_add(W_FREE + (it & 7) * 4, 4, lane);
      if (first) {
        first = false;
        cw_wait(W_QA, 1);
        STAMP(3);
#pragma unroll
        for (int kk = 0; kk < 4; ++kk) qf[kk] = ld128(L, QA_OFF + (n & 1) * 256 + kk * 64 + g4 * 16);
      }
      f32x4 s0 = ZERO4, s1 = ZERO4;
#pragma unroll
      for (int kk = 0; kk < 4; ++kk) { s0 = mfma(kf[kk], qf[kk], s0); s1 = mfma(kf[4 + kk], qf[kk], s1); }
      const u32x4 pf = soft<8>(s0, s1, m, l, acc);
#pragma unroll
      for (int ct = 0; ct < 8; ++ct) acc[ct] = mfma(vf[ct], pf, acc[ct]);
    }
    STAMP(8);
    l = xg_sum(l);
    if (n < 2) {
#pragma unroll
      for (int ct = 0; ct < 8; ++ct) *(LAS f32x4*)(L + MG_OFF + (c * 2 + n) * 512 + ct * 64 + g4 * 16) = acc[ct];
      if (g4 == 0) *(LAS f32x2*)(L + MS_OFF + (c * 2 + n) * 8) = f32x2{m, l};
    }
    if (cw_add_rtn(W_MRG, 1, lane) == 3u) {
      const int hd = lane >> 5;
      f32x2 sv[4]; f32x4 ov[4];
#pragma unroll
      for (int i = 0; i < 4; ++i) { sv[i] = *(LAS f32x2*)(L + MS_OFF + (i * 2 + hd) * 8); ov[i] = *(LAS f32x4*)(L + MG_OFF + i * 1024 + lane * 16); }
      const float M = fmaxf(fmaxf(sv[0][0], sv[1][0]), fmaxf(sv[2][0], sv[3][0]));
      float Ls = 0.f; f32x4 o = ZERO4;
#pragma unroll
      for (int i = 0; i < 4; ++i) { const float w = ex2((sv[i][0] - M) * CS); Ls = fmaf(w, sv[i][1], Ls); o += w * ov[i]; }
      o *= 1.f / Ls;
      const u32 so = WS_AG + (hkv * 16 + j) * AG_STR;
      gst(rws, u32x4{pk(o[0], o[1]), tag, pk(o[2], o[3]), tag}, lane * 16, so);
      if ((lane & 31) == 0) gst(rws, u32x4{asu(M), tag, asu(Ls), tag}, 1024 + hd * 16, so);
    }
    STAMP(9);
    if (s < 2) {
      cw_wait(W_FULL, itw + 4);
      u32x4 wf[24];
      {
        const u32 lp = (8 * g4 + q) * 288 + 8 * p + 32 * c;
#pragma unroll
        for (int ks = 0; ks < 8; ++ks) {
          const u32 ad = ((itw + (ks >> 1)) & 7) * SLOT + (ks & 1) * 9216 + lp;
          wf[ks] = ldtr2(L, ad, ad + 1152); wf[8 + ks] = ldtr2(L, ad + 128, ad + 1280);
          if (c == 0) wf[16 + ks] = ldtr2(L, ad + 256, ad + 1408);
        }
      }
      STAMP(10);
      f32x4 o = ZERO4;
      if (s == 0) {
        u32x4 ov[3], sv[3];
        for (;;) {
          u32 bad = 0;
#pragma unroll
          for (int i = 0; i < 3; ++i) { const u32 so = WS_AG + (hkv * 16 + c * 4 + i) * AG_STR; ov[i] = gld(rws, lane * 16, so); sv[i] = gld(rws, 1024 + (lane >> 5) * 16, so); }
#pragma unroll
          for (int i = 0; i < 3; ++i) bad |= tagx(ov[i], tag) | tagx(sv[i], tag);
          if (allz(bad) || !glb_retry()) break;
        }
        const float M = fmaxf(fmaxf(asf(sv[0][0]), asf(sv[1][0])), asf(sv[2][0]));
        float Ls = 0.f;
#pragma unroll
        for (int i = 0; i < 3; ++i) { const float w = ex2((asf(sv[i][0]) - M) * CS) * asf(sv[i][2]); Ls += w; o += w * f32x4{bflo(ov[i][0]), bfhi(ov[i][0]), bflo(ov[i][2]), bfhi(ov[i][2])}; }
        o *= 1.f / Ls;
      } else {
        u32x4 cv[8];
        for (;;) {
          u32 bad = 0;
#pragma unroll
          for (int i = 0; i < 8; ++i) cv[i] = gld(rws, lane * 32 + (i & 1) * 16, WS_CG + ((hkv * 4 + (i >> 1)) * 1024 + c * 256) * 8);
#pragma unroll
          for (int i = 0; i < 8; ++i) bad |= tagx(cv[i], tag);
          if (allz(bad) || !glb_retry()) break;
        }
#pragma unroll
        for (int i = 0; i < 4; ++i) o += f32x4{asf(cv[2 * i][0]), asf(cv[2 * i][2]), asf(cv[2 * i + 1][0]), asf(cv[2 * i + 1][2])};
      }
      STAMP(11);
      *(LAS u32x2*)(L + OB_OFF + c * OB_STR + lane * 8) = u32x2{pk(o[0], o[1]), pk(o[2], o[3])};
      cw_add(W_OB, 1, lane);
      cw_wait(W_OB, 4);
      f32x4 d0 = ZERO4, d1 = ZERO4, d2 = ZERO4;
#pragma unroll
      for (int ks = 0; ks < 8; ++ks) {
        const u32x4 of = ld128(L, OB_OFF + p * OB_STR + ks * 64 + g4 * 16);
        d0 = mfma(wf[ks], of, d0); d1 = mfma(wf[8 + ks], of, d1);
        if (c == 0) d2 = mfma(wf[16 + ks], of, d2);
      }
      {
        const u32 vo = p * 1152 + g4 * 32 + (q & 1) * 16, so = WS_YG + (((s * 4 + b) * 16 + hkv) * 576 + c * 16) * 8;
        gst(rws, pick2(d0, d1, q, tag), vo + (q >> 1) * 512, so);
        if (c == 0 && q < 2) gst(rws, pick2(d2, d2, q, tag), vo + 1024, so);
      }
      STAMP(12);
    }
    STAMP_END();
    return;
  }

  {
    cw_wait(W_FULL, IT_UK + c + 1);
    u32x4 wu[16];
    {
      const u32 lp = (IT_UK + c) * SLOT + n * 512 + ((g4 ^ swz(n)) << 4);
#pragma unroll
      for (int i = 0; i < 16; ++i) wu[i] = ld128(L, (lp ^ ((i & 3) << 6)) + ((i >> 2) & 1) * 8192 + (i >> 3) * 256);
    }
    cw_add(W_FREE + (IT_UK + c) * 4, 4, lane);
    STAMP(15);
    cw_wait(W_QA, 1);
    STAMP(3);
    f32x4 d[4] = {ZERO4, ZERO4, ZERO4, ZERO4};
#pragma unroll
    for (int i = 0; i < 8; ++i) {
      const u32x4 qv = ld128(L, OB_OFF + p * OB_STR + i * 64 + g4 * 16);
      d[(i >> 2) * 2] = mfma(wu[(i >> 2) * 8 + (i & 3)], qv, d[(i >> 2) * 2]); d[(i >> 2) * 2 + 1] = mfma(wu[(i >> 2) * 8 + 4 + (i & 3)], qv, d[(i >> 2) * 2 + 1]);
    }
    const f32x4 e0 = (q & 1) ? d[1] : d[0], e1 = (q & 1) ? d[3] : d[2], e = (q & 2) ? e1 : e0;
    gst(rws, u32x4{pk(e[0], e[1]), tag, pk(e[2], e[3]), tag}, p * 65536 + (g4 & 1) * 128 + (q >> 1) * 64 + (q & 1) * 32 + (g4 >> 1) * 16, WS_QAG + hkv * 4096 + (b * 4 + c) * 256);
    if (cw_add_rtn(W_QS, 1, lane) == 5u) { if (lane == 0) gst(rws, u32x4{tag, tag, tag, tag}, WS_HINT + (hkv * 4 + b) * 16, 0); cw_add(W_QS, 1, lane); }
    STAMP(4);
  }
  {
    const int ch = c & 1;
    u32x4 qa[8];
    {
      const u32 so = WS_QAG + bq * 65536 + (c >> 1) * 32768 + ch * 2048;
      cw_wait(W_HINT, 1);
      STAMP(5);
      u32x4 t[16];
      for (;;) {
        u32 bad = 0;
#pragma unroll
        for (int i = 0; i < 16; ++i) t[i] = gld(rws, (n >> 1) * 4096 + (n & 1) * 64 + g4 * 16 + i * 128, so);
#pragma unroll
        for (int i = 0; i < 16; ++i) bad |= tagx(t[i], tag);
        if (allz(bad) || !glb_retry()) break;
      }
#pragma unroll
      for (int i = 0; i < 8; ++i) qa[i] = u32x4{t[2 * i][0], t[2 * i][2], t[2 * i + 1][0], t[2 * i + 1][2]};
    }
    STAMP(6);
    float mb = -1e30f, lb = 0.f;
    f32x4 ob[16];
#pragma unroll
    for (int i = 0; i < 16; ++i) ob[i] = ZERO4;
    const u32 lpR = n * 1024 + ((g4 ^ swz(n)) << 4) + ch * 512;
    const u32 lpC = (4 * g4 + q) * 1024 + (((p >> 1) ^ swz(4 * g4 + q)) << 4) + 8 * (p & 1) + ch * 512;
#pragma unroll 1
    for (int st = 0; st < 8; ++st) {
      const int it = IT_C + 2 * st;
      const u32 sb = (it & 7) * SLOT;
      const u32 xw = ((st & 1) ? (4 + c) * SLOT + 16384 : SB_OFF + c * 2048) + lane * 16, xr = ((st & 1) ? (4 + (c ^ 1)) * SLOT + 16384 : SB_OFF + (c ^ 1) * 2048) + lane * 16;
      cw_wait(W_FULL, it + 2);
      u32x4 rf[16], cf[16];
#pragma unroll
      for (int kk = 0; kk < 8; ++kk) { const u32 ak = ((sb + lpR) ^ ((kk & 3) << 6)) + (kk >> 2) * 256; rf[2 * kk] = ld128(L, ak); rf[2 * kk + 1] = ld128(L, ak + SLOT); }
      __builtin_amdgcn_sched_barrier(0);
      f32x4 s0 = ZERO4, s1 = ZERO4;
#pragma unroll
      for (int kk = 0; kk < 8; ++kk) {
        s0 = mfma(rf[2 * kk], qa[kk], s0); s1 = mfma(rf[2 * kk + 1], qa[kk], s1);
        const u32 av = (sb + lpC) ^ (kk << 5);
        cf[kk] = ldtr2(L, av, av + SLOT); cf[8 + kk] = ldtr2(L, av + 256, av + 256 + SLOT);
        __builtin_amdgcn_sched_barrier(0);
      }
      *(LAS f32x4*)(L + xw) = s0;
      *(LAS f32x4*)(L + xw + 1024) = s1;
      cw_st(W_S + c * 4, st + 1);
      if (lane < 2) asm volatile("ds_add_u32 %0, %1" ::"v"(W_FREE + (it & 7) * 4 + lane * 4), "v"(1u) : "memory");
      cw_wait(W_S + (c ^ 1) * 4, st + 1);
      s0 += *(LAS f32x4*)(L + xr); s1 += *(LAS f32x4*)(L + xr + 1024);
      const u32x4 pf = soft<16>(s0, s1, mb, lb, ob);
#pragma unroll
      for (int ct = 0; ct < 16; ++ct) ob[ct] = mfma(cf[ct], pf, ob[ct]);
      if (st == 3) STAMP(16);
      if (st == 4) STAMP(17);
    }
    STAMP(7);
#pragma unroll
    for (int i = 0; i < 8; ++i) {
      auto x = __builtin_amdgcn_permlane16_swap(pk(ob[2 * i][0], ob[2 * i][1]), pk(ob[2 * i + 1][0], ob[2 * i + 1][1]), false, false);
      auto y = __builtin_amdgcn_permlane16_swap(pk(ob[2 * i][2], ob[2 * i][3]), pk(ob[2 * i + 1][2], ob[2 * i + 1][3]), false, false);
      gst(rws, u32x4{x[0], y[0], x[1], y[1]}, (n >> 1) * 512 + (n & 1) * 64 + (g4 & 1) * 32 + (g4 >> 1) * 16 + (i & 3) * 128,
          WS_PB + ((bq * 16 + sp) * 4 + ch * 2 + (i >> 2)) * 8192 + (c >> 1) * 4096);
    }
    lb = xg_sum(lb);
    if (g4 == 0 && ch == 0) *(LAS f32x2*)(L + QA_OFF + ((c >> 1) * 16 + n) * 8) = f32x2{mb, lb};
    VMW(0);
    if (cw_add_rtn(W_BP, 1, lane) == 3u) {
      const f32x2 ml = *(LAS f32x2*)(L + QA_OFF + (lane & 31) * 8);
      if (lane < 32) gst(rws, u32x4{asu(ml[0]), tag, asu(ml[1]), tag}, (lane >> 1) * 2048 + (lane & 1) * 16, WS_SG + (bq * 16 + sp) * 32);
    }
    STAMP(8);
  }
  {
    cw_wait(W_SGF, 1);
    STAMP(10);
    u32x4 pv[8];
#pragma unroll
    for (int i = 0; i < 8; ++i) pv[i] = gld(rws, (lane >> 5) * 32768 + (lane & 31) * 16, WS_PB + ((c * 16 + 2 * i) * 4 + b) * 8192 + hkv * 512);
    const f32x2 st = *(LAS f32x2*)(L + MG_OFF + c * 512 + lane * 8);
    const float w = ex2((st[0] - row_max(st[0])) * CS), cf = w / row_sum(w * st[1]);
    f32x4 o0 = ZERO4, o1 = ZERO4;
#pragma unroll
    for (int i = 0; i < 8; ++i) {
      const float f = asf(__builtin_amdgcn_ds_bpermute((((lane >> 2) & 1) * 16 + 2 * i + (lane >> 5)) * 4, asu(cf)));
      o0 += f * f32x4{bflo(pv[i][0]), bfhi(pv[i][0]), bflo(pv[i][1]), bfhi(pv[i][1])};
      o1 += f * f32x4{bflo(pv[i][2]), bfhi(pv[i][2]), bflo(pv[i][3]), bfhi(pv[i][3])};
    }
#pragma unroll
    for (int i = 0; i < 4; ++i) {
      auto t0 = __builtin_amdgcn_permlane32_swap(asu(o0[i]), asu(o0[i]), false, false); o0[i] = asf(t0[0]) + asf(t0[1]);
      auto t1 = __builtin_amdgcn_permlane32_swap(asu(o1[i]), asu(o1[i]), false, false); o1[i] = asf(t1[0]) + asf(t1[1]);
    }
    if (lane < 32) *(LAS u32x4*)(L + OB_OFF + c * OB_STR + ((lane >> 2) & 1) * 256 + (lane >> 3) * 64 + p * 16) = u32x4{pk(o0[0], o0[1]), pk(o0[2], o0[3]), pk(o1[0], o1[1]), pk(o1[2], o1[3])};
    cw_add(W_OB, 1, lane);
    cw_wait(W_FULL, IT_UV + 4);
    u32x4 wf[16];
    {
      const u32 r0 = 8 * (g4 & 1) + q;
      const u32 lp0 = (8 * g4 + q) * 512 + ((((p >> 1) + 8 * c) ^ swz(r0)) << 4) + 8 * (p & 1);
      const u32 lp1 = (8 * g4 + q + 4) * 512 + ((((p >> 1) + 8 * c) ^ swz(r0 + 4)) << 4) + 8 * (p & 1);
#pragma unroll
      for (int i = 0; i < 16; ++i) wf[i] = ldtr2(L, (lp0 ^ ((i & 3) << 5)) + ((IT_UV + (i >> 2)) & 7) * SLOT, (lp1 ^ ((i & 3) << 5)) + ((IT_UV + (i >> 2)) & 7) * SLOT);
    }
    cw_wait(W_OB, 4);
    STAMP(11);
    f32x4 d[4] = {ZERO4, ZERO4, ZERO4, ZERO4};
#pragma unroll
    for (int ks = 0; ks < 4; ++ks) {
      const u32x4 of = ld128(L, OB_OFF + p * OB_STR + (c >> 1) * 256 + ks * 64 + g4 * 16);
#pragma unroll
      for (int t = 0; t < 4; ++t) d[t] = mfma(wf[ks * 4 + t], of, d[t]);
    }
    {
      const u32 vo = p * 2048 + (q >> 1) * 128 + g4 * 32 + (q & 1) * 16, so = WS_CG + ((hkv * 4 + b) * 1024 + c * 64) * 8;
      gst(rws, pick2(d[0], d[1], q, tag), vo, so); gst(rws, pick2(d[2], d[3], q, tag), vo + 256, so);
    }
    STAMP(12);
  }
  if (c < 2) {
    const int bb = hkv >> 2, c0 = 36 * (hkv & 3);
    u32x4 yv[5];
    for (;;) {
      u32 bad = 0;
#pragma unroll
      for (int i = 0; i < 5; ++i) { const int pi = i < 4 ? 4 * i + g4 : 16 + (g4 & 1); yv[i] = gld(rws, (n * 576 + 2 * pi) * 8, WS_YG + ((c * 4 + b) * 16 * 576 + bb * 144 + c0) * 8); }
#pragma unroll
      for (int i = 0; i < 5; ++i) bad |= tagx(yv[i], tag);
      if (allz(bad) || !glb_retry()) break;
    }
    STAMP(13);
    const rsrc_t ro = mkrs(a.out, 4u * 1152u * 2u);
#pragma unroll
    for (int i = 0; i < 5; ++i) {
      const int pi = i < 4 ? 4 * i + g4 : 16 + (g4 & 1);
      const u32 r = pk(row_sum(asf(yv[i][0])), row_sum(asf(yv[i][2])));
      if (n == 0 && (i < 4 || g4 < 2)) __builtin_amdgcn_raw_buffer_store_b32(r, ro, pi * 4, (bb * 1152 + c * 576 + b * 144 + c0) * 2, 0);
    }
    if (g == 24 && c == 0 && lane == 0) __builtin_amdgcn_raw_buffer_store_b32(tag, rws, 0, WS_EPOCH, SC1);
    STAMP(14);
  }
  STAMP_END();
}

extern "C" void kernel_launch(void* const* d_in, const int* in_sizes, int n_in, void* d_out, int out_size, void* d_ws, size_t ws_size, hipStream_t stream) {
  Args a;
  for (int i = 0; i < 11; ++i) a.in[i] = d_in[i];
  a.out = d_out;
  a.ws = d_ws;
  mk_kernel<<<256, 384, 0, stream>>>(a);
}
```

```cpp
#include <hip/hip_runtime.h>
#include <stdint.h>

#define LAS __attribute__((address_space(3)))
typedef unsigned u32;
typedef u32 u32x2 __attribute__((ext_vector_type(2)));
typedef u32 u32x4 __attribute__((ext_vector_type(4)));
typedef float f32x2 __attribute__((ext_vector_type(2)));
typedef float f32x4 __attribute__((ext_vector_type(4)));
typedef short s16x4 __attribute__((ext_vector_type(4)));
typedef __bf16 bf16x2 __attribute__((ext_vector_type(2)));
typedef __bf16 bf16x8 __attribute__((ext_vector_type(8)));
typedef __amdgpu_buffer_rsrc_t rsrc_t;

#define N0 40
#define N1 40
#define N2 (128 - N0 - N1)
#define POST 4
#define LW_A 28
#define LW_B 28
#define HOLD 12
#define LW_U 44
#define SWAPQ 1
#define HOLD2 40
#define PAUSE_C 1
#define HOLD_ST 1
#define ABL 0
#define POLL_A 6
#define POLL_H 8
#define POLL_B 5
#define AUX_K 2
#define AUX_V 2
#define AUX_C 2
#define AUX_W 2

constexpr int SLOT = 18432, NS = 8, RING = SLOT * NS;
constexpr int CTL = RING;
constexpr int W_FULL = CTL, W_QS = CTL + 4, W_QA = CTL + 8, W_MRG = CTL + 12, W_HINT = CTL + 16, W_SGF = CTL + 20, W_OB = CTL + 24, W_BP = CTL + 28, W_FREE = CTL + 32, W_S = CTL + 64;
constexpr int QA_OFF = CTL + 256;
constexpr int OB_OFF = QA_OFF + 512, OB_STR = 576;
constexpr int SB_OFF = OB_OFF + 4 * OB_STR;
constexpr int MG_OFF = SB_OFF + 8192;
constexpr int MS_OFF = MG_OFF + 4096;
constexpr int LDS_BYTES = MS_OFF + 64;
static_assert(LDS_BYTES <= 163840 && (SB_OFF & 15) == 0 && (MG_OFF & 15) == 0, "lds");

constexpr u32 WS_EPOCH = 0, WS_HINT = 1024;
constexpr u32 WS_QG = 4096;
constexpr u32 WS_QAG = WS_QG + 2 * 4 * 2048 * 8;
constexpr u32 WS_AG = WS_QAG + 4 * 32 * 256 * 8;
constexpr u32 AG_STR = 1152;
constexpr u32 WS_SG = WS_AG + 256 * AG_STR;
constexpr u32 WS_CG = WS_SG + 4 * 16 * 32 * 16;
constexpr u32 WS_YG = WS_CG + 16 * 4 * 4 * 256 * 8;
constexpr u32 WS_PB = WS_YG + 2 * 4 * 16 * 4 * 144 * 8;
constexpr u32 WS_END = WS_PB + 4 * 16 * 4 * 16 * 512;
constexpr u32 WS_DIAG = 8u << 20;
static_assert(WS_END <= WS_DIAG && (WS_PB & 127) == 0, "ws");

constexpr float CS = 0.08838834764831845f * 1.4426950408889634f;
constexpr int SC1 = 16;
constexpr u32 SPIN_BUDGET = 1u << 19;

struct Args { const void* in[11]; void* out; void* ws; };

__device__ __forceinline__ rsrc_t mkrs(const void* p, u32 n) { return __builtin_amdgcn_make_buffer_rsrc((void*)p, 0, n, 0x00020000); }
__device__ __forceinline__ u32 swz(u32 r) { return (((r & 7u) ^ ((r >> 1) & 4u)) << 1) | ((r >> 3) & 1u); }
__device__ __forceinline__ f32x4 mfma(u32x4 a, u32x4 b, f32x4 c) {
  return __builtin_amdgcn_mfma_f32_16x16x32_bf16(__builtin_bit_cast(bf16x8, a), __builtin_bit_cast(bf16x8, b), c, 0, 0, 0);
}
__device__ __forceinline__ u32 pk(float lo, float hi) { f32x2 v = {lo, hi}; return __builtin_bit_cast(u32, __builtin_convertvector(v, bf16x2)); }
__device__ __forceinline__ float bflo(u32 v) { return __builtin_bit_cast(float, v << 16); }
__device__ __forceinline__ float bfhi(u32 v) { return __builtin_bit_cast(float, v & 0xffff0000u); }
__device__ __forceinline__ float asf(u32 v) { return __builtin_bit_cast(float, v); }
__device__ __forceinline__ u32 asu(float v) { return __builtin_bit_cast(u32, v); }
__device__ __forceinline__ float ex2(float x) { return __builtin_amdgcn_exp2f(x); }
__device__ __forceinline__ u32x4 ld128(LAS unsigned char* L, u32 a) { return *(LAS u32x4*)(L + a); }
__device__ __forceinline__ u32x4 ldtr2(LAS unsigned char* L, u32 a0, u32 a1) {
  const u32x2 x = __builtin_bit_cast(u32x2, __builtin_amdgcn_ds_read_tr16_b64_v4i16((LAS s16x4*)(L + a0)));
  const u32x2 y = __builtin_bit_cast(u32x2, __builtin_amdgcn_ds_read_tr16_b64_v4i16((LAS s16x4*)(L + a1)));
  return u32x4{x[0], x[1], y[0], y[1]};
}
__device__ __forceinline__ float xg_max(float x) {
  auto s = __builtin_amdgcn_permlane16_swap(asu(x), asu(x), false, false);
  x = fmaxf(asf(s[0]), asf(s[1]));
  auto t = __builtin_amdgcn_permlane32_swap(asu(x), asu(x), false, false);
  return fmaxf(asf(t[0]), asf(t[1]));
}
__device__ __forceinline__ float xg_sum(float x) {
  auto s = __builtin_amdgcn_permlane16_swap(asu(x), asu(x), false, false);
  x = asf(s[0]) + asf(s[1]);
  auto t = __builtin_amdgcn_permlane32_swap(asu(x), asu(x), false, false);
  return asf(t[0]) + asf(t[1]);
}
template <int CTRL> __device__ __forceinline__ float dpp(float x) { return asf(__builtin_amdgcn_mov_dpp(asu(x), CTRL, 0xf, 0xf, true)); }
__device__ __forceinline__ float row_sum(float x) { x += dpp<0x128>(x); x += dpp<0x124>(x); x += dpp<0x122>(x); x += dpp<0x121>(x); return x; }
__device__ __forceinline__ float row_max(float x) { x = fmaxf(x, dpp<0x128>(x)); x = fmaxf(x, dpp<0x124>(x)); x = fmaxf(x, dpp<0x122>(x)); x = fmaxf(x, dpp<0x121>(x)); return x; }

__device__ __forceinline__ u32 cw_ld(u32 a) { u32 v; asm volatile("ds_read_b32 %0, %1\n\ts_waitcnt lgkmcnt(0)" : "=v"(v) : "v"(a) : "memory"); return __builtin_amdgcn_readfirstlane(v); }
__device__ __forceinline__ void cw_st(u32 a, u32 v) { asm volatile("ds_write_b32 %0, %1" ::"v"(a), "v"(v) : "memory"); }
__device__ __forceinline__ void cw_add(u32 a, u32 v, int lane) { if (lane == 0) asm volatile("ds_add_u32 %0, %1" ::"v"(a), "v"(v) : "memory"); asm volatile("" ::: "memory"); }
__device__ __forceinline__ u32 cw_add_rtn(u32 a, u32 v, int lane) {
  u32 r = 0;
  if (lane == 0) asm volatile("ds_add_rtn_u32 %0, %1, %2\n\ts_waitcnt lgkmcnt(0)" : "=v"(r) : "v"(a), "v"(v) : "memory");
  asm volatile("" ::: "memory");
  return __builtin_amdgcn_readfirstlane(r);
}
__device__ __forceinline__ void cw_wait_(u32 a, u32 want, u32& bud) {
  u32 t;
  asm volatile("1:\n\tds_read_b32 v255, %2\n\ts_waitcnt lgkmcnt(0)\n\tv_readfirstlane_b32 %0, v255\n\ts_cmp_ge_u32 %0, %3\n\ts_cbranch_scc1 2f\n\t"
               "s_sleep 1\n\ts_sub_u32 %1, %1, 1\n\ts_cbranch_scc0 1b\n\ts_mov_b32 %1, 0\n2:"
               : "=&s"(t), "+s"(bud) : "v"(a), "s"(want) : "memory", "scc", "v255");
}
#define cw_wait(a, want) cw_wait_((a), (want), bud)
__device__ __forceinline__ bool glb_retry_(u32& bud) {
  asm volatile("s_sleep 2\n\ts_sub_u32 %0, %0, 16\n\ts_cselect_b32 %0, 0, %0" : "+s"(bud) :: "memory", "scc");
  return bud != 0u;
}
#define glb_retry() glb_retry_(bud)
__device__ __forceinline__ u32x4 gld(rsrc_t r, u32 voff, u32 soff) { return __builtin_amdgcn_raw_buffer_load_b128(r, voff, soff, SC1); }
__device__ __forceinline__ void gst(rsrc_t r, u32x4 v, u32 voff, u32 soff) { __builtin_amdgcn_raw_buffer_store_b128(v, r, voff + soff, 0, SC1); }
__device__ __forceinline__ u32 tagx(u32x4 v, u32 tag) { return (v[1] ^ tag) | (v[3] ^ tag); }
__device__ __forceinline__ bool allz(u32 x) { return __builtin_amdgcn_ballot_w64(x != 0u) == 0ull; }
template <int N> __device__ __forceinline__ void fetch(LAS unsigned char* L, rsrc_t r, u32 voff, u32 ss, u32 tag, u32 dst, u32 ds, u32 flag, u32& bud) {
  u32x4 v[N];
  for (;;) {
    u32 bad = 0;
#pragma unroll
    for (int i = 0; i < N; ++i) v[i] = gld(r, voff + i * ss, 0);
#pragma unroll
    for (int i = 0; i < N; ++i) bad |= tagx(v[i], tag);
    if (allz(bad) || !glb_retry()) break;
  }
#pragma unroll
  for (int i = 0; i < N; ++i) *(LAS u32x2*)(L + dst + i * ds) = u32x2{v[i][0], v[i][2]};
  cw_st(flag, 1u);
}
#define DMA(r, ldsoff, voff, soff, imm, aux) __builtin_amdgcn_raw_ptr_buffer_load_lds((r), (LAS void*)(L + (ldsoff)), 16, (voff), (soff), (imm), (aux))

#define STAMP(i) do {} while (0)
#define STAMP_BEGIN() do {} while (0)
#define STAMP_END() do {} while (0)

#define VMW(n) asm volatile("s_waitcnt vmcnt(" #n ")" ::: "memory")
#define ZERO4 f32x4{0.f, 0.f, 0.f, 0.f}
#define MAX8(a, b) fmaxf(fmaxf(fmaxf(a[0], a[1]), fmaxf(a[2], a[3])), fmaxf(fmaxf(b[0], b[1]), fmaxf(b[2], b[3])))
constexpr float LAZY = 192.f;
template <int NA> __device__ __forceinline__ u32x4 soft(const f32x4 s0, const f32x4 s1, float& m, float& l, f32x4* acc) {
  const float mx = MAX8(s0, s1);
  if (__builtin_amdgcn_ballot_w64(mx > m + LAZY) != 0ull) {
    const float mn = fmaxf(m, xg_max(mx)), al = ex2((m - mn) * CS);
    m = mn; l *= al;
#pragma unroll
    for (int i = 0; i < NA; ++i) acc[i] *= al;
  }
  const float mc = -m * CS;
  float pr[8];
#pragma unroll
  for (int i = 0; i < 4; ++i) { pr[i] = ex2(fmaf(s0[i], CS, mc)); pr[4 + i] = ex2(fmaf(s1[i], CS, mc)); }
  l += ((pr[0] + pr[1]) + (pr[2] + pr[3])) + ((pr[4] + pr[5]) + (pr[6] + pr[7]));
  return u32x4{pk(pr[0], pr[1]), pk(pr[2], pr[3]), pk(pr[4], pr[5]), pk(pr[6], pr[7])};
}
__device__ __forceinline__ u32x4 pick2(const f32x4 d0, const f32x4 d1, int q, u32 tag) {
  const float x0 = (q & 2) ? d1[0] : d0[0], x1 = (q & 2) ? d1[1] : d0[1], x2 = (q & 2) ? d1[2] : d0[2], x3 = (q & 2) ? d1[3] : d0[3];
  return u32x4{asu((q & 1) ? x2 : x0), tag, asu((q & 1) ? x3 : x1), tag};
}

constexpr int IT_UK = 2, IT_C = 6, IT_UV = 22, IT_END = 26;
template <int N> __device__ __forceinline__ void vmw() { asm volatile("s_waitcnt vmcnt(%0)" ::"i"(N) : "memory"); }

__global__ void __launch_bounds__(384, 2) mk_kernel(Args a) {
  __shared__ __attribute__((aligned(16))) unsigned char smem[LDS_BYTES];
  LAS unsigned char* L = (LAS unsigned char*)smem;
  const int lane = threadIdx.x & 63, wave = __builtin_amdgcn_readfirstlane(threadIdx.x >> 6);
  const int g = blockIdx.x, gi = g >> 3, hkv = 2 * (g & 7) + (gi >> 4), j = gi & 15, b = j >> 2, s = j & 3;
  const int nkv = s == 0 ? N0 : s == 1 ? N1 : N2;
  constexpr int it0 = 2;
  const int itw = s < 2 ? 2 + nkv - POST : 1 << 20, nit = s < 2 ? nkv + 6 : s == 2 ? nkv + 2 : IT_END;
  const int br0 = (s >> 1) ^ SWAPQ, sl = 2 * b + (s & 1);
  const int bq = (g & 7) >> 1, sp = (g & 1) * 8 + (gi >> 2);
  u32 bud = SPIN_BUDGET;
  STAMP_BEGIN();

  if (__builtin_expect(wave == 0, 1)) {
    __builtin_amdgcn_s_setprio(3);
    {
      const rsrc_t rw = mkrs(a.in[br0 ? 7 : 3], 576u * 8192u), rx = mkrs(a.in[br0 ? 5 : 0], 4u * 576u * 2u);
      const u32 vo = (lane >> 2) * 8192 + (lane & 3) * 16, so = (hkv * 256 + sl * 32) * 2;
#pragma unroll
      for (int i = 0; i < 36; ++i) DMA(rw, i * 1024, vo, so + i * (16 * 8192), 0, AUX_W);
#pragma unroll
      for (int i = 0; i < 5; ++i) DMA(rx, SB_OFF + i * 1024, lane * 16, i * 1024, 0, 0);
      cw_st(CTL + lane * 4, 0u); asm volatile("s_waitcnt lgkmcnt(0)" ::: "memory"); __builtin_amdgcn_s_barrier();
    }
    u32 vq[4];
#pragma unroll
    for (int q = 0; q < 4; ++q) vq[q] = (lane >> 4) * 256 + (((lane & 15) ^ swz(4 * q + (lane >> 4))) << 4);
    int pub = 0, it = it0;
#define SLOT_WAIT() do { if (it >= NS) { const u32 need = 4u * (it >> 3), wa = W_FREE + (it & 7) * 4; \
      if (cw_ld(wa) < need) { VMW(16); if (it - 1 > pub) cw_st(W_FULL, it - 1); VMW(0); pub = it; cw_st(W_FULL, pub); cw_wait(wa, need); } } } while (0)
#define LSTAMP() do { if (it == 4) STAMP(2); if (it == 8) STAMP(3); if (it == 12) STAMP(4); if (it == 16) STAMP(5); if (it == 20) STAMP(6); if (it == 24) STAMP(7); if (it == 28) STAMP(8); if (it == 32) STAMP(9); if (it == 36) STAMP(10); if (it == 40) STAMP(11); if (it == 44) STAMP(12); } while (0)
#define PACE(LW, k) do { vmw<LW>(); if ((k) == ((LW / 4) & 3) && it - LW / 16 > pub) { pub = it - LW / 16; cw_st(W_FULL, pub); } } while (0)
    if (s < 3) {
      const rsrc_t rk = mkrs(a.in[1], 1u << 26), rv = mkrs(a.in[2], 1u << 26);
      u32 so = ((u32)(b * 16 + hkv) * 4096u + (s == 0 ? 0 : s == 1 ? N0 : N0 + N1) * 32u) * 256u;
      asm volatile("" : "+s"(it));
      if (br0 && HOLD_ST) { VMW(0); pub = 2; cw_st(W_FULL, 2); cw_wait(W_QS, 2); }
      else if (br0) vmw<HOLD>(); else vmw<HOLD2>();
#pragma unroll 1
      for (; it < nit; ++it) {
        const u32 sb = (it & 7) * SLOT;
        SLOT_WAIT();
        LSTAMP();
        if ((u32)(it - itw) >= 4u) {
#pragma unroll
          for (int k = 0; k < 2; ++k) {
            PACE(LW_A, k);
            DMA(rk, sb + k * 4096, vq[0], so + k * 4096, 0, AUX_K); DMA(rk, sb + k * 4096, vq[1], so + k * 4096, 1024, AUX_K);
            DMA(rk, sb + k * 4096, vq[2], so + k * 4096, 2048, AUX_K); DMA(rk, sb + k * 4096, vq[3], so + k * 4096, 3072, AUX_K);
          }
#pragma unroll
          for (int k = 0; k < 2; ++k) {
            PACE(LW_A, k + 2);
            DMA(rv, sb + 8192 + k * 4096, vq[0], so + k * 4096, 0, AUX_V); DMA(rv, sb + 8192 + k * 4096, vq[1], so + k * 4096, 1024, AUX_V);
            DMA(rv, sb + 8192 + k * 4096, vq[2], so + k * 4096, 2048, AUX_V); DMA(rv, sb + 8192 + k * 4096, vq[3], so + k * 4096, 3072, AUX_V);
          }
          so += 8192u;
        } else {
          const rsrc_t r = mkrs(a.in[s ? 10 : 4], 4096u * 1152u);
          const u32 sw = ((u32)hkv * 256u + (it - itw) * 64u) * 1152u + b * 288u;
          int ln = lane; asm volatile("" : "+v"(ln));
#pragma unroll
          for (int i = 0; i < 18; ++i) { if (i < 16 && (i & 3) == 0) PACE(LW_A, i >> 2); const u32 ci = i * 64 + ln; DMA(r, sb + i * 1024, ci * 16 + ((ci * 3641u) >> 16) * 864u, sw, 0, AUX_W); }
        }
        if (it == 2 && br0 && !HOLD_ST) { VMW(16); pub = 2; cw_st(W_FULL, 2); }
      }
    } else {
      asm volatile("" : "+s"(it));
#pragma unroll 1
      for (; it < nit; ++it) {
        const u32 sb = (it & 7) * SLOT;
        if (PAUSE_C && it == IT_C) { VMW(32); cw_st(W_FULL, it - 2); VMW(16); cw_st(W_FULL, it - 1); VMW(0); pub = it; cw_st(W_FULL, it); cw_wait(W_QS, 7); }
        SLOT_WAIT();
        LSTAMP();
        if ((u32)(it - IT_C) < 16u) {
          const rsrc_t r = mkrs(a.in[6], 1u << 24);
          const u32 so = ((u32)bq * 4096u + sp * 256u + (it - IT_C) * 16u) * 1024u;
#pragma unroll
          for (int k = 0; k < 4; ++k) {
            PACE(LW_B, k);
            DMA(r, sb + k * 4096, (lane ^ swz(4 * k + 0)) << 4, so + k * 4096, 0, AUX_C);
            DMA(r, sb + k * 4096, (lane ^ swz(4 * k + 1)) << 4, so + k * 4096, 1024, AUX_C);
            DMA(r, sb + k * 4096, (lane ^ swz(4 * k + 2)) << 4, so + k * 4096, 2048, AUX_C);
            DMA(r, sb + k * 4096, (lane ^ swz(4 * k + 3)) << 4, so + k * 4096, 3072, AUX_C);
          }
        } else {
          const rsrc_t r = mkrs(a.in[it < IT_C ? 8 : 9], 512u * 8192u);
          const u32 so = ((u32)b * 128u + ((it - IT_UK) & 3) * 32u) * 8192u + hkv * 512u;
          int ln = lane; asm volatile("" : "+v"(ln));
#pragma unroll
          for (int i = 0; i < 16; ++i) { if ((i & 3) == 0) PACE(LW_B, i >> 2); DMA(r, sb + i * 1024, (ln >> 5) * 8192 + (((ln & 31) ^ swz((2 * i + (ln >> 5)) & 15)) << 4), so + i * (2 * 8192), 0, AUX_W); }
        }
      }
    }
    VMW(32); if (nit - 2 > pub) cw_st(W_FULL, nit - 2);
    VMW(16); if (nit - 1 > pub) cw_st(W_FULL, nit - 1);
    VMW(0); cw_st(W_FULL, nit);
    STAMP(1);
    STAMP_END();
    return;
  }

  const int c = wave - 1, g4 = lane >> 4, n = lane & 15, q = n >> 2, p = lane & 3;
  const rsrc_t rws = mkrs(a.ws, WS_END);
  const u32 tag = *(const __attribute__((address_space(4))) u32*)((const char*)a.ws + WS_EPOCH) + 1u;

  __builtin_amdgcn_s_barrier();
  if (c == 4) {
    cw_wait(W_FULL, s == 3 ? POLL_B : s == 2 ? POLL_A + 2 : POLL_A);
    cw_wait(W_FREE, 4);
    STAMP(1);
    if (s < 3) fetch<1>(L, rws, lane * 16 + WS_QG + (b * 2048 + hkv * 128) * 8, 0, tag, QA_OFF + lane * 8, 0, W_QA, bud);
    else {
      fetch<4>(L, rws, lane * 16 + WS_QG + (8192 + hkv * 128) * 8, 16384, tag, OB_OFF + lane * 8, OB_STR, W_QA, bud);
      STAMP(3);
      cw_wait(W_FULL, POLL_H);
      fetch<1>(L, rws, lane * 16 + WS_HINT, 0, tag, MG_OFF + lane * 8, 0, W_HINT, bud);
      STAMP(4);
      cw_wait(W_BP, 4);
      fetch<4>(L, rws, n * 32 + (g4 & 1) * 16 + WS_SG + hkv * 2048, 512, tag, MG_OFF + lane * 8, 512, W_SGF, bud);
    }
    STAMP(2);
    STAMP_END();
    return;
  }
  if (c < 2) {
    const u32 ab = (8 * g4 + q) * 64 + 32 * c + 8 * p, ax = SB_OFF + p * 1152 + g4 * 16;
    const u32 vo = (n * 2048 + 2 * g4) * 8, so = WS_QG + (br0 * 8192 + hkv * 128 + sl * 16 + 8 * c) * 8;
    cw_wait(W_FULL, 2);
    STAMP(1);
    u32x4 xr[18], wf[18];
#pragma unroll
    for (int ks = 0; ks < 18; ++ks) { wf[ks] = ldtr2(L, ab + ks * 2048, ab + ks * 2048 + 256); xr[ks] = ld128(L, ax + ks * 64); }
    if (lane < 2) asm volatile("ds_add_u32 %0, %1" ::"v"(W_FREE + lane * 4), "v"(2u) : "memory");
    __builtin_amdgcn_sched_barrier(0);
    f32x4 a0 = ZERO4, a1 = ZERO4;
#pragma unroll
    for (int ks = 0; ks < 18; ks += 2) { a0 = mfma(wf[ks], xr[ks], a0); a1 = mfma(wf[ks + 1], xr[ks + 1], a1); }
    a0 += a1;
    if (n < 4) gst(rws, u32x4{pk(a0[0], a0[1]), tag, pk(a0[2], a0[3]), tag}, vo, so);
    cw_add(W_QS, 1, lane);
    STAMP(2);
  }

  if (s < 3) {
    float m = -1e30f, l = 0.f;
    f32x4 acc[8];
#pragma unroll
    for (int i = 0; i < 8; ++i) acc[i] = ZERO4;
    u32x4 qf[4];
    const u32 lpK = n * 256 + ((g4 ^ swz(n)) << 4);
    const u32 lpV = (4 * g4 + q) * 256 + (((p >> 1) ^ swz(4 * g4 + q)) << 4) + 8 * (p & 1);
    bool first = true;
#pragma unroll 1
    for (int it = it0 + ((c + 2) & 3); it < nit; it += 4) {
      if ((u32)(it - itw) < 4u) continue;
      const u32 sb = (it & 7) * SLOT;
      cw_wait(W_FULL, it + 1);
      u32x4 kf[8], vf[8];
#pragma unroll
      for (int kk = 0; kk < 4; ++kk) { const u32 ak = (sb + lpK) ^ (kk << 6); kf[kk] = ld128(L, ak); kf[4 + kk] = ld128(L, ak + 4096); }
#pragma unroll
      for (int ct = 0; ct < 8; ++ct) { const u32 av = (sb + lpV) ^ (ct << 5); vf[ct] = ldtr2(L, av + 8192, av + 12288); }
      cw_add(W_FREE + (it & 7) * 4, 4, lane);
      if (first) {
        first = false;
        cw_wait(W_QA, 1);
        STAMP(3);
#pragma unroll
        for (int kk = 0; kk < 4; ++kk) qf[kk] = ld128(L, QA_OFF + (n & 1) * 256 + kk * 64 + g4 * 16);
      }
      f32x4 s0 = ZERO4, s1 = ZERO4;
#pragma unroll
      for (int kk = 0; kk < 4; ++kk) { s0 = mfma(kf[kk], qf[kk], s0); s1 = mfma(kf[4 + kk], qf[kk], s1); }
      const u32x4 pf = soft<8>(s0, s1, m, l, acc);
#pragma unroll
      for (int ct = 0; ct < 8; ++ct) acc[ct] = mfma(vf[ct], pf, acc[ct]);
    }
    STAMP(8);
    l = xg_sum(l);
    if (n < 2) {
#pragma unroll
      for (int ct = 0; ct < 8; ++ct) *(LAS f32x4*)(L + MG_OFF + (c * 2 + n) * 512 + ct * 64 + g4 * 16) = acc[ct];
      if (g4 == 0) *(LAS f32x2*)(L + MS_OFF + (c * 2 + n) * 8) = f32x2{m, l};
    }
    if (cw_add_rtn(W_MRG, 1, lane) == 3u) {
      const int hd = lane >> 5;
      f32x2 sv[4]; f32x4 ov[4];
#pragma unroll
      for (int i = 0; i < 4; ++i) { sv[i] = *(LAS f32x2*)(L + MS_OFF + (i * 2 + hd) * 8); ov[i] = *(LAS f32x4*)(L + MG_OFF + i * 1024 + lane * 16); }
      const float M = fmaxf(fmaxf(sv[0][0], sv[1][0]), fmaxf(sv[2][0], sv[3][0]));
      float Ls = 0.f; f32x4 o = ZERO4;
#pragma unroll
      for (int i = 0; i < 4; ++i) { const float w = ex2((sv[i][0] - M) * CS); Ls = fmaf(w, sv[i][1], Ls); o += w * ov[i]; }
      o *= 1.f / Ls;
      const u32 so = WS_AG + (hkv * 16 + j) * AG_STR;
      gst(rws, u32x4{pk(o[0], o[1]), tag, pk(o[2], o[3]), tag}, lane * 16, so);
      if ((lane & 31) == 0) gst(rws, u32x4{asu(M), tag, asu(Ls), tag}, 1024 + hd * 16, so);
    }
    STAMP(9);
    if (s < 2) {
      cw_wait(W_FULL, itw + 4);
      u32x4 wf[24];
      {
        const u32 lp = (8 * g4 + q) * 288 + 8 * p + 32 * c;
#pragma unroll
        for (int ks = 0; ks < 8; ++ks) {
          const u32 ad = ((itw + (ks >> 1)) & 7) * SLOT + (ks & 1) * 9216 + lp;
          wf[ks] = ldtr2(L, ad, ad + 1152); wf[8 + ks] = ldtr2(L, ad + 128, ad + 1280);
          if (c == 0) wf[16 + ks] = ldtr2(L, ad + 256, ad + 1408);
        }
      }
      STAMP(10);
      f32x4 o = ZERO4;
      if (s == 0) {
        u32x4 ov[3], sv[3];
        for (;;) {
          u32 bad = 0;
#pragma unroll
          for (int i = 0; i < 3; ++i) { const u32 so = WS_AG + (hkv * 16 + c * 4 + i) * AG_STR; ov[i] = gld(rws, lane * 16, so); sv[i] = gld(rws, 1024 + (lane >> 5) * 16, so); }
#pragma unroll
          for (int i = 0; i < 3; ++i) bad |= tagx(ov[i], tag) | tagx(sv[i], tag);
          if (allz(bad) || !glb_retry()) break;
        }
        const float M = fmaxf(fmaxf(asf(sv[0][0]), asf(sv[1][0])), asf(sv[2][0]));
        float Ls = 0.f;
#pragma unroll
        for (int i = 0; i < 3; ++i) { const float w = ex2((asf(sv[i][0]) - M) * CS) * asf(sv[i][2]); Ls += w; o += w * f32x4{bflo(ov[i][0]), bfhi(ov[i][0]), bflo(ov[i][2]), bfhi(ov[i][2])}; }
        o *= 1.f / Ls;
      } else {
        u32x4 cv[8];
        for (;;) {
          u32 bad = 0;
#pragma unroll
          for (int i = 0; i < 8; ++i) cv[i] = gld(rws, lane * 32 + (i & 1) * 16, WS_CG + ((hkv * 4 + (i >> 1)) * 1024 + c * 256) * 8);
#pragma unroll
          for (int i = 0; i < 8; ++i) bad |= tagx(cv[i], tag);
          if (allz(bad) || !glb_retry()) break;
        }
#pragma unroll
        for (int i = 0; i < 4; ++i) o += f32x4{asf(cv[2 * i][0]), asf(cv[2 * i][2]), asf(cv[2 * i + 1][0]), asf(cv[2 * i + 1][2])};
      }
      STAMP(11);
      *(LAS u32x2*)(L + OB_OFF + c * OB_STR + lane * 8) = u32x2{pk(o[0], o[1]), pk(o[2], o[3])};
      cw_add(W_OB, 1, lane);
      cw_wait(W_OB, 4);
      f32x4 d0 = ZERO4, d1 = ZERO4, d2 = ZERO4;
#pragma unroll
      for (int ks = 0; ks < 8; ++ks) {
        const u32x4 of = ld128(L, OB_OFF + p * OB_STR + ks * 64 + g4 * 16);
        d0 = mfma(wf[ks], of, d0); d1 = mfma(wf[8 + ks], of, d1);
        if (c == 0) d2 = mfma(wf[16 + ks], of, d2);
      }
      {
        const u32 vo = p * 1152 + g4 * 32 + (q & 1) * 16, so = WS_YG + (((s * 4 + b) * 16 + hkv) * 576 + c * 16) * 8;
        gst(rws, pick2(d0, d1, q, tag), vo + (q >> 1) * 512, so);
        if (c == 0 && q < 2) gst(rws, pick2(d2, d2, q, tag), vo + 1024, so);
      }
      STAMP(12);
    }
    STAMP_END();
    return;
  }

  {
    cw_wait(W_FULL, IT_UK + c + 1);
    u32x4 wu[16];
    {
      const u32 lp = (IT_UK + c) * SLOT + n * 512 + ((g4 ^ swz(n)) << 4);
#pragma unroll
      for (int i = 0; i < 16; ++i) wu[i] = ld128(L, (lp ^ ((i & 3) << 6)) + ((i >> 2) & 1) * 8192 + (i >> 3) * 256);
    }
    cw_add(W_FREE + (IT_UK + c) * 4, 4, lane);
    STAMP(15);
    cw_wait(W_QA, 1);
    STAMP(3);
    f32x4 d[4] = {ZERO4, ZERO4, ZERO4, ZERO4};
#pragma unroll
    for (int i = 0; i < 8; ++i) {
      const u32x4 qv = ld128(L, OB_OFF + p * OB_STR + i * 64 + g4 * 16);
      d[(i >> 2) * 2] = mfma(wu[(i >> 2) * 8 + (i & 3)], qv, d[(i >> 2) * 2]); d[(i >> 2) * 2 + 1] = mfma(wu[(i >> 2) * 8 + 4 + (i & 3)], qv, d[(i >> 2) * 2 + 1]);
    }
    const f32x4 e0 = (q & 1) ? d[1] : d[0], e1 = (q & 1) ? d[3] : d[2], e = (q & 2) ? e1 : e0;
    gst(rws, u32x4{pk(e[0], e[1]), tag, pk(e[2], e[3]), tag}, p * 65536 + (g4 & 1) * 128 + (q >> 1) * 64 + (q & 1) * 32 + (g4 >> 1) * 16, WS_QAG + hkv * 4096 + (b * 4 + c) * 256);
    if (cw_add_rtn(W_QS, 1, lane) == 5u) { if (lane == 0) gst(rws, u32x4{tag, tag, tag, tag}, WS_HINT + (hkv * 4 + b) * 16, 0); cw_add(W_QS, 1, lane); }
    STAMP(4);
  }
  {
    const int ch = c & 1;
    u32x4 qa[8];
    {
      const u32 so = WS_QAG + bq * 65536 + (c >> 1) * 32768 + ch * 2048;
      cw_wait(W_HINT, 1);
      STAMP(5);
      u32x4 t[16];
      for (;;) {
        u32 bad = 0;
#pragma unroll
        for (int i = 0; i < 16; ++i) t[i] = gld(rws, (n >> 1) * 4096 + (n & 1) * 64 + g4 * 16 + i * 128, so);
#pragma unroll
        for (int i = 0; i < 16; ++i) bad |= tagx(t[i], tag);
        if (allz(bad) || !glb_retry()) break;
      }
#pragma unroll
      for (int i = 0; i < 8; ++i) qa[i] = u32x4{t[2 * i][0], t[2 * i][2], t[2 * i + 1][0], t[2 * i + 1][2]};
    }
    STAMP(6);
    float mb = -1e30f, lb = 0.f;
    f32x4 ob[16];
#pragma unroll
    for (int i = 0; i < 16; ++i) ob[i] = ZERO4;
    const u32 lpR = n * 1024 + ((g4 ^ swz(n)) << 4) + ch * 512;
    const u32 lpC = (4 * g4 + q) * 1024 + (((p >> 1) ^ swz(4 * g4 + q)) << 4) + 8 * (p & 1) + ch * 512;
#pragma unroll 1
    for (int st = 0; st < 8; ++st) {
      const int it = IT_C + 2 * st;
      const u32 sb = (it & 7) * SLOT;
      const u32 xw = ((st & 1) ? (4 + c) * SLOT + 16384 : SB_OFF + c * 2048) + lane * 16, xr = ((st & 1) ? (4 + (c ^ 1)) * SLOT + 16384 : SB_OFF + (c ^ 1) * 2048) + lane * 16;
      cw_wait(W_FULL, it + 2);
      u32x4 rf[16], cf[16];
#pragma unroll
      for (int kk = 0; kk < 8; ++kk) { const u32 ak = ((sb + lpR) ^ ((kk & 3) << 6)) + (kk >> 2) * 256; rf[2 * kk] = ld128(L, ak); rf[2 * kk + 1] = ld128(L, ak + SLOT); }
      __builtin_amdgcn_sched_barrier(0);
      f32x4 s0 = ZERO4, s1 = ZERO4;
#pragma unroll
      for (int kk = 0; kk < 8; ++kk) {
        s0 = mfma(rf[2 * kk], qa[kk], s0); s1 = mfma(rf[2 * kk + 1], qa[kk], s1);
        const u32 av = (sb + lpC) ^ (kk << 5);
        cf[kk] = ldtr2(L, av, av + SLOT); cf[8 + kk] = ldtr2(L, av + 256, av + 256 + SLOT);
        __builtin_amdgcn_sched_barrier(0);
      }
      *(LAS f32x4*)(L + xw) = s0;
      *(LAS f32x4*)(L + xw + 1024) = s1;
      cw_st(W_S + c * 4, st + 1);
      if (lane < 2) asm volatile("ds_add_u32 %0, %1" ::"v"(W_FREE + (it & 7) * 4 + lane * 4), "v"(1u) : "memory");
      cw_wait(W_S + (c ^ 1) * 4, st + 1);
      s0 += *(LAS f32x4*)(L + xr); s1 += *(LAS f32x4*)(L + xr + 1024);
      const u32x4 pf = soft<16>(s0, s1, mb, lb, ob);
#pragma unroll
      for (int ct = 0; ct < 16; ++ct) ob[ct] = mfma(cf[ct], pf, ob[ct]);
      if (st == 3) STAMP(16);
      if (st == 4) STAMP(17);
    }
    STAMP(7);
#pragma unroll
    for (int i = 0; i < 8; ++i) {
      auto x = __builtin_amdgcn_permlane16_swap(pk(ob[2 * i][0], ob[2 * i][1]), pk(ob[2 * i + 1][0], ob[2 * i + 1][1]), false, false);
      auto y = __builtin_amdgcn_permlane16_swap(pk(ob[2 * i][2], ob[2 * i][3]), pk(ob[2 * i + 1][2], ob[2 * i + 1][3]), false, false);
      gst(rws, u32x4{x[0], y[0], x[1], y[1]}, (n >> 1) * 512 + (n & 1) * 64 + (g4 & 1) * 32 + (g4 >> 1) * 16 + (i & 3) * 128,
          WS_PB + ((bq * 16 + sp) * 4 + ch * 2 + (i >> 2)) * 8192 + (c >> 1) * 4096);
    }
    lb = xg_sum(lb);
    if (g4 == 0 && ch == 0) *(LAS f32x2*)(L + QA_OFF + ((c >> 1) * 16 + n) * 8) = f32x2{mb, lb};
    VMW(0);
    if (cw_add_rtn(W_BP, 1, lane) == 3u) {
      const f32x2 ml = *(LAS f32x2*)(L + QA_OFF + (lane & 31) * 8);
      if (lane < 32) gst(rws, u32x4{asu(ml[0]), tag, asu(ml[1]), tag}, (lane >> 1) * 2048 + (lane & 1) * 16, WS_SG + (bq * 16 + sp) * 32);
    }
    STAMP(8);
  }
  {
    cw_wait(W_SGF, 1);
    STAMP(10);
    u32x4 pv[8];
#pragma unroll
    for (int i = 0; i < 8; ++i) pv[i] = gld(rws, (lane >> 5) * 32768 + (lane & 31) * 16, WS_PB + ((c * 16 + 2 * i) * 4 + b) * 8192 + hkv * 512);
    const f32x2 st = *(LAS f32x2*)(L + MG_OFF + c * 512 + lane * 8);
    const float w = ex2((st[0] - row_max(st[0])) * CS), cf = w / row_sum(w * st[1]);
    f32x4 o0 = ZERO4, o1 = ZERO4;
#pragma unroll
    for (int i = 0; i < 8; ++i) {
      const float f = asf(__builtin_amdgcn_ds_bpermute((((lane >> 2) & 1) * 16 + 2 * i + (lane >> 5)) * 4, asu(cf)));
      o0 += f * f32x4{bflo(pv[i][0]), bfhi(pv[i][0]), bflo(pv[i][1]), bfhi(pv[i][1])};
      o1 += f * f32x4{bflo(pv[i][2]), bfhi(pv[i][2]), bflo(pv[i][3]), bfhi(pv[i][3])};
    }
#pragma unroll
    for (int i = 0; i < 4; ++i) {
      auto t0 = __builtin_amdgcn_permlane32_swap(asu(o0[i]), asu(o0[i]), false, false); o0[i] = asf(t0[0]) + asf(t0[1]);
      auto t1 = __builtin_amdgcn_permlane32_swap(asu(o1[i]), asu(o1[i]), false, false); o1[i] = asf(t1[0]) + asf(t1[1]);
    }
    if (lane < 32) *(LAS u32x4*)(L + OB_OFF + c * OB_STR + ((lane >> 2) & 1) * 256 + (lane >> 3) * 64 + p * 16) = u32x4{pk(o0[0], o0[1]), pk(o0[2], o0[3]), pk(o1[0], o1[1]), pk(o1[2], o1[3])};
    cw_add(W_OB, 1, lane);
    cw_wait(W_FULL, IT_UV + 4);
    u32x4 wf[16];
    {
      const u32 r0 = 8 * (g4 & 1) + q;
      const u32 lp0 = (8 * g4 + q) * 512 + ((((p >> 1) + 8 * c) ^ swz(r0)) << 4) + 8 * (p & 1);
      const u32 lp1 = (8 * g4 + q + 4) * 512 + ((((p >> 1) + 8 * c) ^ swz(r0 + 4)) << 4) + 8 * (p & 1);
#pragma unroll
      for (int i = 0; i < 16; ++i) wf[i] = ldtr2(L, (lp0 ^ ((i & 3) << 5)) + ((IT_UV + (i >> 2)) & 7) * SLOT, (lp1 ^ ((i & 3) << 5)) + ((IT_UV + (i >> 2)) & 7) * SLOT);
    }
    cw_wait(W_OB, 4);
    STAMP(11);
    f32x4 d[4] = {ZERO4, ZERO4, ZERO4, ZERO4};
#pragma unroll
    for (int ks = 0; ks < 4; ++ks) {
      const u32x4 of = ld128(L, OB_OFF + p * OB_STR + (c >> 1) * 256 + ks * 64 + g4 * 16);
#pragma unroll
      for (int t = 0; t < 4; ++t) d[t] = mfma(wf[ks * 4 + t], of, d[t]);
    }
    {
      const u32 vo = p * 2048 + (q >> 1) * 128 + g4 * 32 + (q & 1) * 16, so = WS_CG + ((hkv * 4 + b) * 1024 + c * 64) * 8;
      gst(rws, pick2(d[0], d[1], q, tag), vo, so); gst(rws, pick2(d[2], d[3], q, tag), vo + 256, so);
    }
    STAMP(12);
  }
  if (c < 2) {
    const int bb = hkv >> 2, c0 = 36 * (hkv & 3);
    u32x4 yv[5];
    for (;;) {
      u32 bad = 0;
#pragma unroll
      for (int i = 0; i < 5; ++i) { const int pi = i < 4 ? 4 * i + g4 : 16 + (g4 & 1); yv[i] = gld(rws, (n * 576 + 2 * pi) * 8, WS_YG + ((c * 4 + b) * 16 * 576 + bb * 144 + c0) * 8); }
#pragma unroll
      for (int i = 0; i < 5; ++i) bad |= tagx(yv[i], tag);
      if (allz(bad) || !glb_retry()) break;
    }
    STAMP(13);
    const rsrc_t ro = mkrs(a.out, 4u * 1152u * 2u);
#pragma unroll
    for (int i = 0; i < 5; ++i) {
      const int pi = i < 4 ? 4 * i + g4 : 16 + (g4 & 1);
      const u32 r = pk(row_sum(asf(yv[i][0])), row_sum(asf(yv[i][2])));
      if (n == 0 && (i < 4 || g4 < 2)) __builtin_amdgcn_raw_buffer_store_b32(r, ro, pi * 4, (bb * 1152 + c * 576 + b * 144 + c0) * 2, 0);
    }
    if (g == 24 && c == 0 && lane == 0) __builtin_amdgcn_raw_buffer_store_b32(tag, rws, 0, WS_EPOCH, SC1);
    STAMP(14);
  }
  STAMP_END();
}

extern "C" void kernel_launch(void* const* d_in, const int* in_sizes, int n_in, void* d_out, int out_size, void* d_ws, size_t ws_size, hipStream_t stream) {
  Args a;
  for (int i = 0; i < 11; ++i) a.in[i] = d_in[i];
  a.out = d_out;
  a.ws = d_ws;
  mk_kernel<<<256, 384, 0, stream>>>(a);
}
```

```cpp
#define DIAG 1
#include <stdio.h>
#include <hip/hip_runtime.h>
#include <stdint.h>

#define LAS __attribute__((address_space(3)))
typedef unsigned u32;
typedef u32 u32x2 __attribute__((ext_vector_type(2)));
typedef u32 u32x4 __attribute__((ext_vector_type(4)));
typedef float f32x2 __attribute__((ext_vector_type(2)));
typedef float f32x4 __attribute__((ext_vector_type(4)));
typedef short s16x4 __attribute__((ext_vector_type(4)));
typedef __bf16 bf16x2 __attribute__((ext_vector_type(2)));
typedef __bf16 bf16x8 __attribute__((ext_vector_type(8)));
typedef __amdgpu_buffer_rsrc_t rsrc_t;

#define N0 40
#define N1 40
#define N2 (128 - N0 - N1)
#define POST 4
#define LW_A 28
#define LW_B 28
#define HOLD 12
#define LW_U 44
#define SWAPQ 1
#define HOLD2 40
#define PAUSE_C 1
#define HOLD_ST 1
#define ABL 0
#define POLL_A 6
#define POLL_H 8
#define POLL_B 5
#define AUX_K 2
#define AUX_V 2
#define AUX_C 2
#define AUX_W 2

constexpr int SLOT = 18432, NS = 8, RING = SLOT * NS;
constexpr int CTL = RING;
constexpr int W_FULL = CTL, W_QS = CTL + 4, W_QA = CTL + 8, W_MRG = CTL + 12, W_HINT = CTL + 16, W_SGF = CTL + 20, W_OB = CTL + 24, W_BP = CTL + 28, W_FREE = CTL + 32, W_S = CTL + 64;
constexpr int QA_OFF = CTL + 256;
constexpr int OB_OFF = QA_OFF + 512, OB_STR = 576;
constexpr int SB_OFF = OB_OFF + 4 * OB_STR;
constexpr int MG_OFF = SB_OFF + 8192;
constexpr int MS_OFF = MG_OFF + 4096;
constexpr int LDS_BYTES = MS_OFF + 64;
static_assert(LDS_BYTES <= 163840 && (SB_OFF & 15) == 0 && (MG_OFF & 15) == 0, "lds");

constexpr u32 WS_EPOCH = 0, WS_HINT = 1024;
constexpr u32 WS_QG = 4096;
constexpr u32 WS_QAG = WS_QG + 2 * 4 * 2048 * 8;
constexpr u32 WS_AG = WS_QAG + 4 * 32 * 256 * 8;
constexpr u32 AG_STR = 1152;
constexpr u32 WS_SG = WS_AG + 256 * AG_STR;
constexpr u32 WS_CG = WS_SG + 4 * 16 * 32 * 16;
constexpr u32 WS_YG = WS_CG + 16 * 4 * 4 * 256 * 8;
constexpr u32 WS_PB = WS_YG + 2 * 4 * 16 * 4 * 144 * 8;
constexpr u32 WS_END = WS_PB + 4 * 16 * 4 * 16 * 512;
constexpr u32 WS_DIAG = 8u << 20;
static_assert(WS_END <= WS_DIAG && (WS_PB & 127) == 0, "ws");

constexpr float CS = 0.08838834764831845f * 1.4426950408889634f;
constexpr int SC1 = 16;
constexpr u32 SPIN_BUDGET = 1u << 19;

struct Args { const void* in[11]; void* out; void* ws; };

__device__ __forceinline__ rsrc_t mkrs(const void* p, u32 n) { return __builtin_amdgcn_make_buffer_rsrc((void*)p, 0, n, 0x00020000); }
__device__ __forceinline__ u32 swz(u32 r) { return (((r & 7u) ^ ((r >> 1) & 4u)) << 1) | ((r >> 3) & 1u); }
__device__ __forceinline__ f32x4 mfma(u32x4 a, u32x4 b, f32x4 c) {
  return __builtin_amdgcn_mfma_f32_16x16x32_bf16(__builtin_bit_cast(bf16x8, a), __builtin_bit_cast(bf16x8, b), c, 0, 0, 0);
}
__device__ __forceinline__ u32 pk(float lo, float hi) { f32x2 v = {lo, hi}; return __builtin_bit_cast(u32, __builtin_convertvector(v, bf16x2)); }
__device__ __forceinline__ float bflo(u32 v) { return __builtin_bit_cast(float, v << 16); }
__device__ __forceinline__ float bfhi(u32 v) { return __builtin_bit_cast(float, v & 0xffff0000u); }
__device__ __forceinline__ float asf(u32 v) { return __builtin_bit_cast(float, v); }
__device__ __forceinline__ u32 asu(float v) { return __builtin_bit_cast(u32, v); }
__device__ __forceinline__ float ex2(float x) { return __builtin_amdgcn_exp2f(x); }
__device__ __forceinline__ u32x4 ld128(LAS unsigned char* L, u32 a) { return *(LAS u32x4*)(L + a); }
__device__ __forceinline__ u32x4 ldtr2(LAS unsigned char* L, u32 a0, u32 a1) {
  const u32x2 x = __builtin_bit_cast(u32x2, __builtin_amdgcn_ds_read_tr16_b64_v4i16((LAS s16x4*)(L + a0)));
  const u32x2 y = __builtin_bit_cast(u32x2, __builtin_amdgcn_ds_read_tr16_b64_v4i16((LAS s16x4*)(L + a1)));
  return u32x4{x[0], x[1], y[0], y[1]};
}
__device__ __forceinline__ float xg_max(float x) {
  auto s = __builtin_amdgcn_permlane16_swap(asu(x), asu(x), false, false);
  x = fmaxf(asf(s[0]), asf(s[1]));
  auto t = __builtin_amdgcn_permlane32_swap(asu(x), asu(x), false, false);
  return fmaxf(asf(t[0]), asf(t[1]));
}
__device__ __forceinline__ float xg_sum(float x) {
  auto s = __builtin_amdgcn_permlane16_swap(asu(x), asu(x), false, false);
  x = asf(s[0]) + asf(s[1]);
  auto t = __builtin_amdgcn_permlane32_swap(asu(x), asu(x), false, false);
  return asf(t[0]) + asf(t[1]);
}
template <int CTRL> __device__ __forceinline__ float dpp(float x) { return asf(__builtin_amdgcn_mov_dpp(asu(x), CTRL, 0xf, 0xf, true)); }
__device__ __forceinline__ float row_sum(float x) { x += dpp<0x128>(x); x += dpp<0x124>(x); x += dpp<0x122>(x); x += dpp<0x121>(x); return x; }
__device__ __forceinline__ float row_max(float x) { x = fmaxf(x, dpp<0x128>(x)); x = fmaxf(x, dpp<0x124>(x)); x = fmaxf(x, dpp<0x122>(x)); x = fmaxf(x, dpp<0x121>(x)); return x; }

__device__ __forceinline__ u32 cw_ld(u32 a) { u32 v; asm volatile("ds_read_b32 %0, %1\n\ts_waitcnt lgkmcnt(0)" : "=v"(v) : "v"(a) : "memory"); return __builtin_amdgcn_readfirstlane(v); }
__device__ __forceinline__ void cw_st(u32 a, u32 v) { asm volatile("ds_write_b32 %0, %1" ::"v"(a), "v"(v) : "memory"); }
__device__ __forceinline__ void cw_add(u32 a, u32 v, int lane) { if (lane == 0) asm volatile("ds_add_u32 %0, %1" ::"v"(a), "v"(v) : "memory"); asm volatile("" ::: "memory"); }
__device__ __forceinline__ u32 cw_add_rtn(u32 a, u32 v, int lane) {
  u32 r = 0;
  if (lane == 0) asm volatile("ds_add_rtn_u32 %0, %1, %2\n\ts_waitcnt lgkmcnt(0)" : "=v"(r) : "v"(a), "v"(v) : "memory");
  asm volatile("" ::: "memory");
  return __builtin_amdgcn_readfirstlane(r);
}
__device__ __forceinline__ void cw_wait_(u32 a, u32 want, u32& bud) {
  u32 t;
  asm volatile("1:\n\tds_read_b32 v255, %2\n\ts_waitcnt lgkmcnt(0)\n\tv_readfirstlane_b32 %0, v255\n\ts_cmp_ge_u32 %0, %3\n\ts_cbranch_scc1 2f\n\t"
               "s_sleep 1\n\ts_sub_u32 %1, %1, 1\n\ts_cbranch_scc0 1b\n\ts_mov_b32 %1, 0\n2:"
               : "=&s"(t), "+s"(bud) : "v"(a), "s"(want) : "memory", "scc", "v255");
}
#define cw_wait(a, want) cw_wait_((a), (want), bud)
__device__ __forceinline__ bool glb_retry_(u32& bud) {
  asm volatile("s_sleep 2\n\ts_sub_u32 %0, %0, 16\n\ts_cselect_b32 %0, 0, %0" : "+s"(bud) :: "memory", "scc");
  return bud != 0u;
}
#define glb_retry() glb_retry_(bud)
__device__ __forceinline__ u32x4 gld(rsrc_t r, u32 voff, u32 soff) { return __builtin_amdgcn_raw_buffer_load_b128(r, voff, soff, SC1); }
__device__ __forceinline__ void gst(rsrc_t r, u32x4 v, u32 voff, u32 soff) { __builtin_amdgcn_raw_buffer_store_b128(v, r, voff + soff, 0, SC1); }
__device__ __forceinline__ u32 tagx(u32x4 v, u32 tag) { return (v[1] ^ tag) | (v[3] ^ tag); }
__device__ __forceinline__ bool allz(u32 x) { return __builtin_amdgcn_ballot_w64(x != 0u) == 0ull; }
template <int N> __device__ __forceinline__ void fetch(LAS unsigned char* L, rsrc_t r, u32 voff, u32 ss, u32 tag, u32 dst, u32 ds, u32 flag, u32& bud) {
  u32x4 v[N];
  for (;;) {
    u32 bad = 0;
#pragma unroll
    for (int i = 0; i < N; ++i) v[i] = gld(r, voff + i * ss, 0);
#pragma unroll
    for (int i = 0; i < N; ++i) bad |= tagx(v[i], tag);
    if (allz(bad) || !glb_retry()) break;
  }
#pragma unroll
  for (int i = 0; i < N; ++i) *(LAS u32x2*)(L + dst + i * ds) = u32x2{v[i][0], v[i][2]};
  cw_st(flag, 1u);
}
#define DMA(r, ldsoff, voff, soff, imm, aux) __builtin_amdgcn_raw_ptr_buffer_load_lds((r), (LAS void*)(L + (ldsoff)), 16, (voff), (soff), (imm), (aux))

#define WLANE(x, i) asm volatile("v_writelane_b32 %0, %1, %2" : "+v"(stv) : "s"((u32)(x)), "i"(i))
#define STAMP(i) do { unsigned long long t_; asm volatile("s_memtime %0\n\ts_waitcnt lgkmcnt(0)" : "=s"(t_) :: "memory"); WLANE(t_, (i)); } while (0)
#define STAMP_BEGIN() u32 stv = 0; do { unsigned long long r_ = __builtin_amdgcn_s_memrealtime(); WLANE(r_, 30); STAMP(0); } while (0)
#define STAMP_END() do { STAMP(29); unsigned long long r_ = __builtin_amdgcn_s_memrealtime(); WLANE(r_, 31); \
    if ((threadIdx.x & 63) < 32) ((u32*)((char*)a.ws + WS_DIAG))[(blockIdx.x * 6 + wave) * 32 + (threadIdx.x & 31)] = stv; } while (0)

#define VMW(n) asm volatile("s_waitcnt vmcnt(" #n ")" ::: "memory")
#define ZERO4 f32x4{0.f, 0.f, 0.f, 0.f}
#define MAX8(a, b) fmaxf(fmaxf(fmaxf(a[0], a[1]), fmaxf(a[2], a[3])), fmaxf(fmaxf(b[0], b[1]), fmaxf(b[2], b[3])))
constexpr float LAZY = 192.f;
template <int NA> __device__ __forceinline__ u32x4 soft(const f32x4 s0, const f32x4 s1, float& m, float& l, f32x4* acc) {
  const float mx = MAX8(s0, s1);
  if (__builtin_amdgcn_ballot_w64(mx > m + LAZY) != 0ull) {
    const float mn = fmaxf(m, xg_max(mx)), al = ex2((m - mn) * CS);
    m = mn; l *= al;
#pragma unroll
    for (int i = 0; i < NA; ++i) acc[i] *= al;
  }
  const float mc = -m * CS;
  float pr[8];
#pragma unroll
  for (int i = 0; i < 4; ++i) { pr[i] = ex2(fmaf(s0[i], CS, mc)); pr[4 + i] = ex2(fmaf(s1[i], CS, mc)); }
  l += ((pr[0] + pr[1]) + (pr[2] + pr[3])) + ((pr[4] + pr[5]) + (pr[6] + pr[7]));
  return u32x4{pk(pr[0], pr[1]), pk(pr[2], pr[3]), pk(pr[4], pr[5]), pk(pr[6], pr[7])};
}
__device__ __forceinline__ u32x4 pick2(const f32x4 d0, const f32x4 d1, int q, u32 tag) {
  const float x0 = (q & 2) ? d1[0] : d0[0], x1 = (q & 2) ? d1[1] : d0[1], x2 = (q & 2) ? d1[2] : d0[2], x3 = (q & 2) ? d1[3] : d0[3];
  return u32x4{asu((q & 1) ? x2 : x0), tag, asu((q & 1) ? x3 : x1), tag};
}

constexpr int IT_UK = 2, IT_C = 6, IT_UV = 22, IT_END = 26;
template <int N> __device__ __forceinline__ void vmw() { asm volatile("s_waitcnt vmcnt(%0)" ::"i"(N) : "memory"); }

__global__ void __launch_bounds__(384, 2) mk_kernel(Args a) {
  __shared__ __attribute__((aligned(16))) unsigned char smem[LDS_BYTES];
  LAS unsigned char* L = (LAS unsigned char*)smem;
  const int lane = threadIdx.x & 63, wave = __builtin_amdgcn_readfirstlane(threadIdx.x >> 6);
  const int g = blockIdx.x, gi = g >> 3, hkv = 2 * (g & 7) + (gi >> 4), j = gi & 15, b = j >> 2, s = j & 3;
  const int nkv = s == 0 ? N0 : s == 1 ? N1 : N2;
  constexpr int it0 = 2;
  const int itw = s < 2 ? 2 + nkv - POST : 1 << 20, nit = s < 2 ? nkv + 6 : s == 2 ? nkv + 2 : IT_END;
  const int br0 = (s >> 1) ^ SWAPQ, sl = 2 * b + (s & 1);
  const int bq = (g & 7) >> 1, sp = (g & 1) * 8 + (gi >> 2);
  u32 bud = SPIN_BUDGET;
  STAMP_BEGIN();

  if (__builtin_expect(wave == 0, 1)) {
    __builtin_amdgcn_s_setprio(3);
    {
      const rsrc_t rw = mkrs(a.in[br0 ? 7 : 3], 576u * 8192u), rx = mkrs(a.in[br0 ? 5 : 0], 4u * 576u * 2u);
      const u32 vo = (lane >> 2) * 8192 + (lane & 3) * 16, so = (hkv * 256 + sl * 32) * 2;
#pragma unroll
      for (int i = 0; i < 36; ++i) DMA(rw, i * 1024, vo, so + i * (16 * 8192), 0, AUX_W);
#pragma unroll
      for (int i = 0; i < 5; ++i) DMA(rx, SB_OFF + i * 1024, lane * 16, i * 1024, 0, 0);
      cw_st(CTL + lane * 4, 0u); asm volatile("s_waitcnt lgkmcnt(0)" ::: "memory"); __builtin_amdgcn_s_barrier();
    }
    u32 vq[4];
#pragma unroll
    for (int q = 0; q < 4; ++q) vq[q] = (lane >> 4) * 256 + (((lane & 15) ^ swz(4 * q + (lane >> 4))) << 4);
    int pub = 0, it = it0;
#define SLOT_WAIT() do { if (it >= NS) { const u32 need = 4u * (it >> 3), wa = W_FREE + (it & 7) * 4; \
      if (cw_ld(wa) < need) { VMW(16); if (it - 1 > pub) cw_st(W_FULL, it - 1); VMW(0); pub = it; cw_st(W_FULL, pub); cw_wait(wa, need); } } } while (0)
#define LSTAMP() do { if (it == 4) STAMP(2); if (it == 8) STAMP(3); if (it == 12) STAMP(4); if (it == 16) STAMP(5); if (it == 20) STAMP(6); if (it == 24) STAMP(7); if (it == 28) STAMP(8); if (it == 32) STAMP(9); if (it == 36) STAMP(10); if (it == 40) STAMP(11); if (it == 44) STAMP(12); } while (0)
#define PACE(LW, k) do { vmw<LW>(); if ((k) == ((LW / 4) & 3) && it - LW / 16 > pub) { pub = it - LW / 16; cw_st(W_FULL, pub); } } while (0)
    if (s < 3) {
      const rsrc_t rk = mkrs(a.in[1], 1u << 26), rv = mkrs(a.in[2], 1u << 26);
      u32 so = ((u32)(b * 16 + hkv) * 4096u + (s == 0 ? 0 : s == 1 ? N0 : N0 + N1) * 32u) * 256u;
      asm volatile("" : "+s"(it));
      if (br0 && HOLD_ST) { VMW(0); pub = 2; cw_st(W_FULL, 2); cw_wait(W_QS, 2); }
      else if (br0) vmw<HOLD>(); else vmw<HOLD2>();
#pragma unroll 1
      for (; it < nit; ++it) {
        const u32 sb = (it & 7) * SLOT;
        SLOT_WAIT();
        LSTAMP();
        if ((u32)(it - itw) >= 4u) {
#pragma unroll
          for (int k = 0; k < 2; ++k) {
            PACE(LW_A, k);
            DMA(rk, sb + k * 4096, vq[0], so + k * 4096, 0, AUX_K); DMA(rk, sb + k * 4096, vq[1], so + k * 4096, 1024, AUX_K);
            DMA(rk, sb + k * 4096, vq[2], so + k * 4096, 2048, AUX_K); DMA(rk, sb + k * 4096, vq[3], so + k * 4096, 3072, AUX_K);
          }
#pragma unroll
          for (int k = 0; k < 2; ++k) {
            PACE(LW_A, k + 2);
            DMA(rv, sb + 8192 + k * 4096, vq[0], so + k * 4096, 0, AUX_V); DMA(rv, sb + 8192 + k * 4096, vq[1], so + k * 4096, 1024, AUX_V);
            DMA(rv, sb + 8192 + k * 4096, vq[2], so + k * 4096, 2048, AUX_V); DMA(rv, sb + 8192 + k * 4096, vq[3], so + k * 4096, 3072, AUX_V);
          }
          so += 8192u;
        } else {
          const rsrc_t r = mkrs(a.in[s ? 10 : 4], 4096u * 1152u);
          const u32 sw = ((u32)hkv * 256u + (it - itw) * 64u) * 1152u + b * 288u;
          int ln = lane; asm volatile("" : "+v"(ln));
#pragma unroll
          for (int i = 0; i < 18; ++i) { if (i < 16 && (i & 3) == 0) PACE(LW_A, i >> 2); const u32 ci = i * 64 + ln; DMA(r, sb + i * 1024, ci * 16 + ((ci * 3641u) >> 16) * 864u, sw, 0, AUX_W); }
        }
        if (it == 2 && br0 && !HOLD_ST) { VMW(16); pub = 2; cw_st(W_FULL, 2); }
      }
    } else {
      asm volatile("" : "+s"(it));
#pragma unroll 1
      for (; it < nit; ++it) {
        const u32 sb = (it & 7) * SLOT;
        if (PAUSE_C && it == IT_C) { VMW(32); cw_st(W_FULL, it - 2); VMW(16); cw_st(W_FULL, it - 1); VMW(0); pub = it; cw_st(W_FULL, it); cw_wait(W_QS, 7); }
        SLOT_WAIT();
        LSTAMP();
        if ((u32)(it - IT_C) < 16u) {
          const rsrc_t r = mkrs(a.in[6], 1u << 24);
          const u32 so = ((u32)bq * 4096u + sp * 256u + (it - IT_C) * 16u) * 1024u;
#pragma unroll
          for (int k = 0; k < 4; ++k) {
            PACE(LW_B, k);
            DMA(r, sb + k * 4096, (lane ^ swz(4 * k + 0)) << 4, so + k * 4096, 0, AUX_C);
            DMA(r, sb + k * 4096, (lane ^ swz(4 * k + 1)) << 4, so + k * 4096, 1024, AUX_C);
            DMA(r, sb + k * 4096, (lane ^ swz(4 * k + 2)) << 4, so + k * 4096, 2048, AUX_C);
            DMA(r, sb + k * 4096, (lane ^ swz(4 * k + 3)) << 4, so + k * 4096, 3072, AUX_C);
          }
        } else {
          const rsrc_t r = mkrs(a.in[it < IT_C ? 8 : 9], 512u * 8192u);
          const u32 so = ((u32)b * 128u + ((it - IT_UK) & 3) * 32u) * 8192u + hkv * 512u;
          int ln = lane; asm volatile("" : "+v"(ln));
#pragma unroll
          for (int i = 0; i < 16; ++i) { if ((i & 3) == 0) PACE(LW_B, i >> 2); DMA(r, sb + i * 1024, (ln >> 5) * 8192 + (((ln & 31) ^ swz((2 * i + (ln >> 5)) & 15)) << 4), so + i * (2 * 8192), 0, AUX_W); }
        }
      }
    }
    VMW(32); if (nit - 2 > pub) cw_st(W_FULL, nit - 2);
    VMW(16); if (nit - 1 > pub) cw_st(W_FULL, nit - 1);
    VMW(0); cw_st(W_FULL, nit);
    STAMP(1);
    STAMP_END();
    return;
  }

  const int c = wave - 1, g4 = lane >> 4, n = lane & 15, q = n >> 2, p = lane & 3;
  const rsrc_t rws = mkrs(a.ws, WS_END);
  const u32 tag = *(const __attribute__((address_space(4))) u32*)((const char*)a.ws + WS_EPOCH) + 1u;

  __builtin_amdgcn_s_barrier();
  if (c == 4) {
    cw_wait(W_FULL, s == 3 ? POLL_B : s == 2 ? POLL_A + 2 : POLL_A);
    cw_wait(W_FREE, 4);
    STAMP(1);
    if (s < 3) fetch<1>(L, rws, lane * 16 + WS_QG + (b * 2048 + hkv * 128) * 8, 0, tag, QA_OFF + lane * 8, 0, W_QA, bud);
    else {
      fetch<4>(L, rws, lane * 16 + WS_QG + (8192 + hkv * 128) * 8, 16384, tag, OB_OFF + lane * 8, OB_STR, W_QA, bud);
      STAMP(3);
      cw_wait(W_FULL, POLL_H);
      fetch<1>(L, rws, lane * 16 + WS_HINT, 0, tag, MG_OFF + lane * 8, 0, W_HINT, bud);
      STAMP(4);
      cw_wait(W_BP, 4);
      fetch<4>(L, rws, n * 32 + (g4 & 1) * 16 + WS_SG + hkv * 2048, 512, tag, MG_OFF + lane * 8, 512, W_SGF, bud);
    }
    STAMP(2);
    STAMP_END();
    return;
  }
  if (c < 2) {
    const u32 ab = (8 * g4 + q) * 64 + 32 * c + 8 * p, ax = SB_OFF + p * 1152 + g4 * 16;
    const u32 vo = (n * 2048 + 2 * g4) * 8, so = WS_QG + (br0 * 8192 + hkv * 128 + sl * 16 + 8 * c) * 8;
    cw_wait(W_FULL, 2);
    STAMP(1);
    u32x4 xr[18], wf[18];
#pragma unroll
    for (int ks = 0; ks < 18; ++ks) { wf[ks] = ldtr2(L, ab + ks * 2048, ab + ks * 2048 + 256); xr[ks] = ld128(L, ax + ks * 64); }
    if (lane < 2) asm volatile("ds_add_u32 %0, %1" ::"v"(W_FREE + lane * 4), "v"(2u) : "memory");
    __builtin_amdgcn_sched_barrier(0);
    f32x4 a0 = ZERO4, a1 = ZERO4;
#pragma unroll
    for (int ks = 0; ks < 18; ks += 2) { a0 = mfma(wf[ks], xr[ks], a0); a1 = mfma(wf[ks + 1], xr[ks + 1], a1); }
    a0 += a1;
    if (n < 4) gst(rws, u32x4{pk(a0[0], a0[1]), tag, pk(a0[2], a0[3]), tag}, vo, so);
    cw_add(W_QS, 1, lane);
    STAMP(2);
  }

  if (s < 3) {
    float m = -1e30f, l = 0.f;
    f32x4 acc[8];
#pragma unroll
    for (int i = 0; i < 8; ++i) acc[i] = ZERO4;
    u32x4 qf[4];
    const u32 lpK = n * 256 + ((g4 ^ swz(n)) << 4);
    const u32 lpV = (4 * g4 + q) * 256 + (((p >> 1) ^ swz(4 * g4 + q)) << 4) + 8 * (p & 1);
    bool first = true;
#pragma unroll 1
    for (int it = it0 + ((c + 2) & 3); it < nit; it += 4) {
      if ((u32)(it - itw) < 4u) continue;
      const u32 sb = (it & 7) * SLOT;
      cw_wait(W_FULL, it + 1);
      u32x4 kf[8], vf[8];
#pragma unroll
      for (int kk = 0; kk < 4; ++kk) { const u32 ak = (sb + lpK) ^ (kk << 6); kf[kk] = ld128(L, ak); kf[4 + kk] = ld128(L, ak + 4096); }
#pragma unroll
      for (int ct = 0; ct < 8; ++ct) { const u32 av = (sb + lpV) ^ (ct << 5); vf[ct] = ldtr2(L, av + 8192, av + 12288); }
      cw_add(W_FREE + (it & 7) * 4, 4, lane);
      if (first) {
        first = false;
        cw_wait(W_QA, 1);
        STAMP(3);
#pragma unroll
        for (int kk = 0; kk < 4; ++kk) qf[kk] = ld128(L, QA_OFF + (n & 1) * 256 + kk * 64 + g4 * 16);
      }
      f32x4 s0 = ZERO4, s1 = ZERO4;
#pragma unroll
      for (int kk = 0; kk < 4; ++kk) { s0 = mfma(kf[kk], qf[kk], s0); s1 = mfma(kf[4 + kk], qf[kk], s1); }
      const u32x4 pf = soft<8>(s0, s1, m, l, acc);
#pragma unroll
      for (int ct = 0; ct < 8; ++ct) acc[ct] = mfma(vf[ct], pf, acc[ct]);
    }
    STAMP(8);
    l = xg_sum(l);
    if (n < 2) {
#pragma unroll
      for (int ct = 0; ct < 8; ++ct) *(LAS f32x4*)(L + MG_OFF + (c * 2 + n) * 512 + ct * 64 + g4 * 16) = acc[ct];
      if (g4 == 0) *(LAS f32x2*)(L + MS_OFF + (c * 2 + n) * 8) = f32x2{m, l};
    }
    if (cw_add_rtn(W_MRG, 1, lane) == 3u) {
      const int hd = lane >> 5;
      f32x2 sv[4]; f32x4 ov[4];
#pragma unroll
      for (int i = 0; i < 4; ++i) { sv[i] = *(LAS f32x2*)(L + MS_OFF + (i * 2 + hd) * 8); ov[i] = *(LAS f32x4*)(L + MG_OFF + i * 1024 + lane * 16); }
      const float M = fmaxf(fmaxf(sv[0][0], sv[1][0]), fmaxf(sv[2][0], sv[3][0]));
      float Ls = 0.f; f32x4 o = ZERO4;
#pragma unroll
      for (int i = 0; i < 4; ++i) { const float w = ex2((sv[i][0] - M) * CS); Ls = fmaf(w, sv[i][1], Ls); o += w * ov[i]; }
      o *= 1.f / Ls;
      const u32 so = WS_AG + (hkv * 16 + j) * AG_STR;
      gst(rws, u32x4{pk(o[0], o[1]), tag, pk(o[2], o[3]), tag}, lane * 16, so);
      if ((lane & 31) == 0) gst(rws, u32x4{asu(M), tag, asu(Ls), tag}, 1024 + hd * 16, so);
    }
    STAMP(9);
    if (s < 2) {
      cw_wait(W_FULL, itw + 4);
      u32x4 wf[24];
      {
        const u32 lp = (8 * g4 + q) * 288 + 8 * p + 32 * c;
#pragma unroll
        for (int ks = 0; ks < 8; ++ks) {
          const u32 ad = ((itw + (ks >> 1)) & 7) * SLOT + (ks & 1) * 9216 + lp;
          wf[ks] = ldtr2(L, ad, ad + 1152); wf[8 + ks] = ldtr2(L, ad + 128, ad + 1280);
          if (c == 0) wf[16 + ks] = ldtr2(L, ad + 256, ad + 1408);
        }
      }
      STAMP(10);
      f32x4 o = ZERO4;
      if (s == 0) {
        u32x4 ov[3], sv[3];
        for (;;) {
          u32 bad = 0;
#pragma unroll
          for (int i = 0; i < 3; ++i) { const u32 so = WS_AG + (hkv * 16 + c * 4 + i) * AG_STR; ov[i] = gld(rws, lane * 16, so); sv[i] = gld(rws, 1024 + (lane >> 5) * 16, so); }
#pragma unroll
          for (int i = 0; i < 3; ++i) bad |= tagx(ov[i], tag) | tagx(sv[i], tag);
          if (allz(bad) || !glb_retry()) break;
        }
        const float M = fmaxf(fmaxf(asf(sv[0][0]), asf(sv[1][0])), asf(sv[2][0]));
        float Ls = 0.f;
#pragma unroll
        for (int i = 0; i < 3; ++i) { const float w = ex2((asf(sv[i][0]) - M) * CS) * asf(sv[i][2]); Ls += w; o += w * f32x4{bflo(ov[i][0]), bfhi(ov[i][0]), bflo(ov[i][2]), bfhi(ov[i][2])}; }
        o *= 1.f / Ls;
      } else {
        u32x4 cv[8];
        for (;;) {
          u32 bad = 0;
#pragma unroll
          for (int i = 0; i < 8; ++i) cv[i] = gld(rws, lane * 32 + (i & 1) * 16, WS_CG + ((hkv * 4 + (i >> 1)) * 1024 + c * 256) * 8);
#pragma unroll
          for (int i = 0; i < 8; ++i) bad |= tagx(cv[i], tag);
          if (allz(bad) || !glb_retry()) break;
        }
#pragma unroll
        for (int i = 0; i < 4; ++i) o += f32x4{asf(cv[2 * i][0]), asf(cv[2 * i][2]), asf(cv[2 * i + 1][0]), asf(cv[2 * i + 1][2])};
      }
      STAMP(11);
      *(LAS u32x2*)(L + OB_OFF + c * OB_STR + lane * 8) = u32x2{pk(o[0], o[1]), pk(o[2], o[3])};
      cw_add(W_OB, 1, lane);
      cw_wait(W_OB, 4);
      f32x4 d0 = ZERO4, d1 = ZERO4, d2 = ZERO4;
#pragma unroll
      for (int ks = 0; ks < 8; ++ks) {
        const u32x4 of = ld128(L, OB_OFF + p * OB_STR + ks * 64 + g4 * 16);
        d0 = mfma(wf[ks], of, d0); d1 = mfma(wf[8 + ks], of, d1);
        if (c == 0) d2 = mfma(wf[16 + ks], of, d2);
      }
      {
        const u32 vo = p * 1152 + g4 * 32 + (q & 1) * 16, so = WS_YG + (((s * 4 + b) * 16 + hkv) * 576 + c * 16) * 8;
        gst(rws, pick2(d0, d1, q, tag), vo + (q >> 1) * 512, so);
        if (c == 0 && q < 2) gst(rws, pick2(d2, d2, q, tag), vo + 1024, so);
      }
      STAMP(12);
    }
    STAMP_END();
    return;
  }

  {
    cw_wait(W_FULL, IT_UK + c + 1);
    u32x4 wu[16];
    {
      const u32 lp = (IT_UK + c) * SLOT + n * 512 + ((g4 ^ swz(n)) << 4);
#pragma unroll
      for (int i = 0; i < 16; ++i) wu[i] = ld128(L, (lp ^ ((i & 3) << 6)) + ((i >> 2) & 1) * 8192 + (i >> 3) * 256);
    }
    cw_add(W_FREE + (IT_UK + c) * 4, 4, lane);
    STAMP(15);
    cw_wait(W_QA, 1);
    STAMP(3);
    f32x4 d[4] = {ZERO4, ZERO4, ZERO4, ZERO4};
#pragma unroll
    for (int i = 0; i < 8; ++i) {
      const u32x4 qv = ld128(L, OB_OFF + p * OB_STR + i * 64 + g4 * 16);
      d[(i >> 2) * 2] = mfma(wu[(i >> 2) * 8 + (i & 3)], qv, d[(i >> 2) * 2]); d[(i >> 2) * 2 + 1] = mfma(wu[(i >> 2) * 8 + 4 + (i & 3)], qv, d[(i >> 2) * 2 + 1]);
    }
    const f32x4 e0 = (q & 1) ? d[1] : d[0], e1 = (q & 1) ? d[3] : d[2], e = (q & 2) ? e1 : e0;
    gst(rws, u32x4{pk(e[0], e[1]), tag, pk(e[2], e[3]), tag}, p * 65536 + (g4 & 1) * 128 + (q >> 1) * 64 + (q & 1) * 32 + (g4 >> 1) * 16, WS_QAG + hkv * 4096 + (b * 4 + c) * 256);
    if (cw_add_rtn(W_QS, 1, lane) == 5u) { if (lane == 0) gst(rws, u32x4{tag, tag, tag, tag}, WS_HINT + (hkv * 4 + b) * 16, 0); cw_add(W_QS, 1, lane); }
    STAMP(4);
  }
  {
    const int ch = c & 1;
    u32x4 qa[8];
    {
      const u32 so = WS_QAG + bq * 65536 + (c >> 1) * 32768 + ch * 2048;
      cw_wait(W_HINT, 1);
      STAMP(5);
      u32x4 t[16];
      for (;;) {
        u32 bad = 0;
#pragma unroll
        for (int i = 0; i < 16; ++i) t[i] = gld(rws, (n >> 1) * 4096 + (n & 1) * 64 + g4 * 16 + i * 128, so);
#pragma unroll
        for (int i = 0; i < 16; ++i) bad |= tagx(t[i], tag);
        if (allz(bad) || !glb_retry()) break;
      }
#pragma unroll
      for (int i = 0; i < 8; ++i) qa[i] = u32x4{t[2 * i][0], t[2 * i][2], t[2 * i + 1][0], t[2 * i + 1][2]};
    }
    STAMP(6);
    float mb = -1e30f, lb = 0.f;
    f32x4 ob[16];
#pragma unroll
    for (int i = 0; i < 16; ++i) ob[i] = ZERO4;
    const u32 lpR = n * 1024 + ((g4 ^ swz(n)) << 4) + ch * 512;
    const u32 lpC = (4 * g4 + q) * 1024 + (((p >> 1) ^ swz(4 * g4 + q)) << 4) + 8 * (p & 1) + ch * 512;
#pragma unroll 1
    for (int st = 0; st < 8; ++st) {
      const int it = IT_C + 2 * st;
      const u32 sb = (it & 7) * SLOT;
      const u32 xw = ((st & 1) ? (4 + c) * SLOT + 16384 : SB_OFF + c * 2048) + lane * 16, xr = ((st & 1) ? (4 + (c ^ 1)) * SLOT + 16384 : SB_OFF + (c ^ 1) * 2048) + lane * 16;
      cw_wait(W_FULL, it + 2);
      u32x4 rf[16], cf[16];
#pragma unroll
      for (int kk = 0; kk < 8; ++kk) { const u32 ak = ((sb + lpR) ^ ((kk & 3) << 6)) + (kk >> 2) * 256; rf[2 * kk] = ld128(L, ak); rf[2 * kk + 1] = ld128(L, ak + SLOT); }
      __builtin_amdgcn_sched_barrier(0);
      f32x4 s0 = ZERO4, s1 = ZERO4;
#pragma unroll
      for (int kk = 0; kk < 8; ++kk) {
        s0 = mfma(rf[2 * kk], qa[kk], s0); s1 = mfma(rf[2 * kk + 1], qa[kk], s1);
        const u32 av = (sb + lpC) ^ (kk << 5);
        cf[kk] = ldtr2(L, av, av + SLOT); cf[8 + kk] = ldtr2(L, av + 256, av + 256 + SLOT);
        __builtin_amdgcn_sched_barrier(0);
      }
      *(LAS f32x4*)(L + xw) = s0;
      *(LAS f32x4*)(L + xw + 1024) = s1;
      cw_st(W_S + c * 4, st + 1);
      if (lane < 2) asm volatile("ds_add_u32 %0, %1" ::"v"(W_FREE + (it & 7) * 4 + lane * 4), "v"(1u) : "memory");
      cw_wait(W_S + (c ^ 1) * 4, st + 1);
      s0 += *(LAS f32x4*)(L + xr); s1 += *(LAS f32x4*)(L + xr + 1024);
      const u32x4 pf = soft<16>(s0, s1, mb, lb, ob);
#pragma unroll
      for (int ct = 0; ct < 16; ++ct) ob[ct] = mfma(cf[ct], pf, ob[ct]);
      if (st == 3) STAMP(16);
      if (st == 4) STAMP(17);
    }
    STAMP(7);
#pragma unroll
    for (int i = 0; i < 8; ++i) {
      auto x = __builtin_amdgcn_permlane16_swap(pk(ob[2 * i][0], ob[2 * i][1]), pk(ob[2 * i + 1][0], ob[2 * i + 1][1]), false, false);
      auto y = __builtin_amdgcn_permlane16_swap(pk(ob[2 * i][2], ob[2 * i][3]), pk(ob[2 * i + 1][2], ob[2 * i + 1][3]), false, false);
      gst(rws, u32x4{x[0], y[0], x[1], y[1]}, (n >> 1) * 512 + (n & 1) * 64 + (g4 & 1) * 32 + (g4 >> 1) * 16 + (i & 3) * 128,
          WS_PB + ((bq * 16 + sp) * 4 + ch * 2 + (i >> 2)) * 8192 + (c >> 1) * 4096);
    }
    lb = xg_sum(lb);
    if (g4 == 0 && ch == 0) *(LAS f32x2*)(L + QA_OFF + ((c >> 1) * 16 + n) * 8) = f32x2{mb, lb};
    VMW(0);
    if (cw_add_rtn(W_BP, 1, lane) == 3u) {
      const f32x2 ml = *(LAS f32x2*)(L + QA_OFF + (lane & 31) * 8);
      if (lane < 32) gst(rws, u32x4{asu(ml[0]), tag, asu(ml[1]), tag}, (lane >> 1) * 2048 + (lane & 1) * 16, WS_SG + (bq * 16 + sp) * 32);
    }
    STAMP(8);
  }
  {
    cw_wait(W_SGF, 1);
    STAMP(10);
    u32x4 pv[8];
#pragma unroll
    for (int i = 0; i < 8; ++i) pv[i] = gld(rws, (lane >> 5) * 32768 + (lane & 31) * 16, WS_PB + ((c * 16 + 2 * i) * 4 + b) * 8192 + hkv * 512);
    const f32x2 st = *(LAS f32x2*)(L + MG_OFF + c * 512 + lane * 8);
    const float w = ex2((st[0] - row_max(st[0])) * CS), cf = w / row_sum(w * st[1]);
    f32x4 o0 = ZERO4, o1 = ZERO4;
#pragma unroll
    for (int i = 0; i < 8; ++i) {
      const float f = asf(__builtin_amdgcn_ds_bpermute((((lane >> 2) & 1) * 16 + 2 * i + (lane >> 5)) * 4, asu(cf)));
      o0 += f * f32x4{bflo(pv[i][0]), bfhi(pv[i][0]), bflo(pv[i][1]), bfhi(pv[i][1])};
      o1 += f * f32x4{bflo(pv[i][2]), bfhi(pv[i][2]), bflo(pv[i][3]), bfhi(pv[i][3])};
    }
#pragma unroll
    for (int i = 0; i < 4; ++i) {
      auto t0 = __builtin_amdgcn_permlane32_swap(asu(o0[i]), asu(o0[i]), false, false); o0[i] = asf(t0[0]) + asf(t0[1]);
      auto t1 = __builtin_amdgcn_permlane32_swap(asu(o1[i]), asu(o1[i]), false, false); o1[i] = asf(t1[0]) + asf(t1[1]);
    }
    if (lane < 32) *(LAS u32x4*)(L + OB_OFF + c * OB_STR + ((lane >> 2) & 1) * 256 + (lane >> 3) * 64 + p * 16) = u32x4{pk(o0[0], o0[1]), pk(o0[2], o0[3]), pk(o1[0], o1[1]), pk(o1[2], o1[3])};
    cw_add(W_OB, 1, lane);
    cw_wait(W_FULL, IT_UV + 4);
    u32x4 wf[16];
    {
      const u32 r0 = 8 * (g4 & 1) + q;
      const u32 lp0 = (8 * g4 + q) * 512 + ((((p >> 1) + 8 * c) ^ swz(r0)) << 4) + 8 * (p & 1);
      const u32 lp1 = (8 * g4 + q + 4) * 512 + ((((p >> 1) + 8 * c) ^ swz(r0 + 4)) << 4) + 8 * (p & 1);
#pragma unroll
      for (int i = 0; i < 16; ++i) wf[i] = ldtr2(L, (lp0 ^ ((i & 3) << 5)) + ((IT_UV + (i >> 2)) & 7) * SLOT, (lp1 ^ ((i & 3) << 5)) + ((IT_UV + (i >> 2)) & 7) * SLOT);
    }
    cw_wait(W_OB, 4);
    STAMP(11);
    f32x4 d[4] = {ZERO4, ZERO4, ZERO4, ZERO4};
#pragma unroll
    for (int ks = 0; ks < 4; ++ks) {
      const u32x4 of = ld128(L, OB_OFF + p * OB_STR + (c >> 1) * 256 + ks * 64 + g4 * 16);
#pragma unroll
      for (int t = 0; t < 4; ++t) d[t] = mfma(wf[ks * 4 + t], of, d[t]);
    }
    {
      const u32 vo = p * 2048 + (q >> 1) * 128 + g4 * 32 + (q & 1) * 16, so = WS_CG + ((hkv * 4 + b) * 1024 + c * 64) * 8;
      gst(rws, pick2(d[0], d[1], q, tag), vo, so); gst(rws, pick2(d[2], d[3], q, tag), vo + 256, so);
    }
    STAMP(12);
  }
  if (c < 2) {
    const int bb = hkv >> 2, c0 = 36 * (hkv & 3);
    u32x4 yv[5];
    for (;;) {
      u32 bad = 0;
#pragma unroll
      for (int i = 0; i < 5; ++i) { const int pi = i < 4 ? 4 * i + g4 : 16 + (g4 & 1); yv[i] = gld(rws, (n * 576 + 2 * pi) * 8, WS_YG + ((c * 4 + b) * 16 * 576 + bb * 144 + c0) * 8); }
#pragma unroll
      for (int i = 0; i < 5; ++i) bad |= tagx(yv[i], tag);
      if (allz(bad) || !glb_retry()) break;
    }
    STAMP(13);
    const rsrc_t ro = mkrs(a.out, 4u * 1152u * 2u);
#pragma unroll
    for (int i = 0; i < 5; ++i) {
      const int pi = i < 4 ? 4 * i + g4 : 16 + (g4 & 1);
      const u32 r = pk(row_sum(asf(yv[i][0])), row_sum(asf(yv[i][2])));
      if (n == 0 && (i < 4 || g4 < 2)) __builtin_amdgcn_raw_buffer_store_b32(r, ro, pi * 4, (bb * 1152 + c * 576 + b * 144 + c0) * 2, 0);
    }
    if (g == 24 && c == 0 && lane == 0) __builtin_amdgcn_raw_buffer_store_b32(tag, rws, 0, WS_EPOCH, SC1);
    STAMP(14);
  }
  STAMP_END();
}

__global__ void dump_kernel(const u32* d, Args a) {
  u32 t0 = d[30];
  for (int i = 0; i < 256 * 6; ++i) { u32 t = d[i * 32 + 30]; if ((int)(t - t0) < 0) t0 = t; }
  printf("ptrs: %p %p %p %p ws %p\n", a.in[1], a.in[2], a.in[3], a.in[6], a.ws);
  for (int r = 0; r < 4; ++r) for (int w = 0; w < 6; ++w) {
    if (w == 2 || w == 3) continue;
    printf("r%d w%d:", r, w);
    for (int i = 0; i < 30; ++i) { float sum = 0, mx = 0; int cnt = 0;
      for (int g = 0; g < 256; ++g) { if (((g >> 3) & 3) != r) continue;
        const u32* e = d + (g * 6 + w) * 32; const float f = (float)(e[29] - e[0]) / ((e[31] - e[30]) * 0.01f);
        if (e[i]) { float t = (e[30] - t0) * 0.01f + (e[i] - e[0]) / f; sum += t; mx = fmaxf(mx, t); cnt++; } }
      if (cnt) printf(" [%d]%.1f/%.1f", i, sum / cnt, mx); }
    printf("\n");
  }
}
extern "C" void kernel_launch(void* const* d_in, const int* in_sizes, int n_in, void* d_out, int out_size, void* d_ws, size_t ws_size, hipStream_t stream) {
  Args a;
  for (int i = 0; i < 11; ++i) a.in[i] = d_in[i];
  a.out = d_out;
  a.ws = d_ws;
  mk_kernel<<<256, 384, 0, stream>>>(a);
  dump_kernel<<<1, 1, 0, stream>>>((const u32*)((char*)d_ws + WS_DIAG), a);
}
```

```cpp
#include <hip/hip_runtime.h>
#include <stdint.h>

#define LAS __attribute__((address_space(3)))
typedef unsigned u32;
typedef u32 u32x2 __attribute__((ext_vector_type(2)));
typedef u32 u32x4 __attribute__((ext_vector_type(4)));
typedef float f32x2 __attribute__((ext_vector_type(2)));
typedef float f32x4 __attribute__((ext_vector_type(4)));
typedef short s16x4 __attribute__((ext_vector_type(4)));
typedef __bf16 bf16x2 __attribute__((ext_vector_type(2)));
typedef __bf16 bf16x8 __attribute__((ext_vector_type(8)));
typedef __amdgpu_buffer_rsrc_t rsrc_t;

#define N0 40
#define N1 40
#define N2 (128 - N0 - N1)
#define POST 4
#define LW_A 28
#define LW_B 28
#define HOLD 12
#define LW_U 44
#define SWAPQ 1
#define HOLD2 40
#define PAUSE_C 1
#define HOLD_ST 1
#define ABL 0
#define POLL_A 6
#define POLL_H 8
#define POLL_B 5
#define AUX_K 2
#define AUX_V 2
#define AUX_C 2
#define AUX_W 2

constexpr int SLOT = 18432, NS = 8, RING = SLOT * NS;
constexpr int CTL = RING;
constexpr int W_FULL = CTL, W_QS = CTL + 4, W_QA = CTL + 8, W_MRG = CTL + 12, W_HINT = CTL + 16, W_SGF = CTL + 20, W_OB = CTL + 24, W_BP = CTL + 28, W_FREE = CTL + 32, W_S = CTL + 64;
constexpr int QA_OFF = CTL + 256;
constexpr int OB_OFF = QA_OFF + 512, OB_STR = 576;
constexpr int SB_OFF = OB_OFF + 4 * OB_STR;
constexpr int MG_OFF = SB_OFF + 8192;
constexpr int MS_OFF = MG_OFF + 4096;
constexpr int LDS_BYTES = MS_OFF + 64;
static_assert(LDS_BYTES <= 163840 && (SB_OFF & 15) == 0 && (MG_OFF & 15) == 0, "lds");

constexpr u32 WS_EPOCH = 0, WS_HINT = 1024;
constexpr u32 WS_QG = 4096;
constexpr u32 WS_QAG = WS_QG + 2 * 4 * 2048 * 8;
constexpr u32 WS_AG = WS_QAG + 4 * 32 * 256 * 8;
constexpr u32 AG_STR = 1152;
constexpr u32 WS_SG = WS_AG + 256 * AG_STR;
constexpr u32 WS_CG = WS_SG + 4 * 16 * 32 * 16;
constexpr u32 WS_YG = WS_CG + 16 * 4 * 4 * 256 * 8;
constexpr u32 WS_PB = WS_YG + 2 * 4 * 16 * 4 * 144 * 8;
constexpr u32 WS_END = WS_PB + 4 * 16 * 4 * 16 * 512;
constexpr u32 WS_DIAG = 8u << 20;
static_assert(WS_END <= WS_DIAG && (WS_PB & 127) == 0, "ws");

constexpr float CS = 0.08838834764831845f * 1.4426950408889634f;
constexpr int SC1 = 16;
constexpr u32 SPIN_BUDGET = 1u << 19;

struct Args { const void* in[11]; void* out; void* ws; };

__device__ __forceinline__ rsrc_t mkrs(const void* p, u32 n) { return __builtin_amdgcn_make_buffer_rsrc((void*)p, 0, n, 0x00020000); }
__device__ __forceinline__ u32 swz(u32 r) { return (((r & 7u) ^ ((r >> 1) & 4u)) << 1) | ((r >> 3) & 1u); }
__device__ __forceinline__ f32x4 mfma(u32x4 a, u32x4 b, f32x4 c) {
  return __builtin_amdgcn_mfma_f32_16x16x32_bf16(__builtin_bit_cast(bf16x8, a), __builtin_bit_cast(bf16x8, b), c, 0, 0, 0);
}
__device__ __forceinline__ u32 pk(float lo, float hi) { f32x2 v = {lo, hi}; return __builtin_bit_cast(u32, __builtin_convertvector(v, bf16x2)); }
__device__ __forceinline__ float bflo(u32 v) { return __builtin_bit_cast(float, v << 16); }
__device__ __forceinline__ float bfhi(u32 v) { return __builtin_bit_cast(float, v & 0xffff0000u); }
__device__ __forceinline__ float asf(u32 v) { return __builtin_bit_cast(float, v); }
__device__ __forceinline__ u32 asu(float v) { return __builtin_bit_cast(u32, v); }
__device__ __forceinline__ float ex2(float x) { return __builtin_amdgcn_exp2f(x); }
__device__ __forceinline__ u32x4 ld128(LAS unsigned char* L, u32 a) { return *(LAS u32x4*)(L + a); }
__device__ __forceinline__ u32x4 ldtr2(LAS unsigned char* L, u32 a0, u32 a1) {
  const u32x2 x = __builtin_bit_cast(u32x2, __builtin_amdgcn_ds_read_tr16_b64_v4i16((LAS s16x4*)(L + a0)));
  const u32x2 y = __builtin_bit_cast(u32x2, __builtin_amdgcn_ds_read_tr16_b64_v4i16((LAS s16x4*)(L + a1)));
  return u32x4{x[0], x[1], y[0], y[1]};
}
__device__ __forceinline__ float xg_max(float x) {
  auto s = __builtin_amdgcn_permlane16_swap(asu(x), asu(x), false, false);
  x = fmaxf(asf(s[0]), asf(s[1]));
  auto t = __builtin_amdgcn_permlane32_swap(asu(x), asu(x), false, false);
  return fmaxf(asf(t[0]), asf(t[1]));
}
__device__ __forceinline__ float xg_sum(float x) {
  auto s = __builtin_amdgcn_permlane16_swap(asu(x), asu(x), false, false);
  x = asf(s[0]) + asf(s[1]);
  auto t = __builtin_amdgcn_permlane32_swap(asu(x), asu(x), false, false);
  return asf(t[0]) + asf(t[1]);
}
template <int CTRL> __device__ __forceinline__ float dpp(float x) { return asf(__builtin_amdgcn_mov_dpp(asu(x), CTRL, 0xf, 0xf, true)); }
__device__ __forceinline__ float row_sum(float x) { x += dpp<0x128>(x); x += dpp<0x124>(x); x += dpp<0x122>(x); x += dpp<0x121>(x); return x; }
__device__ __forceinline__ float row_max(float x) { x = fmaxf(x, dpp<0x128>(x)); x = fmaxf(x, dpp<0x124>(x)); x = fmaxf(x, dpp<0x122>(x)); x = fmaxf(x, dpp<0x121>(x)); return x; }

__device__ __forceinline__ u32 cw_ld(u32 a) { u32 v; asm volatile("ds_read_b32 %0, %1\n\ts_waitcnt lgkmcnt(0)" : "=v"(v) : "v"(a) : "memory"); return __builtin_amdgcn_readfirstlane(v); }
__device__ __forceinline__ void cw_st(u32 a, u32 v) { asm volatile("ds_write_b32 %0, %1" ::"v"(a), "v"(v) : "memory"); }
__device__ __forceinline__ void cw_add(u32 a, u32 v, int lane) { if (lane == 0) asm volatile("ds_add_u32 %0, %1" ::"v"(a), "v"(v) : "memory"); asm volatile("" ::: "memory"); }
__device__ __forceinline__ u32 cw_add_rtn(u32 a, u32 v, int lane) {
  u32 r = 0;
  if (lane == 0) asm volatile("ds_add_rtn_u32 %0, %1, %2\n\ts_waitcnt lgkmcnt(0)" : "=v"(r) : "v"(a), "v"(v) : "memory");
  asm volatile("" ::: "memory");
  return __builtin_amdgcn_readfirstlane(r);
}
__device__ __forceinline__ void cw_wait_(u32 a, u32 want, u32& bud) {
  u32 t;
  asm volatile("1:\n\tds_read_b32 v255, %2\n\ts_waitcnt lgkmcnt(0)\n\tv_readfirstlane_b32 %0, v255\n\ts_cmp_ge_u32 %0, %3\n\ts_cbranch_scc1 2f\n\t"
               "s_sleep 1\n\ts_sub_u32 %1, %1, 1\n\ts_cbranch_scc0 1b\n\ts_mov_b32 %1, 0\n2:"
               : "=&s"(t), "+s"(bud) : "v"(a), "s"(want) : "memory", "scc", "v255");
}
#define cw_wait(a, want) cw_wait_((a), (want), bud)
__device__ __forceinline__ bool glb_retry_(u32& bud) {
  asm volatile("s_sleep 2\n\ts_sub_u32 %0, %0, 16\n\ts_cselect_b32 %0, 0, %0" : "+s"(bud) :: "memory", "scc");
  return bud != 0u;
}
#define glb_retry() glb_retry_(bud)
__device__ __forceinline__ u32x4 gld(rsrc_t r, u32 voff, u32 soff) { return __builtin_amdgcn_raw_buffer_load_b128(r, voff, soff, SC1); }
__device__ __forceinline__ void gst(rsrc_t r, u32x4 v, u32 voff, u32 soff) { __builtin_amdgcn_raw_buffer_store_b128(v, r, voff + soff, 0, SC1); }
__device__ __forceinline__ u32 tagx(u32x4 v, u32 tag) { return (v[1] ^ tag) | (v[3] ^ tag); }
__device__ __forceinline__ bool allz(u32 x) { return __builtin_amdgcn_ballot_w64(x != 0u) == 0ull; }
template <int N> __device__ __forceinline__ void fetch(LAS unsigned char* L, rsrc_t r, u32 voff, u32 ss, u32 tag, u32 dst, u32 ds, u32 flag, u32& bud) {
  u32x4 v[N];
  for (;;) {
    u32 bad = 0;
#pragma unroll
    for (int i = 0; i < N; ++i) v[i] = gld(r, voff + i * ss, 0);
#pragma unroll
    for (int i = 0; i < N; ++i) bad |= tagx(v[i], tag);
    if (allz(bad) || !glb_retry()) break;
  }
#pragma unroll
  for (int i = 0; i < N; ++i) *(LAS u32x2*)(L + dst + i * ds) = u32x2{v[i][0], v[i][2]};
  cw_st(flag, 1u);
}
#define DMA(r, ldsoff, voff, soff, imm, aux) __builtin_amdgcn_raw_ptr_buffer_load_lds((r), (LAS void*)(L + (ldsoff)), 16, (voff), (soff), (imm), (aux))

#define STAMP(i) do {} while (0)
#define STAMP_BEGIN() do {} while (0)
#define STAMP_END() do {} while (0)

#define VMW(n) asm volatile("s_waitcnt vmcnt(" #n ")" ::: "memory")
#define ZERO4 f32x4{0.f, 0.f, 0.f, 0.f}
#define MAX8(a, b) fmaxf(fmaxf(fmaxf(a[0], a[1]), fmaxf(a[2], a[3])), fmaxf(fmaxf(b[0], b[1]), fmaxf(b[2], b[3])))
constexpr float LAZY = 192.f;
template <int NA> __device__ __forceinline__ u32x4 soft(const f32x4 s0, const f32x4 s1, float& m, float& l, f32x4* acc) {
  const float mx = MAX8(s0, s1);
  if (__builtin_amdgcn_ballot_w64(mx > m + LAZY) != 0ull) {
    const float mn = fmaxf(m, xg_max(mx)), al = ex2((m - mn) * CS);
    m = mn; l *= al;
#pragma unroll
    for (int i = 0; i < NA; ++i) acc[i] *= al;
  }
  const float mc = -m * CS;
  float pr[8];
#pragma unroll
  for (int i = 0; i < 4; ++i) { pr[i] = ex2(fmaf(s0[i], CS, mc)); pr[4 + i] = ex2(fmaf(s1[i], CS, mc)); }
  l += ((pr[0] + pr[1]) + (pr[2] + pr[3])) + ((pr[4] + pr[5]) + (pr[6] + pr[7]));
  return u32x4{pk(pr[0], pr[1]), pk(pr[2], pr[3]), pk(pr[4], pr[5]), pk(pr[6], pr[7])};
}
__device__ __forceinline__ u32x4 pick2(const f32x4 d0, const f32x4 d1, int q, u32 tag) {
  const float x0 = (q & 2) ? d1[0] : d0[0], x1 = (q & 2) ? d1[1] : d0[1], x2 = (q & 2) ? d1[2] : d0[2], x3 = (q & 2) ? d1[3] : d0[3];
  return u32x4{asu((q & 1) ? x2 : x0), tag, asu((q & 1) ? x3 : x1), tag};
}

constexpr int IT_UK = 2, IT_C = 6, IT_UV = 22, IT_END = 26;
template <int N> __device__ __forceinline__ void vmw() { asm volatile("s_waitcnt vmcnt(%0)" ::"i"(N) : "memory"); }

__global__ void __launch_bounds__(384, 2) mk_kernel(Args a) {
  __shared__ __attribute__((aligned(16))) unsigned char smem[LDS_BYTES];
  LAS unsigned char* L = (LAS unsigned char*)smem;
  const int lane = threadIdx.x & 63, wave = __builtin_amdgcn_readfirstlane(threadIdx.x >> 6);
  const int g = blockIdx.x, gi = g >> 3, hkv = 2 * (g & 7) + (gi >> 4), j = gi & 15, b = j >> 2, s = j & 3;
  const int nkv = s == 0 ? N0 : s == 1 ? N1 : N2;
  constexpr int it0 = 2;
  const int itw = s < 2 ? 2 + nkv - POST : 1 << 20, nit = s < 2 ? nkv + 6 : s == 2 ? nkv + 2 : IT_END;
  const int br0 = (s >> 1) ^ SWAPQ, sl = 2 * b + (s & 1);
  const int bq = (g & 7) >> 1, sp = (g & 1) * 8 + (gi >> 2);
  u32 bud = SPIN_BUDGET;
  STAMP_BEGIN();

  if (__builtin_expect(wave == 0, 1)) {
    __builtin_amdgcn_s_setprio(3);
    {
      const rsrc_t rw = mkrs(a.in[br0 ? 7 : 3], 576u * 8192u), rx = mkrs(a.in[br0 ? 5 : 0], 4u * 576u * 2u);
      const u32 vo = (lane >> 2) * 8192 + (lane & 3) * 16, so = (hkv * 256 + sl * 32) * 2;
#pragma unroll
      for (int i = 0; i < 36; ++i) DMA(rw, i * 1024, vo, so + i * (16 * 8192), 0, AUX_W);
#pragma unroll
      for (int i = 0; i < 5; ++i) DMA(rx, SB_OFF + i * 1024, lane * 16, i * 1024, 0, 0);
      cw_st(CTL + lane * 4, 0u); asm volatile("s_waitcnt lgkmcnt(0)" ::: "memory"); __builtin_amdgcn_s_barrier();
    }
    u32 vq[4];
#pragma unroll
    for (int q = 0; q < 4; ++q) vq[q] = (lane >> 4) * 256 + (((lane & 15) ^ swz(4 * q + (lane >> 4))) << 4);
    int pub = 0, it = it0;
#define SLOT_WAIT() do { if (it >= NS) { const u32 need = 4u * (it >> 3), wa = W_FREE + (it & 7) * 4; \
      if (cw_ld(wa) < need) { VMW(16); if (it - 1 > pub) cw_st(W_FULL, it - 1); VMW(0); pub = it; cw_st(W_FULL, pub); cw_wait(wa, need); } } } while (0)
#define LSTAMP() do { if (it == 4) STAMP(2); if (it == 8) STAMP(3); if (it == 12) STAMP(4); if (it == 16) STAMP(5); if (it == 20) STAMP(6); if (it == 24) STAMP(7); if (it == 28) STAMP(8); if (it == 32) STAMP(9); if (it == 36) STAMP(10); if (it == 40) STAMP(11); if (it == 44) STAMP(12); } while (0)
#define PACE(LW, k) do { vmw<LW>(); if ((k) == ((LW / 4) & 3) && it - LW / 16 > pub) { pub = it - LW / 16; cw_st(W_FULL, pub); } } while (0)
    if (s < 3) {
      const rsrc_t rk = mkrs(a.in[1], 1u << 26), rv = mkrs(a.in[2], 1u << 26);
      u32 so = ((u32)(b * 16 + hkv) * 4096u + (s == 0 ? 0 : s == 1 ? N0 : N0 + N1) * 32u) * 256u;
      asm volatile("" : "+s"(it));
      if (br0 && HOLD_ST) { VMW(0); pub = 2; cw_st(W_FULL, 2); cw_wait(W_QS, 2); }
      else if (br0) vmw<HOLD>(); else vmw<HOLD2>();
#pragma unroll 1
      for (; it < nit; ++it) {
        const u32 sb = (it & 7) * SLOT;
        SLOT_WAIT();
        LSTAMP();
        if ((u32)(it - itw) >= 4u) {
#pragma unroll
          for (int k = 0; k < 2; ++k) {
            PACE(LW_A, k);
            DMA(rk, sb + k * 4096, vq[0], so + k * 4096, 0, AUX_K); DMA(rk, sb + k * 4096, vq[1], so + k * 4096, 1024, AUX_K);
            DMA(rk, sb + k * 4096, vq[2], so + k * 4096, 2048, AUX_K); DMA(rk, sb + k * 4096, vq[3], so + k * 4096, 3072, AUX_K);
          }
#pragma unroll
          for (int k = 0; k < 2; ++k) {
            PACE(LW_A, k + 2);
            DMA(rv, sb + 8192 + k * 4096, vq[0], so + k * 4096, 0, AUX_V); DMA(rv, sb + 8192 + k * 4096, vq[1], so + k * 4096, 1024, AUX_V);
            DMA(rv, sb + 8192 + k * 4096, vq[2], so + k * 4096, 2048, AUX_V); DMA(rv, sb + 8192 + k * 4096, vq[3], so + k * 4096, 3072, AUX_V);
          }
          so += 8192u;
        } else {
          const rsrc_t r = mkrs(a.in[s ? 10 : 4], 4096u * 1152u);
          const u32 sw = ((u32)hkv * 256u + (it - itw) * 64u) * 1152u + b * 288u;
          int ln = lane; asm volatile("" : "+v"(ln));
#pragma unroll
          for (int i = 0; i < 18; ++i) { if (i < 16 && (i & 3) == 0) PACE(LW_A, i >> 2); const u32 ci = i * 64 + ln; DMA(r, sb + i * 1024, ci * 16 + ((ci * 3641u) >> 16) * 864u, sw, 0, AUX_W); }
        }
        if (it == 2 && br0 && !HOLD_ST) { VMW(16); pub = 2; cw_st(W_FULL, 2); }
      }
    } else {
      asm volatile("" : "+s"(it));
#pragma unroll 1
      for (; it < nit; ++it) {
        const u32 sb = (it & 7) * SLOT;
        if (PAUSE_C && it == IT_C) { VMW(32); cw_st(W_FULL, it - 2); VMW(16); cw_st(W_FULL, it - 1); VMW(0); pub = it; cw_st(W_FULL, it); cw_wait(W_QS, 7); }
        SLOT_WAIT();
        LSTAMP();
        if ((u32)(it - IT_C) < 16u) {
          const rsrc_t r = mkrs(a.in[6], 1u << 24);
          const u32 so = ((u32)bq * 4096u + sp * 256u + (it - IT_C) * 16u) * 1024u;
#pragma unroll
          for (int k = 0; k < 4; ++k) {
            PACE(LW_B, k);
            DMA(r, sb + k * 4096, (lane ^ swz(4 * k + 0)) << 4, so + k * 4096, 0, AUX_C);
            DMA(r, sb + k * 4096, (lane ^ swz(4 * k + 1)) << 4, so + k * 4096, 1024, AUX_C);
            DMA(r, sb + k * 4096, (lane ^ swz(4 * k + 2)) << 4, so + k * 4096, 2048, AUX_C);
            DMA(r, sb + k * 4096, (lane ^ swz(4 * k + 3)) << 4, so + k * 4096, 3072, AUX_C);
          }
        } else {
          const rsrc_t r = mkrs(a.in[it < IT_C ? 8 : 9], 512u * 8192u);
          const u32 so = ((u32)b * 128u + ((it - IT_UK) & 3) * 32u) * 8192u + hkv * 512u;
          int ln = lane; asm volatile("" : "+v"(ln));
#pragma unroll
          for (int i = 0; i < 16; ++i) { if ((i & 3) == 0) PACE(LW_B, i >> 2); DMA(r, sb + i * 1024, (ln >> 5) * 8192 + (((ln & 31) ^ swz((2 * i + (ln >> 5)) & 15)) << 4), so + i * (2 * 8192), 0, AUX_W); }
        }
      }
    }
    VMW(32); if (nit - 2 > pub) cw_st(W_FULL, nit - 2);
    VMW(16); if (nit - 1 > pub) cw_st(W_FULL, nit - 1);
    VMW(0); cw_st(W_FULL, nit);
    STAMP(1);
    STAMP_END();
    return;
  }

  const int c = wave - 1, g4 = lane >> 4, n = lane & 15, q = n >> 2, p = lane & 3;
  const rsrc_t rws = mkrs(a.ws, WS_END);
  const u32 tag = *(const __attribute__((address_space(4))) u32*)((const char*)a.ws + WS_EPOCH) + 1u;

  __builtin_amdgcn_s_barrier();
  if (c == 4) {
    cw_wait(W_FULL, s == 3 ? POLL_B : s == 2 ? POLL_A + 2 : POLL_A);
    cw_wait(W_FREE, 4);
    STAMP(1);
    if (s < 3) fetch<1>(L, rws, lane * 16 + WS_QG + (b * 2048 + hkv * 128) * 8, 0, tag, QA_OFF + lane * 8, 0, W_QA, bud);
    else {
      fetch<4>(L, rws, lane * 16 + WS_QG + (8192 + hkv * 128) * 8, 16384, tag, OB_OFF + lane * 8, OB_STR, W_QA, bud);
      STAMP(3);
      cw_wait(W_FULL, POLL_H);
      fetch<1>(L, rws, lane * 16 + WS_HINT, 0, tag, MG_OFF + lane * 8, 0, W_HINT, bud);
      STAMP(4);
      cw_wait(W_BP, 4);
      fetch<4>(L, rws, n * 32 + (g4 & 1) * 16 + WS_SG + hkv * 2048, 512, tag, MG_OFF + lane * 8, 512, W_SGF, bud);
    }
    STAMP(2);
    STAMP_END();
    return;
  }
  if (c < 2) {
    const u32 ab = (8 * g4 + q) * 64 + 32 * c + 8 * p, ax = SB_OFF + p * 1152 + g4 * 16;
    const u32 vo = (n * 2048 + 2 * g4) * 8, so = WS_QG + (br0 * 8192 + hkv * 128 + sl * 16 + 8 * c) * 8;
    cw_wait(W_FULL, 2);
    STAMP(1);
    u32x4 xr[18], wf[18];
#pragma unroll
    for (int ks = 0; ks < 18; ++ks) { wf[ks] = ldtr2(L, ab + ks * 2048, ab + ks * 2048 + 256); xr[ks] = ld128(L, ax + ks * 64); }
    if (lane < 2) asm volatile("ds_add_u32 %0, %1" ::"v"(W_FREE + lane * 4), "v"(2u) : "memory");
    __builtin_amdgcn_sched_barrier(0);
    f32x4 a0 = ZERO4, a1 = ZERO4;
#pragma unroll
    for (int ks = 0; ks < 18; ks += 2) { a0 = mfma(wf[ks], xr[ks], a0); a1 = mfma(wf[ks + 1], xr[ks + 1], a1); }
    a0 += a1;
    if (n < 4) gst(rws, u32x4{pk(a0[0], a0[1]), tag, pk(a0[2], a0[3]), tag}, vo, so);
    cw_add(W_QS, 1, lane);
    STAMP(2);
  }

  if (s < 3) {
    float m = -1e30f, l = 0.f;
    f32x4 acc[8];
#pragma unroll
    for (int i = 0; i < 8; ++i) acc[i] = ZERO4;
    u32x4 qf[4];
    const u32 lpK = n * 256 + ((g4 ^ swz(n)) << 4);
    const u32 lpV = (4 * g4 + q) * 256 + (((p >> 1) ^ swz(4 * g4 + q)) << 4) + 8 * (p & 1);
    bool first = true;
#pragma unroll 1
    for (int it = it0 + ((c + 2) & 3); it < nit; it += 4) {
      if ((u32)(it - itw) < 4u) continue;
      const u32 sb = (it & 7) * SLOT;
      cw_wait(W_FULL, it + 1);
      u32x4 kf[8], vf[8];
#pragma unroll
      for (int kk = 0; kk < 4; ++kk) { const u32 ak = (sb + lpK) ^ (kk << 6); kf[kk] = ld128(L, ak); kf[4 + kk] = ld128(L, ak + 4096); }
#pragma unroll
      for (int ct = 0; ct < 8; ++ct) { const u32 av = (sb + lpV) ^ (ct << 5); vf[ct] = ldtr2(L, av + 8192, av + 12288); }
      cw_add(W_FREE + (it & 7) * 4, 4, lane);
      if (first) {
        first = false;
        cw_wait(W_QA, 1);
        STAMP(3);
#pragma unroll
        for (int kk = 0; kk < 4; ++kk) qf[kk] = ld128(L, QA_OFF + (n & 1) * 256 + kk * 64 + g4 * 16);
      }
      f32x4 s0 = ZERO4, s1 = ZERO4;
#pragma unroll
      for (int kk = 0; kk < 4; ++kk) { s0 = mfma(kf[kk], qf[kk], s0); s1 = mfma(kf[4 + kk], qf[kk], s1); }
      const u32x4 pf = soft<8>(s0, s1, m, l, acc);
#pragma unroll
      for (int ct = 0; ct < 8; ++ct) acc[ct] = mfma(vf[ct], pf, acc[ct]);
    }
    STAMP(8);
    l = xg_sum(l);
    if (n < 2) {
#pragma unroll
      for (int ct = 0; ct < 8; ++ct) *(LAS f32x4*)(L + MG_OFF + (c * 2 + n) * 512 + ct * 64 + g4 * 16) = acc[ct];
      if (g4 == 0) *(LAS f32x2*)(L + MS_OFF + (c * 2 + n) * 8) = f32x2{m, l};
    }
    if (cw_add_rtn(W_MRG, 1, lane) == 3u) {
      const int hd = lane >> 5;
      f32x2 sv[4]; f32x4 ov[4];
#pragma unroll
      for (int i = 0; i < 4; ++i) { sv[i] = *(LAS f32x2*)(L + MS_OFF + (i * 2 + hd) * 8); ov[i] = *(LAS f32x4*)(L + MG_OFF + i * 1024 + lane * 16); }
      const float M = fmaxf(fmaxf(sv[0][0], sv[1][0]), fmaxf(sv[2][0], sv[3][0]));
      float Ls = 0.f; f32x4 o = ZERO4;
#pragma unroll
      for (int i = 0; i < 4; ++i) { const float w = ex2((sv[i][0] - M) * CS); Ls = fmaf(w, sv[i][1], Ls); o += w * ov[i]; }
      o *= 1.f / Ls;
      const u32 so = WS_AG + (hkv * 16 + j) * AG_STR;
      gst(rws, u32x4{pk(o[0], o[1]), tag, pk(o[2], o[3]), tag}, lane * 16, so);
      if ((lane & 31) == 0) gst(rws, u32x4{asu(M), tag, asu(Ls), tag}, 1024 + hd * 16, so);
    }
    STAMP(9);
    if (s < 2) {
      cw_wait(W_FULL, itw + 4);
      u32x4 wf[24];
      {
        const u32 lp = (8 * g4 + q) * 288 + 8 * p + 32 * c;
#pragma unroll
        for (int ks = 0; ks < 8; ++ks) {
          const u32 ad = ((itw + (ks >> 1)) & 7) * SLOT + (ks & 1) * 9216 + lp;
          wf[ks] = ldtr2(L, ad, ad + 1152); wf[8 + ks] = ldtr2(L, ad + 128, ad + 1280);
          if (c == 0) wf[16 + ks] = ldtr2(L, ad + 256, ad + 1408);
        }
      }
      STAMP(10);
      f32x4 o = ZERO4;
      if (s == 0) {
        u32x4 ov[3], sv[3];
        for (;;) {
          u32 bad = 0;
#pragma unroll
          for (int i = 0; i < 3; ++i) { const u32 so = WS_AG + (hkv * 16 + c * 4 + i) * AG_STR; ov[i] = gld(rws, lane * 16, so); sv[i] = gld(rws, 1024 + (lane >> 5) * 16, so); }
#pragma unroll
          for (int i = 0; i < 3; ++i) bad |= tagx(ov[i], tag) | tagx(sv[i], tag);
          if (allz(bad) || !glb_retry()) break;
        }
        const float M = fmaxf(fmaxf(asf(sv[0][0]), asf(sv[1][0])), asf(sv[2][0]));
        float Ls = 0.f;
#pragma unroll
        for (int i = 0; i < 3; ++i) { const float w = ex2((asf(sv[i][0]) - M) * CS) * asf(sv[i][2]); Ls += w; o += w * f32x4{bflo(ov[i][0]), bfhi(ov[i][0]), bflo(ov[i][2]), bfhi(ov[i][2])}; }
        o *= 1.f / Ls;
      } else {
        u32x4 cv[8];
        for (;;) {
          u32 bad = 0;
#pragma unroll
          for (int i = 0; i < 8; ++i) cv[i] = gld(rws, lane * 32 + (i & 1) * 16, WS_CG + ((hkv * 4 + (i >> 1)) * 1024 + c * 256) * 8);
#pragma unroll
          for (int i = 0; i < 8; ++i) bad |= tagx(cv[i], tag);
          if (allz(bad) || !glb_retry()) break;
        }
#pragma unroll
        for (int i = 0; i < 4; ++i) o += f32x4{asf(cv[2 * i][0]), asf(cv[2 * i][2]), asf(cv[2 * i + 1][0]), asf(cv[2 * i + 1][2])};
      }
      STAMP(11);
      *(LAS u32x2*)(L + OB_OFF + c * OB_STR + lane * 8) = u32x2{pk(o[0], o[1]), pk(o[2], o[3])};
      cw_add(W_OB, 1, lane);
      cw_wait(W_OB, 4);
      f32x4 d0 = ZERO4, d1 = ZERO4, d2 = ZERO4;
#pragma unroll
      for (int ks = 0; ks < 8; ++ks) {
        const u32x4 of = ld128(L, OB_OFF + p * OB_STR + ks * 64 + g4 * 16);
        d0 = mfma(wf[ks], of, d0); d1 = mfma(wf[8 + ks], of, d1);
        if (c == 0) d2 = mfma(wf[16 + ks], of, d2);
      }
      {
        const u32 vo = p * 1152 + g4 * 32 + (q & 1) * 16, so = WS_YG + (((s * 4 + b) * 16 + hkv) * 576 + c * 16) * 8;
        gst(rws, pick2(d0, d1, q, tag), vo + (q >> 1) * 512, so);
        if (c == 0 && q < 2) gst(rws, pick2(d2, d2, q, tag), vo + 1024, so);
      }
      STAMP(12);
    }
    STAMP_END();
    return;
  }

  {
    cw_wait(W_FULL, IT_UK + c + 1);
    u32x4 wu[16];
    {
      const u32 lp = (IT_UK + c) * SLOT + n * 512 + ((g4 ^ swz(n)) << 4);
#pragma unroll
      for (int i = 0; i < 16; ++i) wu[i] = ld128(L, (lp ^ ((i & 3) << 6)) + ((i >> 2) & 1) * 8192 + (i >> 3) * 256);
    }
    cw_add(W_FREE + (IT_UK + c) * 4, 4, lane);
    STAMP(15);
    cw_wait(W_QA, 1);
    STAMP(3);
    f32x4 d[4] = {ZERO4, ZERO4, ZERO4, ZERO4};
#pragma unroll
    for (int i = 0; i < 8; ++i) {
      const u32x4 qv = ld128(L, OB_OFF + p * OB_STR + i * 64 + g4 * 16);
      d[(i >> 2) * 2] = mfma(wu[(i >> 2) * 8 + (i & 3)], qv, d[(i >> 2) * 2]); d[(i >> 2) * 2 + 1] = mfma(wu[(i >> 2) * 8 + 4 + (i & 3)], qv, d[(i >> 2) * 2 + 1]);
    }
    const f32x4 e0 = (q & 1) ? d[1] : d[0], e1 = (q & 1) ? d[3] : d[2], e = (q & 2) ? e1 : e0;
    gst(rws, u32x4{pk(e[0], e[1]), tag, pk(e[2], e[3]), tag}, p * 65536 + (g4 & 1) * 128 + (q >> 1) * 64 + (q & 1) * 32 + (g4 >> 1) * 16, WS_QAG + hkv * 4096 + (b * 4 + c) * 256);
    if (cw_add_rtn(W_QS, 1, lane) == 5u) { if (lane == 0) gst(rws, u32x4{tag, tag, tag, tag}, WS_HINT + (hkv * 4 + b) * 16, 0); cw_add(W_QS, 1, lane); }
    STAMP(4);
  }
  {
    const int ch = c & 1;
    u32x4 qa[8];
    {
      const u32 so = WS_QAG + bq * 65536 + (c >> 1) * 32768 + ch * 2048;
      cw_wait(W_HINT, 1);
      STAMP(5);
      u32x4 t[16];
      for (;;) {
        u32 bad = 0;
#pragma unroll
        for (int i = 0; i < 16; ++i) t[i] = gld(rws, (n >> 1) * 4096 + (n & 1) * 64 + g4 * 16 + i * 128, so);
#pragma unroll
        for (int i = 0; i < 16; ++i) bad |= tagx(t[i], tag);
        if (allz(bad) || !glb_retry()) break;
      }
#pragma unroll
      for (int i = 0; i < 8; ++i) qa[i] = u32x4{t[2 * i][0], t[2 * i][2], t[2 * i + 1][0], t[2 * i + 1][2]};
    }
    STAMP(6);
    float mb = -1e30f, lb = 0.f;
    f32x4 ob[16];
#pragma unroll
    for (int i = 0; i < 16; ++i) ob[i] = ZERO4;
    const u32 lpR = n * 1024 + ((g4 ^ swz(n)) << 4) + ch * 512;
    const u32 lpC = (4 * g4 + q) * 1024 + (((p >> 1) ^ swz(4 * g4 + q)) << 4) + 8 * (p & 1) + ch * 512;
#pragma unroll 1
    for (int st = 0; st < 8; ++st) {
      const int it = IT_C + 2 * st;
      const u32 sb = (it & 7) * SLOT;
      const u32 xw = ((st & 1) ? (4 + c) * SLOT + 16384 : SB_OFF + c * 2048) + lane * 16, xr = ((st & 1) ? (4 + (c ^ 1)) * SLOT + 16384 : SB_OFF + (c ^ 1) * 2048) + lane * 16;
      cw_wait(W_FULL, it + 2);
      u32x4 rf[16], cf[16];
#pragma unroll
      for (int kk = 0; kk < 8; ++kk) { const u32 ak = ((sb + lpR) ^ ((kk & 3) << 6)) + (kk >> 2) * 256; rf[2 * kk] = ld128(L, ak); rf[2 * kk + 1] = ld128(L, ak + SLOT); }
      __builtin_amdgcn_sched_barrier(0);
      f32x4 s0 = ZERO4, s1 = ZERO4;
#pragma unroll
      for (int kk = 0; kk < 8; ++kk) {
        s0 = mfma(rf[2 * kk], qa[kk], s0); s1 = mfma(rf[2 * kk + 1], qa[kk], s1);
        const u32 av = (sb + lpC) ^ (kk << 5);
        cf[kk] = ldtr2(L, av, av + SLOT); cf[8 + kk] = ldtr2(L, av + 256, av + 256 + SLOT);
        __builtin_amdgcn_sched_barrier(0);
      }
      *(LAS f32x4*)(L + xw) = s0;
      *(LAS f32x4*)(L + xw + 1024) = s1;
      cw_st(W_S + c * 4, st + 1);
      if (lane < 2) asm volatile("ds_add_u32 %0, %1" ::"v"(W_FREE + (it & 7) * 4 + lane * 4), "v"(1u) : "memory");
      cw_wait(W_S + (c ^ 1) * 4, st + 1);
      s0 += *(LAS f32x4*)(L + xr); s1 += *(LAS f32x4*)(L + xr + 1024);
      const u32x4 pf = soft<16>(s0, s1, mb, lb, ob);
#pragma unroll
      for (int ct = 0; ct < 16; ++ct) ob[ct] = mfma(cf[ct], pf, ob[ct]);
      if (st == 3) STAMP(16);
      if (st == 4) STAMP(17);
    }
    STAMP(7);
#pragma unroll
    for (int i = 0; i < 8; ++i) {
      auto x = __builtin_amdgcn_permlane16_swap(pk(ob[2 * i][0], ob[2 * i][1]), pk(ob[2 * i + 1][0], ob[2 * i + 1][1]), false, false);
      auto y = __builtin_amdgcn_permlane16_swap(pk(ob[2 * i][2], ob[2 * i][3]), pk(ob[2 * i + 1][2], ob[2 * i + 1][3]), false, false);
      gst(rws, u32x4{x[0], y[0], x[1], y[1]}, (n >> 1) * 512 + (n & 1) * 64 + (g4 & 1) * 32 + (g4 >> 1) * 16 + (i & 3) * 128,
          WS_PB + ((bq * 16 + sp) * 4 + ch * 2 + (i >> 2)) * 8192 + (c >> 1) * 4096);
    }
    lb = xg_sum(lb);
    if (g4 == 0 && ch == 0) *(LAS f32x2*)(L + QA_OFF + ((c >> 1) * 16 + n) * 8) = f32x2{mb, lb};
    VMW(0);
    if (cw_add_rtn(W_BP, 1, lane) == 3u) {
      const f32x2 ml = *(LAS f32x2*)(L + QA_OFF + (lane & 31) * 8);
      if (lane < 32) gst(rws, u32x4{asu(ml[0]), tag, asu(ml[1]), tag}, (lane >> 1) * 2048 + (lane & 1) * 16, WS_SG + (bq * 16 + sp) * 32);
    }
    STAMP(8);
  }
  {
    cw_wait(W_SGF, 1);
    STAMP(10);
    u32x4 pv[8];
#pragma unroll
    for (int i = 0; i < 8; ++i) pv[i] = gld(rws, (lane >> 5) * 32768 + (lane & 31) * 16, WS_PB + ((c * 16 + 2 * i) * 4 + b) * 8192 + hkv * 512);
    const f32x2 st = *(LAS f32x2*)(L + MG_OFF + c * 512 + lane * 8);
    const float w = ex2((st[0] - row_max(st[0])) * CS), cf = w / row_sum(w * st[1]);
    f32x4 o0 = ZERO4, o1 = ZERO4;
#pragma unroll
    for (int i = 0; i < 8; ++i) {
      const float f = asf(__builtin_amdgcn_ds_bpermute((((lane >> 2) & 1) * 16 + 2 * i + (lane >> 5)) * 4, asu(cf)));
      o0 += f * f32x4{bflo(pv[i][0]), bfhi(pv[i][0]), bflo(pv[i][1]), bfhi(pv[i][1])};
      o1 += f * f32x4{bflo(pv[i][2]), bfhi(pv[i][2]), bflo(pv[i][3]), bfhi(pv[i][3])};
    }
#pragma unroll
    for (int i = 0; i < 4; ++i) {
      auto t0 = __builtin_amdgcn_permlane32_swap(asu(o0[i]), asu(o0[i]), false, false); o0[i] = asf(t0[0]) + asf(t0[1]);
      auto t1 = __builtin_amdgcn_permlane32_swap(asu(o1[i]), asu(o1[i]), false, false); o1[i] = asf(t1[0]) + asf(t1[1]);
    }
    if (lane < 32) *(LAS u32x4*)(L + OB_OFF + c * OB_STR + ((lane >> 2) & 1) * 256 + (lane >> 3) * 64 + p * 16) = u32x4{pk(o0[0], o0[1]), pk(o0[2], o0[3]), pk(o1[0], o1[1]), pk(o1[2], o1[3])};
    cw_add(W_OB, 1, lane);
    cw_wait(W_FULL, IT_UV + 4);
    u32x4 wf[16];
    {
      const u32 r0 = 8 * (g4 & 1) + q;
      const u32 lp0 = (8 * g4 + q) * 512 + ((((p >> 1) + 8 * c) ^ swz(r0)) << 4) + 8 * (p & 1);
      const u32 lp1 = (8 * g4 + q + 4) * 512 + ((((p >> 1) + 8 * c) ^ swz(r0 + 4)) << 4) + 8 * (p & 1);
#pragma unroll
      for (int i = 0; i < 16; ++i) wf[i] = ldtr2(L, (lp0 ^ ((i & 3) << 5)) + ((IT_UV + (i >> 2)) & 7) * SLOT, (lp1 ^ ((i & 3) << 5)) + ((IT_UV + (i >> 2)) & 7) * SLOT);
    }
    cw_wait(W_OB, 4);
    STAMP(11);
    f32x4 d[4] = {ZERO4, ZERO4, ZERO4, ZERO4};
#pragma unroll
    for (int ks = 0; ks < 4; ++ks) {
      const u32x4 of = ld128(L, OB_OFF + p * OB_STR + (c >> 1) * 256 + ks * 64 + g4 * 16);
#pragma unroll
      for (int t = 0; t < 4; ++t) d[t] = mfma(wf[ks * 4 + t], of, d[t]);
    }
    {
      const u32 vo = p * 2048 + (q >> 1) * 128 + g4 * 32 + (q & 1) * 16, so = WS_CG + ((hkv * 4 + b) * 1024 + c * 64) * 8;
      gst(rws, pick2(d[0], d[1], q, tag), vo, so); gst(rws, pick2(d[2], d[3], q, tag), vo + 256, so);
    }
    STAMP(12);
  }
  if (c < 2) {
    const int bb = hkv >> 2, c0 = 36 * (hkv & 3);
    u32x4 yv[5];
    for (;;) {
      u32 bad = 0;
#pragma unroll
      for (int i = 0; i < 5; ++i) { const int pi = i < 4 ? 4 * i + g4 : 16 + (g4 & 1); yv[i] = gld(rws, (n * 576 + 2 * pi) * 8, WS_YG + ((c * 4 + b) * 16 * 576 + bb * 144 + c0) * 8); }
#pragma unroll
      for (int i = 0; i < 5; ++i) bad |= tagx(yv[i], tag);
      if (allz(bad) || !glb_retry()) break;
    }
    STAMP(13);
    const rsrc_t ro = mkrs(a.out, 4u * 1152u * 2u);
#pragma unroll
    for (int i = 0; i < 5; ++i) {
      const int pi = i < 4 ? 4 * i + g4 : 16 + (g4 & 1);
      const u32 r = pk(row_sum(asf(yv[i][0])), row_sum(asf(yv[i][2])));
      if (n == 0 && (i < 4 || g4 < 2)) __builtin_amdgcn_raw_buffer_store_b32(r, ro, pi * 4, (bb * 1152 + c * 576 + b * 144 + c0) * 2, 0);
    }
    if (g == 24 && c == 0 && lane == 0) __builtin_amdgcn_raw_buffer_store_b32(tag, rws, 0, WS_EPOCH, SC1);
    STAMP(14);
  }
  STAMP_END();
}

__global__ void pre_kernel(u32* d) { if (threadIdx.x == 0) d[0] = 1; }
extern "C" void kernel_launch(void* const* d_in, const int* in_sizes, int n_in, void* d_out, int out_size, void* d_ws, size_t ws_size, hipStream_t stream) {
  Args a;
  for (int i = 0; i < 11; ++i) a.in[i] = d_in[i];
  a.out = d_out;
  a.ws = d_ws;
  pre_kernel<<<1, 64, 0, stream>>>((u32*)((char*)d_ws + WS_DIAG + (1 << 20)));
  mk_kernel<<<256, 384, 0, stream>>>(a);
}
```

```cpp
#include <hip/hip_runtime.h>
#include <stdint.h>

#define LAS __attribute__((address_space(3)))
typedef unsigned u32;
typedef u32 u32x2 __attribute__((ext_vector_type(2)));
typedef u32 u32x4 __attribute__((ext_vector_type(4)));
typedef float f32x2 __attribute__((ext_vector_type(2)));
typedef float f32x4 __attribute__((ext_vector_type(4)));
typedef short s16x4 __attribute__((ext_vector_type(4)));
typedef __bf16 bf16x2 __attribute__((ext_vector_type(2)));
typedef __bf16 bf16x8 __attribute__((ext_vector_type(8)));
typedef __amdgpu_buffer_rsrc_t rsrc_t;

#define NST 40
constexpr int NU = 128 - 3 * NST, NREC = 3 + NU, NTICK = 4 * NU;
static_assert(NU >= 1 && NREC <= 32, "split");
#define LW_A 28
#define LW_B 28
#define HOLD_ST 0
#define POLL_A 6
#define POLL_A2 8
#define YSLEEP 4
#define GATE1 6
#define GATE2 8
#define AUX_K 2
#define AUX_V 2
#define AUX_C 2
#define AUX_W 2
static_assert(LW_A <= 32, "a ticket is read two items after it was drawn");

constexpr int SLOT = 18432, NS = 8, RING = SLOT * NS;
constexpr int CTL = RING;
constexpr int W_FULL = CTL, W_QS = CTL + 4, W_QA = CTL + 8, W_MRG = CTL + 12, W_HINT = CTL + 16, W_SGF = CTL + 20, W_OB = CTL + 24, W_BP = CTL + 28,
              W_FREE = CTL + 32, W_S = CTL + 64, W_GI = CTL + 80, W_END = CTL + 84, W_PJ = CTL + 88, W_TK = CTL + 96;
constexpr int ST_OFF = CTL + 256;
constexpr int OB_OFF = ST_OFF + 512, OB_STR = 576;
constexpr int SB_OFF = OB_OFF + 4 * OB_STR;
constexpr int QA_OFF = SB_OFF + 5120;
constexpr int MG_OFF = SB_OFF + 8192;
constexpr int MS_OFF = MG_OFF + 4096;
constexpr int LDS_BYTES = MS_OFF + 64;
static_assert(LDS_BYTES <= 163840 && (SB_OFF & 15) == 0 && (MG_OFF & 15) == 0, "lds");

constexpr u32 WS_EPOCH = 0, WS_HINT = 1024;
constexpr u32 WS_TK = 2048;
constexpr u32 WS_QG = 4096;
constexpr u32 WS_QAG = WS_QG + 2 * 4 * 2048 * 8;
constexpr u32 WS_AG = WS_QAG + 4 * 32 * 256 * 8;
constexpr u32 WS_AS = WS_AG + 64 * 32 * 1024;
constexpr u32 WS_SG = WS_AS + 64 * 1024;
constexpr u32 WS_CG = WS_SG + 4 * 16 * 32 * 16;
constexpr u32 WS_YG = WS_CG + 16 * 4 * 4 * 256 * 8;
constexpr u32 WS_PB = WS_YG + 2 * 4 * 32 * 4 * 144 * 8;
constexpr u32 WS_END = WS_PB + 4 * 16 * 4 * 16 * 512;
constexpr u32 WS_DIAG = 8u << 20;
static_assert(WS_END <= WS_DIAG && (WS_PB & 127) == 0, "ws");

constexpr float CS = 0.08838834764831845f * 1.4426950408889634f;
constexpr int SC1 = 16;
constexpr u32 SPIN_BUDGET = 1u << 19;

struct Args { const void* in[11]; void* out; void* ws; };

__device__ __forceinline__ rsrc_t mkrs(const void* p, u32 n) { return __builtin_amdgcn_make_buffer_rsrc((void*)p, 0, n, 0x00020000); }
constexpr __device__ __forceinline__ u32 swz(u32 r) { return (((r & 7u) ^ ((r >> 1) & 4u)) << 1) | ((r >> 3) & 1u); }
__device__ __forceinline__ f32x4 mfma(u32x4 a, u32x4 b, f32x4 c) {
  return __builtin_amdgcn_mfma_f32_16x16x32_bf16(__builtin_bit_cast(bf16x8, a), __builtin_bit_cast(bf16x8, b), c, 0, 0, 0);
}
__device__ __forceinline__ u32 pk(float lo, float hi) { f32x2 v = {lo, hi}; return __builtin_bit_cast(u32, __builtin_convertvector(v, bf16x2)); }
__device__ __forceinline__ float bflo(u32 v) { return __builtin_bit_cast(float, v << 16); }
__device__ __forceinline__ float bfhi(u32 v) { return __builtin_bit_cast(float, v & 0xffff0000u); }
__device__ __forceinline__ float asf(u32 v) { return __builtin_bit_cast(float, v); }
__device__ __forceinline__ u32 asu(float v) { return __builtin_bit_cast(u32, v); }
__device__ __forceinline__ float ex2(float x) { return __builtin_amdgcn_exp2f(x); }
__device__ __forceinline__ u32x4 ld128(LAS unsigned char* L, u32 a) { return *(LAS u32x4*)(L + a); }
__device__ __forceinline__ u32x4 ldtr2(LAS unsigned char* L, u32 a0, u32 a1) {
  const u32x2 x = __builtin_bit_cast(u32x2, __builtin_amdgcn_ds_read_tr16_b64_v4i16((LAS s16x4*)(L + a0)));
  const u32x2 y = __builtin_bit_cast(u32x2, __builtin_amdgcn_ds_read_tr16_b64_v4i16((LAS s16x4*)(L + a1)));
  return u32x4{x[0], x[1], y[0], y[1]};
}
__device__ __forceinline__ float xg_max(float x) {
  auto s = __builtin_amdgcn_permlane16_swap(asu(x), asu(x), false, false);
  x = fmaxf(asf(s[0]), asf(s[1]));
  auto t = __builtin_amdgcn_permlane32_swap(asu(x), asu(x), false, false);
  return fmaxf(asf(t[0]), asf(t[1]));
}
__device__ __forceinline__ float xg_sum(float x) {
  auto s = __builtin_amdgcn_permlane16_swap(asu(x), asu(x), false, false);
  x = asf(s[0]) + asf(s[1]);
  auto t = __builtin_amdgcn_permlane32_swap(asu(x), asu(x), false, false);
  return asf(t[0]) + asf(t[1]);
}
template <int CTRL> __device__ __forceinline__ float dpp(float x) { return asf(__builtin_amdgcn_mov_dpp(asu(x), CTRL, 0xf, 0xf, true)); }
__device__ __forceinline__ float row_sum(float x) { x += dpp<0x128>(x); x += dpp<0x124>(x); x += dpp<0x122>(x); x += dpp<0x121>(x); return x; }
__device__ __forceinline__ float row_max(float x) { x = fmaxf(x, dpp<0x128>(x)); x = fmaxf(x, dpp<0x124>(x)); x = fmaxf(x, dpp<0x122>(x)); x = fmaxf(x, dpp<0x121>(x)); return x; }

__device__ __forceinline__ u32 cw_ld(u32 a) { u32 v; asm volatile("ds_read_b32 %0, %1\n\ts_waitcnt lgkmcnt(0)" : "=v"(v) : "v"(a) : "memory"); return __builtin_amdgcn_readfirstlane(v); }
__device__ __forceinline__ void cw_st(u32 a, u32 v) { asm volatile("ds_write_b32 %0, %1" ::"v"(a), "v"(v) : "memory"); }
__device__ __forceinline__ void cw_add(u32 a, u32 v, int lane) { if (lane == 0) asm volatile("ds_add_u32 %0, %1" ::"v"(a), "v"(v) : "memory"); asm volatile("" ::: "memory"); }
__device__ __forceinline__ u32 cw_add_rtn(u32 a, u32 v, int lane) {
  u32 r = 0;
  if (lane == 0) asm volatile("ds_add_rtn_u32 %0, %1, %2\n\ts_waitcnt lgkmcnt(0)" : "=v"(r) : "v"(a), "v"(v) : "memory");
  asm volatile("" ::: "memory");
  return __builtin_amdgcn_readfirstlane(r);
}
__device__ __forceinline__ void cw_wait_(u32 a, u32 want, u32& bud) {
  u32 t;
  bud = __builtin_amdgcn_readfirstlane(bud); want = __builtin_amdgcn_readfirstlane(want);
  asm volatile("1:\n\tds_read_b32 v255, %2\n\ts_waitcnt lgkmcnt(0)\n\tv_readfirstlane_b32 %0, v255\n\ts_cmp_ge_u32 %0, %3\n\ts_cbranch_scc1 2f\n\t"
               "s_sleep 1\n\ts_sub_u32 %1, %1, 1\n\ts_cbranch_scc0 1b\n\ts_mov_b32 %1, 0\n2:"
               : "=&s"(t), "+s"(bud) : "v"(a), "s"(want) : "memory", "scc", "v255");
}
#define cw_wait(a, want) cw_wait_((a), (want), bud)
__device__ __forceinline__ bool glb_retry_(u32& bud) {
  bud = __builtin_amdgcn_readfirstlane(bud);
  asm volatile("s_sleep 2\n\ts_sub_u32 %0, %0, 16\n\ts_cselect_b32 %0, 0, %0" : "+s"(bud) :: "memory", "scc");
  return bud != 0u;
}
#define glb_retry() glb_retry_(bud)
__device__ __forceinline__ u32x4 gld(rsrc_t r, u32 voff, u32 soff) { return __builtin_amdgcn_raw_buffer_load_b128(r, voff, soff, SC1); }
__device__ __forceinline__ u32x4 gld0(rsrc_t r, u32 voff, u32 soff) { return __builtin_amdgcn_raw_buffer_load_b128(r, voff, soff, 0); }
__device__ __forceinline__ void gst(rsrc_t r, u32x4 v, u32 voff, u32 soff) { __builtin_amdgcn_raw_buffer_store_b128(v, r, voff + soff, 0, SC1); }
__device__ __forceinline__ u32 tagx(u32x4 v, u32 tag) { return (v[1] ^ tag) | (v[3] ^ tag); }
__device__ __forceinline__ bool allz(u32 x) { return __builtin_amdgcn_ballot_w64(x != 0u) == 0ull; }
template <int N> __device__ __forceinline__ void fetch(LAS unsigned char* L, rsrc_t r, u32 voff, u32 ss, u32 tag, u32 dst, u32 ds, u32 flag, u32& bud) {
  u32x4 v[N];
  for (;;) {
    u32 bad = 0;
#pragma unroll
    for (int i = 0; i < N; ++i) v[i] = gld(r, voff + i * ss, 0);
#pragma unroll
    for (int i = 0; i < N; ++i) bad |= tagx(v[i], tag);
    if (allz(bad) || !glb_retry()) break;
  }
#pragma unroll
  for (int i = 0; i < N; ++i) *(LAS u32x2*)(L + dst + i * ds) = u32x2{v[i][0], v[i][2]};
  cw_st(flag, 1u);
}
#define DMA(r, ldsoff, voff, soff, imm, aux) __builtin_amdgcn_raw_ptr_buffer_load_lds((r), (LAS void*)(L + (ldsoff)), 16, (voff), (soff), (imm), (aux))

#define STAMP(i) do {} while (0)
#define STAMP_BEGIN() do {} while (0)
#define STAMP_END() do {} while (0)

#define VMW(n) asm volatile("s_waitcnt vmcnt(" #n ")" ::: "memory")
#define ZERO4 f32x4{0.f, 0.f, 0.f, 0.f}
#define MAX8(a, b) fmaxf(fmaxf(fmaxf(a[0], a[1]), fmaxf(a[2], a[3])), fmaxf(fmaxf(b[0], b[1]), fmaxf(b[2], b[3])))
constexpr float LAZY = 192.f;
template <int NA> __device__ __forceinline__ u32x4 soft(const f32x4 s0, const f32x4 s1, float& m, float& l, f32x4* acc) {
  const float mx = MAX8(s0, s1);
  if (__builtin_amdgcn_ballot_w64(mx > m + LAZY) != 0ull) {
    const float mn = fmaxf(m, xg_max(mx)), al = ex2((m - mn) * CS);
    m = mn; l *= al;
#pragma unroll
    for (int i = 0; i < NA; ++i) acc[i] *= al;
  }
  const float mc = -m * CS;
  float pr[8];
#pragma unroll
  for (int i = 0; i < 4; ++i) { pr[i] = ex2(fmaf(s0[i], CS, mc)); pr[4 + i] = ex2(fmaf(s1[i], CS, mc)); }
  l += ((pr[0] + pr[1]) + (pr[2] + pr[3])) + ((pr[4] + pr[5]) + (pr[6] + pr[7]));
  return u32x4{pk(pr[0], pr[1]), pk(pr[2], pr[3]), pk(pr[4], pr[5]), pk(pr[6], pr[7])};
}
__device__ __forceinline__ u32x4 pick2(const f32x4 d0, const f32x4 d1, int q, u32 tag) {
  const float x0 = (q & 2) ? d1[0] : d0[0], x1 = (q & 2) ? d1[1] : d0[1], x2 = (q & 2) ? d1[2] : d0[2], x3 = (q & 2) ? d1[3] : d0[3];
  return u32x4{asu((q & 1) ? x2 : x0), tag, asu((q & 1) ? x3 : x1), tag};
}

constexpr int IT_UK = 0, IT_C = 4, IT_UV = 20, IT_W = 24;
constexpr u32 ALL = 1u << 20;
template <int N> __device__ __forceinline__ void vmw() { asm volatile("s_waitcnt vmcnt(%0)" ::"i"(N) : "memory"); }

__global__ void __launch_bounds__(384, 2) mk_kernel(Args a) {
  __shared__ __attribute__((aligned(16))) unsigned char smem[LDS_BYTES];
  LAS unsigned char* L = (LAS unsigned char*)smem;
  const int lane = threadIdx.x & 63, wave = __builtin_amdgcn_readfirstlane(threadIdx.x >> 6);
  const int g = blockIdx.x, gi = g >> 3, hkv = 2 * (g & 7) + (gi >> 4), j = gi & 15, b = j >> 2, s = j & 3;
  const int it0 = s < 2 ? 2 : 4, itu = it0 + NST;
  const int ai = (gi >> 2) * 3 + s, am = ai >> 1, ah = ((g & 7) + 8 * (ai & 1) + 3 * am) & 15, ab = am & 3, as = am >> 2;
  const int bq = (g & 7) >> 1, sp = (g & 1) * 8 + (gi >> 2);
  u32 bud = SPIN_BUDGET;
  STAMP_BEGIN();

  if (__builtin_expect(wave == 0, 1)) {
    __builtin_amdgcn_s_setprio(3);
    cw_st(CTL + lane * 4, 0u);
    if (s < 3) {
      const rsrc_t rw = mkrs(a.in[s < 2 ? 7 : 3], 576u * 8192u), rx = mkrs(a.in[s < 2 ? 5 : 0], 4u * 576u * 2u);
      const u32 vo = s < 2 ? (lane >> 2) * 8192 + (lane & 3) * 16 : ((lane >> 2) & 7) * 8192 + (lane & 3) * 16 + (lane >> 5) * 64;
      const u32 st = s < 2 ? 16 * 8192 : 8 * 8192;
      u32 so = hkv * 512 + b * 128 + (s == 1 ? 64 : 0);
#pragma unroll
      for (int i = 0; i < 36; ++i, so += st) DMA(rw, i * 1024, vo, so, 0, AUX_W);
#pragma unroll
      for (int i = 0; i < 5; ++i) DMA(rx, SB_OFF + i * 1024, lane * 16, i * 1024, 0, 0);
      asm volatile("s_waitcnt lgkmcnt(0)" ::: "memory"); __builtin_amdgcn_s_barrier();
      if (s == 2) {
#pragma unroll
        for (int i = 36; i < 72; ++i, so += st) { if ((i & 3) == 0) vmw<40>(); DMA(rw, i * 1024, vo, so, 0, AUX_W); }
      }
    }
    const u32 vu = (lane >> 5) * 8192 + (((lane & 31) ^ ((lane >> 5) << 1)) << 4);
    if (s == 3) {
      const rsrc_t r = mkrs(a.in[8], 512u * 8192u);
      u32 so = (u32)b * (128u * 8192u) + hkv * 512u;
#pragma unroll 1
      for (int t = 0; t < 4; ++t) {
        if (t == 2) { asm volatile("s_waitcnt lgkmcnt(0)" ::: "memory"); __builtin_amdgcn_s_barrier(); }
        if (t == 3) { VMW(32); cw_st(W_FULL, 1); }
#pragma unroll
        for (int i = 0; i < 16; ++i, so += 2 * 8192) DMA(r, t * SLOT + i * 1024, vu ^ (swz((2 * i) & 15) << 4), so, 0, AUX_W);
      }
      VMW(32); cw_st(W_FULL, 2);
    }
    u32 vq[4];
#pragma unroll
    for (int q = 0; q < 4; ++q) vq[q] = (lane >> 4) * 256 + (((lane & 15) ^ swz(4 * q + (lane >> 4))) << 4);
    int pub = 0, it = it0;
#define SLOT_WAIT() do { if (it >= NS) { const u32 need = 4u * (it >> 3), wa = W_FREE + (it & 7) * 4; \
      if (cw_ld(wa) < need) { VMW(16); if (it - 1 > pub) cw_st(W_FULL, it - 1); VMW(0); pub = it; cw_st(W_FULL, pub); cw_wait(wa, need); } } } while (0)
#define LSTAMP() do { if (it == 4) STAMP(2); if (it == 8) STAMP(3); if (it == 12) STAMP(4); if (it == 16) STAMP(5); if (it == 20) STAMP(6); if (it == 24) STAMP(7); if (it == 28) STAMP(8); if (it == 32) STAMP(9); if (it == 36) STAMP(10); if (it == 40) STAMP(11); if (it == 44) STAMP(12); } while (0)
#define PACE(LW, k) do { vmw<LW>(); if ((k) == ((LW / 4) & 3) && it - LW / 16 > pub) { pub = it - LW / 16; cw_st(W_FULL, pub); } } while (0)
    if (s < 3) {
      const rsrc_t rk = mkrs(a.in[1], 1u << 26), rv = mkrs(a.in[2], 1u << 26), rt = mkrs((const char*)a.ws + WS_TK + ah * 128, 4u);
      u32 so = ((u32)(ab * 16 + ah) * 128u + as * NST) * 8192u;
      asm volatile("v_mov_b32 v254, 1" ::: "v254");
      asm volatile("" : "+s"(it));
      if (s < 2 && HOLD_ST) { VMW(0); pub = 2; cw_st(W_FULL, 2); cw_wait(W_QS, 2); }
#pragma unroll 1
      for (;; ++it) {
        const u32 sb = (it & 7) * SLOT;
        SLOT_WAIT();
        LSTAMP();
        PACE(LW_A, 0);
        if (it >= itu) {
          u32 t;
          asm volatile("v_readlane_b32 %0, v254, %1\n\tv_writelane_b32 v254, 1, %1\n\ts_nop 4" : "=&s"(t) : "s"((it - itu) & 3) : "memory", "v254");
          if (t >= (u32)NTICK) break;
          cw_st(W_TK + (it & 7) * 4, t);
          so = (((t & 3u) * 16u + ah) * 128u + 3 * NST + (t >> 2)) * 8192u;
        }
        if (it >= itu - 2) {
          unsigned long long ex;
          asm volatile("s_mov_b64 %0, exec\n\ts_lshl_b64 exec, 1, %2\n\tbuffer_atomic_add v254, off, %1, 0 sc0\n\ts_mov_b64 exec, %0"
                       : "=&s"(ex) : "s"(rt), "s"((it - (itu - 2)) & 3) : "memory", "scc", "v254");
        }
#pragma unroll
        for (int k = 0; k < 2; ++k) {
          if (k) PACE(LW_A, 1);
          DMA(rk, sb + k * 4096, vq[0], so + k * 4096, 0, AUX_K); DMA(rk, sb + k * 4096, vq[1], so + k * 4096, 1024, AUX_K);
          DMA(rk, sb + k * 4096, vq[2], so + k * 4096, 2048, AUX_K); DMA(rk, sb + k * 4096, vq[3], so + k * 4096, 3072, AUX_K);
        }
#pragma unroll
        for (int k = 0; k < 2; ++k) {
          PACE(LW_A, k + 2);
          DMA(rv, sb + 8192 + k * 4096, vq[0], so + k * 4096, 0, AUX_V); DMA(rv, sb + 8192 + k * 4096, vq[1], so + k * 4096, 1024, AUX_V);
          DMA(rv, sb + 8192 + k * 4096, vq[2], so + k * 4096, 2048, AUX_V); DMA(rv, sb + 8192 + k * 4096, vq[3], so + k * 4096, 3072, AUX_V);
        }
        so += 8192u;
      }
      cw_st(W_END, it);
    } else {
      asm volatile("" : "+s"(it));
#pragma unroll 1
      for (; it < IT_W; ++it) {
        const u32 sb = (it & 7) * SLOT;
        if (it == GATE1 || it == GATE2) {
          VMW(16); if (it - 1 > pub) cw_st(W_FULL, it - 1);
          VMW(0); pub = it; cw_st(W_FULL, it);
          if (it == GATE1) cw_wait(W_QS, 5);
          if (it == GATE2) cw_wait(W_GI, 4);
        }
        SLOT_WAIT();
        LSTAMP();
        if (it < IT_UV) {
          const rsrc_t r = mkrs(a.in[6], 1u << 24);
          const u32 so = ((u32)bq * 4096u + sp * 256u + (it - IT_C) * 16u) * 1024u;
#pragma unroll
          for (int k = 0; k < 4; ++k) {
            PACE(LW_B, k);
            DMA(r, sb + k * 4096, (lane ^ swz(4 * k + 0)) << 4, so + k * 4096, 0, AUX_C);
            DMA(r, sb + k * 4096, (lane ^ swz(4 * k + 1)) << 4, so + k * 4096, 1024, AUX_C);
            DMA(r, sb + k * 4096, (lane ^ swz(4 * k + 2)) << 4, so + k * 4096, 2048, AUX_C);
            DMA(r, sb + k * 4096, (lane ^ swz(4 * k + 3)) << 4, so + k * 4096, 3072, AUX_C);
          }
        } else {
          const rsrc_t r = mkrs(a.in[9], 512u * 8192u);
          const u32 so = ((u32)b * 128u + (it - IT_UV) * 32u) * 8192u + hkv * 512u;
#pragma unroll
          for (int i = 0; i < 16; ++i) { if ((i & 3) == 0) PACE(LW_B, i >> 2); DMA(r, sb + i * 1024, vu ^ (swz((2 * i) & 15) << 4), so + i * (2 * 8192), 0, AUX_W); }
        }
      }
    }
    STAMP(13);
    {
      const rsrc_t r = mkrs(a.in[s < 2 ? 4 : 10], 4096u * 1152u);
      u32 sw = ((u32)hkv * 256u + (s & 1) * 128u) * 1152u + b * 288u;
#pragma unroll 1
      for (int t = 0; t < 2; ++t, ++it, sw += 64u * 1152u) {
        const u32 sb = (it & 7) * SLOT;
        SLOT_WAIT();
        int ln = lane; asm volatile("" : "+v"(ln));
#pragma unroll
        for (int i = 0; i < 18; ++i) { if (i < 16 && (i & 3) == 0) PACE(LW_A, i >> 2); const u32 ci = i * 64 + ln; DMA(r, sb + i * 1024, ci * 16 + ((ci * 3641u) >> 16) * 864u, sw, 0, AUX_W); }
      }
    }
    VMW(36); if (it - 2 > pub) cw_st(W_FULL, it - 2);
    VMW(0); cw_st(W_FULL, ALL);
    STAMP(1);
    STAMP_END();
    return;
  }

  const int c = wave - 1, g4 = lane >> 4, n = lane & 15, q = n >> 2, p = lane & 3;
  const rsrc_t rws = mkrs(a.ws, WS_END);
  __builtin_amdgcn_s_barrier();
  const char* wsp = (const char*)a.ws; asm volatile("" : "+s"(wsp));
  const u32 tag = *(const __attribute__((address_space(4))) u32*)(wsp + WS_EPOCH) + 1u;
  if (c == 4) {
    cw_wait(W_FULL, s == 3 ? GATE1 : s == 2 ? POLL_A2 : POLL_A);
    STAMP(1);
    fetch<4>(L, rws, lane * 16 + WS_QG + (s < 3 ? ah * 128 : 8192 + hkv * 128) * 8, 16384, tag, (s < 3 ? QA_OFF : OB_OFF) + lane * 8, s < 3 ? 512 : OB_STR, W_QA, bud);
    if (s == 3) {
      STAMP(3);
      cw_wait(W_QS, 5); cw_wait(W_FULL, GATE2);
      fetch<1>(L, rws, lane * 16 + WS_HINT, 0, tag, MG_OFF + lane * 8, 0, W_HINT, bud);
      STAMP(4);
      cw_wait(W_BP, 4);
      fetch<4>(L, rws, n * 32 + (g4 & 1) * 16 + WS_SG + hkv * 2048, 512, tag, MG_OFF + lane * 8, 512, W_SGF, bud);
    }
    STAMP(2);
    STAMP_END();
    return;
  }
  if (s == 2 || (s < 2 && c < 2)) {
    const int wd = s >> 1;
    const u32 aw = ((g4 * 512) << wd) + (c >> 1) * 512 + q * 64 + (c & 1) * 32 + 8 * p, ax = SB_OFF + p * 1152 + g4 * 16;
    const u32 vo = (n * 2048 + 2 * g4) * 8, so = WS_QG + ((wd ^ 1) * 8192 + hkv * 128 + b * 32 + (s == 1 ? 16 : 0) + 8 * c) * 8;
    if (j == 2 && c == 0) { __builtin_amdgcn_raw_buffer_store_b32(0u, rws, WS_TK + hkv * 128, 0, SC1); VMW(0); }
    cw_wait(W_FULL, it0);
    STAMP(1);
    u32x4 xr[18], wf[18];
#pragma unroll
    for (int ks = 0; ks < 18; ++ks) { const u32 ad = aw + ((ks * 2048) << wd); wf[ks] = ldtr2(L, ad, ad + 256); xr[ks] = ld128(L, ax + ks * 64); }
    if (lane < it0) asm volatile("ds_add_u32 %0, %1" ::"v"(W_FREE + lane * 4), "v"(2u >> wd) : "memory");
    __builtin_amdgcn_sched_barrier(0);
    f32x4 a0 = ZERO4, a1 = ZERO4;
#pragma unroll
    for (int ks = 0; ks < 18; ks += 2) { a0 = mfma(wf[ks], xr[ks], a0); a1 = mfma(wf[ks + 1], xr[ks + 1], a1); }
    a0 += a1;
    if (n < 4) gst(rws, u32x4{pk(a0[0], a0[1]), tag, pk(a0[2], a0[3]), tag}, vo, so);
    cw_add(W_QS, 1, lane);
    STAMP(2);
  }

  int itw = IT_W;
  if (s < 3) {
    float m = -1e30f, l = 0.f;
    f32x4 acc[8];
#pragma unroll
    for (int i = 0; i < 8; ++i) acc[i] = ZERO4;
    u32x4 qf[4], kf[8], vf[8];
    const u32 lpK = n * 256 + ((g4 ^ swz(n)) << 4), lpQ = QA_OFF + (n & 1) * 256 + g4 * 16;
    const u32 lpV = (4 * g4 + q) * 256 + (((p >> 1) ^ swz(4 * g4 + q)) << 4) + 8 * (p & 1);
#define READ_KV() do { const u32 sb = (it & 7) * SLOT; \
      _Pragma("unroll") for (int kk = 0; kk < 4; ++kk) { const u32 ak = (sb + lpK) ^ (kk << 6); kf[kk] = ld128(L, ak); kf[4 + kk] = ld128(L, ak + 4096); } \
      _Pragma("unroll") for (int ct = 0; ct < 8; ++ct) { const u32 av = (sb + lpV) ^ (ct << 5); vf[ct] = ldtr2(L, av + 8192, av + 12288); } \
      cw_add(W_FREE + (it & 7) * 4, 4, lane); } while (0)
#define BLOCK() do { f32x4 s0 = ZERO4, s1 = ZERO4; \
      _Pragma("unroll") for (int kk = 0; kk < 4; ++kk) { s0 = mfma(kf[kk], qf[kk], s0); s1 = mfma(kf[4 + kk], qf[kk], s1); } \
      const u32x4 pf = soft<8>(s0, s1, m, l, acc); \
      _Pragma("unroll") for (int ct = 0; ct < 8; ++ct) acc[ct] = mfma(vf[ct], pf, acc[ct]); } while (0)
    bool first = true;
    int it = it0 + ((c + 2) & 3);
#pragma unroll 1
    for (; it < itu; it += 4) {
      cw_wait(W_FULL, it + 1);
      READ_KV();
      if (first) {
        first = false;
        cw_wait(W_QA, 1);
        STAMP(3);
#pragma unroll
        for (int kk = 0; kk < 4; ++kk) qf[kk] = ld128(L, lpQ + ab * 512 + kk * 64);
      }
      BLOCK();
    }
    STAMP(8);
    l = xg_sum(l);
    if (n < 2) {
#pragma unroll
      for (int ct = 0; ct < 8; ++ct) *(LAS f32x4*)(L + MG_OFF + (c * 2 + n) * 512 + ct * 64 + g4 * 16) = acc[ct];
      if (g4 == 0) *(LAS f32x2*)(L + MS_OFF + (c * 2 + n) * 8) = f32x2{m, l};
    }
    if (cw_add_rtn(W_MRG, 1, lane) == 3u) {
      const int hd = lane >> 5;
      f32x2 sv[4]; f32x4 ov[4];
#pragma unroll
      for (int i = 0; i < 4; ++i) { sv[i] = *(LAS f32x2*)(L + MS_OFF + (i * 2 + hd) * 8); ov[i] = *(LAS f32x4*)(L + MG_OFF + i * 1024 + lane * 16); }
      const float M = fmaxf(fmaxf(sv[0][0], sv[1][0]), fmaxf(sv[2][0], sv[3][0]));
      float Ls = 0.f; f32x4 o = ZERO4;
#pragma unroll
      for (int i = 0; i < 4; ++i) { const float w = ex2((sv[i][0] - M) * CS); Ls = fmaf(w, sv[i][1], Ls); o += w * ov[i]; }
      o *= 1.f / Ls;
      gst(rws, u32x4{pk(o[0], o[1]), tag, pk(o[2], o[3]), tag}, lane * 16, WS_AG + ((ah * 4 + ab) * 32 + as) * 1024);
      if ((lane & 31) == 0) gst(rws, u32x4{asu(M), tag, asu(Ls), tag}, hd * 512, WS_AS + (ah * 4 + ab) * 1024 + as * 16);
    }
    STAMP(9);
#pragma unroll 1
    for (;; it += 4) {
      cw_wait(W_FULL, it + 1);
      itw = cw_ld(W_END);
      if ((itw != 0 && it >= itw) || bud == 0u) break;
      const u32 t = cw_ld(W_TK + (it & 7) * 4);
#pragma unroll
      for (int kk = 0; kk < 4; ++kk) qf[kk] = ld128(L, lpQ + (t & 3u) * 512 + kk * 64);
      READ_KV();
      m = -1e30f; l = 0.f;
#pragma unroll
      for (int i = 0; i < 8; ++i) acc[i] = ZERO4;
      BLOCK();
      l = xg_sum(l);
      const f32x4 e0 = (n & 2) ? acc[1] : acc[0], e1 = (n & 2) ? acc[3] : acc[2], e2 = (n & 2) ? acc[5] : acc[4], e3 = (n & 2) ? acc[7] : acc[6];
      const f32x4 e4 = (n & 4) ? e1 : e0, e5 = (n & 4) ? e3 : e2, o = ((n & 8) ? e5 : e4) * (1.f / l);
      const u32 pr = ah * 4 + (t & 3u), rec = (3 + (t >> 2)) & 31u;
      gst(rws, u32x4{pk(o[0], o[1]), tag, pk(o[2], o[3]), tag}, (n & 1) * 512 + (n >> 1) * 64 + g4 * 16, WS_AG + (pr * 32 + rec) * 1024);
      if (lane < 2) gst(rws, u32x4{asu(m), tag, asu(l), tag}, lane * 512, WS_AS + pr * 1024 + rec * 16);
      STAMP(10);
    }
  } else {
  {
    cw_wait(W_FULL, IT_UK + c + 1);
    u32x4 wu[16];
    {
      const u32 lp = (IT_UK + c) * SLOT + n * 512 + ((g4 ^ swz(n)) << 4);
#pragma unroll
      for (int i = 0; i < 16; ++i) wu[i] = ld128(L, (lp ^ ((i & 3) << 6)) + ((i >> 2) & 1) * 8192 + (i >> 3) * 256);
    }
    cw_add(W_FREE + (IT_UK + c) * 4, 4, lane);
    STAMP(15);
    cw_wait(W_QA, 1);
    STAMP(3);
    f32x4 d[4] = {ZERO4, ZERO4, ZERO4, ZERO4};
#pragma unroll
    for (int i = 0; i < 8; ++i) {
      const u32x4 qv = ld128(L, OB_OFF + p * OB_STR + i * 64 + g4 * 16);
      d[(i >> 2) * 2] = mfma(wu[(i >> 2) * 8 + (i & 3)], qv, d[(i >> 2) * 2]); d[(i >> 2) * 2 + 1] = mfma(wu[(i >> 2) * 8 + 4 + (i & 3)], qv, d[(i >> 2) * 2 + 1]);
    }
    const f32x4 e0 = (q & 1) ? d[1] : d[0], e1 = (q & 1) ? d[3] : d[2], e = (q & 2) ? e1 : e0;
    gst(rws, u32x4{pk(e[0], e[1]), tag, pk(e[2], e[3]), tag}, p * 65536 + (g4 & 1) * 128 + (q >> 1) * 64 + (q & 1) * 32 + (g4 >> 1) * 16, WS_QAG + hkv * 4096 + (b * 4 + c) * 256);
    if (cw_add_rtn(W_QS, 1, lane) == 3u) { if (lane == 0) gst(rws, u32x4{tag, tag, tag, tag}, WS_HINT + (hkv * 4 + b) * 16, 0); cw_add(W_QS, 1, lane); }
    STAMP(4);
  }
  {
    const int ch = c & 1;
    u32x4 qa[8];
    {
      const u32 so = WS_QAG + bq * 65536 + (c >> 1) * 32768 + ch * 2048;
      cw_wait(W_HINT, 1);
      STAMP(5);
      u32x4 t[16];
#pragma unroll
      for (int i = 0; i < 16; ++i) t[i] = gld0(rws, (n >> 1) * 4096 + (n & 1) * 64 + g4 * 16 + i * 128, so);
      cw_add(W_GI, 1, lane);
      for (;;) {
        u32 bad = 0;
#pragma unroll
        for (int i = 0; i < 16; ++i) bad |= tagx(t[i], tag);
        if (allz(bad) || !glb_retry()) break;
#pragma unroll
        for (int i = 0; i < 16; ++i) t[i] = gld(rws, (n >> 1) * 4096 + (n & 1) * 64 + g4 * 16 + i * 128, so);
      }
#pragma unroll
      for (int i = 0; i < 8; ++i) qa[i] = u32x4{t[2 * i][0], t[2 * i][2], t[2 * i + 1][0], t[2 * i + 1][2]};
    }
    STAMP(6);
    float mb = -1e30f, lb = 0.f;
    f32x4 ob[16];
#pragma unroll
    for (int i = 0; i < 16; ++i) ob[i] = ZERO4;
    const u32 lpR = n * 1024 + ((g4 ^ swz(n)) << 4) + ch * 512;
    const u32 lpC = (4 * g4 + q) * 1024 + (((p >> 1) ^ swz(4 * g4 + q)) << 4) + 8 * (p & 1) + ch * 512;
#pragma unroll 1
    for (int st = 0; st < 8; ++st) {
      const int it = IT_C + 2 * st;
      const u32 sb = (it & 7) * SLOT;
      const u32 xw = ((st & 1) ? (4 + c) * SLOT + 16384 : SB_OFF + c * 2048) + lane * 16, xr = ((st & 1) ? (4 + (c ^ 1)) * SLOT + 16384 : SB_OFF + (c ^ 1) * 2048) + lane * 16;
      cw_wait(W_FULL, it + 2);
      u32x4 rf[16], cf[16];
#pragma unroll
      for (int kk = 0; kk < 8; ++kk) { const u32 ak = ((sb + lpR) ^ ((kk & 3) << 6)) + (kk >> 2) * 256; rf[2 * kk] = ld128(L, ak); rf[2 * kk + 1] = ld128(L, ak + SLOT); }
      __builtin_amdgcn_sched_barrier(0);
      f32x4 s0 = ZERO4, s1 = ZERO4;
#pragma unroll
      for (int kk = 0; kk < 8; ++kk) {
        s0 = mfma(rf[2 * kk], qa[kk], s0); s1 = mfma(rf[2 * kk + 1], qa[kk], s1);
        const u32 av = (sb + lpC) ^ (kk << 5);
        cf[kk] = ldtr2(L, av, av + SLOT); cf[8 + kk] = ldtr2(L, av + 256, av + 256 + SLOT);
        __builtin_amdgcn_sched_barrier(0);
      }
      *(LAS f32x4*)(L + xw) = s0;
      *(LAS f32x4*)(L + xw + 1024) = s1;
      cw_st(W_S + c * 4, st + 1);
      if (lane < 2) asm volatile("ds_add_u32 %0, %1" ::"v"(W_FREE + (it & 7) * 4 + lane * 4), "v"(1u) : "memory");
      cw_wait(W_S + (c ^ 1) * 4, st + 1);
      s0 += *(LAS f32x4*)(L + xr); s1 += *(LAS f32x4*)(L + xr + 1024);
      const u32x4 pf = soft<16>(s0, s1, mb, lb, ob);
#pragma unroll
      for (int ct = 0; ct < 16; ++ct) ob[ct] = mfma(cf[ct], pf, ob[ct]);
      if (st == 3) STAMP(16);
      if (st == 4) STAMP(17);
    }
    STAMP(7);
#pragma unroll
    for (int i = 0; i < 8; ++i) {
      auto x = __builtin_amdgcn_permlane16_swap(pk(ob[2 * i][0], ob[2 * i][1]), pk(ob[2 * i + 1][0], ob[2 * i + 1][1]), false, false);
      auto y = __builtin_amdgcn_permlane16_swap(pk(ob[2 * i][2], ob[2 * i][3]), pk(ob[2 * i + 1][2], ob[2 * i + 1][3]), false, false);
      gst(rws, u32x4{x[0], y[0], x[1], y[1]}, (n >> 1) * 512 + (n & 1) * 64 + (g4 & 1) * 32 + (g4 >> 1) * 16 + (i & 3) * 128,
          WS_PB + ((bq * 16 + sp) * 4 + ch * 2 + (i >> 2)) * 8192 + (c >> 1) * 4096);
    }
    lb = xg_sum(lb);
    if (g4 == 0 && ch == 0) *(LAS f32x2*)(L + ST_OFF + ((c >> 1) * 16 + n) * 8) = f32x2{mb, lb};
    VMW(0);
    if (cw_add_rtn(W_BP, 1, lane) == 3u) {
      const f32x2 ml = *(LAS f32x2*)(L + ST_OFF + (lane & 31) * 8);
      if (lane < 32) gst(rws, u32x4{asu(ml[0]), tag, asu(ml[1]), tag}, (lane >> 1) * 2048 + (lane & 1) * 16, WS_SG + (bq * 16 + sp) * 32);
    }
    STAMP(8);
  }
  {
    cw_wait(W_SGF, 1);
    STAMP(10);
    u32x4 pv[8];
#pragma unroll
    for (int i = 0; i < 8; ++i) pv[i] = gld(rws, (lane >> 5) * 32768 + (lane & 31) * 16, WS_PB + ((c * 16 + 2 * i) * 4 + b) * 8192 + hkv * 512);
    const f32x2 st = *(LAS f32x2*)(L + MG_OFF + c * 512 + lane * 8);
    const float w = ex2((st[0] - row_max(st[0])) * CS), cf = w / row_sum(w * st[1]);
    f32x4 o0 = ZERO4, o1 = ZERO4;
#pragma unroll
    for (int i = 0; i < 8; ++i) {
      const float f = asf(__builtin_amdgcn_ds_bpermute((((lane >> 2) & 1) * 16 + 2 * i + (lane >> 5)) * 4, asu(cf)));
      o0 += f * f32x4{bflo(pv[i][0]), bfhi(pv[i][0]), bflo(pv[i][1]), bfhi(pv[i][1])};
      o1 += f * f32x4{bflo(pv[i][2]), bfhi(pv[i][2]), bflo(pv[i][3]), bfhi(pv[i][3])};
    }
#pragma unroll
    for (int i = 0; i < 4; ++i) {
      auto t0 = __builtin_amdgcn_permlane32_swap(asu(o0[i]), asu(o0[i]), false, false); o0[i] = asf(t0[0]) + asf(t0[1]);
      auto t1 = __builtin_amdgcn_permlane32_swap(asu(o1[i]), asu(o1[i]), false, false); o1[i] = asf(t1[0]) + asf(t1[1]);
    }
    if (lane < 32) *(LAS u32x4*)(L + OB_OFF + c * OB_STR + ((lane >> 2) & 1) * 256 + (lane >> 3) * 64 + p * 16) = u32x4{pk(o0[0], o0[1]), pk(o0[2], o0[3]), pk(o1[0], o1[1]), pk(o1[2], o1[3])};
    cw_add(W_OB, 1, lane);
    cw_wait(W_FULL, IT_UV + 4);
    u32x4 wf[16];
    {
      const u32 r0 = 8 * (g4 & 1) + q;
      const u32 lp0 = (8 * g4 + q) * 512 + ((((p >> 1) + 8 * c) ^ swz(r0)) << 4) + 8 * (p & 1);
      const u32 lp1 = (8 * g4 + q + 4) * 512 + ((((p >> 1) + 8 * c) ^ swz(r0 + 4)) << 4) + 8 * (p & 1);
#pragma unroll
      for (int i = 0; i < 16; ++i) wf[i] = ldtr2(L, (lp0 ^ ((i & 3) << 5)) + ((IT_UV + (i >> 2)) & 7) * SLOT, (lp1 ^ ((i & 3) << 5)) + ((IT_UV + (i >> 2)) & 7) * SLOT);
    }
    cw_wait(W_OB, 4);
    STAMP(11);
    f32x4 d[4] = {ZERO4, ZERO4, ZERO4, ZERO4};
#pragma unroll
    for (int ks = 0; ks < 4; ++ks) {
      const u32x4 of = ld128(L, OB_OFF + p * OB_STR + (c >> 1) * 256 + ks * 64 + g4 * 16);
#pragma unroll
      for (int t = 0; t < 4; ++t) d[t] = mfma(wf[ks * 4 + t], of, d[t]);
    }
    {
      const u32 vo = p * 2048 + (q >> 1) * 128 + g4 * 32 + (q & 1) * 16, so = WS_CG + ((hkv * 4 + b) * 1024 + c * 64) * 8;
      gst(rws, pick2(d[0], d[1], q, tag), vo, so); gst(rws, pick2(d[2], d[3], q, tag), vo + 256, so);
    }
    STAMP(12);
  }
  }

  {
    cw_wait(W_FULL, ALL);
    u32x4 wf[12];
    {
      const u32 lp = (8 * g4 + q) * 288 + 8 * p + 32 * c;
#pragma unroll
      for (int ks = 0; ks < 4; ++ks) {
        const u32 ad = ((itw + (ks >> 1)) & 7) * SLOT + (ks & 1) * 9216 + lp;
        wf[ks] = ldtr2(L, ad, ad + 1152); wf[4 + ks] = ldtr2(L, ad + 128, ad + 1280);
        if (c == 0) wf[8 + ks] = ldtr2(L, ad + 256, ad + 1408);
      }
    }
    STAMP(18);
    const u32 ob = SB_OFF + c * OB_STR;
    if (s < 2) {
      constexpr int NL = (NREC + 1) / 2;
      const u32 so = WS_AG + (hkv * 4 + c) * 32768 + s * 512 + (lane & 31) * 16, ss = WS_AS + (hkv * 4 + c) * 1024 + s * 512 + ((lane & 31) < NREC ? lane & 31 : 0) * 16;
      u32x4 ov[NL], sv;
      for (;;) {
        sv = gld(rws, ss, 0);
#pragma unroll
        for (int i = 0; i < NL; ++i) ov[i] = gld(rws, so + (2 * i + 1 < NREC ? (lane >> 5) * 1024 : 0), i * 2048);
        u32 bad = tagx(sv, tag);
#pragma unroll
        for (int i = 0; i < NL; ++i) bad |= tagx(ov[i], tag);
        if (allz(bad) || !glb_retry()) break;
      }
      STAMP(19);
      const float ms = asf(sv[0]), w0 = ex2((ms - xg_max(row_max(ms))) * CS) * asf(sv[2]), w = lane < NREC ? w0 : 0.f, cf = w / xg_sum(row_sum(w));
      f32x4 o = ZERO4;
#pragma unroll
      for (int i = 0; i < NL; ++i) {
        const float f0 = asf(__builtin_amdgcn_readlane(asu(cf), 2 * i)), f1 = asf(__builtin_amdgcn_readlane(asu(cf), 2 * i + 1));
        o += (lane < 32 ? f0 : f1) * f32x4{bflo(ov[i][0]), bfhi(ov[i][0]), bflo(ov[i][2]), bfhi(ov[i][2])};
      }
#pragma unroll
      for (int i = 0; i < 4; ++i) { auto t = __builtin_amdgcn_permlane32_swap(asu(o[i]), asu(o[i]), false, false); o[i] = asf(t[0]) + asf(t[1]); }
      *(LAS u32x2*)(L + ob + (lane & 31) * 8) = u32x2{pk(o[0], o[1]), pk(o[2], o[3])};
    } else {
      const u32 so = WS_CG + (hkv * 4096 + c * 256 + (s & 1) * 128) * 8;
      u32x4 cv[4];
#pragma unroll
      for (int i = 0; i < 4; ++i) cv[i] = gld0(rws, lane * 16, so + i * 8192);
      for (;;) {
        u32 bad = 0;
#pragma unroll
        for (int i = 0; i < 4; ++i) bad |= tagx(cv[i], tag);
        if (allz(bad) || !glb_retry()) break;
#pragma unroll
        for (int i = 0; i < 4; ++i) cv[i] = gld(rws, lane * 16, so + i * 8192);
      }
      STAMP(19);
      *(LAS u32*)(L + ob + lane * 4) = pk((asf(cv[0][0]) + asf(cv[1][0])) + (asf(cv[2][0]) + asf(cv[3][0])), (asf(cv[0][2]) + asf(cv[1][2])) + (asf(cv[2][2]) + asf(cv[3][2])));
    }
    cw_add(W_PJ, 1, lane);
    cw_wait(W_PJ, 4);
    f32x4 d0 = ZERO4, d1 = ZERO4, d2 = ZERO4;
#pragma unroll
    for (int ks = 0; ks < 4; ++ks) {
      const u32x4 of = ld128(L, SB_OFF + p * OB_STR + ks * 64 + g4 * 16);
      d0 = mfma(wf[ks], of, d0); d1 = mfma(wf[4 + ks], of, d1);
      if (c == 0) d2 = mfma(wf[8 + ks], of, d2);
    }
    const u32 vo = p * 1152 + g4 * 32 + (q & 1) * 16, so = WS_YG + ((((s >> 1) * 4 + b) * 32 + hkv * 2 + (s & 1)) * 576 + c * 16) * 8;
    gst(rws, pick2(d0, d1, q, tag), vo + (q >> 1) * 512, so);
    if (c == 0 && q < 2) gst(rws, pick2(d2, d2, q, tag), vo + 1024, so);
    STAMP(20);
  }
  if (s == 3) {
    const int br = c & 1, bb = hkv >> 2, c0 = 36 * (hkv & 3) + 18 * (c >> 1);
    const u32 so = WS_YG + ((br * 4 + b) * 32 * 576 + bb * 144 + c0) * 8;
    for (;;) {
      const u32x4 t = gld(rws, hkv * 2 * 4608, so);
      if (allz(tagx(t, tag)) || !glb_retry()) break;
      __builtin_amdgcn_s_sleep(YSLEEP);
    }
    u32x4 yv[5];
    for (;;) {
      u32 bad = 0;
#pragma unroll
      for (int i = 0; i < 5; ++i) yv[i] = gld(rws, (lane & 31) * 4608 + (i < 4 ? (lane >> 5) * 16 : 0) + i * 32, so);
#pragma unroll
      for (int i = 0; i < 5; ++i) bad |= tagx(yv[i], tag);
      if (allz(bad) || !glb_retry()) break;
    }
    STAMP(21);
    u32 r = 0;
#pragma unroll
    for (int i = 0; i < 5; ++i) {
      float y0 = row_sum(asf(yv[i][0])), y1 = row_sum(asf(yv[i][2]));
      auto t0 = __builtin_amdgcn_permlane16_swap(asu(y0), asu(y0), false, false); auto t1 = __builtin_amdgcn_permlane16_swap(asu(y1), asu(y1), false, false);
      const u32 v = pk(asf(t0[0]) + asf(t0[1]), asf(t1[0]) + asf(t1[1]));
      r = (lane & 31) == i ? v : r;
    }
    if ((lane & 31) < 4 || lane == 4)
      __builtin_amdgcn_raw_buffer_store_b32(r, mkrs(a.out, 4u * 1152u * 2u), ((lane & 31) * 2 + (lane >> 5)) * 4, (bb * 1152 + br * 576 + b * 144 + c0) * 2, 0);
    if (g == 24 && c == 0 && lane == 0) __builtin_amdgcn_raw_buffer_store_b32(tag, rws, 0, WS_EPOCH, SC1);
    STAMP(22);
  }
  STAMP_END();
}

extern "C" void kernel_launch(void* const* d_in, const int* in_sizes, int n_in, void* d_out, int out_size, void* d_ws, size_t ws_size, hipStream_t stream) {
  Args a;
  for (int i = 0; i < 11; ++i) a.in[i] = d_in[i];
  a.out = d_out;
  a.ws = d_ws;
  mk_kernel<<<256, 384, 0, stream>>>(a);
}
```

```cpp
#include <hip/hip_runtime.h>
#include <stdint.h>

#define LAS __attribute__((address_space(3)))
typedef unsigned u32;
typedef u32 u32x2 __attribute__((ext_vector_type(2)));
typedef u32 u32x4 __attribute__((ext_vector_type(4)));
typedef float f32x2 __attribute__((ext_vector_type(2)));
typedef float f32x4 __attribute__((ext_vector_type(4)));
typedef short s16x4 __attribute__((ext_vector_type(4)));
typedef __bf16 bf16x2 __attribute__((ext_vector_type(2)));
typedef __bf16 bf16x8 __attribute__((ext_vector_type(8)));
typedef __amdgpu_buffer_rsrc_t rsrc_t;

#define N0 37
#define N1 38
#define N2 40
constexpr int NSUM = N0 + N1 + N2, NU = 128 - NSUM, NREC = 3 + NU, NTICK = 4 * NU;
static_assert(NU >= 1 && NREC <= 16, "split");
#define LW_A 32
#define LW_B 28
#define HOLD_ST 1
#define POLL_A 9
#define POLL_A0 7
#define GATE1 8
#define GATE2 12
#define WTRIG 0
#define POLL_C (N1 + 2)
#define WMODE 3
#define OUT_AUX 0
#define CODE_PF 1
#define LW_W 44
#define FSLEEP 32
#define POLL_B GATE1
#define POLL_H GATE2
#define CSLEEP 2
#define AUX_K 2
#define AUX_V 2
#define AUX_C 2
#define AUX_W 2
static_assert(LW_A <= 32, "a ticket is read two items after it was drawn");

constexpr int SLOT = 18432, NS = 8, RING = SLOT * NS;
constexpr int CTL = RING;
constexpr int W_FULL = CTL, W_KF = CTL + 4, W_QA = CTL + 8, W_MRG = CTL + 12, W_HINT = CTL + 16, W_SGF = CTL + 20, W_OB = CTL + 24, W_BP = CTL + 28,
              W_FREE = CTL + 32, W_S = CTL + 64, W_GI = CTL + 80, W_QS = CTL + 84, W_PJ = CTL + 88, W_WT = CTL + 92, W_MF = CTL + 96, W_O2 = CTL + 100, W_TK = CTL + 128;
constexpr int ST_OFF = CTL + 256;
constexpr int OB_OFF = ST_OFF + 512, OB_STR = 576;
constexpr int SB_OFF = OB_OFF + 4 * OB_STR;
constexpr int QA_OFF = SB_OFF + 5120;
constexpr int MG_OFF = SB_OFF + 8192;
constexpr int MS_OFF = MG_OFF + 4096;
constexpr int LDS_BYTES = MS_OFF + 64;
static_assert(LDS_BYTES <= 163840 && (SB_OFF & 15) == 0 && (MG_OFF & 15) == 0, "lds");

constexpr u32 WS_EPOCH = 0, WS_HINT = 1024;
constexpr u32 WS_TK = 2048;
constexpr u32 WS_QG = 4096;
constexpr u32 WS_QAG = WS_QG + 2 * 4 * 2048 * 8;
constexpr u32 WS_AG = WS_QAG + 4 * 32 * 256 * 8;
constexpr u32 WS_AS = WS_AG + 64 * 16 * 1024;
constexpr u32 WS_SG = WS_AS + 64 * 2048;
constexpr u32 WS_CG = WS_SG + 4 * 16 * 32 * 16;
constexpr u32 WS_YG = WS_CG + 16 * 4 * 4 * 256 * 8;
constexpr u32 WS_PB = WS_YG + 2 * 32 * 18432;
constexpr u32 WS_END = WS_PB + 4 * 16 * 4 * 16 * 512;
constexpr u32 WS_DIAG = 8u << 20;
static_assert(WS_END <= WS_DIAG && (WS_PB & 127) == 0, "ws");

constexpr float CS = 0.08838834764831845f * 1.4426950408889634f;
constexpr int SC1 = 16;
constexpr u32 CODE_BYTES = 28672;
constexpr u32 SPIN_BUDGET = 1u << 19;

struct Args { const void* in[11]; void* out; void* ws; };

__device__ __forceinline__ rsrc_t mkrs(const void* p, u32 n) { return __builtin_amdgcn_make_buffer_rsrc((void*)p, 0, n, 0x00020000); }
constexpr __device__ __forceinline__ u32 swz(u32 r) { return (((r & 7u) ^ ((r >> 1) & 4u)) << 1) | ((r >> 3) & 1u); }
__device__ __forceinline__ f32x4 mfma(u32x4 a, u32x4 b, f32x4 c) {
  return __builtin_amdgcn_mfma_f32_16x16x32_bf16(__builtin_bit_cast(bf16x8, a), __builtin_bit_cast(bf16x8, b), c, 0, 0, 0);
}
__device__ __forceinline__ u32 pk(float lo, float hi) { f32x2 v = {lo, hi}; return __builtin_bit_cast(u32, __builtin_convertvector(v, bf16x2)); }
__device__ __forceinline__ float bflo(u32 v) { return __builtin_bit_cast(float, v << 16); }
__device__ __forceinline__ float bfhi(u32 v) { return __builtin_bit_cast(float, v & 0xffff0000u); }
__device__ __forceinline__ float asf(u32 v) { return __builtin_bit_cast(float, v); }
__device__ __forceinline__ u32 asu(float v) { return __builtin_bit_cast(u32, v); }
__device__ __forceinline__ float ex2(float x) { return __builtin_amdgcn_exp2f(x); }
__device__ __forceinline__ u32x4 ld128(LAS unsigned char* L, u32 a) { return *(LAS u32x4*)(L + a); }
__device__ __forceinline__ u32x4 ldtr2(LAS unsigned char* L, u32 a0, u32 a1) {
  const u32x2 x = __builtin_bit_cast(u32x2, __builtin_amdgcn_ds_read_tr16_b64_v4i16((LAS s16x4*)(L + a0)));
  const u32x2 y = __builtin_bit_cast(u32x2, __builtin_amdgcn_ds_read_tr16_b64_v4i16((LAS s16x4*)(L + a1)));
  return u32x4{x[0], x[1], y[0], y[1]};
}
__device__ __forceinline__ float xg_max(float x) {
  auto s = __builtin_amdgcn_permlane16_swap(asu(x), asu(x), false, false);
  x = fmaxf(asf(s[0]), asf(s[1]));
  auto t = __builtin_amdgcn_permlane32_swap(asu(x), asu(x), false, false);
  return fmaxf(asf(t[0]), asf(t[1]));
}
__device__ __forceinline__ float xg_sum(float x) {
  auto s = __builtin_amdgcn_permlane16_swap(asu(x), asu(x), false, false);
  x = asf(s[0]) + asf(s[1]);
  auto t = __builtin_amdgcn_permlane32_swap(asu(x), asu(x), false, false);
  return asf(t[0]) + asf(t[1]);
}
template <int CTRL> __device__ __forceinline__ float dpp(float x) { return asf(__builtin_amdgcn_mov_dpp(asu(x), CTRL, 0xf, 0xf, true)); }
__device__ __forceinline__ float row_sum(float x) { x += dpp<0x128>(x); x += dpp<0x124>(x); x += dpp<0x122>(x); x += dpp<0x121>(x); return x; }
__device__ __forceinline__ float row_max(float x) { x = fmaxf(x, dpp<0x128>(x)); x = fmaxf(x, dpp<0x124>(x)); x = fmaxf(x, dpp<0x122>(x)); x = fmaxf(x, dpp<0x121>(x)); return x; }

__device__ __forceinline__ u32 cw_ld(u32 a) { u32 v; asm volatile("ds_read_b32 %0, %1\n\ts_waitcnt lgkmcnt(0)" : "=v"(v) : "v"(a) : "memory"); return __builtin_amdgcn_readfirstlane(v); }
__device__ __forceinline__ void cw_st(u32 a, u32 v) { asm volatile("ds_write_b32 %0, %1" ::"v"(a), "v"(v) : "memory"); }
__device__ __forceinline__ void cw_full(u32 v) { asm volatile("ds_write2_b32 %0, %1, %1 offset1:1" ::"v"(W_FULL), "v"(v) : "memory"); }
__device__ __forceinline__ void cw_add(u32 a, u32 v, int lane) { if (lane == 0) asm volatile("ds_add_u32 %0, %1" ::"v"(a), "v"(v) : "memory"); asm volatile("" ::: "memory"); }
__device__ __forceinline__ u32 cw_add_rtn(u32 a, u32 v, int lane) {
  u32 r = 0;
  if (lane == 0) asm volatile("ds_add_rtn_u32 %0, %1, %2\n\ts_waitcnt lgkmcnt(0)" : "=v"(r) : "v"(a), "v"(v) : "memory");
  asm volatile("" ::: "memory");
  return __builtin_amdgcn_readfirstlane(r);
}
__device__ __forceinline__ u32 cw_wait_(u32 a, u32 want, u32& bud) {
  u32 t;
  bud = __builtin_amdgcn_readfirstlane(bud); want = __builtin_amdgcn_readfirstlane(want);
  asm volatile("1:\n\tds_read_b32 v255, %2\n\ts_waitcnt lgkmcnt(0)\n\tv_readfirstlane_b32 %0, v255\n\ts_cmp_ge_u32 %0, %3\n\ts_cbranch_scc1 2f\n\t"
               "s_sleep 1\n\ts_sub_u32 %1, %1, 1\n\ts_cbranch_scc0 1b\n\ts_mov_b32 %1, 0\n2:\n\ts_nop 1"
               : "=&s"(t), "+s"(bud) : "v"(a), "s"(want) : "memory", "scc", "v255");
  return t;
}
#define cw_wait(a, want) cw_wait_((a), (want), bud)
__device__ __forceinline__ bool glb_retry_(u32& bud) {
  bud = __builtin_amdgcn_readfirstlane(bud);
  asm volatile("s_sleep 1\n\ts_sub_u32 %0, %0, 16\n\ts_cselect_b32 %0, 0, %0" : "+s"(bud) :: "memory", "scc");
  return bud != 0u;
}
#define glb_retry() glb_retry_(bud)
__device__ __forceinline__ u32x4 gld(rsrc_t r, u32 voff, u32 soff) { return __builtin_amdgcn_raw_buffer_load_b128(r, voff, soff, SC1); }
__device__ __forceinline__ u32x4 gld0(rsrc_t r, u32 voff, u32 soff) { return __builtin_amdgcn_raw_buffer_load_b128(r, voff, soff, 0); }
__device__ __forceinline__ void gst(rsrc_t r, u32x4 v, u32 voff, u32 soff) { __builtin_amdgcn_raw_buffer_store_b128(v, r, voff + soff, 0, SC1); }
__device__ __forceinline__ u32 tagx(u32x4 v, u32 tag) { return (v[1] ^ tag) | (v[3] ^ tag); }
__device__ __forceinline__ bool allz(u32 x) { return __builtin_amdgcn_ballot_w64(x != 0u) == 0ull; }
template <int N> __device__ __forceinline__ void fetch(LAS unsigned char* L, rsrc_t r, u32 voff, u32 ss, u32 tag, u32 dst, u32 ds, u32 flag, u32& bud) {
  u32x4 v[N];
  for (;;) {
    u32 bad = 0;
#pragma unroll
    for (int i = 0; i < N; ++i) v[i] = gld(r, voff + i * ss, 0);
#pragma unroll
    for (int i = 0; i < N; ++i) bad |= tagx(v[i], tag);
    if (allz(bad) || !glb_retry()) break;
  }
#pragma unroll
  for (int i = 0; i < N; ++i) *(LAS u32x2*)(L + dst + i * ds) = u32x2{v[i][0], v[i][2]};
  cw_st(flag, 1u);
}
#define DMA(r, ldsoff, voff, soff, imm, aux) __builtin_amdgcn_raw_ptr_buffer_load_lds((r), (LAS void*)(L + (ldsoff)), 16, (voff), (soff), (imm), (aux))

#define STAMP(i) do {} while (0)
#define STAMP_BEGIN() do {} while (0)
#define STAMP_END() do {} while (0)

#define VMW(n) asm volatile("s_waitcnt vmcnt(" #n ")" ::: "memory")
#define ZERO4 f32x4{0.f, 0.f, 0.f, 0.f}
#define MAX8(a, b) fmaxf(fmaxf(fmaxf(a[0], a[1]), fmaxf(a[2], a[3])), fmaxf(fmaxf(b[0], b[1]), fmaxf(b[2], b[3])))
constexpr float LAZY = 192.f;
template <int NA> __device__ __forceinline__ u32x4 soft(const f32x4 s0, const f32x4 s1, float& m, float& l, f32x4* acc) {
  const float mx = MAX8(s0, s1);
  if (__builtin_amdgcn_ballot_w64(mx > m + LAZY) != 0ull) {
    const float mn = fmaxf(m, xg_max(mx)), al = ex2((m - mn) * CS);
    m = mn; l *= al;
#pragma unroll
    for (int i = 0; i < NA; ++i) acc[i] *= al;
  }
  const float mc = -m * CS;
  float pr[8];
#pragma unroll
  for (int i = 0; i < 4; ++i) { pr[i] = ex2(fmaf(s0[i], CS, mc)); pr[4 + i] = ex2(fmaf(s1[i], CS, mc)); }
  l += ((pr[0] + pr[1]) + (pr[2] + pr[3])) + ((pr[4] + pr[5]) + (pr[6] + pr[7]));
  return u32x4{pk(pr[0], pr[1]), pk(pr[2], pr[3]), pk(pr[4], pr[5]), pk(pr[6], pr[7])};
}
__device__ __forceinline__ u32x4 pick2(const f32x4 d0, const f32x4 d1, int q, u32 tag) {
  const float x0 = (q & 2) ? d1[0] : d0[0], x1 = (q & 2) ? d1[1] : d0[1], x2 = (q & 2) ? d1[2] : d0[2], x3 = (q & 2) ? d1[3] : d0[3];
  return u32x4{asu((q & 1) ? x2 : x0), tag, asu((q & 1) ? x3 : x1), tag};
}

constexpr int NPR = (NREC + 1) / 2;
template <int I> __device__ __forceinline__ void merge_rec(f32x4* o, const u32x4* t, float w) {
  if constexpr (I < NPR) { o[I & 1] += dpp<0x150 + 2 * I>(w) * f32x4{bflo(t[I][0]), bfhi(t[I][0]), bflo(t[I][2]), bfhi(t[I][2])}; merge_rec<I + 1>(o, t, w); }
}

constexpr int IT_UK = 0, IT_C = 4, IT_UV = 20, IT_W = 24;
constexpr u32 ALL = 1u << 20;
template <int N> __device__ __forceinline__ void vmw() { asm volatile("s_waitcnt vmcnt(%0)" ::"i"(N) : "memory"); }

__device__ __forceinline__ void read_w(LAS unsigned char* L, u32x4* wf, int c, int lane) {
  const u32 lp = (8 * (lane >> 4) + ((lane >> 2) & 3)) * 576 + 8 * (lane & 3) + 128 * c, t2 = c < 4 ? 64 : 0;
#pragma unroll
  for (int i = 0; i < 16; ++i) { const u32 ad = (i >> 2) * SLOT + lp + (i & 1) * 32 + ((i & 2) ? t2 : 0); wf[i] = ldtr2(L, ad, ad + 2304); }
}
__device__ __forceinline__ void project(LAS unsigned char* L, const u32x4* wf, rsrc_t rws, u32 so, u32 tag, int c, int lane, u32& bud) {
  const int g4 = lane >> 4, q = (lane >> 2) & 3, p = lane & 3;
  u32 ao = OB_OFF + p * OB_STR + g4 * 16, vo = so + c * 512 + p * 4608 + (q >> 1) * 128 + g4 * 32 + (q & 1) * 16;
  asm volatile("" : "+v"(ao), "+v"(vo));
  cw_wait(W_PJ, 4);
  f32x4 d[4] = {ZERO4, ZERO4, ZERO4, ZERO4};
  u32x4 of[4];
#pragma unroll
  for (int ks = 0; ks < 4; ++ks) of[ks] = ld128(L, ao + ks * 64);
  __builtin_amdgcn_sched_barrier(0);
#pragma unroll
  for (int ks = 0; ks < 4; ++ks) { d[0] = mfma(wf[ks * 4], of[ks], d[0]); d[1] = mfma(wf[ks * 4 + 1], of[ks], d[1]); }
#pragma unroll
  for (int ks = 0; ks < 4; ++ks) { d[2] = mfma(wf[ks * 4 + 2], of[ks], d[2]); d[3] = mfma(wf[ks * 4 + 3], of[ks], d[3]); }
  gst(rws, pick2(d[0], d[1], q, tag), vo, 0);
  if (c < 4) gst(rws, pick2(d[2], d[3], q, tag), vo + 256, 0);
}
__device__ __forceinline__ void final_sum(rsrc_t rws, void* out, int br, int hkv, int b, int i, u32 tag, int lane, u32& bud) {
  const int c0 = 36 * hkv + 8 * i + 2 * (lane >> 5);
  u32 vo = WS_YG + (br * 32 + (lane & 31)) * 18432 + (b * 576 + c0) * 8, oo = (b * 1152 + br * 576 + c0 + (lane & 1) * 4) * 2;
  const u32 v2 = i < 4 ? 32 : 0;
  const rsrc_t ro = mkrs(out, 4u * 1152u * 2u);
  asm volatile("" : "+v"(vo), "+v"(oo));
  u32x4 y, z;
  for (;;) { y = gld(rws, vo, 0); z = gld(rws, vo, v2); if (allz(tagx(y, tag) | tagx(z, tag)) || !glb_retry()) break; }
  float r[4] = {asf(y[0]), asf(y[2]), asf(z[0]), asf(z[2])};
#pragma unroll
  for (int k = 0; k < 4; ++k) { auto t = __builtin_amdgcn_permlane16_swap(asu(r[k]), asu(r[k]), false, false); r[k] = row_sum(asf(t[0]) + asf(t[1])); }
  if ((lane & 31) < (i < 4 ? 2 : 1)) __builtin_amdgcn_raw_buffer_store_b32((lane & 1) ? pk(r[2], r[3]) : pk(r[0], r[1]), ro, oo, 0, OUT_AUX);
}

__global__ void __launch_bounds__(384, 2) mk_kernel(Args a) {
  __shared__ __attribute__((aligned(16))) unsigned char smem[LDS_BYTES];
  LAS unsigned char* L = (LAS unsigned char*)smem;
  const int lane = threadIdx.x & 63, wave = __builtin_amdgcn_readfirstlane(threadIdx.x >> 6);
  const int g = blockIdx.x, gi = g >> 3, hkv = 2 * (g & 7) + (gi >> 4), j = gi & 15, b = j >> 2, s = j & 3;
  STAMP_BEGIN();
  if (wave == CODE_PF - 1) {
    unsigned long long pc; asm volatile("s_getpc_b64 %0" : "=s"(pc));
    DMA(mkrs((const void*)(pc & ~255ull), CODE_BYTES), MG_OFF, lane * 16, gi * 1024, 0, 0);
  }
  const u32 vu = (lane >> 5) * 8192 + (((lane & 31) ^ ((lane >> 5) << 1)) << 4);
  const u32 vw = ((lane >> 2) & 7) * 8192 + (lane & 3) * 16 + (lane >> 5) * 64;
  if (wave == 0) {
    if (s != 1) {
      const rsrc_t rw = mkrs(a.in[s == 0 ? 7 : s == 2 ? 3 : 8], s < 3 ? 576u * 8192u : 512u * 8192u);
      const u32 vo = s < 3 ? vw : vu, st = s < 3 ? 8 * 8192 : 2 * 8192, xm = s == 3 ? 0xf0u : 0u;
      u32 so = hkv * 512 + (s < 3 ? b * 128 : b * (128u * 8192u));
#pragma unroll
      for (int i = 0; i < 32; ++i, so += st) DMA(rw, i * 1024 + (i >= 16 && s == 3 ? 2048 : 0), vo ^ ((swz((2 * i) & 15) << 4) & xm), so, 0, AUX_W);
    }
    STAMP(14);
    cw_st(CTL + lane * 4, 0u);
    asm volatile("s_waitcnt lgkmcnt(0)" ::: "memory");
  }
  __builtin_amdgcn_s_barrier();
  const int ab = gi >> 3, ah = ((g & 7) + 2 * (gi & 4) + 3 * (ab + 4 * s)) & 15, as = s;
  const int ITU = 4 + (s == 0 ? N0 : s == 1 ? N1 : N2);
  const int bq = (g & 7) >> 1, sp = (g & 1) * 8 + (gi >> 2);
  u32 bud = SPIN_BUDGET;

  if (wave == 0) {
    __builtin_amdgcn_s_setprio(3);
    if ((s & 1) == 0) {
      const rsrc_t rw = mkrs(a.in[s ? 3 : 7], 576u * 8192u), rx = mkrs(a.in[s ? 0 : 5], 4u * 576u * 2u);
      u32 so = hkv * 512 + b * 128 + 32 * 65536;
#pragma unroll
      for (int i = 0; i < 5; ++i) DMA(rx, SB_OFF + i * 1024, lane * 16, i * 1024, 0, 0);
#pragma unroll 1
      for (int i = 32; i < 72; i += 4, so += 4 * 65536) {
        vmw<40>();
        DMA(rw, i * 1024, vw, so, 0, AUX_W); DMA(rw, i * 1024 + 1024, vw, so + 65536, 0, AUX_W); DMA(rw, i * 1024 + 2048, vw, so + 2 * 65536, 0, AUX_W); DMA(rw, i * 1024 + 3072, vw, so + 3 * 65536, 0, AUX_W);
      }
      VMW(36); cw_full(2);
    } else if (s == 3) {
      const rsrc_t r = mkrs(a.in[8], 512u * 8192u);
      u32 so = ((u32)b * 128u + 64u) * 8192u + hkv * 512u;
#pragma unroll 1
      for (int t = 2; t < 4; ++t) {
        if (t == 3) { VMW(32); cw_full(1); }
#pragma unroll
        for (int i = 0; i < 16; ++i, so += 2 * 8192) DMA(r, t * SLOT + i * 1024, vu ^ (swz((2 * i) & 15) << 4), so, 0, AUX_W);
      }
      VMW(32); cw_full(2);
    }
    u32 vq[4];
#pragma unroll
    for (int q = 0; q < 4; ++q) vq[q] = (lane >> 4) * 256 + (((lane & 15) ^ swz(4 * q + (lane >> 4))) << 4);
    int pub = 0, it = 4;
#define SLOT_WAIT() do { if (it >= NS) { const u32 need = 4u * (it >> 3), wa = W_FREE + (it & 7) * 4; \
      if (cw_ld(wa) < need) { VMW(16); if (it - 1 > pub) cw_full(it - 1); VMW(0); pub = it; cw_full(pub); cw_wait(wa, need); } } } while (0)
#define LSTAMP() do { if (it == 4) STAMP(2); if (it == 8) STAMP(3); if (it == 12) STAMP(4); if (it == 16) STAMP(5); if (it == 20) STAMP(6); if (it == 24) STAMP(7); if (it == 28) STAMP(8); if (it == 32) STAMP(9); if (it == 36) STAMP(10); if (it == 40) STAMP(11); if (it == 44) STAMP(12); } while (0)
#define PACE(LW, k) do { vmw<LW>(); if ((k) == ((LW / 4) & 3) && it - LW / 16 > pub) { pub = it - LW / 16; cw_full(pub); } } while (0)
#define LOAD_W(in_, LW) do { const rsrc_t r = mkrs(a.in[in_], 4096u * 1152u); u32 wo[9]; int ln = lane; asm volatile("" : "+v"(ln)); \
      _Pragma("unroll") for (int i = 0; i < 9; ++i) { const u32 ci = i * 64 + ln; wo[i] = ci * 16 + ((ci * 3641u) >> 17) * 576u; } \
      u32 sw = ((u32)hkv * 256u + (b & 1) * 128u) * 1152u + (b >> 1) * 576u; \
      _Pragma("unroll 1") for (int t = 0; t < 8; ++t, sw += 16u * 1152u) { _Pragma("unroll") for (int i = 0; i < 9; ++i) { if (i % 3 == 0) vmw<LW>(); DMA(r, t * 9216 + i * 1024, wo[i], sw, 0, AUX_W); } } } while (0)
    if (s == 1) LOAD_W(10, 32);
    asm volatile("" : "+s"(it));
    if (s < 3) {
      const rsrc_t rk = mkrs(a.in[1], 1u << 26), rv = mkrs(a.in[2], 1u << 26), rt = mkrs((const char*)a.ws + WS_TK + ah * 128, 4u);
      u32 so = ((u32)(ab * 16 + ah) * 128u + (s == 0 ? 0 : s == 1 ? N0 : N0 + N1)) * 8192u;
      asm volatile("v_mov_b32 v254, 1" ::: "v254");
      if (s == 0 && HOLD_ST) { VMW(0); pub = 4; cw_full(4); cw_wait(W_QS, 4); }
#pragma unroll 1
      for (;; ++it) {
        const u32 sb = (it & 7) * SLOT;
        SLOT_WAIT();
        LSTAMP();
        PACE(LW_A, 0);
        if (it >= ITU) {
          u32 t;
          asm volatile("v_readlane_b32 %0, v254, %1\n\tv_writelane_b32 v254, 1, %1\n\ts_nop 4" : "=&s"(t) : "s"(it & 3) : "memory", "v254");
          if (t >= (u32)NTICK) break;
          cw_st(W_TK + (it & 15) * 4, (it << 8) | t);
          so = (((t & 3u) * 16u + ah) * 128u + NSUM + (t >> 2)) * 8192u;
        }
        if (it >= ITU - 2) {
          unsigned long long ex;
          asm volatile("s_mov_b64 %0, exec\n\ts_lshl_b64 exec, 1, %2\n\tbuffer_atomic_add v254, off, %1, 0 sc0\n\ts_mov_b64 exec, %0"
                       : "=&s"(ex) : "s"(rt), "s"((it + 2) & 3) : "memory", "scc", "v254");
        }
#pragma unroll
        for (int k = 0; k < 2; ++k) {
          if (k) PACE(LW_A, 1);
          DMA(rk, sb + k * 4096, vq[0], so + k * 4096, 0, AUX_K); DMA(rk, sb + k * 4096, vq[1], so + k * 4096, 1024, AUX_K);
          DMA(rk, sb + k * 4096, vq[2], so + k * 4096, 2048, AUX_K); DMA(rk, sb + k * 4096, vq[3], so + k * 4096, 3072, AUX_K);
        }
#pragma unroll
        for (int k = 0; k < 2; ++k) {
          PACE(LW_A, k + 2);
          DMA(rv, sb + 8192 + k * 4096, vq[0], so + k * 4096, 0, AUX_V); DMA(rv, sb + 8192 + k * 4096, vq[1], so + k * 4096, 1024, AUX_V);
          DMA(rv, sb + 8192 + k * 4096, vq[2], so + k * 4096, 2048, AUX_V); DMA(rv, sb + 8192 + k * 4096, vq[3], so + k * 4096, 3072, AUX_V);
        }
        so += 8192u;
      }
      cw_st(W_TK + ((it + (lane & 3)) & 15) * 4, ((it + (lane & 3)) << 8) | 255u);
      STAMP(13);
    } else {
#pragma unroll 1
      for (; it < IT_W; ++it) {
        const u32 sb = (it & 7) * SLOT;
        if (it == GATE1 || it == GATE2) {
          VMW(16); if (it - 1 > pub) cw_full(it - 1);
          VMW(0); pub = it; cw_full(it);
          if (it == GATE1) cw_wait(W_QS, 5);
          if (it == GATE2) cw_wait(W_GI, 4);
        }
        SLOT_WAIT();
        LSTAMP();
        if (it < IT_UV) {
          const rsrc_t r = mkrs(a.in[6], 1u << 24);
          const u32 so = ((u32)bq * 4096u + sp * 256u + (it - IT_C) * 16u) * 1024u;
#pragma unroll
          for (int k = 0; k < 4; ++k) {
            PACE(LW_B, k);
            DMA(r, sb + k * 4096, (lane ^ swz(4 * k + 0)) << 4, so + k * 4096, 0, AUX_C);
            DMA(r, sb + k * 4096, (lane ^ swz(4 * k + 1)) << 4, so + k * 4096, 1024, AUX_C);
            DMA(r, sb + k * 4096, (lane ^ swz(4 * k + 2)) << 4, so + k * 4096, 2048, AUX_C);
            DMA(r, sb + k * 4096, (lane ^ swz(4 * k + 3)) << 4, so + k * 4096, 3072, AUX_C);
          }
        } else {
          const rsrc_t r = mkrs(a.in[9], 512u * 8192u);
          const u32 so = ((u32)b * 128u + (it & 3) * 32u) * 8192u + hkv * 512u;
#pragma unroll
          for (int i = 0; i < 16; ++i) { if ((i & 3) == 0) PACE(LW_B, i >> 2); DMA(r, sb + i * 1024, vu ^ (swz((2 * i) & 15) << 4), so + i * (2 * 8192), 0, AUX_W); }
        }
      }
      VMW(0); pub = it; cw_full(it);
      cw_wait(W_GI, 8);
      STAMP(15);
      LOAD_W(4, LW_W);
    }
    VMW(16); if (it - 1 > pub) cw_full(it - 1);
    VMW(8); cw_st(W_KF, ALL);
    VMW(0); cw_full(ALL);
    STAMP(1);
    STAMP_END();
    return;
  }

  const int c = wave - 1, g4 = lane >> 4, n = lane & 15, q = n >> 2, p = lane & 3;
  const rsrc_t rws = mkrs(a.ws, WS_END);
  const char* wsp = (const char*)a.ws; asm volatile("" : "+s"(wsp));
  const u32 tag = *(const __attribute__((address_space(4))) u32*)(wsp + WS_EPOCH) + 1u;
  const u32 yso = WS_YG + ((s == 1 ? 32 : 0) + hkv * 2 + (b & 1)) * 18432 + (b >> 1) * 2304;
  if (c == 4) {
    u32x4 wf[16];
    if (s == 1) {
      cw_wait(W_FULL, 4);
      read_w(L, wf, 4, lane);
      cw_st(W_MF, 1u);
    }
    cw_wait(W_FULL, s == 3 ? POLL_B : s == 0 ? POLL_A0 : POLL_A);
    STAMP(1);
    fetch<4>(L, rws, lane * 16 + WS_QG + (s < 3 ? ah * 128 : 8192 + hkv * 128) * 8, 16384, tag, (s < 3 ? QA_OFF : OB_OFF) + lane * 8, s < 3 ? 512 : OB_STR, W_QA, bud);
    STAMP(3);
    if (s == 3) {
      cw_wait(W_QS, 5); cw_wait(W_FULL, POLL_H);
      fetch<1>(L, rws, lane * 16 + WS_HINT, 0, tag, MG_OFF + lane * 8, 0, W_HINT, bud);
      STAMP(4);
      cw_wait(W_BP, 4);
      fetch<4>(L, rws, n * 32 + (g4 & 1) * 16 + WS_SG + hkv * 2048, 512, tag, MG_OFF + lane * 8, 512, W_SGF, bud);
      STAMP(2);
      for (int i = 0; i < 64; ++i) {
        if (__builtin_amdgcn_readfirstlane(__builtin_amdgcn_raw_buffer_load_b32(rws, WS_TK + hkv * 128, 0, SC1)) >= (u32)(WTRIG)) break;
        __builtin_amdgcn_s_sleep(4); asm volatile("" ::: "memory");
      }
      cw_st(W_WT, 1u);
      cw_wait(W_FULL, ALL);
      read_w(L, wf, 4, lane);
    } else if (s == 1) {
      cw_wait(W_FULL, POLL_C);
      const u32 so = WS_CG + hkv * 32768 + (b & 1) * 1024;
      for (;;) {
        const u32x4 t = gld(rws, q * 8192 + (p & 1) * 512, so);
        if (allz(tagx(t, tag)) || !glb_retry()) break;
        __builtin_amdgcn_s_sleep(16);
      }
      u32x4 cv[16];
      for (;;) {
        u32 bad = 0;
#pragma unroll
        for (int i = 0; i < 16; ++i) cv[i] = gld(rws, lane * 16 + (i & 1) * 2048, so + (i >> 1) * 4096);
#pragma unroll
        for (int i = 0; i < 16; ++i) bad |= tagx(cv[i], tag);
        if (allz(bad) || !glb_retry()) break;
      }
#pragma unroll
      for (int i = 0; i < 4; ++i)
        *(LAS u32*)(L + OB_OFF + i * OB_STR + lane * 4) = pk((asf(cv[i][0]) + asf(cv[4 + i][0])) + (asf(cv[8 + i][0]) + asf(cv[12 + i][0])), (asf(cv[i][2]) + asf(cv[4 + i][2])) + (asf(cv[8 + i][2]) + asf(cv[12 + i][2])));
      cw_st(W_PJ, 4u);
      STAMP(2);
    }
    if (s & 1) {
      project(L, wf, rws, yso, tag, 4, lane, bud);
    } else {
      cw_wait(W_O2, 4);
      __builtin_amdgcn_s_sleep(FSLEEP);
      final_sum(rws, a.out, s == 0, hkv, b, 4, tag, lane, bud);
      if (g == 16 && lane == 0) __builtin_amdgcn_raw_buffer_store_b32(tag, rws, 0, WS_EPOCH, SC1);
      STAMP(22);
    }
    STAMP_END();
    return;
  }

  if (s < 3) {
    u32x4 wf[16];
    if (s == 1) {
      cw_wait(W_FULL, 4);
      read_w(L, wf, c, lane);
      cw_wait(W_MF, 1);
      if (lane < 4) asm volatile("ds_add_u32 %0, %1" ::"v"(W_FREE + lane * 4), "v"(1u) : "memory");
    } else {
      const u32 aw = g4 * 1024 + (c >> 1) * 512 + q * 64 + (c & 1) * 32 + 8 * p, ax = SB_OFF + p * 1152 + g4 * 16;
      const u32 vo = (n * 2048 + 2 * g4) * 8, so = WS_QG + ((s ? 0 : 8192) + hkv * 128 + b * 32 + 8 * c) * 8;
      if (j == 2 && c == 0) { __builtin_amdgcn_raw_buffer_store_b32(0u, rws, WS_TK + hkv * 128, 0, SC1); VMW(0); }
      cw_wait(W_FULL, 2);
      u32x4 xr[18], wq[9];
#pragma unroll
      for (int ks = 0; ks < 18; ++ks) xr[ks] = ld128(L, ax + ks * 64);
      f32x4 a0 = ZERO4, a1 = ZERO4;
#pragma unroll
      for (int hf = 0; hf < 2; ++hf) {
        if (hf) { cw_wait(W_FULL, 4); STAMP(1); }
#pragma unroll
        for (int ks = 0; ks < 9; ++ks) wq[ks] = ldtr2(L, aw + (hf * 9 + ks) * 4096, aw + (hf * 9 + ks) * 4096 + 256);
        if (lane < 2) asm volatile("ds_add_u32 %0, %1" ::"v"(W_FREE + hf * 8 + lane * 4), "v"(1u) : "memory");
        __builtin_amdgcn_sched_barrier(0);
#pragma unroll
        for (int ks = 0; ks < 9; ++ks) { if (ks & 1) a1 = mfma(wq[ks], xr[hf * 9 + ks], a1); else a0 = mfma(wq[ks], xr[hf * 9 + ks], a0); }
        __builtin_amdgcn_sched_barrier(0);
      }
      a0 += a1;
      if (n < 4) gst(rws, u32x4{pk(a0[0], a0[1]), tag, pk(a0[2], a0[3]), tag}, vo, so);
      cw_add(W_QS, 1, lane);
      STAMP(2);
    }
    float m = -1e30f, l = 0.f;
    f32x4 acc[8];
#pragma unroll
    for (int i = 0; i < 8; ++i) acc[i] = ZERO4;
    u32x4 qf[4], kf[8], vf[8];
    const u32 lpK = n * 256 + ((g4 ^ swz(n)) << 4), lpQ = QA_OFF + (n & 1) * 256 + g4 * 16;
    const u32 lpV = (4 * g4 + q) * 256 + (((p >> 1) ^ swz(4 * g4 + q)) << 4) + 8 * (p & 1);
#define READ_K() _Pragma("unroll") for (int kk = 0; kk < 4; ++kk) { const u32 ak = (sb + lpK) ^ (kk << 6); kf[kk] = ld128(L, ak); kf[4 + kk] = ld128(L, ak + 4096); }
#define READ_V() do { _Pragma("unroll") for (int ct = 0; ct < 8; ++ct) { const u32 av = (sb + lpV) ^ (ct << 5); vf[ct] = ldtr2(L, av + 8192, av + 12288); } \
      cw_add(W_FREE + (it & 7) * 4, 4, lane); } while (0)
#define SCORES() f32x4 s0 = ZERO4, s1 = ZERO4; \
      _Pragma("unroll") for (int kk = 0; kk < 4; ++kk) { s0 = mfma(kf[kk], qf[kk], s0); s1 = mfma(kf[4 + kk], qf[kk], s1); }
#define PICK8(e) const f32x4 e##0 = (n & 2) ? acc[1] : acc[0], e##1 = (n & 2) ? acc[3] : acc[2], e##2 = (n & 2) ? acc[5] : acc[4], e##3 = (n & 2) ? acc[7] : acc[6], \
      e##4 = (n & 4) ? e##1 : e##0, e##5 = (n & 4) ? e##3 : e##2, e = (n & 8) ? e##5 : e##4
    const u32 vrec = (n & 1) * 512 + (n >> 1) * 64 + g4 * 16;
    bool first = true;
    int it = 4 + c;
#pragma unroll 1
    for (; it < ITU; it += 4) {
      const u32 sb = (it & 7) * SLOT;
      cw_wait(W_FULL, it + 1);
      READ_K(); READ_V();
      if (first) {
        first = false;
        cw_wait(W_QA, 1);
        STAMP(3);
#pragma unroll
        for (int kk = 0; kk < 4; ++kk) qf[kk] = ld128(L, lpQ + ab * 512 + kk * 64);
      }
      SCORES();
      const u32x4 pf = soft<8>(s0, s1, m, l, acc);
#pragma unroll
      for (int ct = 0; ct < 8; ++ct) acc[ct] = mfma(vf[ct], pf, acc[ct]);
    }
    STAMP(8);
    {
      l = xg_sum(l);
      PICK8(e);
      *(LAS f32x4*)(L + MG_OFF + c * 1024 + lane * 16) = e;
      if (lane < 2) *(LAS f32x2*)(L + MS_OFF + (c * 2 + lane) * 8) = f32x2{m, l};
      if (cw_add_rtn(W_MRG, 1, lane) == 3u) {
        f32x2 sv[4]; f32x4 ov[4];
#pragma unroll
        for (int i = 0; i < 4; ++i) { sv[i] = *(LAS f32x2*)(L + MS_OFF + (i * 2 + (n & 1)) * 8); ov[i] = *(LAS f32x4*)(L + MG_OFF + i * 1024 + lane * 16); }
        const float M = fmaxf(fmaxf(sv[0][0], sv[1][0]), fmaxf(sv[2][0], sv[3][0]));
        float Ls = 0.f; f32x4 o = ZERO4;
#pragma unroll
        for (int i = 0; i < 4; ++i) { const float w = ex2((sv[i][0] - M) * CS); Ls = fmaf(w, sv[i][1], Ls); o += w * ov[i]; }
        o *= __builtin_amdgcn_rcpf(Ls);
        gst(rws, u32x4{pk(o[0], o[1]), tag, pk(o[2], o[3]), tag}, vrec, WS_AG + ((ah * 4 + ab) * 16 + as) * 1024);
        if (lane < 2) gst(rws, u32x4{asu(M), tag, asu(Ls), tag}, lane * 16, WS_AS + (ah * 4 + ab) * 2048 + as * 128);
      }
    }
    STAMP(9);
#pragma unroll 1
    for (;; it += 4) {
      const u32 t = cw_wait(W_TK + (it & 15) * 4, it << 8) & 255u, sb = (it & 7) * SLOT;
      if (t >= (u32)NTICK || bud == 0u) break;
#pragma unroll
      for (int kk = 0; kk < 4; ++kk) qf[kk] = ld128(L, lpQ + (t & 3u) * 512 + kk * 64);
      const u32 pr = ah * 4 + (t & 3u), rec = 3 + (t >> 2), va = vrec + WS_AG + (pr * 16 + rec) * 1024, vs = lane * 16 + WS_AS + pr * 2048 + rec * 128;
      cw_wait(W_FULL, it + 1);
      READ_K(); READ_V();
      SCORES();
      m = xg_max(MAX8(s0, s1));
      const float mc = -m * CS;
#pragma unroll
      for (int i = 0; i < 4; ++i) { s0[i] = ex2(fmaf(s0[i], CS, mc)); s1[i] = ex2(fmaf(s1[i], CS, mc)); }
      const u32x4 pf = {pk(s0[0], s0[1]), pk(s0[2], s0[3]), pk(s1[0], s1[1]), pk(s1[2], s1[3])};
      f32x4 e0 = ZERO4, e1 = ZERO4;
#pragma unroll
      for (int ct = 0; ct < 8; ct += 2) { e0 = mfma(vf[ct], (n >> 1) == ct ? pf : u32x4{0u, 0u, 0u, 0u}, e0); e1 = mfma(vf[ct + 1], (n >> 1) == ct + 1 ? pf : u32x4{0u, 0u, 0u, 0u}, e1); }
      s0 += s1;
      l = xg_sum((s0[0] + s0[1]) + (s0[2] + s0[3]));
      e0 = (e0 + e1) * __builtin_amdgcn_rcpf(l);
      gst(rws, u32x4{pk(e0[0], e0[1]), tag, pk(e0[2], e0[3]), tag}, va, 0);
      if (lane < 2) gst(rws, u32x4{asu(m), tag, asu(l), tag}, vs, 0);
      STAMP(10);
    }
    if (s == 1) {
      STAMP(18);
      project(L, wf, rws, yso, tag, c, lane, bud);
      STAMP(20);
    } else {
      cw_add(W_O2, 1, lane);
      cw_wait(W_O2, 4);
      __builtin_amdgcn_s_sleep(FSLEEP);
      final_sum(rws, a.out, s == 0, hkv, b, c, tag, lane, bud);
      STAMP(22);
    }
    STAMP_END();
    return;
  }

  {
    cw_wait(W_FULL, IT_UK + c + 1);
    u32x4 wu[16];
    {
      const u32 lp = (IT_UK + c) * SLOT + n * 512 + ((g4 ^ swz(n)) << 4);
#pragma unroll
      for (int i = 0; i < 16; ++i) wu[i] = ld128(L, (lp ^ ((i & 3) << 6)) + ((i >> 2) & 1) * 8192 + (i >> 3) * 256);
    }
    cw_add(W_FREE + (IT_UK + c) * 4, 4, lane);
    STAMP(15);
    cw_wait(W_QA, 1);
    STAMP(3);
    f32x4 d[4] = {ZERO4, ZERO4, ZERO4, ZERO4};
#pragma unroll
    for (int i = 0; i < 8; ++i) {
      const u32x4 qv = ld128(L, OB_OFF + p * OB_STR + i * 64 + g4 * 16);
      d[(i >> 2) * 2] = mfma(wu[(i >> 2) * 8 + (i & 3)], qv, d[(i >> 2) * 2]); d[(i >> 2) * 2 + 1] = mfma(wu[(i >> 2) * 8 + 4 + (i & 3)], qv, d[(i >> 2) * 2 + 1]);
    }
    const f32x4 e0 = (q & 1) ? d[1] : d[0], e1 = (q & 1) ? d[3] : d[2], e = (q & 2) ? e1 : e0;
    gst(rws, u32x4{pk(e[0], e[1]), tag, pk(e[2], e[3]), tag}, p * 65536 + (g4 & 1) * 128 + (q >> 1) * 64 + (q & 1) * 32 + (g4 >> 1) * 16, WS_QAG + hkv * 4096 + (b * 4 + c) * 256);
    if (cw_add_rtn(W_QS, 1, lane) == 3u) { if (lane == 0) gst(rws, u32x4{tag, tag, tag, tag}, WS_HINT + (hkv * 4 + b) * 16, 0); cw_add(W_QS, 1, lane); }
    STAMP(4);
  }
  {
    const int ch = c & 1;
    u32x4 qa[8];
    {
      const u32 so = WS_QAG + bq * 65536 + (c >> 1) * 32768 + ch * 2048;
      cw_wait(W_HINT, 1);
      STAMP(5);
      u32x4 t[16];
#pragma unroll
      for (int i = 0; i < 16; ++i) t[i] = gld0(rws, (n >> 1) * 4096 + (n & 1) * 64 + g4 * 16 + i * 128, so);
      cw_add(W_GI, 1, lane);
      for (;;) {
        u32 bad = 0;
#pragma unroll
        for (int i = 0; i < 16; ++i) bad |= tagx(t[i], tag);
        if (allz(bad) || !glb_retry()) break;
#pragma unroll
        for (int i = 0; i < 16; ++i) t[i] = gld(rws, (n >> 1) * 4096 + (n & 1) * 64 + g4 * 16 + i * 128, so);
      }
#pragma unroll
      for (int i = 0; i < 8; ++i) qa[i] = u32x4{t[2 * i][0], t[2 * i][2], t[2 * i + 1][0], t[2 * i + 1][2]};
    }
    STAMP(6);
    float mb = -1e30f, lb = 0.f;
    f32x4 ob[16];
#pragma unroll
    for (int i = 0; i < 16; ++i) ob[i] = ZERO4;
    const u32 lpR = n * 1024 + ((g4 ^ swz(n)) << 4) + ch * 512;
    const u32 lpC = (4 * g4 + q) * 1024 + (((p >> 1) ^ swz(4 * g4 + q)) << 4) + 8 * (p & 1) + ch * 512;
#pragma unroll 1
    for (int st = 0; st < 8; ++st) {
      const int it = IT_C + 2 * st;
      const u32 sb = (it & 7) * SLOT;
      const u32 xw = ((st & 1) ? (4 + c) * SLOT + 16384 : SB_OFF + c * 2048) + lane * 16, xr = ((st & 1) ? (4 + (c ^ 1)) * SLOT + 16384 : SB_OFF + (c ^ 1) * 2048) + lane * 16;
      cw_wait(W_FULL, it + 2);
      u32x4 rf[16], cf[16];
#pragma unroll
      for (int kk = 0; kk < 8; ++kk) { const u32 ak = ((sb + lpR) ^ ((kk & 3) << 6)) + (kk >> 2) * 256; rf[2 * kk] = ld128(L, ak); rf[2 * kk + 1] = ld128(L, ak + SLOT); }
      __builtin_amdgcn_sched_barrier(0);
      f32x4 s0 = ZERO4, s1 = ZERO4;
#pragma unroll
      for (int kk = 0; kk < 8; ++kk) {
        s0 = mfma(rf[2 * kk], qa[kk], s0); s1 = mfma(rf[2 * kk + 1], qa[kk], s1);
        const u32 av = (sb + lpC) ^ (kk << 5);
        cf[kk] = ldtr2(L, av, av + SLOT); cf[8 + kk] = ldtr2(L, av + 256, av + 256 + SLOT);
        __builtin_amdgcn_sched_barrier(0);
      }
      *(LAS f32x4*)(L + xw) = s0;
      *(LAS f32x4*)(L + xw + 1024) = s1;
      cw_st(W_S + c * 4, st + 1);
      if (lane < 2) asm volatile("ds_add_u32 %0, %1" ::"v"(W_FREE + (it & 7) * 4 + lane * 4), "v"(1u) : "memory");
      cw_wait(W_S + (c ^ 1) * 4, st + 1);
      s0 += *(LAS f32x4*)(L + xr); s1 += *(LAS f32x4*)(L + xr + 1024);
      const u32x4 pf = soft<16>(s0, s1, mb, lb, ob);
#pragma unroll
      for (int ct = 0; ct < 16; ++ct) ob[ct] = mfma(cf[ct], pf, ob[ct]);
      if (st == 3) STAMP(16);
      if (st == 4) STAMP(17);
    }
    STAMP(7);
#pragma unroll
    for (int i = 0; i < 8; ++i) {
      auto x = __builtin_amdgcn_permlane16_swap(pk(ob[2 * i][0], ob[2 * i][1]), pk(ob[2 * i + 1][0], ob[2 * i + 1][1]), false, false);
      auto y = __builtin_amdgcn_permlane16_swap(pk(ob[2 * i][2], ob[2 * i][3]), pk(ob[2 * i + 1][2], ob[2 * i + 1][3]), false, false);
      gst(rws, u32x4{x[0], y[0], x[1], y[1]}, (n >> 1) * 512 + (n & 1) * 64 + (g4 & 1) * 32 + (g4 >> 1) * 16 + (i & 3) * 128,
          WS_PB + ((bq * 16 + sp) * 4 + ch * 2 + (i >> 2)) * 8192 + (c >> 1) * 4096);
    }
    lb = xg_sum(lb);
    if (g4 == 0 && ch == 0) *(LAS f32x2*)(L + ST_OFF + ((c >> 1) * 16 + n) * 8) = f32x2{mb, lb};
    VMW(0);
    if (cw_add_rtn(W_BP, 1, lane) == 3u) {
      const f32x2 ml = *(LAS f32x2*)(L + ST_OFF + (lane & 31) * 8);
      if (lane < 32) gst(rws, u32x4{asu(ml[0]), tag, asu(ml[1]), tag}, (lane >> 1) * 2048 + (lane & 1) * 16, WS_SG + (bq * 16 + sp) * 32);
      cw_add(W_BP, 1, lane);
    }
    STAMP(8);
  }
  {
    cw_wait(W_SGF, 1);
    STAMP(10);
    u32x4 pv[8];
#pragma unroll
    for (int i = 0; i < 8; ++i) pv[i] = gld(rws, (lane >> 5) * 32768 + (lane & 31) * 16, WS_PB + ((c * 16 + 2 * i) * 4 + b) * 8192 + hkv * 512);
    cw_add(W_GI, 1, lane);
    const f32x2 st = *(LAS f32x2*)(L + MG_OFF + c * 512 + lane * 8);
    const float w = ex2((st[0] - row_max(st[0])) * CS), cf = w / row_sum(w * st[1]);
    f32x4 o0 = ZERO4, o1 = ZERO4;
#pragma unroll
    for (int i = 0; i < 8; ++i) {
      const float f = asf(__builtin_amdgcn_ds_bpermute((((lane >> 2) & 1) * 16 + 2 * i + (lane >> 5)) * 4, asu(cf)));
      o0 += f * f32x4{bflo(pv[i][0]), bfhi(pv[i][0]), bflo(pv[i][1]), bfhi(pv[i][1])};
      o1 += f * f32x4{bflo(pv[i][2]), bfhi(pv[i][2]), bflo(pv[i][3]), bfhi(pv[i][3])};
    }
#pragma unroll
    for (int i = 0; i < 4; ++i) {
      auto t0 = __builtin_amdgcn_permlane32_swap(asu(o0[i]), asu(o0[i]), false, false); o0[i] = asf(t0[0]) + asf(t0[1]);
      auto t1 = __builtin_amdgcn_permlane32_swap(asu(o1[i]), asu(o1[i]), false, false); o1[i] = asf(t1[0]) + asf(t1[1]);
    }
    if (lane < 32) *(LAS u32x4*)(L + OB_OFF + c * OB_STR + ((lane >> 2) & 1) * 256 + (lane >> 3) * 64 + p * 16) = u32x4{pk(o0[0], o0[1]), pk(o0[2], o0[3]), pk(o1[0], o1[1]), pk(o1[2], o1[3])};
    cw_add(W_OB, 1, lane);
    cw_wait(W_FULL, IT_UV + 4);
    u32x4 wf[16];
    {
      const u32 r0 = 8 * (g4 & 1) + q;
      const u32 lp0 = (8 * g4 + q) * 512 + ((((p >> 1) + 8 * c) ^ swz(r0)) << 4) + 8 * (p & 1);
      const u32 lp1 = (8 * g4 + q + 4) * 512 + ((((p >> 1) + 8 * c) ^ swz(r0 + 4)) << 4) + 8 * (p & 1);
#pragma unroll
      for (int i = 0; i < 16; ++i) wf[i] = ldtr2(L, (lp0 ^ ((i & 3) << 5)) + ((IT_UV + (i >> 2)) & 7) * SLOT, (lp1 ^ ((i & 3) << 5)) + ((IT_UV + (i >> 2)) & 7) * SLOT);
    }
    cw_wait(W_OB, 4);
    STAMP(11);
    f32x4 d[4] = {ZERO4, ZERO4, ZERO4, ZERO4};
#pragma unroll
    for (int ks = 0; ks < 4; ++ks) {
      const u32x4 of = ld128(L, OB_OFF + p * OB_STR + (c >> 1) * 256 + ks * 64 + g4 * 16);
#pragma unroll
      for (int t = 0; t < 4; ++t) d[t] = mfma(wf[ks * 4 + t], of, d[t]);
    }
    {
      const u32 vo = p * 2048 + (q >> 1) * 128 + g4 * 32 + (q & 1) * 16, so = WS_CG + ((hkv * 4 + b) * 1024 + c * 64) * 8;
      gst(rws, pick2(d[0], d[1], q, tag), vo, so); gst(rws, pick2(d[2], d[3], q, tag), vo + 256, so);
    }
    STAMP(12);
  }
  {
    cw_add(W_O2, 1, lane);
    cw_wait(W_O2, 4);
    const int rn = n ^ (lane >> 5);
    const u32 so = WS_AG + (hkv * 4 + c) * 16384 + (b & 1) * 512, ss = WS_AS + (hkv * 4 + c) * 2048 + (b & 1) * 16 + (rn < NREC ? rn : 0) * 128;
    const u32 lr = (4 + c) * SLOT, v0 = (lane & 31) * 16, v1 = v0 + (lane >> 5) * 1024;
    u32x4 wf[16], sv, ov[NPR];
    u32 todo = 0x5555u & ((1u << NREC) - 1u);
    {
      u32 ask = todo;
      while (cw_ld(W_FULL) < ALL && bud != 0u) {
        sv = gld(rws, ss, 0);
        const unsigned long long sb = __builtin_amdgcn_ballot_w64(tagx(sv, tag) != 0u);
        const u32 nw = ask & ~((u32)sb | (u32)(sb >> 32));
        for (u32 k = nw; k; k &= k - 1u) { const u32 i = __builtin_ctz(k); DMA(rws, lr + i * 512, i + 1 < NREC ? v1 : v0, so + i * 1024, 0, SC1); }
        ask &= ~nw;
        bud = bud < 16u ? 0u : bud - 16u;
        __builtin_amdgcn_s_sleep(CSLEEP);
      }
      todo = ask | 0x10000u;
    }
    read_w(L, wf, c, lane);
    STAMP(18);
    for (;;) {
      sv = gld(rws, ss, 0);
      for (u32 k = todo & 0xffffu; k; k &= k - 1u) { const u32 i = __builtin_ctz(k); DMA(rws, lr + i * 512, i + 1 < NREC ? v1 : v0, so + i * 1024, 0, SC1); }
      VMW(0);
      const unsigned long long sb = __builtin_amdgcn_ballot_w64(tagx(sv, tag) != 0u);
      if (todo & ~(((u32)sb | (u32)(sb >> 32)) & 0xffffu)) {
#pragma unroll
        for (int i = 0; i < NPR; ++i) ov[i] = ld128(L, lr + lane * 16 + i * 1024);
        u32 bad = 0;
#pragma unroll
        for (int i = 0; i < NPR; ++i) bad |= (tagx(ov[i], tag) != 0u) << (2 * i);
        if (allz(bad)) todo = 0u;
        else { bad |= __builtin_amdgcn_mov_dpp(bad, 0x128, 0xf, 0xf, true); bad |= __builtin_amdgcn_mov_dpp(bad, 0x124, 0xf, 0xf, true); bad |= __builtin_amdgcn_mov_dpp(bad, 0x122, 0xf, 0xf, true); bad |= __builtin_amdgcn_mov_dpp(bad, 0x121, 0xf, 0xf, true);
          todo = __builtin_amdgcn_readlane(bad, 0) | __builtin_amdgcn_readlane(bad, 16) | __builtin_amdgcn_readlane(bad, 32) | __builtin_amdgcn_readlane(bad, 48); }
      }
      if (todo == 0u && sb == 0ull) break;
      bud = bud < 16u ? 0u : bud - 16u;
      if (bud == 0u) break;
      asm volatile("" ::: "memory");
    }
    STAMP(19);
    const float ms = asf(sv[0]), w0 = ex2((ms - row_max(ms)) * CS) * asf(sv[2]), w = rn < NREC ? w0 : 0.f;
    f32x4 o[2] = {ZERO4, ZERO4};
    merge_rec<0>(o, ov, w);
    o[0] += o[1];
    const float rs = __builtin_amdgcn_rcpf(asf(__builtin_amdgcn_readfirstlane(asu(row_sum(w)))));
#pragma unroll
    for (int i = 0; i < 4; ++i) { auto t = __builtin_amdgcn_permlane32_swap(asu(o[0][i]), asu(o[0][i]), false, false); o[0][i] = (asf(t[0]) + asf(t[1])) * rs; }
    *(LAS u32x2*)(L + OB_OFF + c * OB_STR + (lane & 31) * 8) = u32x2{pk(o[0][0], o[0][1]), pk(o[0][2], o[0][3])};
    cw_add(W_PJ, 1, lane);
    project(L, wf, rws, yso, tag, c, lane, bud);
    STAMP(20);
  }
  STAMP_END();
}

extern "C" void kernel_launch(void* const* d_in, const int* in_sizes, int n_in, void* d_out, int out_size, void* d_ws, size_t ws_size, hipStream_t stream) {
  Args a;
  for (int i = 0; i < 11; ++i) a.in[i] = d_in[i];
  a.out = d_out;
  a.ws = d_ws;
  mk_kernel<<<256, 384, 0, stream>>>(a);
}
```

```cpp
#include <hip/hip_runtime.h>
#include <stdint.h>

#define LAS __attribute__((address_space(3)))
typedef unsigned u32;
typedef u32 u32x2 __attribute__((ext_vector_type(2)));
typedef u32 u32x4 __attribute__((ext_vector_type(4)));
typedef float f32x2 __attribute__((ext_vector_type(2)));
typedef float f32x4 __attribute__((ext_vector_type(4)));
typedef short s16x4 __attribute__((ext_vector_type(4)));
typedef __bf16 bf16x2 __attribute__((ext_vector_type(2)));
typedef __bf16 bf16x8 __attribute__((ext_vector_type(8)));
typedef __amdgpu_buffer_rsrc_t rsrc_t;

#define N0 37
#define N1 38
#define N2 40
constexpr int NSUM = N0 + N1 + N2, NU = 128 - NSUM, NREC = 3 + NU, NTICK = 4 * NU;
static_assert(NU >= 1 && NREC <= 16, "split");
#define LW_A 32
#define LW_B 28
#define HOLD_ST 1
#define POLL_A 9
#define POLL_A0 7
#define GATE1 8
#define GATE2 12
#define WTRIG 0
#define POLL_C (N1 + 2)
#define WMODE 3
#define OUT_AUX 0
#define CODE_PF 1
#define LW_W 44
#define FSLEEP 32
#define POLL_B GATE1
#define POLL_H GATE2
#define CSLEEP 2
#define TLBW 1
#define AUX_K 2
#define AUX_V 2
#define AUX_C 2
#define AUX_W 2
static_assert(LW_A <= 32, "a ticket is read two items after it was drawn");

constexpr int SLOT = 18432, NS = 8, RING = SLOT * NS;
constexpr int CTL = RING;
constexpr int W_FULL = CTL, W_KF = CTL + 4, W_QA = CTL + 8, W_MRG = CTL + 12, W_HINT = CTL + 16, W_SGF = CTL + 20, W_OB = CTL + 24, W_BP = CTL + 28,
              W_FREE = CTL + 32, W_S = CTL + 64, W_GI = CTL + 80, W_QS = CTL + 84, W_PJ = CTL + 88, W_WT = CTL + 92, W_MF = CTL + 96, W_O2 = CTL + 100, W_TK = CTL + 128;
constexpr int ST_OFF = CTL + 256;
constexpr int OB_OFF = ST_OFF + 512, OB_STR = 576;
constexpr int SB_OFF = OB_OFF + 4 * OB_STR;
constexpr int QA_OFF = SB_OFF + 5120;
constexpr int MG_OFF = SB_OFF + 8192;
constexpr int MS_OFF = MG_OFF + 4096;
constexpr int LDS_BYTES = MS_OFF + 64;
static_assert(LDS_BYTES <= 163840 && (SB_OFF & 15) == 0 && (MG_OFF & 15) == 0, "lds");

constexpr u32 WS_EPOCH = 0, WS_HINT = 1024;
constexpr u32 WS_TK = 2048;
constexpr u32 WS_QG = 4096;
constexpr u32 WS_QAG = WS_QG + 2 * 4 * 2048 * 8;
constexpr u32 WS_AG = WS_QAG + 4 * 32 * 256 * 8;
constexpr u32 WS_AS = WS_AG + 64 * 16 * 1024;
constexpr u32 WS_SG = WS_AS + 64 * 2048;
constexpr u32 WS_CG = WS_SG + 4 * 16 * 32 * 16;
constexpr u32 WS_YG = WS_CG + 16 * 4 * 4 * 256 * 8;
constexpr u32 WS_PB = WS_YG + 2 * 32 * 18432;
constexpr u32 WS_END = WS_PB + 4 * 16 * 4 * 16 * 512;
constexpr u32 WS_DIAG = 8u << 20;
static_assert(WS_END <= WS_DIAG && (WS_PB & 127) == 0, "ws");

constexpr float CS = 0.08838834764831845f * 1.4426950408889634f;
constexpr int SC1 = 16;
constexpr u32 CODE_BYTES = 28672;
constexpr u32 SPIN_BUDGET = 1u << 19;

struct Args { const void* in[11]; void* out; void* ws; };

__device__ __forceinline__ rsrc_t mkrs(const void* p, u32 n) { return __builtin_amdgcn_make_buffer_rsrc((void*)p, 0, n, 0x00020000); }
constexpr __device__ __forceinline__ u32 swz(u32 r) { return (((r & 7u) ^ ((r >> 1) & 4u)) << 1) | ((r >> 3) & 1u); }
__device__ __forceinline__ f32x4 mfma(u32x4 a, u32x4 b, f32x4 c) {
  return __builtin_amdgcn_mfma_f32_16x16x32_bf16(__builtin_bit_cast(bf16x8, a), __builtin_bit_cast(bf16x8, b), c, 0, 0, 0);
}
__device__ __forceinline__ u32 pk(float lo, float hi) { f32x2 v = {lo, hi}; return __builtin_bit_cast(u32, __builtin_convertvector(v, bf16x2)); }
__device__ __forceinline__ float bflo(u32 v) { return __builtin_bit_cast(float, v << 16); }
__device__ __forceinline__ float bfhi(u32 v) { return __builtin_bit_cast(float, v & 0xffff0000u); }
__device__ __forceinline__ float asf(u32 v) { return __builtin_bit_cast(float, v); }
__device__ __forceinline__ u32 asu(float v) { return __builtin_bit_cast(u32, v); }
__device__ __forceinline__ float ex2(float x) { return __builtin_amdgcn_exp2f(x); }
__device__ __forceinline__ u32x4 ld128(LAS unsigned char* L, u32 a) { return *(LAS u32x4*)(L + a); }
__device__ __forceinline__ u32x4 ldtr2(LAS unsigned char* L, u32 a0, u32 a1) {
  const u32x2 x = __builtin_bit_cast(u32x2, __builtin_amdgcn_ds_read_tr16_b64_v4i16((LAS s16x4*)(L + a0)));
  const u32x2 y = __builtin_bit_cast(u32x2, __builtin_amdgcn_ds_read_tr16_b64_v4i16((LAS s16x4*)(L + a1)));
  return u32x4{x[0], x[1], y[0], y[1]};
}
__device__ __forceinline__ float xg_max(float x) {
  auto s = __builtin_amdgcn_permlane16_swap(asu(x), asu(x), false, false);
  x = fmaxf(asf(s[0]), asf(s[1]));
  auto t = __builtin_amdgcn_permlane32_swap(asu(x), asu(x), false, false);
  return fmaxf(asf(t[0]), asf(t[1]));
}
__device__ __forceinline__ float xg_sum(float x) {
  auto s = __builtin_amdgcn_permlane16_swap(asu(x), asu(x), false, false);
  x = asf(s[0]) + asf(s[1]);
  auto t = __builtin_amdgcn_permlane32_swap(asu(x), asu(x), false, false);
  return asf(t[0]) + asf(t[1]);
}
template <int CTRL> __device__ __forceinline__ float dpp(float x) { return asf(__builtin_amdgcn_mov_dpp(asu(x), CTRL, 0xf, 0xf, true)); }
__device__ __forceinline__ float row_sum(float x) { x += dpp<0x128>(x); x += dpp<0x124>(x); x += dpp<0x122>(x); x += dpp<0x121>(x); return x; }
__device__ __forceinline__ float row_max(float x) { x = fmaxf(x, dpp<0x128>(x)); x = fmaxf(x, dpp<0x124>(x)); x = fmaxf(x, dpp<0x122>(x)); x = fmaxf(x, dpp<0x121>(x)); return x; }

__device__ __forceinline__ u32 cw_ld(u32 a) { u32 v; asm volatile("ds_read_b32 %0, %1\n\ts_waitcnt lgkmcnt(0)" : "=v"(v) : "v"(a) : "memory"); return __builtin_amdgcn_readfirstlane(v); }
__device__ __forceinline__ void cw_st(u32 a, u32 v) { asm volatile("ds_write_b32 %0, %1" ::"v"(a), "v"(v) : "memory"); }
__device__ __forceinline__ void cw_full(u32 v) { asm volatile("ds_write2_b32 %0, %1, %1 offset1:1" ::"v"(W_FULL), "v"(v) : "memory"); }
__device__ __forceinline__ void cw_add(u32 a, u32 v, int lane) { if (lane == 0) asm volatile("ds_add_u32 %0, %1" ::"v"(a), "v"(v) : "memory"); asm volatile("" ::: "memory"); }
__device__ __forceinline__ u32 cw_add_rtn(u32 a, u32 v, int lane) {
  u32 r = 0;
  if (lane == 0) asm volatile("ds_add_rtn_u32 %0, %1, %2\n\ts_waitcnt lgkmcnt(0)" : "=v"(r) : "v"(a), "v"(v) : "memory");
  asm volatile("" ::: "memory");
  return __builtin_amdgcn_readfirstlane(r);
}
__device__ __forceinline__ u32 cw_wait_(u32 a, u32 want, u32& bud) {
  u32 t;
  bud = __builtin_amdgcn_readfirstlane(bud); want = __builtin_amdgcn_readfirstlane(want);
  asm volatile("1:\n\tds_read_b32 v255, %2\n\ts_waitcnt lgkmcnt(0)\n\tv_readfirstlane_b32 %0, v255\n\ts_cmp_ge_u32 %0, %3\n\ts_cbranch_scc1 2f\n\t"
               "s_sleep 1\n\ts_sub_u32 %1, %1, 1\n\ts_cbranch_scc0 1b\n\ts_mov_b32 %1, 0\n2:\n\ts_nop 1"
               : "=&s"(t), "+s"(bud) : "v"(a), "s"(want) : "memory", "scc", "v255");
  return t;
}
#define cw_wait(a, want) cw_wait_((a), (want), bud)
__device__ __forceinline__ bool glb_retry_(u32& bud) {
  bud = __builtin_amdgcn_readfirstlane(bud);
  asm volatile("s_sleep 1\n\ts_sub_u32 %0, %0, 16\n\ts_cselect_b32 %0, 0, %0" : "+s"(bud) :: "memory", "scc");
  return bud != 0u;
}
#define glb_retry() glb_retry_(bud)
__device__ __forceinline__ u32x4 gld(rsrc_t r, u32 voff, u32 soff) { return __builtin_amdgcn_raw_buffer_load_b128(r, voff, soff, SC1); }
__device__ __forceinline__ u32x4 gld0(rsrc_t r, u32 voff, u32 soff) { return __builtin_amdgcn_raw_buffer_load_b128(r, voff, soff, 0); }
__device__ __forceinline__ void gst(rsrc_t r, u32x4 v, u32 voff, u32 soff) { __builtin_amdgcn_raw_buffer_store_b128(v, r, voff + soff, 0, SC1); }
__device__ __forceinline__ u32 tagx(u32x4 v, u32 tag) { return (v[1] ^ tag) | (v[3] ^ tag); }
__device__ __forceinline__ bool allz(u32 x) { return __builtin_amdgcn_ballot_w64(x != 0u) == 0ull; }
template <int N> __device__ __forceinline__ void fetch(LAS unsigned char* L, rsrc_t r, u32 voff, u32 ss, u32 tag, u32 dst, u32 ds, u32 flag, u32& bud) {
  u32x4 v[N];
  for (;;) {
    u32 bad = 0;
#pragma unroll
    for (int i = 0; i < N; ++i) v[i] = gld(r, voff + i * ss, 0);
#pragma unroll
    for (int i = 0; i < N; ++i) bad |= tagx(v[i], tag);
    if (allz(bad) || !glb_retry()) break;
  }
#pragma unroll
  for (int i = 0; i < N; ++i) *(LAS u32x2*)(L + dst + i * ds) = u32x2{v[i][0], v[i][2]};
  cw_st(flag, 1u);
}
#define DMA(r, ldsoff, voff, soff, imm, aux) __builtin_amdgcn_raw_ptr_buffer_load_lds((r), (LAS void*)(L + (ldsoff)), 16, (voff), (soff), (imm), (aux))

#define STAMP(i) do {} while (0)
#define STAMP_BEGIN() do {} while (0)
#define STAMP_END() do {} while (0)

#define VMW(n) asm volatile("s_waitcnt vmcnt(" #n ")" ::: "memory")
#define ZERO4 f32x4{0.f, 0.f, 0.f, 0.f}
#define MAX8(a, b) fmaxf(fmaxf(fmaxf(a[0], a[1]), fmaxf(a[2], a[3])), fmaxf(fmaxf(b[0], b[1]), fmaxf(b[2], b[3])))
constexpr float LAZY = 192.f;
template <int NA> __device__ __forceinline__ u32x4 soft(const f32x4 s0, const f32x4 s1, float& m, float& l, f32x4* acc) {
  const float mx = MAX8(s0, s1);
  if (__builtin_amdgcn_ballot_w64(mx > m + LAZY) != 0ull) {
    const float mn = fmaxf(m, xg_max(mx)), al = ex2((m - mn) * CS);
    m = mn; l *= al;
#pragma unroll
    for (int i = 0; i < NA; ++i) acc[i] *= al;
  }
  const float mc = -m * CS;
  float pr[8];
#pragma unroll
  for (int i = 0; i < 4; ++i) { pr[i] = ex2(fmaf(s0[i], CS, mc)); pr[4 + i] = ex2(fmaf(s1[i], CS, mc)); }
  l += ((pr[0] + pr[1]) + (pr[2] + pr[3])) + ((pr[4] + pr[5]) + (pr[6] + pr[7]));
  return u32x4{pk(pr[0], pr[1]), pk(pr[2], pr[3]), pk(pr[4], pr[5]), pk(pr[6], pr[7])};
}
__device__ __forceinline__ u32x4 pick2(const f32x4 d0, const f32x4 d1, int q, u32 tag) {
  const float x0 = (q & 2) ? d1[0] : d0[0], x1 = (q & 2) ? d1[1] : d0[1], x2 = (q & 2) ? d1[2] : d0[2], x3 = (q & 2) ? d1[3] : d0[3];
  return u32x4{asu((q & 1) ? x2 : x0), tag, asu((q & 1) ? x3 : x1), tag};
}

constexpr int NPR = (NREC + 1) / 2;
template <int I> __device__ __forceinline__ void merge_rec(f32x4* o, const u32x4* t, float w) {
  if constexpr (I < NPR) { o[I & 1] += dpp<0x150 + 2 * I>(w) * f32x4{bflo(t[I][0]), bfhi(t[I][0]), bflo(t[I][2]), bfhi(t[I][2])}; merge_rec<I + 1>(o, t, w); }
}

constexpr int IT_UK = 0, IT_C = 4, IT_UV = 20, IT_W = 24;
constexpr u32 ALL = 1u << 20;
template <int N> __device__ __forceinline__ void vmw() { asm volatile("s_waitcnt vmcnt(%0)" ::"i"(N) : "memory"); }

__device__ __forceinline__ void read_w(LAS unsigned char* L, u32x4* wf, int c, int lane) {
  const u32 lp = (8 * (lane >> 4) + ((lane >> 2) & 3)) * 576 + 8 * (lane & 3) + 128 * c, t2 = c < 4 ? 64 : 0;
#pragma unroll
  for (int i = 0; i < 16; ++i) { const u32 ad = (i >> 2) * SLOT + lp + (i & 1) * 32 + ((i & 2) ? t2 : 0); wf[i] = ldtr2(L, ad, ad + 2304); }
}
__device__ __forceinline__ void project(LAS unsigned char* L, const u32x4* wf, rsrc_t rws, u32 so, u32 tag, int c, int lane, u32& bud) {
  const int g4 = lane >> 4, q = (lane >> 2) & 3, p = lane & 3;
  u32 ao = OB_OFF + p * OB_STR + g4 * 16, vo = so + c * 512 + p * 4608 + (q >> 1) * 128 + g4 * 32 + (q & 1) * 16;
  asm volatile("" : "+v"(ao), "+v"(vo));
  cw_wait(W_PJ, 4);
  f32x4 d[4] = {ZERO4, ZERO4, ZERO4, ZERO4};
  u32x4 of[4];
#pragma unroll
  for (int ks = 0; ks < 4; ++ks) of[ks] = ld128(L, ao + ks * 64);
  __builtin_amdgcn_sched_barrier(0);
#pragma unroll
  for (int ks = 0; ks < 4; ++ks) { d[0] = mfma(wf[ks * 4], of[ks], d[0]); d[1] = mfma(wf[ks * 4 + 1], of[ks], d[1]); }
#pragma unroll
  for (int ks = 0; ks < 4; ++ks) { d[2] = mfma(wf[ks * 4 + 2], of[ks], d[2]); d[3] = mfma(wf[ks * 4 + 3], of[ks], d[3]); }
  gst(rws, pick2(d[0], d[1], q, tag), vo, 0);
  if (c < 4) gst(rws, pick2(d[2], d[3], q, tag), vo + 256, 0);
}
__device__ __forceinline__ void final_sum(rsrc_t rws, void* out, int br, int hkv, int b, int i, u32 tag, int lane, u32& bud) {
  const int c0 = 36 * hkv + 8 * i + 2 * (lane >> 5);
  u32 vo = WS_YG + (br * 32 + (lane & 31)) * 18432 + (b * 576 + c0) * 8, oo = (b * 1152 + br * 576 + c0 + (lane & 1) * 4) * 2;
  const u32 v2 = i < 4 ? 32 : 0;
  const rsrc_t ro = mkrs(out, 4u * 1152u * 2u);
  asm volatile("" : "+v"(vo), "+v"(oo));
  u32x4 y, z;
  for (;;) { y = gld(rws, vo, 0); z = gld(rws, vo, v2); if (allz(tagx(y, tag) | tagx(z, tag)) || !glb_retry()) break; }
  float r[4] = {asf(y[0]), asf(y[2]), asf(z[0]), asf(z[2])};
#pragma unroll
  for (int k = 0; k < 4; ++k) { auto t = __builtin_amdgcn_permlane16_swap(asu(r[k]), asu(r[k]), false, false); r[k] = row_sum(asf(t[0]) + asf(t[1])); }
  if ((lane & 31) < (i < 4 ? 2 : 1)) __builtin_amdgcn_raw_buffer_store_b32((lane & 1) ? pk(r[2], r[3]) : pk(r[0], r[1]), ro, oo, 0, OUT_AUX);
}

__global__ void __launch_bounds__(384, 2) mk_kernel(Args a) {
  __shared__ __attribute__((aligned(16))) unsigned char smem[LDS_BYTES];
  LAS unsigned char* L = (LAS unsigned char*)smem;
  const int lane = threadIdx.x & 63, wave = __builtin_amdgcn_readfirstlane(threadIdx.x >> 6);
  const int g = blockIdx.x, gi = g >> 3, hkv = 2 * (g & 7) + (gi >> 4), j = gi & 15, b = j >> 2, s = j & 3;
  STAMP_BEGIN();
  if (wave == CODE_PF - 1) {
    unsigned long long pc; asm volatile("s_getpc_b64 %0" : "=s"(pc));
    DMA(mkrs((const void*)(pc & ~255ull), CODE_BYTES), MG_OFF, lane * 16, gi * 1024, 0, 0);
  }
  const u32 vu = (lane >> 5) * 8192 + (((lane & 31) ^ ((lane >> 5) << 1)) << 4);
  const u32 vw = ((lane >> 2) & 7) * 8192 + (lane & 3) * 16 + (lane >> 5) * 64;
  if (wave == 1) {
    const int ab_ = gi >> 3, ah_ = ((g & 7) + 2 * (gi & 4) + 3 * (ab_ + 4 * s)) & 15;
#define TOUCH(r, vo, so) __builtin_amdgcn_raw_ptr_buffer_load_lds((r), (LAS void*)(L + MG_OFF + 1024), 4, (vo), (so), 0, 2)
    if (s < 3) { TOUCH(mkrs(a.in[1], 1u << 26), (TLBW > 1 ? lane & 3 : ab_) << 24, ah_ << 20); TOUCH(mkrs(a.in[2], 1u << 26), (TLBW > 1 ? lane & 3 : ab_) << 24, ah_ << 20); }
    else { TOUCH(mkrs(a.in[6], 1u << 24), 0, (((g & 7) >> 1) * 4096u + ((g & 1) * 8 + (gi >> 2)) * 256u) * 1024u);
      if (TLBW > 1) { TOUCH(mkrs(a.in[9], 512u * 8192u), 0, (u32)b * (128u * 8192u)); TOUCH(mkrs(a.in[4], 4096u * 1152u), 0, (u32)hkv * (256u * 1152u)); } }
    if (TLBW > 2) __builtin_amdgcn_raw_ptr_buffer_load_lds(mkrs(a.ws, WS_END), (LAS void*)(L + MG_OFF + 1024), 4, (lane & 3) * (1u << 21) + 512, 0, 0, SC1);
  }
  if (wave == 0) {
    if (s != 1) {
      const rsrc_t rw = mkrs(a.in[s == 0 ? 7 : s == 2 ? 3 : 8], s < 3 ? 576u * 8192u : 512u * 8192u);
      const u32 vo = s < 3 ? vw : vu, st = s < 3 ? 8 * 8192 : 2 * 8192, xm = s == 3 ? 0xf0u : 0u;
      u32 so = hkv * 512 + (s < 3 ? b * 128 : b * (128u * 8192u));
#pragma unroll
      for (int i = 0; i < 32; ++i, so += st) DMA(rw, i * 1024 + (i >= 16 && s == 3 ? 2048 : 0), vo ^ ((swz((2 * i) & 15) << 4) & xm), so, 0, AUX_W);
    }
    STAMP(14);
    cw_st(CTL + lane * 4, 0u);
    asm volatile("s_waitcnt lgkmcnt(0)" ::: "memory");
  }
  __builtin_amdgcn_s_barrier();
  const int ab = gi >> 3, ah = ((g & 7) + 2 * (gi & 4) + 3 * (ab + 4 * s)) & 15, as = s;
  const int ITU = 4 + (s == 0 ? N0 : s == 1 ? N1 : N2);
  const int bq = (g & 7) >> 1, sp = (g & 1) * 8 + (gi >> 2);
  u32 bud = SPIN_BUDGET;

  if (wave == 0) {
    __builtin_amdgcn_s_setprio(3);
    if ((s & 1) == 0) {
      const rsrc_t rw = mkrs(a.in[s ? 3 : 7], 576u * 8192u), rx = mkrs(a.in[s ? 0 : 5], 4u * 576u * 2u);
      u32 so = hkv * 512 + b * 128 + 32 * 65536;
#pragma unroll
      for (int i = 0; i < 5; ++i) DMA(rx, SB_OFF + i * 1024, lane * 16, i * 1024, 0, 0);
#pragma unroll 1
      for (int i = 32; i < 72; i += 4, so += 4 * 65536) {
        vmw<40>();
        DMA(rw, i * 1024, vw, so, 0, AUX_W); DMA(rw, i * 1024 + 1024, vw, so + 65536, 0, AUX_W); DMA(rw, i * 1024 + 2048, vw, so + 2 * 65536, 0, AUX_W); DMA(rw, i * 1024 + 3072, vw, so + 3 * 65536, 0, AUX_W);
      }
      VMW(36); cw_full(2);
    } else if (s == 3) {
      const rsrc_t r = mkrs(a.in[8], 512u * 8192u);
      u32 so = ((u32)b * 128u + 64u) * 8192u + hkv * 512u;
#pragma unroll 1
      for (int t = 2; t < 4; ++t) {
        if (t == 3) { VMW(32); cw_full(1); }
#pragma unroll
        for (int i = 0; i < 16; ++i, so += 2 * 8192) DMA(r, t * SLOT + i * 1024, vu ^ (swz((2 * i) & 15) << 4), so, 0, AUX_W);
      }
      VMW(32); cw_full(2);
    }
    u32 vq[4];
#pragma unroll
    for (int q = 0; q < 4; ++q) vq[q] = (lane >> 4) * 256 + (((lane & 15) ^ swz(4 * q + (lane >> 4))) << 4);
    int pub = 0, it = 4;
#define SLOT_WAIT() do { if (it >= NS) { const u32 need = 4u * (it >> 3), wa = W_FREE + (it & 7) * 4; \
      if (cw_ld(wa) < need) { VMW(16); if (it - 1 > pub) cw_full(it - 1); VMW(0); pub = it; cw_full(pub); cw_wait(wa, need); } } } while (0)
#define LSTAMP() do { if (it == 4) STAMP(2); if (it == 8) STAMP(3); if (it == 12) STAMP(4); if (it == 16) STAMP(5); if (it == 20) STAMP(6); if (it == 24) STAMP(7); if (it == 28) STAMP(8); if (it == 32) STAMP(9); if (it == 36) STAMP(10); if (it == 40) STAMP(11); if (it == 44) STAMP(12); } while (0)
#define PACE(LW, k) do { vmw<LW>(); if ((k) == ((LW / 4) & 3) && it - LW / 16 > pub) { pub = it - LW / 16; cw_full(pub); } } while (0)
#define LOAD_W(in_, LW) do { const rsrc_t r = mkrs(a.in[in_], 4096u * 1152u); u32 wo[9]; int ln = lane; asm volatile("" : "+v"(ln)); \
      _Pragma("unroll") for (int i = 0; i < 9; ++i) { const u32 ci = i * 64 + ln; wo[i] = ci * 16 + ((ci * 3641u) >> 17) * 576u; } \
      u32 sw = ((u32)hkv * 256u + (b & 1) * 128u) * 1152u + (b >> 1) * 576u; \
      _Pragma("unroll 1") for (int t = 0; t < 8; ++t, sw += 16u * 1152u) { _Pragma("unroll") for (int i = 0; i < 9; ++i) { if (i % 3 == 0) vmw<LW>(); DMA(r, t * 9216 + i * 1024, wo[i], sw, 0, AUX_W); } } } while (0)
    if (s == 1) LOAD_W(10, 32);
    asm volatile("" : "+s"(it));
    if (s < 3) {
      const rsrc_t rk = mkrs(a.in[1], 1u << 26), rv = mkrs(a.in[2], 1u << 26), rt = mkrs((const char*)a.ws + WS_TK + ah * 128, 4u);
      u32 so = ((u32)(ab * 16 + ah) * 128u + (s == 0 ? 0 : s == 1 ? N0 : N0 + N1)) * 8192u;
      asm volatile("v_mov_b32 v254, 1" ::: "v254");
      if (s == 0 && HOLD_ST) { VMW(0); pub = 4; cw_full(4); cw_wait(W_QS, 4); }
#pragma unroll 1
      for (;; ++it) {
        const u32 sb = (it & 7) * SLOT;
        SLOT_WAIT();
        LSTAMP();
        PACE(LW_A, 0);
        if (it >= ITU) {
          u32 t;
          asm volatile("v_readlane_b32 %0, v254, %1\n\tv_writelane_b32 v254, 1, %1\n\ts_nop 4" : "=&s"(t) : "s"(it & 3) : "memory", "v254");
          if (t >= (u32)NTICK) break;
          cw_st(W_TK + (it & 15) * 4, (it << 8) | t);
          so = (((t & 3u) * 16u + ah) * 128u + NSUM + (t >> 2)) * 8192u;
        }
        if (it >= ITU - 2) {
          unsigned long long ex;
          asm volatile("s_mov_b64 %0, exec\n\ts_lshl_b64 exec, 1, %2\n\tbuffer_atomic_add v254, off, %1, 0 sc0\n\ts_mov_b64 exec, %0"
                       : "=&s"(ex) : "s"(rt), "s"((it + 2) & 3) : "memory", "scc", "v254");
        }
#pragma unroll
        for (int k = 0; k < 2; ++k) {
          if (k) PACE(LW_A, 1);
          DMA(rk, sb + k * 4096, vq[0], so + k * 4096, 0, AUX_K); DMA(rk, sb + k * 4096, vq[1], so + k * 4096, 1024, AUX_K);
          DMA(rk, sb + k * 4096, vq[2], so + k * 4096, 2048, AUX_K); DMA(rk, sb + k * 4096, vq[3], so + k * 4096, 3072, AUX_K);
        }
#pragma unroll
        for (int k = 0; k < 2; ++k) {
          PACE(LW_A, k + 2);
          DMA(rv, sb + 8192 + k * 4096, vq[0], so + k * 4096, 0, AUX_V); DMA(rv, sb + 8192 + k * 4096, vq[1], so + k * 4096, 1024, AUX_V);
          DMA(rv, sb + 8192 + k * 4096, vq[2], so + k * 4096, 2048, AUX_V); DMA(rv, sb + 8192 + k * 4096, vq[3], so + k * 4096, 3072, AUX_V);
        }
        so += 8192u;
      }
      cw_st(W_TK + ((it + (lane & 3)) & 15) * 4, ((it + (lane & 3)) << 8) | 255u);
      STAMP(13);
    } else {
#pragma unroll 1
      for (; it < IT_W; ++it) {
        const u32 sb = (it & 7) * SLOT;
        if (it == GATE1 || it == GATE2) {
          VMW(16); if (it - 1 > pub) cw_full(it - 1);
          VMW(0); pub = it; cw_full(it);
          if (it == GATE1) cw_wait(W_QS, 5);
          if (it == GATE2) cw_wait(W_GI, 4);
        }
        SLOT_WAIT();
        LSTAMP();
        if (it < IT_UV) {
          const rsrc_t r = mkrs(a.in[6], 1u << 24);
          const u32 so = ((u32)bq * 4096u + sp * 256u + (it - IT_C) * 16u) * 1024u;
#pragma unroll
          for (int k = 0; k < 4; ++k) {
            PACE(LW_B, k);
            DMA(r, sb + k * 4096, (lane ^ swz(4 * k + 0)) << 4, so + k * 4096, 0, AUX_C);
            DMA(r, sb + k * 4096, (lane ^ swz(4 * k + 1)) << 4, so + k * 4096, 1024, AUX_C);
            DMA(r, sb + k * 4096, (lane ^ swz(4 * k + 2)) << 4, so + k * 4096, 2048, AUX_C);
            DMA(r, sb + k * 4096, (lane ^ swz(4 * k + 3)) << 4, so + k * 4096, 3072, AUX_C);
          }
        } else {
          const rsrc_t r = mkrs(a.in[9], 512u * 8192u);
          const u32 so = ((u32)b * 128u + (it & 3) * 32u) * 8192u + hkv * 512u;
#pragma unroll
          for (int i = 0; i < 16; ++i) { if ((i & 3) == 0) PACE(LW_B, i >> 2); DMA(r, sb + i * 1024, vu ^ (swz((2 * i) & 15) << 4), so + i * (2 * 8192), 0, AUX_W); }
        }
      }
      VMW(0); pub = it; cw_full(it);
      cw_wait(W_GI, 8);
      STAMP(15);
      LOAD_W(4, LW_W);
    }
    VMW(16); if (it - 1 > pub) cw_full(it - 1);
    VMW(8); cw_st(W_KF, ALL);
    VMW(0); cw_full(ALL);
    STAMP(1);
    STAMP_END();
    return;
  }

  const int c = wave - 1, g4 = lane >> 4, n = lane & 15, q = n >> 2, p = lane & 3;
  const rsrc_t rws = mkrs(a.ws, WS_END);
  const char* wsp = (const char*)a.ws; asm volatile("" : "+s"(wsp));
  const u32 tag = *(const __attribute__((address_space(4))) u32*)(wsp + WS_EPOCH) + 1u;
  const u32 yso = WS_YG + ((s == 1 ? 32 : 0) + hkv * 2 + (b & 1)) * 18432 + (b >> 1) * 2304;
  if (c == 4) {
    u32x4 wf[16];
    if (s == 1) {
      cw_wait(W_FULL, 4);
      read_w(L, wf, 4, lane);
      cw_st(W_MF, 1u);
    }
    cw_wait(W_FULL, s == 3 ? POLL_B : s == 0 ? POLL_A0 : POLL_A);
    STAMP(1);
    fetch<4>(L, rws, lane * 16 + WS_QG + (s < 3 ? ah * 128 : 8192 + hkv * 128) * 8, 16384, tag, (s < 3 ? QA_OFF : OB_OFF) + lane * 8, s < 3 ? 512 : OB_STR, W_QA, bud);
    STAMP(3);
    if (s == 3) {
      cw_wait(W_QS, 5); cw_wait(W_FULL, POLL_H);
      fetch<1>(L, rws, lane * 16 + WS_HINT, 0, tag, MG_OFF + lane * 8, 0, W_HINT, bud);
      STAMP(4);
      cw_wait(W_BP, 4);
      fetch<4>(L, rws, n * 32 + (g4 & 1) * 16 + WS_SG + hkv * 2048, 512, tag, MG_OFF + lane * 8, 512, W_SGF, bud);
      STAMP(2);
      for (int i = 0; i < 64; ++i) {
        if (__builtin_amdgcn_readfirstlane(__builtin_amdgcn_raw_buffer_load_b32(rws, WS_TK + hkv * 128, 0, SC1)) >= (u32)(WTRIG)) break;
        __builtin_amdgcn_s_sleep(4); asm volatile("" ::: "memory");
      }
      cw_st(W_WT, 1u);
      cw_wait(W_FULL, ALL);
      read_w(L, wf, 4, lane);
    } else if (s == 1) {
      cw_wait(W_FULL, POLL_C);
      const u32 so = WS_CG + hkv * 32768 + (b & 1) * 1024;
      for (;;) {
        const u32x4 t = gld(rws, q * 8192 + (p & 1) * 512, so);
        if (allz(tagx(t, tag)) || !glb_retry()) break;
        __builtin_amdgcn_s_sleep(16);
      }
      u32x4 cv[16];
      for (;;) {
        u32 bad = 0;
#pragma unroll
        for (int i = 0; i < 16; ++i) cv[i] = gld(rws, lane * 16 + (i & 1) * 2048, so + (i >> 1) * 4096);
#pragma unroll
        for (int i = 0; i < 16; ++i) bad |= tagx(cv[i], tag);
        if (allz(bad) || !glb_retry()) break;
      }
#pragma unroll
      for (int i = 0; i < 4; ++i)
        *(LAS u32*)(L + OB_OFF + i * OB_STR + lane * 4) = pk((asf(cv[i][0]) + asf(cv[4 + i][0])) + (asf(cv[8 + i][0]) + asf(cv[12 + i][0])), (asf(cv[i][2]) + asf(cv[4 + i][2])) + (asf(cv[8 + i][2]) + asf(cv[12 + i][2])));
      cw_st(W_PJ, 4u);
      STAMP(2);
    }
    if (s & 1) {
      project(L, wf, rws, yso, tag, 4, lane, bud);
    } else {
      cw_wait(W_O2, 4);
      __builtin_amdgcn_s_sleep(FSLEEP);
      final_sum(rws, a.out, s == 0, hkv, b, 4, tag, lane, bud);
      if (g == 16 && lane == 0) __builtin_amdgcn_raw_buffer_store_b32(tag, rws, 0, WS_EPOCH, SC1);
      STAMP(22);
    }
    STAMP_END();
    return;
  }

  if (s < 3) {
    u32x4 wf[16];
    if (s == 1) {
      cw_wait(W_FULL, 4);
      read_w(L, wf, c, lane);
      cw_wait(W_MF, 1);
      if (lane < 4) asm volatile("ds_add_u32 %0, %1" ::"v"(W_FREE + lane * 4), "v"(1u) : "memory");
    } else {
      const u32 aw = g4 * 1024 + (c >> 1) * 512 + q * 64 + (c & 1) * 32 + 8 * p, ax = SB_OFF + p * 1152 + g4 * 16;
      const u32 vo = (n * 2048 + 2 * g4) * 8, so = WS_QG + ((s ? 0 : 8192) + hkv * 128 + b * 32 + 8 * c) * 8;
      if (j == 2 && c == 0) { __builtin_amdgcn_raw_buffer_store_b32(0u, rws, WS_TK + hkv * 128, 0, SC1); VMW(0); }
      cw_wait(W_FULL, 2);
      u32x4 xr[18], wq[9];
#pragma unroll
      for (int ks = 0; ks < 18; ++ks) xr[ks] = ld128(L, ax + ks * 64);
      f32x4 a0 = ZERO4, a1 = ZERO4;
#pragma unroll
      for (int hf = 0; hf < 2; ++hf) {
        if (hf) { cw_wait(W_FULL, 4); STAMP(1); }
#pragma unroll
        for (int ks = 0; ks < 9; ++ks) wq[ks] = ldtr2(L, aw + (hf * 9 + ks) * 4096, aw + (hf * 9 + ks) * 4096 + 256);
        if (lane < 2) asm volatile("ds_add_u32 %0, %1" ::"v"(W_FREE + hf * 8 + lane * 4), "v"(1u) : "memory");
        __builtin_amdgcn_sched_barrier(0);
#pragma unroll
        for (int ks = 0; ks < 9; ++ks) { if (ks & 1) a1 = mfma(wq[ks], xr[hf * 9 + ks], a1); else a0 = mfma(wq[ks], xr[hf * 9 + ks], a0); }
        __builtin_amdgcn_sched_barrier(0);
      }
      a0 += a1;
      if (n < 4) gst(rws, u32x4{pk(a0[0], a0[1]), tag, pk(a0[2], a0[3]), tag}, vo, so);
      cw_add(W_QS, 1, lane);
      STAMP(2);
    }
    float m = -1e30f, l = 0.f;
    f32x4 acc[8];
#pragma unroll
    for (int i = 0; i < 8; ++i) acc[i] = ZERO4;
    u32x4 qf[4], kf[8], vf[8];
    const u32 lpK = n * 256 + ((g4 ^ swz(n)) << 4), lpQ = QA_OFF + (n & 1) * 256 + g4 * 16;
    const u32 lpV = (4 * g4 + q) * 256 + (((p >> 1) ^ swz(4 * g4 + q)) << 4) + 8 * (p & 1);
#define READ_K() _Pragma("unroll") for (int kk = 0; kk < 4; ++kk) { const u32 ak = (sb + lpK) ^ (kk << 6); kf[kk] = ld128(L, ak); kf[4 + kk] = ld128(L, ak + 4096); }
#define READ_V() do { _Pragma("unroll") for (int ct = 0; ct < 8; ++ct) { const u32 av = (sb + lpV) ^ (ct << 5); vf[ct] = ldtr2(L, av + 8192, av + 12288); } \
      cw_add(W_FREE + (it & 7) * 4, 4, lane); } while (0)
#define SCORES() f32x4 s0 = ZERO4, s1 = ZERO4; \
      _Pragma("unroll") for (int kk = 0; kk < 4; ++kk) { s0 = mfma(kf[kk], qf[kk], s0); s1 = mfma(kf[4 + kk], qf[kk], s1); }
#define PICK8(e) const f32x4 e##0 = (n & 2) ? acc[1] : acc[0], e##1 = (n & 2) ? acc[3] : acc[2], e##2 = (n & 2) ? acc[5] : acc[4], e##3 = (n & 2) ? acc[7] : acc[6], \
      e##4 = (n & 4) ? e##1 : e##0, e##5 = (n & 4) ? e##3 : e##2, e = (n & 8) ? e##5 : e##4
    const u32 vrec = (n & 1) * 512 + (n >> 1) * 64 + g4 * 16;
    bool first = true;
    int it = 4 + c;
#pragma unroll 1
    for (; it < ITU; it += 4) {
      const u32 sb = (it & 7) * SLOT;
      cw_wait(W_FULL, it + 1);
      READ_K(); READ_V();
      if (first) {
        first = false;
        cw_wait(W_QA, 1);
        STAMP(3);
#pragma unroll
        for (int kk = 0; kk < 4; ++kk) qf[kk] = ld128(L, lpQ + ab * 512 + kk * 64);
      }
      SCORES();
      const u32x4 pf = soft<8>(s0, s1, m, l, acc);
#pragma unroll
      for (int ct = 0; ct < 8; ++ct) acc[ct] = mfma(vf[ct], pf, acc[ct]);
    }
    STAMP(8);
    {
      l = xg_sum(l);
      PICK8(e);
      *(LAS f32x4*)(L + MG_OFF + c * 1024 + lane * 16) = e;
      if (lane < 2) *(LAS f32x2*)(L + MS_OFF + (c * 2 + lane) * 8) = f32x2{m, l};
      if (cw_add_rtn(W_MRG, 1, lane) == 3u) {
        f32x2 sv[4]; f32x4 ov[4];
#pragma unroll
        for (int i = 0; i < 4; ++i) { sv[i] = *(LAS f32x2*)(L + MS_OFF + (i * 2 + (n & 1)) * 8); ov[i] = *(LAS f32x4*)(L + MG_OFF + i * 1024 + lane * 16); }
        const float M = fmaxf(fmaxf(sv[0][0], sv[1][0]), fmaxf(sv[2][0], sv[3][0]));
        float Ls = 0.f; f32x4 o = ZERO4;
#pragma unroll
        for (int i = 0; i < 4; ++i) { const float w = ex2((sv[i][0] - M) * CS); Ls = fmaf(w, sv[i][1], Ls); o += w * ov[i]; }
        o *= __builtin_amdgcn_rcpf(Ls);
        gst(rws, u32x4{pk(o[0], o[1]), tag, pk(o[2], o[3]), tag}, vrec, WS_AG + ((ah * 4 + ab) * 16 + as) * 1024);
        if (lane < 2) gst(rws, u32x4{asu(M), tag, asu(Ls), tag}, lane * 16, WS_AS + (ah * 4 + ab) * 2048 + as * 128);
      }
    }
    STAMP(9);
#pragma unroll 1
    for (;; it += 4) {
      const u32 t = cw_wait(W_TK + (it & 15) * 4, it << 8) & 255u, sb = (it & 7) * SLOT;
      if (t >= (u32)NTICK || bud == 0u) break;
#pragma unroll
      for (int kk = 0; kk < 4; ++kk) qf[kk] = ld128(L, lpQ + (t & 3u) * 512 + kk * 64);
      const u32 pr = ah * 4 + (t & 3u), rec = 3 + (t >> 2), va = vrec + WS_AG + (pr * 16 + rec) * 1024, vs = lane * 16 + WS_AS + pr * 2048 + rec * 128;
      cw_wait(W_FULL, it + 1);
      READ_K(); READ_V();
      SCORES();
      m = xg_max(MAX8(s0, s1));
      const float mc = -m * CS;
#pragma unroll
      for (int i = 0; i < 4; ++i) { s0[i] = ex2(fmaf(s0[i], CS, mc)); s1[i] = ex2(fmaf(s1[i], CS, mc)); }
      const u32x4 pf = {pk(s0[0], s0[1]), pk(s0[2], s0[3]), pk(s1[0], s1[1]), pk(s1[2], s1[3])};
      f32x4 e0 = ZERO4, e1 = ZERO4;
#pragma unroll
      for (int ct = 0; ct < 8; ct += 2) { e0 = mfma(vf[ct], (n >> 1) == ct ? pf : u32x4{0u, 0u, 0u, 0u}, e0); e1 = mfma(vf[ct + 1], (n >> 1) == ct + 1 ? pf : u32x4{0u, 0u, 0u, 0u}, e1); }
      s0 += s1;
      l = xg_sum((s0[0] + s0[1]) + (s0[2] + s0[3]));
      e0 = (e0 + e1) * __builtin_amdgcn_rcpf(l);
      gst(rws, u32x4{pk(e0[0], e0[1]), tag, pk(e0[2], e0[3]), tag}, va, 0);
      if (lane < 2) gst(rws, u32x4{asu(m), tag, asu(l), tag}, vs, 0);
      STAMP(10);
    }
    if (s == 1) {
      STAMP(18);
      project(L, wf, rws, yso, tag, c, lane, bud);
      STAMP(20);
    } else {
      cw_add(W_O2, 1, lane);
      cw_wait(W_O2, 4);
      __builtin_amdgcn_s_sleep(FSLEEP);
      final_sum(rws, a.out, s == 0, hkv, b, c, tag, lane, bud);
      STAMP(22);
    }
    STAMP_END();
    return;
  }

  {
    cw_wait(W_FULL, IT_UK + c + 1);
    u32x4 wu[16];
    {
      const u32 lp = (IT_UK + c) * SLOT + n * 512 + ((g4 ^ swz(n)) << 4);
#pragma unroll
      for (int i = 0; i < 16; ++i) wu[i] = ld128(L, (lp ^ ((i & 3) << 6)) + ((i >> 2) & 1) * 8192 + (i >> 3) * 256);
    }
    cw_add(W_FREE + (IT_UK + c) * 4, 4, lane);
    STAMP(15);
    cw_wait(W_QA, 1);
    STAMP(3);
    f32x4 d[4] = {ZERO4, ZERO4, ZERO4, ZERO4};
#pragma unroll
    for (int i = 0; i < 8; ++i) {
      const u32x4 qv = ld128(L, OB_OFF + p * OB_STR + i * 64 + g4 * 16);
      d[(i >> 2) * 2] = mfma(wu[(i >> 2) * 8 + (i & 3)], qv, d[(i >> 2) * 2]); d[(i >> 2) * 2 + 1] = mfma(wu[(i >> 2) * 8 + 4 + (i & 3)], qv, d[(i >> 2) * 2 + 1]);
    }
    const f32x4 e0 = (q & 1) ? d[1] : d[0], e1 = (q & 1) ? d[3] : d[2], e = (q & 2) ? e1 : e0;
    gst(rws, u32x4{pk(e[0], e[1]), tag, pk(e[2], e[3]), tag}, p * 65536 + (g4 & 1) * 128 + (q >> 1) * 64 + (q & 1) * 32 + (g4 >> 1) * 16, WS_QAG + hkv * 4096 + (b * 4 + c) * 256);
    if (cw_add_rtn(W_QS, 1, lane) == 3u) { if (lane == 0) gst(rws, u32x4{tag, tag, tag, tag}, WS_HINT + (hkv * 4 + b) * 16, 0); cw_add(W_QS, 1, lane); }
    STAMP(4);
  }
  {
    const int ch = c & 1;
    u32x4 qa[8];
    {
      const u32 so = WS_QAG + bq * 65536 + (c >> 1) * 32768 + ch * 2048;
      cw_wait(W_HINT, 1);
      STAMP(5);
      u32x4 t[16];
#pragma unroll
      for (int i = 0; i < 16; ++i) t[i] = gld0(rws, (n >> 1) * 4096 + (n & 1) * 64 + g4 * 16 + i * 128, so);
      cw_add(W_GI, 1, lane);
      for (;;) {
        u32 bad = 0;
#pragma unroll
        for (int i = 0; i < 16; ++i) bad |= tagx(t[i], tag);
        if (allz(bad) || !glb_retry()) break;
#pragma unroll
        for (int i = 0; i < 16; ++i) t[i] = gld(rws, (n >> 1) * 4096 + (n & 1) * 64 + g4 * 16 + i * 128, so);
      }
#pragma unroll
      for (int i = 0; i < 8; ++i) qa[i] = u32x4{t[2 * i][0], t[2 * i][2], t[2 * i + 1][0], t[2 * i + 1][2]};
    }
    STAMP(6);
    float mb = -1e30f, lb = 0.f;
    f32x4 ob[16];
#pragma unroll
    for (int i = 0; i < 16; ++i) ob[i] = ZERO4;
    const u32 lpR = n * 1024 + ((g4 ^ swz(n)) << 4) + ch * 512;
    const u32 lpC = (4 * g4 + q) * 1024 + (((p >> 1) ^ swz(4 * g4 + q)) << 4) + 8 * (p & 1) + ch * 512;
#pragma unroll 1
    for (int st = 0; st < 8; ++st) {
      const int it = IT_C + 2 * st;
      const u32 sb = (it & 7) * SLOT;
      const u32 xw = ((st & 1) ? (4 + c) * SLOT + 16384 : SB_OFF + c * 2048) + lane * 16, xr = ((st & 1) ? (4 + (c ^ 1)) * SLOT + 16384 : SB_OFF + (c ^ 1) * 2048) + lane * 16;
      cw_wait(W_FULL, it + 2);
      u32x4 rf[16], cf[16];
#pragma unroll
      for (int kk = 0; kk < 8; ++kk) { const u32 ak = ((sb + lpR) ^ ((kk & 3) << 6)) + (kk >> 2) * 256; rf[2 * kk] = ld128(L, ak); rf[2 * kk + 1] = ld128(L, ak + SLOT); }
      __builtin_amdgcn_sched_barrier(0);
      f32x4 s0 = ZERO4, s1 = ZERO4;
#pragma unroll
      for (int kk = 0; kk < 8; ++kk) {
        s0 = mfma(rf[2 * kk], qa[kk], s0); s1 = mfma(rf[2 * kk + 1], qa[kk], s1);
        const u32 av = (sb + lpC) ^ (kk << 5);
        cf[kk] = ldtr2(L, av, av + SLOT); cf[8 + kk] = ldtr2(L, av + 256, av + 256 + SLOT);
        __builtin_amdgcn_sched_barrier(0);
      }
      *(LAS f32x4*)(L + xw) = s0;
      *(LAS f32x4*)(L + xw + 1024) = s1;
      cw_st(W_S + c * 4, st + 1);
      if (lane < 2) asm volatile("ds_add_u32 %0, %1" ::"v"(W_FREE + (it & 7) * 4 + lane * 4), "v"(1u) : "memory");
      cw_wait(W_S + (c ^ 1) * 4, st + 1);
      s0 += *(LAS f32x4*)(L + xr); s1 += *(LAS f32x4*)(L + xr + 1024);
      const u32x4 pf = soft<16>(s0, s1, mb, lb, ob);
#pragma unroll
      for (int ct = 0; ct < 16; ++ct) ob[ct] = mfma(cf[ct], pf, ob[ct]);
      if (st == 3) STAMP(16);
      if (st == 4) STAMP(17);
    }
    STAMP(7);
#pragma unroll
    for (int i = 0; i < 8; ++i) {
      auto x = __builtin_amdgcn_permlane16_swap(pk(ob[2 * i][0], ob[2 * i][1]), pk(ob[2 * i + 1][0], ob[2 * i + 1][1]), false, false);
      auto y = __builtin_amdgcn_permlane16_swap(pk(ob[2 * i][2], ob[2 * i][3]), pk(ob[2 * i + 1][2], ob[2 * i + 1][3]), false, false);
      gst(rws, u32x4{x[0], y[0], x[1], y[1]}, (n >> 1) * 512 + (n & 1) * 64 + (g4 & 1) * 32 + (g4 >> 1) * 16 + (i & 3) * 128,
          WS_PB + ((bq * 16 + sp) * 4 + ch * 2 + (i >> 2)) * 8192 + (c >> 1) * 4096);
    }
    lb = xg_sum(lb);
    if (g4 == 0 && ch == 0) *(LAS f32x2*)(L + ST_OFF + ((c >> 1) * 16 + n) * 8) = f32x2{mb, lb};
    VMW(0);
    if (cw_add_rtn(W_BP, 1, lane) == 3u) {
      const f32x2 ml = *(LAS f32x2*)(L + ST_OFF + (lane & 31) * 8);
      if (lane < 32) gst(rws, u32x4{asu(ml[0]), tag, asu(ml[1]), tag}, (lane >> 1) * 2048 + (lane & 1) * 16, WS_SG + (bq * 16 + sp) * 32);
      cw_add(W_BP, 1, lane);
    }
    STAMP(8);
  }
  {
    cw_wait(W_SGF, 1);
    STAMP(10);
    u32x4 pv[8];
#pragma unroll
    for (int i = 0; i < 8; ++i) pv[i] = gld(rws, (lane >> 5) * 32768 + (lane & 31) * 16, WS_PB + ((c * 16 + 2 * i) * 4 + b) * 8192 + hkv * 512);
    cw_add(W_GI, 1, lane);
    const f32x2 st = *(LAS f32x2*)(L + MG_OFF + c * 512 + lane * 8);
    const float w = ex2((st[0] - row_max(st[0])) * CS), cf = w / row_sum(w * st[1]);
    f32x4 o0 = ZERO4, o1 = ZERO4;
#pragma unroll
    for (int i = 0; i < 8; ++i) {
      const float f = asf(__builtin_amdgcn_ds_bpermute((((lane >> 2) & 1) * 16 + 2 * i + (lane >> 5)) * 4, asu(cf)));
      o0 += f * f32x4{bflo(pv[i][0]), bfhi(pv[i][0]), bflo(pv[i][1]), bfhi(pv[i][1])};
      o1 += f * f32x4{bflo(pv[i][2]), bfhi(pv[i][2]), bflo(pv[i][3]), bfhi(pv[i][3])};
    }
#pragma unroll
    for (int i = 0; i < 4; ++i) {
      auto t0 = __builtin_amdgcn_permlane32_swap(asu(o0[i]), asu(o0[i]), false, false); o0[i] = asf(t0[0]) + asf(t0[1]);
      auto t1 = __builtin_amdgcn_permlane32_swap(asu(o1[i]), asu(o1[i]), false, false); o1[i] = asf(t1[0]) + asf(t1[1]);
    }
    if (lane < 32) *(LAS u32x4*)(L + OB_OFF + c * OB_STR + ((lane >> 2) & 1) * 256 + (lane >> 3) * 64 + p * 16) = u32x4{pk(o0[0], o0[1]), pk(o0[2], o0[3]), pk(o1[0], o1[1]), pk(o1[2], o1[3])};
    cw_add(W_OB, 1, lane);
    cw_wait(W_FULL, IT_UV + 4);
    u32x4 wf[16];
    {
      const u32 r0 = 8 * (g4 & 1) + q;
      const u32 lp0 = (8 * g4 + q) * 512 + ((((p >> 1) + 8 * c) ^ swz(r0)) << 4) + 8 * (p & 1);
      const u32 lp1 = (8 * g4 + q + 4) * 512 + ((((p >> 1) + 8 * c) ^ swz(r0 + 4)) << 4) + 8 * (p & 1);
#pragma unroll
      for (int i = 0; i < 16; ++i) wf[i] = ldtr2(L, (lp0 ^ ((i & 3) << 5)) + ((IT_UV + (i >> 2)) & 7) * SLOT, (lp1 ^ ((i & 3) << 5)) + ((IT_UV + (i >> 2)) & 7) * SLOT);
    }
    cw_wait(W_OB, 4);
    STAMP(11);
    f32x4 d[4] = {ZERO4, ZERO4, ZERO4, ZERO4};
#pragma unroll
    for (int ks = 0; ks < 4; ++ks) {
      const u32x4 of = ld128(L, OB_OFF + p * OB_STR + (c >> 1) * 256 + ks * 64 + g4 * 16);
#pragma unroll
      for (int t = 0; t < 4; ++t) d[t] = mfma(wf[ks * 4 + t], of, d[t]);
    }
    {
      const u32 vo = p * 2048 + (q >> 1) * 128 + g4 * 32 + (q & 1) * 16, so = WS_CG + ((hkv * 4 + b) * 1024 + c * 64) * 8;
      gst(rws, pick2(d[0], d[1], q, tag), vo, so); gst(rws, pick2(d[2], d[3], q, tag), vo + 256, so);
    }
    STAMP(12);
  }
  {
    cw_add(W_O2, 1, lane);
    cw_wait(W_O2, 4);
    const int rn = n ^ (lane >> 5);
    const u32 so = WS_AG + (hkv * 4 + c) * 16384 + (b & 1) * 512, ss = WS_AS + (hkv * 4 + c) * 2048 + (b & 1) * 16 + (rn < NREC ? rn : 0) * 128;
    const u32 lr = (4 + c) * SLOT, v0 = (lane & 31) * 16, v1 = v0 + (lane >> 5) * 1024;
    u32x4 wf[16], sv, ov[NPR];
    u32 todo = 0x5555u & ((1u << NREC) - 1u);
    {
      u32 ask = todo;
      while (cw_ld(W_FULL) < ALL && bud != 0u) {
        sv = gld(rws, ss, 0);
        const unsigned long long sb = __builtin_amdgcn_ballot_w64(tagx(sv, tag) != 0u);
        const u32 nw = ask & ~((u32)sb | (u32)(sb >> 32));
        for (u32 k = nw; k; k &= k - 1u) { const u32 i = __builtin_ctz(k); DMA(rws, lr + i * 512, i + 1 < NREC ? v1 : v0, so + i * 1024, 0, SC1); }
        ask &= ~nw;
        bud = bud < 16u ? 0u : bud - 16u;
        __builtin_amdgcn_s_sleep(CSLEEP);
      }
      todo = ask | 0x10000u;
    }
    read_w(L, wf, c, lane);
    STAMP(18);
    for (;;) {
      sv = gld(rws, ss, 0);
      for (u32 k = todo & 0xffffu; k; k &= k - 1u) { const u32 i = __builtin_ctz(k); DMA(rws, lr + i * 512, i + 1 < NREC ? v1 : v0, so + i * 1024, 0, SC1); }
      VMW(0);
      const unsigned long long sb = __builtin_amdgcn_ballot_w64(tagx(sv, tag) != 0u);
      if (todo & ~(((u32)sb | (u32)(sb >> 32)) & 0xffffu)) {
#pragma unroll
        for (int i = 0; i < NPR; ++i) ov[i] = ld128(L, lr + lane * 16 + i * 1024);
        u32 bad = 0;
#pragma unroll
        for (int i = 0; i < NPR; ++i) bad |= (tagx(ov[i], tag) != 0u) << (2 * i);
        if (allz(bad)) todo = 0u;
        else { bad |= __builtin_amdgcn_mov_dpp(bad, 0x128, 0xf, 0xf, true); bad |= __builtin_amdgcn_mov_dpp(bad, 0x124, 0xf, 0xf, true); bad |= __builtin_amdgcn_mov_dpp(bad, 0x122, 0xf, 0xf, true); bad |= __builtin_amdgcn_mov_dpp(bad, 0x121, 0xf, 0xf, true);
          todo = __builtin_amdgcn_readlane(bad, 0) | __builtin_amdgcn_readlane(bad, 16) | __builtin_amdgcn_readlane(bad, 32) | __builtin_amdgcn_readlane(bad, 48); }
      }
      if (todo == 0u && sb == 0ull) break;
      bud = bud < 16u ? 0u : bud - 16u;
      if (bud == 0u) break;
      asm volatile("" ::: "memory");
    }
    STAMP(19);
    const float ms = asf(sv[0]), w0 = ex2((ms - row_max(ms)) * CS) * asf(sv[2]), w = rn < NREC ? w0 : 0.f;
    f32x4 o[2] = {ZERO4, ZERO4};
    merge_rec<0>(o, ov, w);
    o[0] += o[1];
    const float rs = __builtin_amdgcn_rcpf(asf(__builtin_amdgcn_readfirstlane(asu(row_sum(w)))));
#pragma unroll
    for (int i = 0; i < 4; ++i) { auto t = __builtin_amdgcn_permlane32_swap(asu(o[0][i]), asu(o[0][i]), false, false); o[0][i] = (asf(t[0]) + asf(t[1])) * rs; }
    *(LAS u32x2*)(L + OB_OFF + c * OB_STR + (lane & 31) * 8) = u32x2{pk(o[0][0], o[0][1]), pk(o[0][2], o[0][3])};
    cw_add(W_PJ, 1, lane);
    project(L, wf, rws, yso, tag, c, lane, bud);
    STAMP(20);
  }
  STAMP_END();
}

extern "C" void kernel_launch(void* const* d_in, const int* in_sizes, int n_in, void* d_out, int out_size, void* d_ws, size_t ws_size, hipStream_t stream) {
  Args a;
  for (int i = 0; i < 11; ++i) a.in[i] = d_in[i];
  a.out = d_out;
  a.ws = d_ws;
  mk_kernel<<<256, 384, 0, stream>>>(a);
}
```

```cpp
#define DIAG 1
#include <hip/hip_runtime.h>
#include <stdint.h>

#define LAS __attribute__((address_space(3)))
typedef unsigned u32;
typedef u32 u32x2 __attribute__((ext_vector_type(2)));
typedef u32 u32x4 __attribute__((ext_vector_type(4)));
typedef float f32x2 __attribute__((ext_vector_type(2)));
typedef float f32x4 __attribute__((ext_vector_type(4)));
typedef short s16x4 __attribute__((ext_vector_type(4)));
typedef __bf16 bf16x2 __attribute__((ext_vector_type(2)));
typedef __bf16 bf16x8 __attribute__((ext_vector_type(8)));
typedef __amdgpu_buffer_rsrc_t rsrc_t;

#define N0 37
#define N1 38
#define N2 40
constexpr int NSUM = N0 + N1 + N2, NU = 128 - NSUM, NREC = 3 + NU, NTICK = 4 * NU;
static_assert(NU >= 1 && NREC <= 16, "split");
#define LW_A 32
#define LW_B 28
#define HOLD_ST 1
#define POLL_A 9
#define POLL_A0 7
#define GATE1 8
#define GATE2 12
#define WTRIG 0
#define POLL_C (N1 + 2)
#define WMODE 3
#define OUT_AUX 0
#define CODE_PF 1
#define LW_W 44
#define FSLEEP 32
#define POLL_B GATE1
#define POLL_H GATE2
#define CSLEEP 2
#define AUX_K 2
#define AUX_V 2
#define AUX_C 2
#define AUX_W 2
static_assert(LW_A <= 32, "a ticket is read two items after it was drawn");

constexpr int SLOT = 18432, NS = 8, RING = SLOT * NS;
constexpr int CTL = RING;
constexpr int W_FULL = CTL, W_KF = CTL + 4, W_QA = CTL + 8, W_MRG = CTL + 12, W_HINT = CTL + 16, W_SGF = CTL + 20, W_OB = CTL + 24, W_BP = CTL + 28,
              W_FREE = CTL + 32, W_S = CTL + 64, W_GI = CTL + 80, W_QS = CTL + 84, W_PJ = CTL + 88, W_WT = CTL + 92, W_MF = CTL + 96, W_O2 = CTL + 100, W_TK = CTL + 128;
constexpr int ST_OFF = CTL + 256;
constexpr int OB_OFF = ST_OFF + 512, OB_STR = 576;
constexpr int SB_OFF = OB_OFF + 4 * OB_STR;
constexpr int QA_OFF = SB_OFF + 5120;
constexpr int MG_OFF = SB_OFF + 8192;
constexpr int MS_OFF = MG_OFF + 4096;
constexpr int LDS_BYTES = MS_OFF + 64;
static_assert(LDS_BYTES <= 163840 && (SB_OFF & 15) == 0 && (MG_OFF & 15) == 0, "lds");

constexpr u32 WS_EPOCH = 0, WS_HINT = 1024;
constexpr u32 WS_TK = 2048;
constexpr u32 WS_QG = 4096;
constexpr u32 WS_QAG = WS_QG + 2 * 4 * 2048 * 8;
constexpr u32 WS_AG = WS_QAG + 4 * 32 * 256 * 8;
constexpr u32 WS_AS = WS_AG + 64 * 16 * 1024;
constexpr u32 WS_SG = WS_AS + 64 * 2048;
constexpr u32 WS_CG = WS_SG + 4 * 16 * 32 * 16;
constexpr u32 WS_YG = WS_CG + 16 * 4 * 4 * 256 * 8;
constexpr u32 WS_PB = WS_YG + 2 * 32 * 18432;
constexpr u32 WS_END = WS_PB + 4 * 16 * 4 * 16 * 512;
constexpr u32 WS_DIAG = 8u << 20;
static_assert(WS_END <= WS_DIAG && (WS_PB & 127) == 0, "ws");

constexpr float CS = 0.08838834764831845f * 1.4426950408889634f;
constexpr int SC1 = 16;
constexpr u32 CODE_BYTES = 28672;
constexpr u32 SPIN_BUDGET = 1u << 19;

struct Args { const void* in[11]; void* out; void* ws; };

__device__ __forceinline__ rsrc_t mkrs(const void* p, u32 n) { return __builtin_amdgcn_make_buffer_rsrc((void*)p, 0, n, 0x00020000); }
constexpr __device__ __forceinline__ u32 swz(u32 r) { return (((r & 7u) ^ ((r >> 1) & 4u)) << 1) | ((r >> 3) & 1u); }
__device__ __forceinline__ f32x4 mfma(u32x4 a, u32x4 b, f32x4 c) {
  return __builtin_amdgcn_mfma_f32_16x16x32_bf16(__builtin_bit_cast(bf16x8, a), __builtin_bit_cast(bf16x8, b), c, 0, 0, 0);
}
__device__ __forceinline__ u32 pk(float lo, float hi) { f32x2 v = {lo, hi}; return __builtin_bit_cast(u32, __builtin_convertvector(v, bf16x2)); }
__device__ __forceinline__ float bflo(u32 v) { return __builtin_bit_cast(float, v << 16); }
__device__ __forceinline__ float bfhi(u32 v) { return __builtin_bit_cast(float, v & 0xffff0000u); }
__device__ __forceinline__ float asf(u32 v) { return __builtin_bit_cast(float, v); }
__device__ __forceinline__ u32 asu(float v) { return __builtin_bit_cast(u32, v); }
__device__ __forceinline__ float ex2(float x) { return __builtin_amdgcn_exp2f(x); }
__device__ __forceinline__ u32x4 ld128(LAS unsigned char* L, u32 a) { return *(LAS u32x4*)(L + a); }
__device__ __forceinline__ u32x4 ldtr2(LAS unsigned char* L, u32 a0, u32 a1) {
  const u32x2 x = __builtin_bit_cast(u32x2, __builtin_amdgcn_ds_read_tr16_b64_v4i16((LAS s16x4*)(L + a0)));
  const u32x2 y = __builtin_bit_cast(u32x2, __builtin_amdgcn_ds_read_tr16_b64_v4i16((LAS s16x4*)(L + a1)));
  return u32x4{x[0], x[1], y[0], y[1]};
}
__device__ __forceinline__ float xg_max(float x) {
  auto s = __builtin_amdgcn_permlane16_swap(asu(x), asu(x), false, false);
  x = fmaxf(asf(s[0]), asf(s[1]));
  auto t = __builtin_amdgcn_permlane32_swap(asu(x), asu(x), false, false);
  return fmaxf(asf(t[0]), asf(t[1]));
}
__device__ __forceinline__ float xg_sum(float x) {
  auto s = __builtin_amdgcn_permlane16_swap(asu(x), asu(x), false, false);
  x = asf(s[0]) + asf(s[1]);
  auto t = __builtin_amdgcn_permlane32_swap(asu(x), asu(x), false, false);
  return asf(t[0]) + asf(t[1]);
}
template <int CTRL> __device__ __forceinline__ float dpp(float x) { return asf(__builtin_amdgcn_mov_dpp(asu(x), CTRL, 0xf, 0xf, true)); }
__device__ __forceinline__ float row_sum(float x) { x += dpp<0x128>(x); x += dpp<0x124>(x); x += dpp<0x122>(x); x += dpp<0x121>(x); return x; }
__device__ __forceinline__ float row_max(float x) { x = fmaxf(x, dpp<0x128>(x)); x = fmaxf(x, dpp<0x124>(x)); x = fmaxf(x, dpp<0x122>(x)); x = fmaxf(x, dpp<0x121>(x)); return x; }

__device__ __forceinline__ u32 cw_ld(u32 a) { u32 v; asm volatile("ds_read_b32 %0, %1\n\ts_waitcnt lgkmcnt(0)" : "=v"(v) : "v"(a) : "memory"); return __builtin_amdgcn_readfirstlane(v); }
__device__ __forceinline__ void cw_st(u32 a, u32 v) { asm volatile("ds_write_b32 %0, %1" ::"v"(a), "v"(v) : "memory"); }
__device__ __forceinline__ void cw_full(u32 v) { asm volatile("ds_write2_b32 %0, %1, %1 offset1:1" ::"v"(W_FULL), "v"(v) : "memory"); }
__device__ __forceinline__ void cw_add(u32 a, u32 v, int lane) { if (lane == 0) asm volatile("ds_add_u32 %0, %1" ::"v"(a), "v"(v) : "memory"); asm volatile("" ::: "memory"); }
__device__ __forceinline__ u32 cw_add_rtn(u32 a, u32 v, int lane) {
  u32 r = 0;
  if (lane == 0) asm volatile("ds_add_rtn_u32 %0, %1, %2\n\ts_waitcnt lgkmcnt(0)" : "=v"(r) : "v"(a), "v"(v) : "memory");
  asm volatile("" ::: "memory");
  return __builtin_amdgcn_readfirstlane(r);
}
__device__ __forceinline__ u32 cw_wait_(u32 a, u32 want, u32& bud) {
  u32 t;
  bud = __builtin_amdgcn_readfirstlane(bud); want = __builtin_amdgcn_readfirstlane(want);
  asm volatile("1:\n\tds_read_b32 v255, %2\n\ts_waitcnt lgkmcnt(0)\n\tv_readfirstlane_b32 %0, v255\n\ts_cmp_ge_u32 %0, %3\n\ts_cbranch_scc1 2f\n\t"
               "s_sleep 1\n\ts_sub_u32 %1, %1, 1\n\ts_cbranch_scc0 1b\n\ts_mov_b32 %1, 0\n2:\n\ts_nop 1"
               : "=&s"(t), "+s"(bud) : "v"(a), "s"(want) : "memory", "scc", "v255");
  return t;
}
#define cw_wait(a, want) cw_wait_((a), (want), bud)
__device__ __forceinline__ bool glb_retry_(u32& bud) {
  bud = __builtin_amdgcn_readfirstlane(bud);
  asm volatile("s_sleep 1\n\ts_sub_u32 %0, %0, 16\n\ts_cselect_b32 %0, 0, %0" : "+s"(bud) :: "memory", "scc");
  return bud != 0u;
}
#define glb_retry() glb_retry_(bud)
__device__ __forceinline__ u32x4 gld(rsrc_t r, u32 voff, u32 soff) { return __builtin_amdgcn_raw_buffer_load_b128(r, voff, soff, SC1); }
__device__ __forceinline__ u32x4 gld0(rsrc_t r, u32 voff, u32 soff) { return __builtin_amdgcn_raw_buffer_load_b128(r, voff, soff, 0); }
__device__ __forceinline__ void gst(rsrc_t r, u32x4 v, u32 voff, u32 soff) { __builtin_amdgcn_raw_buffer_store_b128(v, r, voff + soff, 0, SC1); }
__device__ __forceinline__ u32 tagx(u32x4 v, u32 tag) { return (v[1] ^ tag) | (v[3] ^ tag); }
__device__ __forceinline__ bool allz(u32 x) { return __builtin_amdgcn_ballot_w64(x != 0u) == 0ull; }
template <int N> __device__ __forceinline__ void fetch(LAS unsigned char* L, rsrc_t r, u32 voff, u32 ss, u32 tag, u32 dst, u32 ds, u32 flag, u32& bud) {
  u32x4 v[N];
  for (;;) {
    u32 bad = 0;
#pragma unroll
    for (int i = 0; i < N; ++i) v[i] = gld(r, voff + i * ss, 0);
#pragma unroll
    for (int i = 0; i < N; ++i) bad |= tagx(v[i], tag);
    if (allz(bad) || !glb_retry()) break;
  }
#pragma unroll
  for (int i = 0; i < N; ++i) *(LAS u32x2*)(L + dst + i * ds) = u32x2{v[i][0], v[i][2]};
  cw_st(flag, 1u);
}
#define DMA(r, ldsoff, voff, soff, imm, aux) __builtin_amdgcn_raw_ptr_buffer_load_lds((r), (LAS void*)(L + (ldsoff)), 16, (voff), (soff), (imm), (aux))

#define WLANE(x, i) asm volatile("v_writelane_b32 %0, %1, %2" : "+v"(stv) : "s"((u32)(x)), "i"(i))
#define SKIPST(i) false
#define STAMP(i) do { if (SKIPST(i)) break; unsigned long long t_; asm volatile("s_memtime %0\n\ts_waitcnt lgkmcnt(0)" : "=s"(t_) :: "memory"); WLANE(t_, (i)); } while (0)
#define STAMP_BEGIN() u32 stv = 0; do { unsigned long long r_ = __builtin_amdgcn_s_memrealtime(); WLANE(r_, 30); STAMP(0); } while (0)
#define STAMP_END() do { STAMP(29); unsigned long long r_ = __builtin_amdgcn_s_memrealtime(); WLANE(r_, 31); \
    if ((threadIdx.x & 63) < 32) ((u32*)((char*)a.ws + WS_DIAG))[(blockIdx.x * 6 + wave) * 32 + (threadIdx.x & 31)] = stv; } while (0)

#define VMW(n) asm volatile("s_waitcnt vmcnt(" #n ")" ::: "memory")
#define ZERO4 f32x4{0.f, 0.f, 0.f, 0.f}
#define MAX8(a, b) fmaxf(fmaxf(fmaxf(a[0], a[1]), fmaxf(a[2], a[3])), fmaxf(fmaxf(b[0], b[1]), fmaxf(b[2], b[3])))
constexpr float LAZY = 192.f;
template <int NA> __device__ __forceinline__ u32x4 soft(const f32x4 s0, const f32x4 s1, float& m, float& l, f32x4* acc) {
  const float mx = MAX8(s0, s1);
  if (__builtin_amdgcn_ballot_w64(mx > m + LAZY) != 0ull) {
    const float mn = fmaxf(m, xg_max(mx)), al = ex2((m - mn) * CS);
    m = mn; l *= al;
#pragma unroll
    for (int i = 0; i < NA; ++i) acc[i] *= al;
  }
  const float mc = -m * CS;
  float pr[8];
#pragma unroll
  for (int i = 0; i < 4; ++i) { pr[i] = ex2(fmaf(s0[i], CS, mc)); pr[4 + i] = ex2(fmaf(s1[i], CS, mc)); }
  l += ((pr[0] + pr[1]) + (pr[2] + pr[3])) + ((pr[4] + pr[5]) + (pr[6] + pr[7]));
  return u32x4{pk(pr[0], pr[1]), pk(pr[2], pr[3]), pk(pr[4], pr[5]), pk(pr[6], pr[7])};
}
__device__ __forceinline__ u32x4 pick2(const f32x4 d0, const f32x4 d1, int q, u32 tag) {
  const float x0 = (q & 2) ? d1[0] : d0[0], x1 = (q & 2) ? d1[1] : d0[1], x2 = (q & 2) ? d1[2] : d0[2], x3 = (q & 2) ? d1[3] : d0[3];
  return u32x4{asu((q & 1) ? x2 : x0), tag, asu((q & 1) ? x3 : x1), tag};
}

constexpr int NPR = (NREC + 1) / 2;
template <int I> __device__ __forceinline__ void merge_rec(f32x4* o, const u32x4* t, float w) {
  if constexpr (I < NPR) { o[I & 1] += dpp<0x150 + 2 * I>(w) * f32x4{bflo(t[I][0]), bfhi(t[I][0]), bflo(t[I][2]), bfhi(t[I][2])}; merge_rec<I + 1>(o, t, w); }
}

constexpr int IT_UK = 0, IT_C = 4, IT_UV = 20, IT_W = 24;
constexpr u32 ALL = 1u << 20;
template <int N> __device__ __forceinline__ void vmw() { asm volatile("s_waitcnt vmcnt(%0)" ::"i"(N) : "memory"); }

__device__ __forceinline__ void read_w(LAS unsigned char* L, u32x4* wf, int c, int lane) {
  const u32 lp = (8 * (lane >> 4) + ((lane >> 2) & 3)) * 576 + 8 * (lane & 3) + 128 * c, t2 = c < 4 ? 64 : 0;
#pragma unroll
  for (int i = 0; i < 16; ++i) { const u32 ad = (i >> 2) * SLOT + lp + (i & 1) * 32 + ((i & 2) ? t2 : 0); wf[i] = ldtr2(L, ad, ad + 2304); }
}
__device__ __forceinline__ void project(LAS unsigned char* L, const u32x4* wf, rsrc_t rws, u32 so, u32 tag, int c, int lane, u32& bud) {
  const int g4 = lane >> 4, q = (lane >> 2) & 3, p = lane & 3;
  u32 ao = OB_OFF + p * OB_STR + g4 * 16, vo = so + c * 512 + p * 4608 + (q >> 1) * 128 + g4 * 32 + (q & 1) * 16;
  asm volatile("" : "+v"(ao), "+v"(vo));
  cw_wait(W_PJ, 4);
  f32x4 d[4] = {ZERO4, ZERO4, ZERO4, ZERO4};
  u32x4 of[4];
#pragma unroll
  for (int ks = 0; ks < 4; ++ks) of[ks] = ld128(L, ao + ks * 64);
  __builtin_amdgcn_sched_barrier(0);
#pragma unroll
  for (int ks = 0; ks < 4; ++ks) { d[0] = mfma(wf[ks * 4], of[ks], d[0]); d[1] = mfma(wf[ks * 4 + 1], of[ks], d[1]); }
#pragma unroll
  for (int ks = 0; ks < 4; ++ks) { d[2] = mfma(wf[ks * 4 + 2], of[ks], d[2]); d[3] = mfma(wf[ks * 4 + 3], of[ks], d[3]); }
  gst(rws, pick2(d[0], d[1], q, tag), vo, 0);
  if (c < 4) gst(rws, pick2(d[2], d[3], q, tag), vo + 256, 0);
}
__device__ __forceinline__ void final_sum(rsrc_t rws, void* out, int br, int hkv, int b, int i, u32 tag, int lane, u32& bud) {
  const int c0 = 36 * hkv + 8 * i + 2 * (lane >> 5);
  u32 vo = WS_YG + (br * 32 + (lane & 31)) * 18432 + (b * 576 + c0) * 8, oo = (b * 1152 + br * 576 + c0 + (lane & 1) * 4) * 2;
  const u32 v2 = i < 4 ? 32 : 0;
  const rsrc_t ro = mkrs(out, 4u * 1152u * 2u);
  asm volatile("" : "+v"(vo), "+v"(oo));
  u32x4 y, z;
  for (;;) { y = gld(rws, vo, 0); z = gld(rws, vo, v2); if (allz(tagx(y, tag) | tagx(z, tag)) || !glb_retry()) break; }
  float r[4] = {asf(y[0]), asf(y[2]), asf(z[0]), asf(z[2])};
#pragma unroll
  for (int k = 0; k < 4; ++k) { auto t = __builtin_amdgcn_permlane16_swap(asu(r[k]), asu(r[k]), false, false); r[k] = row_sum(asf(t[0]) + asf(t[1])); }
  if ((lane & 31) < (i < 4 ? 2 : 1)) __builtin_amdgcn_raw_buffer_store_b32((lane & 1) ? pk(r[2], r[3]) : pk(r[0], r[1]), ro, oo, 0, OUT_AUX);
}

__global__ void __launch_bounds__(384, 2) mk_kernel(Args a) {
  __shared__ __attribute__((aligned(16))) unsigned char smem[LDS_BYTES];
  LAS unsigned char* L = (LAS unsigned char*)smem;
  const int lane = threadIdx.x & 63, wave = __builtin_amdgcn_readfirstlane(threadIdx.x >> 6);
  const int g = blockIdx.x, gi = g >> 3, hkv = 2 * (g & 7) + (gi >> 4), j = gi & 15, b = j >> 2, s = j & 3;
  STAMP_BEGIN();
  if (wave == CODE_PF - 1) {
    unsigned long long pc; asm volatile("s_getpc_b64 %0" : "=s"(pc));
    DMA(mkrs((const void*)(pc & ~255ull), CODE_BYTES), MG_OFF, lane * 16, gi * 1024, 0, 0);
  }
  const u32 vu = (lane >> 5) * 8192 + (((lane & 31) ^ ((lane >> 5) << 1)) << 4);
  const u32 vw = ((lane >> 2) & 7) * 8192 + (lane & 3) * 16 + (lane >> 5) * 64;
  if (wave == 0) {
    if (s != 1) {
      const rsrc_t rw = mkrs(a.in[s == 0 ? 7 : s == 2 ? 3 : 8], s < 3 ? 576u * 8192u : 512u * 8192u);
      const u32 vo = s < 3 ? vw : vu, st = s < 3 ? 8 * 8192 : 2 * 8192, xm = s == 3 ? 0xf0u : 0u;
      u32 so = hkv * 512 + (s < 3 ? b * 128 : b * (128u * 8192u));
#pragma unroll
      for (int i = 0; i < 32; ++i, so += st) DMA(rw, i * 1024 + (i >= 16 && s == 3 ? 2048 : 0), vo ^ ((swz((2 * i) & 15) << 4) & xm), so, 0, AUX_W);
    }
    STAMP(14);
    cw_st(CTL + lane * 4, 0u);
    asm volatile("s_waitcnt lgkmcnt(0)" ::: "memory");
  }
  __builtin_amdgcn_s_barrier();
  const int ab = gi >> 3, ah = ((g & 7) + 2 * (gi & 4) + 3 * (ab + 4 * s)) & 15, as = s;
  const int ITU = 4 + (s == 0 ? N0 : s == 1 ? N1 : N2);
  const int bq = (g & 7) >> 1, sp = (g & 1) * 8 + (gi >> 2);
  u32 bud = SPIN_BUDGET;

  if (wave == 0) {
    __builtin_amdgcn_s_setprio(3);
    if ((s & 1) == 0) {
      const rsrc_t rw = mkrs(a.in[s ? 3 : 7], 576u * 8192u), rx = mkrs(a.in[s ? 0 : 5], 4u * 576u * 2u);
      u32 so = hkv * 512 + b * 128 + 32 * 65536;
#pragma unroll
      for (int i = 0; i < 5; ++i) DMA(rx, SB_OFF + i * 1024, lane * 16, i * 1024, 0, 0);
#pragma unroll 1
      for (int i = 32; i < 72; i += 4, so += 4 * 65536) {
        vmw<40>();
        DMA(rw, i * 1024, vw, so, 0, AUX_W); DMA(rw, i * 1024 + 1024, vw, so + 65536, 0, AUX_W); DMA(rw, i * 1024 + 2048, vw, so + 2 * 65536, 0, AUX_W); DMA(rw, i * 1024 + 3072, vw, so + 3 * 65536, 0, AUX_W);
      }
      VMW(36); cw_full(2);
    } else if (s == 3) {
      const rsrc_t r = mkrs(a.in[8], 512u * 8192u);
      u32 so = ((u32)b * 128u + 64u) * 8192u + hkv * 512u;
#pragma unroll 1
      for (int t = 2; t < 4; ++t) {
        if (t == 3) { VMW(32); cw_full(1); }
#pragma unroll
        for (int i = 0; i < 16; ++i, so += 2 * 8192) DMA(r, t * SLOT + i * 1024, vu ^ (swz((2 * i) & 15) << 4), so, 0, AUX_W);
      }
      VMW(32); cw_full(2);
    }
    u32 vq[4];
#pragma unroll
    for (int q = 0; q < 4; ++q) vq[q] = (lane >> 4) * 256 + (((lane & 15) ^ swz(4 * q + (lane >> 4))) << 4);
    int pub = 0, it = 4;
#define SLOT_WAIT() do { if (it >= NS) { const u32 need = 4u * (it >> 3), wa = W_FREE + (it & 7) * 4; \
      if (cw_ld(wa) < need) { VMW(16); if (it - 1 > pub) cw_full(it - 1); VMW(0); pub = it; cw_full(pub); cw_wait(wa, need); } } } while (0)
#define LSTAMP() do { if (it == 4) STAMP(2); if (it == 8) STAMP(3); if (it == 12) STAMP(4); if (it == 16) STAMP(5); if (it == 20) STAMP(6); if (it == 24) STAMP(7); if (it == 28) STAMP(8); if (it == 32) STAMP(9); if (it == 36) STAMP(10); if (it == 40) STAMP(11); if (it == 44) STAMP(12); } while (0)
#define PACE(LW, k) do { vmw<LW>(); if ((k) == ((LW / 4) & 3) && it - LW / 16 > pub) { pub = it - LW / 16; cw_full(pub); } } while (0)
#define LOAD_W(in_, LW) do { const rsrc_t r = mkrs(a.in[in_], 4096u * 1152u); u32 wo[9]; int ln = lane; asm volatile("" : "+v"(ln)); \
      _Pragma("unroll") for (int i = 0; i < 9; ++i) { const u32 ci = i * 64 + ln; wo[i] = ci * 16 + ((ci * 3641u) >> 17) * 576u; } \
      u32 sw = ((u32)hkv * 256u + (b & 1) * 128u) * 1152u + (b >> 1) * 576u; \
      _Pragma("unroll 1") for (int t = 0; t < 8; ++t, sw += 16u * 1152u) { _Pragma("unroll") for (int i = 0; i < 9; ++i) { if (i % 3 == 0) vmw<LW>(); DMA(r, t * 9216 + i * 1024, wo[i], sw, 0, AUX_W); } } } while (0)
    if (s == 1) LOAD_W(10, 32);
    asm volatile("" : "+s"(it));
    if (s < 3) {
      const rsrc_t rk = mkrs(a.in[1], 1u << 26), rv = mkrs(a.in[2], 1u << 26), rt = mkrs((const char*)a.ws + WS_TK + ah * 128, 4u);
      u32 so = ((u32)(ab * 16 + ah) * 128u + (s == 0 ? 0 : s == 1 ? N0 : N0 + N1)) * 8192u;
      asm volatile("v_mov_b32 v254, 1" ::: "v254");
      if (s == 0 && HOLD_ST) { VMW(0); pub = 4; cw_full(4); cw_wait(W_QS, 4); }
#pragma unroll 1
      for (;; ++it) {
        const u32 sb = (it & 7) * SLOT;
        SLOT_WAIT();
        LSTAMP();
        PACE(LW_A, 0);
        if (it >= ITU) {
          u32 t;
          asm volatile("v_readlane_b32 %0, v254, %1\n\tv_writelane_b32 v254, 1, %1\n\ts_nop 4" : "=&s"(t) : "s"(it & 3) : "memory", "v254");
          if (t >= (u32)NTICK) break;
          cw_st(W_TK + (it & 15) * 4, (it << 8) | t);
          so = (((t & 3u) * 16u + ah) * 128u + NSUM + (t >> 2)) * 8192u;
        }
        if (it >= ITU - 2) {
          unsigned long long ex;
          asm volatile("s_mov_b64 %0, exec\n\ts_lshl_b64 exec, 1, %2\n\tbuffer_atomic_add v254, off, %1, 0 sc0\n\ts_mov_b64 exec, %0"
                       : "=&s"(ex) : "s"(rt), "s"((it + 2) & 3) : "memory", "scc", "v254");
        }
#pragma unroll
        for (int k = 0; k < 2; ++k) {
          if (k) PACE(LW_A, 1);
          DMA(rk, sb + k * 4096, vq[0], so + k * 4096, 0, AUX_K); DMA(rk, sb + k * 4096, vq[1], so + k * 4096, 1024, AUX_K);
          DMA(rk, sb + k * 4096, vq[2], so + k * 4096, 2048, AUX_K); DMA(rk, sb + k * 4096, vq[3], so + k * 4096, 3072, AUX_K);
        }
#pragma unroll
        for (int k = 0; k < 2; ++k) {
          PACE(LW_A, k + 2);
          DMA(rv, sb + 8192 + k * 4096, vq[0], so + k * 4096, 0, AUX_V); DMA(rv, sb + 8192 + k * 4096, vq[1], so + k * 4096, 1024, AUX_V);
          DMA(rv, sb + 8192 + k * 4096, vq[2], so + k * 4096, 2048, AUX_V); DMA(rv, sb + 8192 + k * 4096, vq[3], so + k * 4096, 3072, AUX_V);
        }
        so += 8192u;
      }
      cw_st(W_TK + ((it + (lane & 3)) & 15) * 4, ((it + (lane & 3)) << 8) | 255u);
      STAMP(13);
    } else {
#pragma unroll 1
      for (; it < IT_W; ++it) {
        const u32 sb = (it & 7) * SLOT;
        if (it == GATE1 || it == GATE2) {
          VMW(16); if (it - 1 > pub) cw_full(it - 1);
          VMW(0); pub = it; cw_full(it);
          if (it == GATE1) cw_wait(W_QS, 5);
          if (it == GATE2) cw_wait(W_GI, 4);
        }
        SLOT_WAIT();
        LSTAMP();
        if (it < IT_UV) {
          const rsrc_t r = mkrs(a.in[6], 1u << 24);
          const u32 so = ((u32)bq * 4096u + sp * 256u + (it - IT_C) * 16u) * 1024u;
#pragma unroll
          for (int k = 0; k < 4; ++k) {
            PACE(LW_B, k);
            DMA(r, sb + k * 4096, (lane ^ swz(4 * k + 0)) << 4, so + k * 4096, 0, AUX_C);
            DMA(r, sb + k * 4096, (lane ^ swz(4 * k + 1)) << 4, so + k * 4096, 1024, AUX_C);
            DMA(r, sb + k * 4096, (lane ^ swz(4 * k + 2)) << 4, so + k * 4096, 2048, AUX_C);
            DMA(r, sb + k * 4096, (lane ^ swz(4 * k + 3)) << 4, so + k * 4096, 3072, AUX_C);
          }
        } else {
          const rsrc_t r = mkrs(a.in[9], 512u * 8192u);
          const u32 so = ((u32)b * 128u + (it & 3) * 32u) * 8192u + hkv * 512u;
#pragma unroll
          for (int i = 0; i < 16; ++i) { if ((i & 3) == 0) PACE(LW_B, i >> 2); DMA(r, sb + i * 1024, vu ^ (swz((2 * i) & 15) << 4), so + i * (2 * 8192), 0, AUX_W); }
        }
      }
      VMW(0); pub = it; cw_full(it);
      cw_wait(W_GI, 8);
      STAMP(15);
      LOAD_W(4, LW_W);
    }
    VMW(16); if (it - 1 > pub) cw_full(it - 1);
    VMW(8); cw_st(W_KF, ALL);
    VMW(0); cw_full(ALL);
    STAMP(1);
    STAMP_END();
    return;
  }

  const int c = wave - 1, g4 = lane >> 4, n = lane & 15, q = n >> 2, p = lane & 3;
  const rsrc_t rws = mkrs(a.ws, WS_END);
  const char* wsp = (const char*)a.ws; asm volatile("" : "+s"(wsp));
  const u32 tag = *(const __attribute__((address_space(4))) u32*)(wsp + WS_EPOCH) + 1u;
  const u32 yso = WS_YG + ((s == 1 ? 32 : 0) + hkv * 2 + (b & 1)) * 18432 + (b >> 1) * 2304;
  if (c == 4) {
    u32x4 wf[16];
    if (s == 1) {
      cw_wait(W_FULL, 4);
      read_w(L, wf, 4, lane);
      cw_st(W_MF, 1u);
    }
    cw_wait(W_FULL, s == 3 ? POLL_B : s == 0 ? POLL_A0 : POLL_A);
    STAMP(1);
    fetch<4>(L, rws, lane * 16 + WS_QG + (s < 3 ? ah * 128 : 8192 + hkv * 128) * 8, 16384, tag, (s < 3 ? QA_OFF : OB_OFF) + lane * 8, s < 3 ? 512 : OB_STR, W_QA, bud);
    STAMP(3);
    if (s == 3) {
      cw_wait(W_QS, 5); cw_wait(W_FULL, POLL_H);
      fetch<1>(L, rws, lane * 16 + WS_HINT, 0, tag, MG_OFF + lane * 8, 0, W_HINT, bud);
      STAMP(4);
      cw_wait(W_BP, 4);
      fetch<4>(L, rws, n * 32 + (g4 & 1) * 16 + WS_SG + hkv * 2048, 512, tag, MG_OFF + lane * 8, 512, W_SGF, bud);
      STAMP(2);
      for (int i = 0; i < 64; ++i) {
        if (__builtin_amdgcn_readfirstlane(__builtin_amdgcn_raw_buffer_load_b32(rws, WS_TK + hkv * 128, 0, SC1)) >= (u32)(WTRIG)) break;
        __builtin_amdgcn_s_sleep(4); asm volatile("" ::: "memory");
      }
      cw_st(W_WT, 1u);
      cw_wait(W_FULL, ALL);
      read_w(L, wf, 4, lane);
    } else if (s == 1) {
      cw_wait(W_FULL, POLL_C);
      const u32 so = WS_CG + hkv * 32768 + (b & 1) * 1024;
      for (;;) {
        const u32x4 t = gld(rws, q * 8192 + (p & 1) * 512, so);
        if (allz(tagx(t, tag)) || !glb_retry()) break;
        __builtin_amdgcn_s_sleep(16);
      }
      u32x4 cv[16];
      for (;;) {
        u32 bad = 0;
#pragma unroll
        for (int i = 0; i < 16; ++i) cv[i] = gld(rws, lane * 16 + (i & 1) * 2048, so + (i >> 1) * 4096);
#pragma unroll
        for (int i = 0; i < 16; ++i) bad |= tagx(cv[i], tag);
        if (allz(bad) || !glb_retry()) break;
      }
#pragma unroll
      for (int i = 0; i < 4; ++i)
        *(LAS u32*)(L + OB_OFF + i * OB_STR + lane * 4) = pk((asf(cv[i][0]) + asf(cv[4 + i][0])) + (asf(cv[8 + i][0]) + asf(cv[12 + i][0])), (asf(cv[i][2]) + asf(cv[4 + i][2])) + (asf(cv[8 + i][2]) + asf(cv[12 + i][2])));
      cw_st(W_PJ, 4u);
      STAMP(2);
    }
    if (s & 1) {
      project(L, wf, rws, yso, tag, 4, lane, bud);
    } else {
      cw_wait(W_O2, 4);
      __builtin_amdgcn_s_sleep(FSLEEP);
      final_sum(rws, a.out, s == 0, hkv, b, 4, tag, lane, bud);
      if (g == 16 && lane == 0) __builtin_amdgcn_raw_buffer_store_b32(tag, rws, 0, WS_EPOCH, SC1);
      STAMP(22);
    }
    STAMP_END();
    return;
  }

  if (s < 3) {
    u32x4 wf[16];
    if (s == 1) {
      cw_wait(W_FULL, 4);
      read_w(L, wf, c, lane);
      cw_wait(W_MF, 1);
      if (lane < 4) asm volatile("ds_add_u32 %0, %1" ::"v"(W_FREE + lane * 4), "v"(1u) : "memory");
    } else {
      const u32 aw = g4 * 1024 + (c >> 1) * 512 + q * 64 + (c & 1) * 32 + 8 * p, ax = SB_OFF + p * 1152 + g4 * 16;
      const u32 vo = (n * 2048 + 2 * g4) * 8, so = WS_QG + ((s ? 0 : 8192) + hkv * 128 + b * 32 + 8 * c) * 8;
      if (j == 2 && c == 0) { __builtin_amdgcn_raw_buffer_store_b32(0u, rws, WS_TK + hkv * 128, 0, SC1); VMW(0); }
      cw_wait(W_FULL, 2);
      u32x4 xr[18], wq[9];
#pragma unroll
      for (int ks = 0; ks < 18; ++ks) xr[ks] = ld128(L, ax + ks * 64);
      f32x4 a0 = ZERO4, a1 = ZERO4;
#pragma unroll
      for (int hf = 0; hf < 2; ++hf) {
        if (hf) { cw_wait(W_FULL, 4); STAMP(1); }
#pragma unroll
        for (int ks = 0; ks < 9; ++ks) wq[ks] = ldtr2(L, aw + (hf * 9 + ks) * 4096, aw + (hf * 9 + ks) * 4096 + 256);
        if (lane < 2) asm volatile("ds_add_u32 %0, %1" ::"v"(W_FREE + hf * 8 + lane * 4), "v"(1u) : "memory");
        __builtin_amdgcn_sched_barrier(0);
#pragma unroll
        for (int ks = 0; ks < 9; ++ks) { if (ks & 1) a1 = mfma(wq[ks], xr[hf * 9 + ks], a1); else a0 = mfma(wq[ks], xr[hf * 9 + ks], a0); }
        __builtin_amdgcn_sched_barrier(0);
      }
      a0 += a1;
      if (n < 4) gst(rws, u32x4{pk(a0[0], a0[1]), tag, pk(a0[2], a0[3]), tag}, vo, so);
      cw_add(W_QS, 1, lane);
      STAMP(2);
    }
    float m = -1e30f, l = 0.f;
    f32x4 acc[8];
#pragma unroll
    for (int i = 0; i < 8; ++i) acc[i] = ZERO4;
    u32x4 qf[4], kf[8], vf[8];
    const u32 lpK = n * 256 + ((g4 ^ swz(n)) << 4), lpQ = QA_OFF + (n & 1) * 256 + g4 * 16;
    const u32 lpV = (4 * g4 + q) * 256 + (((p >> 1) ^ swz(4 * g4 + q)) << 4) + 8 * (p & 1);
#define READ_K() _Pragma("unroll") for (int kk = 0; kk < 4; ++kk) { const u32 ak = (sb + lpK) ^ (kk << 6); kf[kk] = ld128(L, ak); kf[4 + kk] = ld128(L, ak + 4096); }
#define READ_V() do { _Pragma("unroll") for (int ct = 0; ct < 8; ++ct) { const u32 av = (sb + lpV) ^ (ct << 5); vf[ct] = ldtr2(L, av + 8192, av + 12288); } \
      cw_add(W_FREE + (it & 7) * 4, 4, lane); } while (0)
#define SCORES() f32x4 s0 = ZERO4, s1 = ZERO4; \
      _Pragma("unroll") for (int kk = 0; kk < 4; ++kk) { s0 = mfma(kf[kk], qf[kk], s0); s1 = mfma(kf[4 + kk], qf[kk], s1); }
#define PICK8(e) const f32x4 e##0 = (n & 2) ? acc[1] : acc[0], e##1 = (n & 2) ? acc[3] : acc[2], e##2 = (n & 2) ? acc[5] : acc[4], e##3 = (n & 2) ? acc[7] : acc[6], \
      e##4 = (n & 4) ? e##1 : e##0, e##5 = (n & 4) ? e##3 : e##2, e = (n & 8) ? e##5 : e##4
    const u32 vrec = (n & 1) * 512 + (n >> 1) * 64 + g4 * 16;
    bool first = true;
    int it = 4 + c;
#pragma unroll 1
    for (; it < ITU; it += 4) {
      const u32 sb = (it & 7) * SLOT;
      cw_wait(W_FULL, it + 1);
      READ_K(); READ_V();
      if (first) {
        first = false;
        cw_wait(W_QA, 1);
        STAMP(3);
#pragma unroll
        for (int kk = 0; kk < 4; ++kk) qf[kk] = ld128(L, lpQ + ab * 512 + kk * 64);
      }
      SCORES();
      const u32x4 pf = soft<8>(s0, s1, m, l, acc);
#pragma unroll
      for (int ct = 0; ct < 8; ++ct) acc[ct] = mfma(vf[ct], pf, acc[ct]);
    }
    STAMP(8);
    {
      l = xg_sum(l);
      PICK8(e);
      *(LAS f32x4*)(L + MG_OFF + c * 1024 + lane * 16) = e;
      if (lane < 2) *(LAS f32x2*)(L + MS_OFF + (c * 2 + lane) * 8) = f32x2{m, l};
      if (cw_add_rtn(W_MRG, 1, lane) == 3u) {
        f32x2 sv[4]; f32x4 ov[4];
#pragma unroll
        for (int i = 0; i < 4; ++i) { sv[i] = *(LAS f32x2*)(L + MS_OFF + (i * 2 + (n & 1)) * 8); ov[i] = *(LAS f32x4*)(L + MG_OFF + i * 1024 + lane * 16); }
        const float M = fmaxf(fmaxf(sv[0][0], sv[1][0]), fmaxf(sv[2][0], sv[3][0]));
        float Ls = 0.f; f32x4 o = ZERO4;
#pragma unroll
        for (int i = 0; i < 4; ++i) { const float w = ex2((sv[i][0] - M) * CS); Ls = fmaf(w, sv[i][1], Ls); o += w * ov[i]; }
        o *= __builtin_amdgcn_rcpf(Ls);
        gst(rws, u32x4{pk(o[0], o[1]), tag, pk(o[2], o[3]), tag}, vrec, WS_AG + ((ah * 4 + ab) * 16 + as) * 1024);
        if (lane < 2) gst(rws, u32x4{asu(M), tag, asu(Ls), tag}, lane * 16, WS_AS + (ah * 4 + ab) * 2048 + as * 128);
      }
    }
    STAMP(9);
#pragma unroll 1
    for (;; it += 4) {
      const u32 t = cw_wait(W_TK + (it & 15) * 4, it << 8) & 255u, sb = (it & 7) * SLOT;
      if (t >= (u32)NTICK || bud == 0u) break;
#pragma unroll
      for (int kk = 0; kk < 4; ++kk) qf[kk] = ld128(L, lpQ + (t & 3u) * 512 + kk * 64);
      const u32 pr = ah * 4 + (t & 3u), rec = 3 + (t >> 2), va = vrec + WS_AG + (pr * 16 + rec) * 1024, vs = lane * 16 + WS_AS + pr * 2048 + rec * 128;
      cw_wait(W_FULL, it + 1);
      READ_K(); READ_V();
      SCORES();
      m = xg_max(MAX8(s0, s1));
      const float mc = -m * CS;
#pragma unroll
      for (int i = 0; i < 4; ++i) { s0[i] = ex2(fmaf(s0[i], CS, mc)); s1[i] = ex2(fmaf(s1[i], CS, mc)); }
      const u32x4 pf = {pk(s0[0], s0[1]), pk(s0[2], s0[3]), pk(s1[0], s1[1]), pk(s1[2], s1[3])};
      f32x4 e0 = ZERO4, e1 = ZERO4;
#pragma unroll
      for (int ct = 0; ct < 8; ct += 2) { e0 = mfma(vf[ct], (n >> 1) == ct ? pf : u32x4{0u, 0u, 0u, 0u}, e0); e1 = mfma(vf[ct + 1], (n >> 1) == ct + 1 ? pf : u32x4{0u, 0u, 0u, 0u}, e1); }
      s0 += s1;
      l = xg_sum((s0[0] + s0[1]) + (s0[2] + s0[3]));
      e0 = (e0 + e1) * __builtin_amdgcn_rcpf(l);
      gst(rws, u32x4{pk(e0[0], e0[1]), tag, pk(e0[2], e0[3]), tag}, va, 0);
      if (lane < 2) gst(rws, u32x4{asu(m), tag, asu(l), tag}, vs, 0);
      STAMP(10);
    }
    if (s == 1) {
      STAMP(18);
      project(L, wf, rws, yso, tag, c, lane, bud);
      STAMP(20);
    } else {
      cw_add(W_O2, 1, lane);
      cw_wait(W_O2, 4);
      __builtin_amdgcn_s_sleep(FSLEEP);
      final_sum(rws, a.out, s == 0, hkv, b, c, tag, lane, bud);
      STAMP(22);
    }
    STAMP_END();
    return;
  }

  {
    cw_wait(W_FULL, IT_UK + c + 1);
    u32x4 wu[16];
    {
      const u32 lp = (IT_UK + c) * SLOT + n * 512 + ((g4 ^ swz(n)) << 4);
#pragma unroll
      for (int i = 0; i < 16; ++i) wu[i] = ld128(L, (lp ^ ((i & 3) << 6)) + ((i >> 2) & 1) * 8192 + (i >> 3) * 256);
    }
    cw_add(W_FREE + (IT_UK + c) * 4, 4, lane);
    STAMP(15);
    cw_wait(W_QA, 1);
    STAMP(3);
    f32x4 d[4] = {ZERO4, ZERO4, ZERO4, ZERO4};
#pragma unroll
    for (int i = 0; i < 8; ++i) {
      const u32x4 qv = ld128(L, OB_OFF + p * OB_STR + i * 64 + g4 * 16);
      d[(i >> 2) * 2] = mfma(wu[(i >> 2) * 8 + (i & 3)], qv, d[(i >> 2) * 2]); d[(i >> 2) * 2 + 1] = mfma(wu[(i >> 2) * 8 + 4 + (i & 3)], qv, d[(i >> 2) * 2 + 1]);
    }
    const f32x4 e0 = (q & 1) ? d[1] : d[0], e1 = (q & 1) ? d[3] : d[2], e = (q & 2) ? e1 : e0;
    gst(rws, u32x4{pk(e[0], e[1]), tag, pk(e[2], e[3]), tag}, p * 65536 + (g4 & 1) * 128 + (q >> 1) * 64 + (q & 1) * 32 + (g4 >> 1) * 16, WS_QAG + hkv * 4096 + (b * 4 + c) * 256);
    if (cw_add_rtn(W_QS, 1, lane) == 3u) { if (lane == 0) gst(rws, u32x4{tag, tag, tag, tag}, WS_HINT + (hkv * 4 + b) * 16, 0); cw_add(W_QS, 1, lane); }
    STAMP(4);
  }
  {
    const int ch = c & 1;
    u32x4 qa[8];
    {
      const u32 so = WS_QAG + bq * 65536 + (c >> 1) * 32768 + ch * 2048;
      cw_wait(W_HINT, 1);
      STAMP(5);
      u32x4 t[16];
#pragma unroll
      for (int i = 0; i < 16; ++i) t[i] = gld0(rws, (n >> 1) * 4096 + (n & 1) * 64 + g4 * 16 + i * 128, so);
      cw_add(W_GI, 1, lane);
      for (;;) {
        u32 bad = 0;
#pragma unroll
        for (int i = 0; i < 16; ++i) bad |= tagx(t[i], tag);
        if (allz(bad) || !glb_retry()) break;
#pragma unroll
        for (int i = 0; i < 16; ++i) t[i] = gld(rws, (n >> 1) * 4096 + (n & 1) * 64 + g4 * 16 + i * 128, so);
      }
#pragma unroll
      for (int i = 0; i < 8; ++i) qa[i] = u32x4{t[2 * i][0], t[2 * i][2], t[2 * i + 1][0], t[2 * i + 1][2]};
    }
    STAMP(6);
    float mb = -1e30f, lb = 0.f;
    f32x4 ob[16];
#pragma unroll
    for (int i = 0; i < 16; ++i) ob[i] = ZERO4;
    const u32 lpR = n * 1024 + ((g4 ^ swz(n)) << 4) + ch * 512;
    const u32 lpC = (4 * g4 + q) * 1024 + (((p >> 1) ^ swz(4 * g4 + q)) << 4) + 8 * (p & 1) + ch * 512;
#pragma unroll 1
    for (int st = 0; st < 8; ++st) {
      const int it = IT_C + 2 * st;
      const u32 sb = (it & 7) * SLOT;
      const u32 xw = ((st & 1) ? (4 + c) * SLOT + 16384 : SB_OFF + c * 2048) + lane * 16, xr = ((st & 1) ? (4 + (c ^ 1)) * SLOT + 16384 : SB_OFF + (c ^ 1) * 2048) + lane * 16;
      cw_wait(W_FULL, it + 2);
      u32x4 rf[16], cf[16];
#pragma unroll
      for (int kk = 0; kk < 8; ++kk) { const u32 ak = ((sb + lpR) ^ ((kk & 3) << 6)) + (kk >> 2) * 256; rf[2 * kk] = ld128(L, ak); rf[2 * kk + 1] = ld128(L, ak + SLOT); }
      __builtin_amdgcn_sched_barrier(0);
      f32x4 s0 = ZERO4, s1 = ZERO4;
#pragma unroll
      for (int kk = 0; kk < 8; ++kk) {
        s0 = mfma(rf[2 * kk], qa[kk], s0); s1 = mfma(rf[2 * kk + 1], qa[kk], s1);
        const u32 av = (sb + lpC) ^ (kk << 5);
        cf[kk] = ldtr2(L, av, av + SLOT); cf[8 + kk] = ldtr2(L, av + 256, av + 256 + SLOT);
        __builtin_amdgcn_sched_barrier(0);
      }
      *(LAS f32x4*)(L + xw) = s0;
      *(LAS f32x4*)(L + xw + 1024) = s1;
      cw_st(W_S + c * 4, st + 1);
      if (lane < 2) asm volatile("ds_add_u32 %0, %1" ::"v"(W_FREE + (it & 7) * 4 + lane * 4), "v"(1u) : "memory");
      cw_wait(W_S + (c ^ 1) * 4, st + 1);
      s0 += *(LAS f32x4*)(L + xr); s1 += *(LAS f32x4*)(L + xr + 1024);
      const u32x4 pf = soft<16>(s0, s1, mb, lb, ob);
#pragma unroll
      for (int ct = 0; ct < 16; ++ct) ob[ct] = mfma(cf[ct], pf, ob[ct]);
      if (st == 3) STAMP(16);
      if (st == 4) STAMP(17);
    }
    STAMP(7);
#pragma unroll
    for (int i = 0; i < 8; ++i) {
      auto x = __builtin_amdgcn_permlane16_swap(pk(ob[2 * i][0], ob[2 * i][1]), pk(ob[2 * i + 1][0], ob[2 * i + 1][1]), false, false);
      auto y = __builtin_amdgcn_permlane16_swap(pk(ob[2 * i][2], ob[2 * i][3]), pk(ob[2 * i + 1][2], ob[2 * i + 1][3]), false, false);
      gst(rws, u32x4{x[0], y[0], x[1], y[1]}, (n >> 1) * 512 + (n & 1) * 64 + (g4 & 1) * 32 + (g4 >> 1) * 16 + (i & 3) * 128,
          WS_PB + ((bq * 16 + sp) * 4 + ch * 2 + (i >> 2)) * 8192 + (c >> 1) * 4096);
    }
    lb = xg_sum(lb);
    if (g4 == 0 && ch == 0) *(LAS f32x2*)(L + ST_OFF + ((c >> 1) * 16 + n) * 8) = f32x2{mb, lb};
    VMW(0);
    if (cw_add_rtn(W_BP, 1, lane) == 3u) {
      const f32x2 ml = *(LAS f32x2*)(L + ST_OFF + (lane & 31) * 8);
      if (lane < 32) gst(rws, u32x4{asu(ml[0]), tag, asu(ml[1]), tag}, (lane >> 1) * 2048 + (lane & 1) * 16, WS_SG + (bq * 16 + sp) * 32);
      cw_add(W_BP, 1, lane);
    }
    STAMP(8);
  }
  {
    cw_wait(W_SGF, 1);
    STAMP(10);
    u32x4 pv[8];
#pragma unroll
    for (int i = 0; i < 8; ++i) pv[i] = gld(rws, (lane >> 5) * 32768 + (lane & 31) * 16, WS_PB + ((c * 16 + 2 * i) * 4 + b) * 8192 + hkv * 512);
    cw_add(W_GI, 1, lane);
    const f32x2 st = *(LAS f32x2*)(L + MG_OFF + c * 512 + lane * 8);
    const float w = ex2((st[0] - row_max(st[0])) * CS), cf = w / row_sum(w * st[1]);
    f32x4 o0 = ZERO4, o1 = ZERO4;
#pragma unroll
    for (int i = 0; i < 8; ++i) {
      const float f = asf(__builtin_amdgcn_ds_bpermute((((lane >> 2) & 1) * 16 + 2 * i + (lane >> 5)) * 4, asu(cf)));
      o0 += f * f32x4{bflo(pv[i][0]), bfhi(pv[i][0]), bflo(pv[i][1]), bfhi(pv[i][1])};
      o1 += f * f32x4{bflo(pv[i][2]), bfhi(pv[i][2]), bflo(pv[i][3]), bfhi(pv[i][3])};
    }
#pragma unroll
    for (int i = 0; i < 4; ++i) {
      auto t0 = __builtin_amdgcn_permlane32_swap(asu(o0[i]), asu(o0[i]), false, false); o0[i] = asf(t0[0]) + asf(t0[1]);
      auto t1 = __builtin_amdgcn_permlane32_swap(asu(o1[i]), asu(o1[i]), false, false); o1[i] = asf(t1[0]) + asf(t1[1]);
    }
    if (lane < 32) *(LAS u32x4*)(L + OB_OFF + c * OB_STR + ((lane >> 2) & 1) * 256 + (lane >> 3) * 64 + p * 16) = u32x4{pk(o0[0], o0[1]), pk(o0[2], o0[3]), pk(o1[0], o1[1]), pk(o1[2], o1[3])};
    cw_add(W_OB, 1, lane);
    cw_wait(W_FULL, IT_UV + 4);
    u32x4 wf[16];
    {
      const u32 r0 = 8 * (g4 & 1) + q;
      const u32 lp0 = (8 * g4 + q) * 512 + ((((p >> 1) + 8 * c) ^ swz(r0)) << 4) + 8 * (p & 1);
      const u32 lp1 = (8 * g4 + q + 4) * 512 + ((((p >> 1) + 8 * c) ^ swz(r0 + 4)) << 4) + 8 * (p & 1);
#pragma unroll
      for (int i = 0; i < 16; ++i) wf[i] = ldtr2(L, (lp0 ^ ((i & 3) << 5)) + ((IT_UV + (i >> 2)) & 7) * SLOT, (lp1 ^ ((i & 3) << 5)) + ((IT_UV + (i >> 2)) & 7) * SLOT);
    }
    cw_wait(W_OB, 4);
    STAMP(11);
    f32x4 d[4] = {ZERO4, ZERO4, ZERO4, ZERO4};
#pragma unroll
    for (int ks = 0; ks < 4; ++ks) {
      const u32x4 of = ld128(L, OB_OFF + p * OB_STR + (c >> 1) * 256 + ks * 64 + g4 * 16);
#pragma unroll
      for (int t = 0; t < 4; ++t) d[t] = mfma(wf[ks * 4 + t], of, d[t]);
    }
    {
      const u32 vo = p * 2048 + (q >> 1) * 128 + g4 * 32 + (q & 1) * 16, so = WS_CG + ((hkv * 4 + b) * 1024 + c * 64) * 8;
      gst(rws, pick2(d[0], d[1], q, tag), vo, so); gst(rws, pick2(d[2], d[3], q, tag), vo + 256, so);
    }
    STAMP(12);
  }
  {
    cw_add(W_O2, 1, lane);
    cw_wait(W_O2, 4);
    const int rn = n ^ (lane >> 5);
    const u32 so = WS_AG + (hkv * 4 + c) * 16384 + (b & 1) * 512, ss = WS_AS + (hkv * 4 + c) * 2048 + (b & 1) * 16 + (rn < NREC ? rn : 0) * 128;
    const u32 lr = (4 + c) * SLOT, v0 = (lane & 31) * 16, v1 = v0 + (lane >> 5) * 1024;
    u32x4 wf[16], sv, ov[NPR];
    u32 todo = 0x5555u & ((1u << NREC) - 1u);
    {
      u32 ask = todo;
      while (cw_ld(W_FULL) < ALL && bud != 0u) {
        sv = gld(rws, ss, 0);
        const unsigned long long sb = __builtin_amdgcn_ballot_w64(tagx(sv, tag) != 0u);
        const u32 nw = ask & ~((u32)sb | (u32)(sb >> 32));
        for (u32 k = nw; k; k &= k - 1u) { const u32 i = __builtin_ctz(k); DMA(rws, lr + i * 512, i + 1 < NREC ? v1 : v0, so + i * 1024, 0, SC1); }
        ask &= ~nw;
        bud = bud < 16u ? 0u : bud - 16u;
        __builtin_amdgcn_s_sleep(CSLEEP);
      }
      todo = ask | 0x10000u;
    }
    read_w(L, wf, c, lane);
    STAMP(18);
    for (;;) {
      sv = gld(rws, ss, 0);
      for (u32 k = todo & 0xffffu; k; k &= k - 1u) { const u32 i = __builtin_ctz(k); DMA(rws, lr + i * 512, i + 1 < NREC ? v1 : v0, so + i * 1024, 0, SC1); }
      VMW(0);
      const unsigned long long sb = __builtin_amdgcn_ballot_w64(tagx(sv, tag) != 0u);
      if (todo & ~(((u32)sb | (u32)(sb >> 32)) & 0xffffu)) {
#pragma unroll
        for (int i = 0; i < NPR; ++i) ov[i] = ld128(L, lr + lane * 16 + i * 1024);
        u32 bad = 0;
#pragma unroll
        for (int i = 0; i < NPR; ++i) bad |= (tagx(ov[i], tag) != 0u) << (2 * i);
        if (allz(bad)) todo = 0u;
        else { bad |= __builtin_amdgcn_mov_dpp(bad, 0x128, 0xf, 0xf, true); bad |= __builtin_amdgcn_mov_dpp(bad, 0x124, 0xf, 0xf, true); bad |= __builtin_amdgcn_mov_dpp(bad, 0x122, 0xf, 0xf, true); bad |= __builtin_amdgcn_mov_dpp(bad, 0x121, 0xf, 0xf, true);
          todo = __builtin_amdgcn_readlane(bad, 0) | __builtin_amdgcn_readlane(bad, 16) | __builtin_amdgcn_readlane(bad, 32) | __builtin_amdgcn_readlane(bad, 48); }
      }
      if (todo == 0u && sb == 0ull) break;
      bud = bud < 16u ? 0u : bud - 16u;
      if (bud == 0u) break;
      asm volatile("" ::: "memory");
    }
    STAMP(19);
    const float ms = asf(sv[0]), w0 = ex2((ms - row_max(ms)) * CS) * asf(sv[2]), w = rn < NREC ? w0 : 0.f;
    f32x4 o[2] = {ZERO4, ZERO4};
    merge_rec<0>(o, ov, w);
    o[0] += o[1];
    const float rs = __builtin_amdgcn_rcpf(asf(__builtin_amdgcn_readfirstlane(asu(row_sum(w)))));
#pragma unroll
    for (int i = 0; i < 4; ++i) { auto t = __builtin_amdgcn_permlane32_swap(asu(o[0][i]), asu(o[0][i]), false, false); o[0][i] = (asf(t[0]) + asf(t[1])) * rs; }
    *(LAS u32x2*)(L + OB_OFF + c * OB_STR + (lane & 31) * 8) = u32x2{pk(o[0][0], o[0][1]), pk(o[0][2], o[0][3])};
    cw_add(W_PJ, 1, lane);
    project(L, wf, rws, yso, tag, c, lane, bud);
    STAMP(20);
  }
  STAMP_END();
}

__global__ void probe_kernel(const u32* d) {
  __shared__ float red[256]; __shared__ u32 t0s[256]; __shared__ float cnt[256];
  const unsigned long long st = __builtin_amdgcn_s_memrealtime();
  const int g = threadIdx.x;
  u32 t0 = d[g * 6 * 32 + 30];
  t0s[g] = t0; __syncthreads();
  for (int i = 0; i < 256; ++i) if ((int)(t0s[i] - t0) < 0) t0 = t0s[i];
  float v = 0, c = 0;
  for (int w = 0; w < 6; ++w) {
    if (0 >= 0 && w != 0) continue;
    if (2 >= 0 && ((g >> 3) & 3) != 2) continue;
    const u32* e = d + (g * 6 + w) * 32; const float f = (float)(e[29] - e[0]) / ((e[31] - e[30]) * 0.01f);
    if (e[3]) { v = fmaxf(v, (e[30] - t0) * 0.01f + (e[3] - e[0]) / f); c = 1; }
  }
  red[g] = v; cnt[g] = c; __syncthreads();
  float mx = 0, sum = 0, n = 0;
  for (int i = 0; i < 256; ++i) { mx = fmaxf(mx, red[i]); sum += red[i]; n += cnt[i]; }
  float t = sum / n;
  if (!(t >= 0.f && t < 200.f)) t = 200.f;
  const unsigned long long ticks = (unsigned long long)(t * 2000.f) + 5000ull;
  while (__builtin_amdgcn_s_memrealtime() - st < ticks) __builtin_amdgcn_s_sleep(1);
}
extern "C" void kernel_launch(void* const* d_in, const int* in_sizes, int n_in, void* d_out, int out_size, void* d_ws, size_t ws_size, hipStream_t stream) {
  Args a;
  for (int i = 0; i < 11; ++i) a.in[i] = d_in[i];
  a.out = d_out;
  a.ws = d_ws;
  mk_kernel<<<256, 384, 0, stream>>>(a);
  probe_kernel<<<1, 256, 0, stream>>>((const u32*)((char*)d_ws + WS_DIAG));
}
```

```cpp
#include <hip/hip_runtime.h>
#include <stdint.h>

#define LAS __attribute__((address_space(3)))
typedef unsigned u32;
typedef u32 u32x2 __attribute__((ext_vector_type(2)));
typedef u32 u32x4 __attribute__((ext_vector_type(4)));
typedef float f32x2 __attribute__((ext_vector_type(2)));
typedef float f32x4 __attribute__((ext_vector_type(4)));
typedef short s16x4 __attribute__((ext_vector_type(4)));
typedef __bf16 bf16x2 __attribute__((ext_vector_type(2)));
typedef __bf16 bf16x8 __attribute__((ext_vector_type(8)));
typedef __amdgpu_buffer_rsrc_t rsrc_t;

#define N0 37
#define N1 38
#define N2 40
constexpr int NSUM = N0 + N1 + N2, NU = 128 - NSUM, NREC = 3 + NU, NTICK = 4 * NU;
static_assert(NU >= 1 && NREC <= 16, "split");
#define LW_A 32
#define LW_B 28
#define HOLD_ST 1
#define POLL_A 9
#define POLL_A0 7
#define GATE1 8
#define GATE2 12
#define WTRIG 0
#define POLL_C (N1 + 2)
#define WMODE 3
#define OUT_AUX 16
#define CODE_PF 1
#define LW_W 44
#define FSLEEP 16
#define POLL_B GATE1
#define POLL_H GATE2
#define CSLEEP 2
#define AUX_K 2
#define AUX_V 2
#define AUX_C 2
#define AUX_W 2
static_assert(LW_A <= 32, "a ticket is read two items after it was drawn");

constexpr int SLOT = 18432, NS = 8, RING = SLOT * NS;
constexpr int CTL = RING;
constexpr int W_FULL = CTL, W_KF = CTL + 4, W_QA = CTL + 8, W_MRG = CTL + 12, W_HINT = CTL + 16, W_SGF = CTL + 20, W_OB = CTL + 24, W_BP = CTL + 28,
              W_FREE = CTL + 32, W_S = CTL + 64, W_GI = CTL + 80, W_QS = CTL + 84, W_PJ = CTL + 88, W_WT = CTL + 92, W_MF = CTL + 96, W_O2 = CTL + 100, W_TK = CTL + 128;
constexpr int ST_OFF = CTL + 256;
constexpr int OB_OFF = ST_OFF + 512, OB_STR = 576;
constexpr int SB_OFF = OB_OFF + 4 * OB_STR;
constexpr int QA_OFF = SB_OFF + 5120;
constexpr int MG_OFF = SB_OFF + 8192;
constexpr int MS_OFF = MG_OFF + 4096;
constexpr int LDS_BYTES = MS_OFF + 64;
static_assert(LDS_BYTES <= 163840 && (SB_OFF & 15) == 0 && (MG_OFF & 15) == 0, "lds");

constexpr u32 WS_EPOCH = 0, WS_HINT = 1024;
constexpr u32 WS_TK = 2048;
constexpr u32 WS_QG = 4096;
constexpr u32 WS_QAG = WS_QG + 2 * 4 * 2048 * 8;
constexpr u32 WS_AG = WS_QAG + 4 * 32 * 256 * 8;
constexpr u32 WS_AS = WS_AG + 64 * 16 * 1024;
constexpr u32 WS_SG = WS_AS + 64 * 2048;
constexpr u32 WS_CG = WS_SG + 4 * 16 * 32 * 16;
constexpr u32 WS_YG = WS_CG + 16 * 4 * 4 * 256 * 8;
constexpr u32 WS_PB = WS_YG + 2 * 32 * 18432;
constexpr u32 WS_END = WS_PB + 4 * 16 * 4 * 16 * 512;
constexpr u32 WS_DIAG = 8u << 20;
static_assert(WS_END <= WS_DIAG && (WS_PB & 127) == 0, "ws");

constexpr float CS = 0.08838834764831845f * 1.4426950408889634f;
constexpr int SC1 = 16;
constexpr u32 CODE_BYTES = 31744;
constexpr u32 SPIN_BUDGET = 1u << 19;

struct Args { const void* in[11]; void* out; void* ws; };

__device__ __forceinline__ rsrc_t mkrs(const void* p, u32 n) { return __builtin_amdgcn_make_buffer_rsrc((void*)p, 0, n, 0x00020000); }
constexpr __device__ __forceinline__ u32 swz(u32 r) { return (((r & 7u) ^ ((r >> 1) & 4u)) << 1) | ((r >> 3) & 1u); }
__device__ __forceinline__ f32x4 mfma(u32x4 a, u32x4 b, f32x4 c) {
  return __builtin_amdgcn_mfma_f32_16x16x32_bf16(__builtin_bit_cast(bf16x8, a), __builtin_bit_cast(bf16x8, b), c, 0, 0, 0);
}
__device__ __forceinline__ u32 pk(float lo, float hi) { f32x2 v = {lo, hi}; return __builtin_bit_cast(u32, __builtin_convertvector(v, bf16x2)); }
__device__ __forceinline__ float bflo(u32 v) { return __builtin_bit_cast(float, v << 16); }
__device__ __forceinline__ float bfhi(u32 v) { return __builtin_bit_cast(float, v & 0xffff0000u); }
__device__ __forceinline__ float asf(u32 v) { return __builtin_bit_cast(float, v); }
__device__ __forceinline__ u32 asu(float v) { return __builtin_bit_cast(u32, v); }
__device__ __forceinline__ float ex2(float x) { return __builtin_amdgcn_exp2f(x); }
__device__ __forceinline__ u32x4 ld128(LAS unsigned char* L, u32 a) { return *(LAS u32x4*)(L + a); }
__device__ __forceinline__ u32x4 ldtr2(LAS unsigned char* L, u32 a0, u32 a1) {
  const u32x2 x = __builtin_bit_cast(u32x2, __builtin_amdgcn_ds_read_tr16_b64_v4i16((LAS s16x4*)(L + a0)));
  const u32x2 y = __builtin_bit_cast(u32x2, __builtin_amdgcn_ds_read_tr16_b64_v4i16((LAS s16x4*)(L + a1)));
  return u32x4{x[0], x[1], y[0], y[1]};
}
__device__ __forceinline__ float xg_max(float x) {
  auto s = __builtin_amdgcn_permlane16_swap(asu(x), asu(x), false, false);
  x = fmaxf(asf(s[0]), asf(s[1]));
  auto t = __builtin_amdgcn_permlane32_swap(asu(x), asu(x), false, false);
  return fmaxf(asf(t[0]), asf(t[1]));
}
__device__ __forceinline__ float xg_sum(float x) {
  auto s = __builtin_amdgcn_permlane16_swap(asu(x), asu(x), false, false);
  x = asf(s[0]) + asf(s[1]);
  auto t = __builtin_amdgcn_permlane32_swap(asu(x), asu(x), false, false);
  return asf(t[0]) + asf(t[1]);
}
template <int CTRL> __device__ __forceinline__ float dpp(float x) { return asf(__builtin_amdgcn_mov_dpp(asu(x), CTRL, 0xf, 0xf, true)); }
__device__ __forceinline__ float row_sum(float x) { x += dpp<0x128>(x); x += dpp<0x124>(x); x += dpp<0x122>(x); x += dpp<0x121>(x); return x; }
__device__ __forceinline__ float row_max(float x) { x = fmaxf(x, dpp<0x128>(x)); x = fmaxf(x, dpp<0x124>(x)); x = fmaxf(x, dpp<0x122>(x)); x = fmaxf(x, dpp<0x121>(x)); return x; }

__device__ __forceinline__ u32 cw_ld(u32 a) { u32 v; asm volatile("ds_read_b32 %0, %1\n\ts_waitcnt lgkmcnt(0)" : "=v"(v) : "v"(a) : "memory"); return __builtin_amdgcn_readfirstlane(v); }
__device__ __forceinline__ void cw_st(u32 a, u32 v) { asm volatile("ds_write_b32 %0, %1" ::"v"(a), "v"(v) : "memory"); }
__device__ __forceinline__ void cw_full(u32 v) { asm volatile("ds_write2_b32 %0, %1, %1 offset1:1" ::"v"(W_FULL), "v"(v) : "memory"); }
__device__ __forceinline__ void cw_add(u32 a, u32 v, int lane) { if (lane == 0) asm volatile("ds_add_u32 %0, %1" ::"v"(a), "v"(v) : "memory"); asm volatile("" ::: "memory"); }
__device__ __forceinline__ u32 cw_add_rtn(u32 a, u32 v, int lane) {
  u32 r = 0;
  if (lane == 0) asm volatile("ds_add_rtn_u32 %0, %1, %2\n\ts_waitcnt lgkmcnt(0)" : "=v"(r) : "v"(a), "v"(v) : "memory");
  asm volatile("" ::: "memory");
  return __builtin_amdgcn_readfirstlane(r);
}
__device__ __forceinline__ u32 cw_wait_(u32 a, u32 want, u32& bud) {
  u32 t;
  bud = __builtin_amdgcn_readfirstlane(bud); want = __builtin_amdgcn_readfirstlane(want);
  asm volatile("1:\n\tds_read_b32 v255, %2\n\ts_waitcnt lgkmcnt(0)\n\tv_readfirstlane_b32 %0, v255\n\ts_cmp_ge_u32 %0, %3\n\ts_cbranch_scc1 2f\n\t"
               "s_sleep 1\n\ts_sub_u32 %1, %1, 1\n\ts_cbranch_scc0 1b\n\ts_mov_b32 %1, 0\n2:\n\ts_nop 1"
               : "=&s"(t), "+s"(bud) : "v"(a), "s"(want) : "memory", "scc", "v255");
  return t;
}
#define cw_wait(a, want) cw_wait_((a), (want), bud)
__device__ __forceinline__ bool glb_retry_(u32& bud) {
  bud = __builtin_amdgcn_readfirstlane(bud);
  asm volatile("s_sleep 1\n\ts_sub_u32 %0, %0, 16\n\ts_cselect_b32 %0, 0, %0" : "+s"(bud) :: "memory", "scc");
  return bud != 0u;
}
#define glb_retry() glb_retry_(bud)
__device__ __forceinline__ u32x4 gld(rsrc_t r, u32 voff, u32 soff) { return __builtin_amdgcn_raw_buffer_load_b128(r, voff, soff, SC1); }
__device__ __forceinline__ u32x4 gld0(rsrc_t r, u32 voff, u32 soff) { return __builtin_amdgcn_raw_buffer_load_b128(r, voff, soff, 0); }
__device__ __forceinline__ void gst(rsrc_t r, u32x4 v, u32 voff, u32 soff) { __builtin_amdgcn_raw_buffer_store_b128(v, r, voff + soff, 0, SC1); }
__device__ __forceinline__ u32 tagx(u32x4 v, u32 tag) { return (v[1] ^ tag) | (v[3] ^ tag); }
__device__ __forceinline__ bool allz(u32 x) { return __builtin_amdgcn_ballot_w64(x != 0u) == 0ull; }
template <int N> __device__ __forceinline__ void fetch(LAS unsigned char* L, rsrc_t r, u32 voff, u32 ss, u32 tag, u32 dst, u32 ds, u32 flag, u32& bud) {
  u32x4 v[N];
  for (;;) {
    u32 bad = 0;
#pragma unroll
    for (int i = 0; i < N; ++i) v[i] = gld(r, voff + i * ss, 0);
#pragma unroll
    for (int i = 0; i < N; ++i) bad |= tagx(v[i], tag);
    if (allz(bad) || !glb_retry()) break;
  }
#pragma unroll
  for (int i = 0; i < N; ++i) *(LAS u32x2*)(L + dst + i * ds) = u32x2{v[i][0], v[i][2]};
  cw_st(flag, 1u);
}
#define DMA(r, ldsoff, voff, soff, imm, aux) __builtin_amdgcn_raw_ptr_buffer_load_lds((r), (LAS void*)(L + (ldsoff)), 16, (voff), (soff), (imm), (aux))

#define STAMP(i) do {} while (0)
#define STAMP_BEGIN() do {} while (0)
#define STAMP_END() do {} while (0)

#define VMW(n) asm volatile("s_waitcnt vmcnt(" #n ")" ::: "memory")
#define ZERO4 f32x4{0.f, 0.f, 0.f, 0.f}
#define MAX8(a, b) fmaxf(fmaxf(fmaxf(a[0], a[1]), fmaxf(a[2], a[3])), fmaxf(fmaxf(b[0], b[1]), fmaxf(b[2], b[3])))
constexpr float LAZY = 192.f;
template <int NA> __device__ __forceinline__ u32x4 soft(const f32x4 s0, const f32x4 s1, float& m, float& l, f32x4* acc) {
  const float mx = MAX8(s0, s1);
  if (__builtin_amdgcn_ballot_w64(mx > m + LAZY) != 0ull) {
    const float mn = fmaxf(m, xg_max(mx)), al = ex2((m - mn) * CS);
    m = mn; l *= al;
#pragma unroll
    for (int i = 0; i < NA; ++i) acc[i] *= al;
  }
  const float mc = -m * CS;
  float pr[8];
#pragma unroll
  for (int i = 0; i < 4; ++i) { pr[i] = ex2(fmaf(s0[i], CS, mc)); pr[4 + i] = ex2(fmaf(s1[i], CS, mc)); }
  l += ((pr[0] + pr[1]) + (pr[2] + pr[3])) + ((pr[4] + pr[5]) + (pr[6] + pr[7]));
  return u32x4{pk(pr[0], pr[1]), pk(pr[2], pr[3]), pk(pr[4], pr[5]), pk(pr[6], pr[7])};
}
__device__ __forceinline__ u32x4 pick2(const f32x4 d0, const f32x4 d1, int q, u32 tag) {
  const float x0 = (q & 2) ? d1[0] : d0[0], x1 = (q & 2) ? d1[1] : d0[1], x2 = (q & 2) ? d1[2] : d0[2], x3 = (q & 2) ? d1[3] : d0[3];
  return u32x4{asu((q & 1) ? x2 : x0), tag, asu((q & 1) ? x3 : x1), tag};
}

constexpr int NPR = (NREC + 1) / 2;
template <int I> __device__ __forceinline__ void merge_rec(f32x4* o, const u32x4* t, float w) {
  if constexpr (I < NPR) { o[I & 1] += dpp<0x150 + 2 * I>(w) * f32x4{bflo(t[I][0]), bfhi(t[I][0]), bflo(t[I][2]), bfhi(t[I][2])}; merge_rec<I + 1>(o, t, w); }
}

constexpr int IT_UK = 0, IT_C = 4, IT_UV = 20, IT_W = 24;
constexpr u32 ALL = 1u << 20;
template <int N> __device__ __forceinline__ void vmw() { asm volatile("s_waitcnt vmcnt(%0)" ::"i"(N) : "memory"); }

__device__ __forceinline__ void read_w(LAS unsigned char* L, u32x4* wf, int c, int lane) {
  const u32 lp = (8 * (lane >> 4) + ((lane >> 2) & 3)) * 576 + 8 * (lane & 3) + 128 * c, t2 = c < 4 ? 64 : 0;
#pragma unroll
  for (int i = 0; i < 16; ++i) { const u32 ad = (i >> 2) * SLOT + lp + (i & 1) * 32 + ((i & 2) ? t2 : 0); wf[i] = ldtr2(L, ad, ad + 2304); }
}
__device__ __forceinline__ void project(LAS unsigned char* L, const u32x4* wf, rsrc_t rws, u32 so, u32 tag, int c, int lane, u32& bud) {
  const int g4 = lane >> 4, q = (lane >> 2) & 3, p = lane & 3;
  u32 ao = OB_OFF + p * OB_STR + g4 * 16, vo = so + c * 512 + p * 4608 + (q >> 1) * 128 + g4 * 32 + (q & 1) * 16;
  asm volatile("" : "+v"(ao), "+v"(vo));
  cw_wait(W_PJ, 4);
  f32x4 d[4] = {ZERO4, ZERO4, ZERO4, ZERO4};
  u32x4 of[4];
#pragma unroll
  for (int ks = 0; ks < 4; ++ks) of[ks] = ld128(L, ao + ks * 64);
  __builtin_amdgcn_sched_barrier(0);
#pragma unroll
  for (int ks = 0; ks < 4; ++ks) { d[0] = mfma(wf[ks * 4], of[ks], d[0]); d[1] = mfma(wf[ks * 4 + 1], of[ks], d[1]); }
#pragma unroll
  for (int ks = 0; ks < 4; ++ks) { d[2] = mfma(wf[ks * 4 + 2], of[ks], d[2]); d[3] = mfma(wf[ks * 4 + 3], of[ks], d[3]); }
  gst(rws, pick2(d[0], d[1], q, tag), vo, 0);
  if (c < 4) gst(rws, pick2(d[2], d[3], q, tag), vo + 256, 0);
}
__device__ __forceinline__ void final_sum(rsrc_t rws, void* out, int br, int hkv, int b, int i, u32 tag, int lane, u32& bud) {
  const int c0 = 36 * hkv + 8 * i + 2 * (lane >> 5);
  u32 vo = WS_YG + (br * 32 + (lane & 31)) * 18432 + (b * 576 + c0) * 8, oo = (b * 1152 + br * 576 + c0 + (lane & 1) * 4) * 2;
  const u32 v2 = i < 4 ? 32 : 0;
  const rsrc_t ro = mkrs(out, 4u * 1152u * 2u);
  asm volatile("" : "+v"(vo), "+v"(oo));
  u32x4 y, z;
  for (;;) { y = gld(rws, vo, 0); z = gld(rws, vo, v2); if (allz(tagx(y, tag) | tagx(z, tag)) || !glb_retry()) break; }
  float r[4] = {asf(y[0]), asf(y[2]), asf(z[0]), asf(z[2])};
#pragma unroll
  for (int k = 0; k < 4; ++k) { auto t = __builtin_amdgcn_permlane16_swap(asu(r[k]), asu(r[k]), false, false); r[k] = row_sum(asf(t[0]) + asf(t[1])); }
  if ((lane & 31) < (i < 4 ? 2 : 1)) __builtin_amdgcn_raw_buffer_store_b32((lane & 1) ? pk(r[2], r[3]) : pk(r[0], r[1]), ro, oo, 0, OUT_AUX);
}

__global__ void __launch_bounds__(384, 2) mk_kernel(Args a) {
  __shared__ __attribute__((aligned(16))) unsigned char smem[LDS_BYTES];
  LAS unsigned char* L = (LAS unsigned char*)smem;
  const int lane = threadIdx.x & 63, wave = __builtin_amdgcn_readfirstlane(threadIdx.x >> 6);
  const int g = blockIdx.x, gi = g >> 3, hkv = 2 * (g & 7) + (gi >> 4), j = gi & 15, b = j >> 2, s = j & 3;
  STAMP_BEGIN();
  if (wave == CODE_PF - 1) {
    unsigned long long pc; asm volatile("s_getpc_b64 %0" : "=s"(pc));
    DMA(mkrs((const void*)(pc & ~255ull), CODE_BYTES), MG_OFF, lane * 16, gi * 1024, 0, 0);
  }
  const u32 vu = (lane >> 5) * 8192 + (((lane & 31) ^ ((lane >> 5) << 1)) << 4);
  const u32 vw = ((lane >> 2) & 7) * 8192 + (lane & 3) * 16 + (lane >> 5) * 64;
  if (wave == 0) {
    if (s != 1) {
      const rsrc_t rw = mkrs(a.in[s == 0 ? 7 : s == 2 ? 3 : 8], s < 3 ? 576u * 8192u : 512u * 8192u);
      const u32 vo = s < 3 ? vw : vu, st = s < 3 ? 8 * 8192 : 2 * 8192, xm = s == 3 ? 0xf0u : 0u;
      u32 so = hkv * 512 + (s < 3 ? b * 128 : b * (128u * 8192u));
#pragma unroll
      for (int i = 0; i < 32; ++i, so += st) DMA(rw, i * 1024 + (i >= 16 && s == 3 ? 2048 : 0), vo ^ ((swz((2 * i) & 15) << 4) & xm), so, 0, AUX_W);
    }
    STAMP(14);
    cw_st(CTL + lane * 4, 0u);
    asm volatile("s_waitcnt lgkmcnt(0)" ::: "memory");
  }
  __builtin_amdgcn_s_barrier();
  const int ab = gi >> 3, ah = ((g & 7) + 2 * (gi & 4) + 3 * (ab + 4 * s)) & 15, as = s;
  const int ITU = 4 + (s == 0 ? N0 : s == 1 ? N1 : N2);
  const int bq = (g & 7) >> 1, sp = (g & 1) * 8 + (gi >> 2);
  u32 bud = SPIN_BUDGET;

  if (wave == 0) {
    __builtin_amdgcn_s_setprio(3);
    if ((s & 1) == 0) {
      const rsrc_t rw = mkrs(a.in[s ? 3 : 7], 576u * 8192u), rx = mkrs(a.in[s ? 0 : 5], 4u * 576u * 2u);
      u32 so = hkv * 512 + b * 128 + 32 * 65536;
#pragma unroll
      for (int i = 0; i < 5; ++i) DMA(rx, SB_OFF + i * 1024, lane * 16, i * 1024, 0, 0);
#pragma unroll 1
      for (int i = 32; i < 72; i += 4, so += 4 * 65536) {
        vmw<40>();
        DMA(rw, i * 1024, vw, so, 0, AUX_W); DMA(rw, i * 1024 + 1024, vw, so + 65536, 0, AUX_W); DMA(rw, i * 1024 + 2048, vw, so + 2 * 65536, 0, AUX_W); DMA(rw, i * 1024 + 3072, vw, so + 3 * 65536, 0, AUX_W);
      }
      VMW(36); cw_full(2);
    } else if (s == 3) {
      const rsrc_t r = mkrs(a.in[8], 512u * 8192u);
      u32 so = ((u32)b * 128u + 64u) * 8192u + hkv * 512u;
#pragma unroll 1
      for (int t = 2; t < 4; ++t) {
        if (t == 3) { VMW(32); cw_full(1); }
#pragma unroll
        for (int i = 0; i < 16; ++i, so += 2 * 8192) DMA(r, t * SLOT + i * 1024, vu ^ (swz((2 * i) & 15) << 4), so, 0, AUX_W);
      }
      VMW(32); cw_full(2);
    }
    u32 vq[4];
#pragma unroll
    for (int q = 0; q < 4; ++q) vq[q] = (lane >> 4) * 256 + (((lane & 15) ^ swz(4 * q + (lane >> 4))) << 4);
    int pub = 0, it = 4;
#define SLOT_WAIT() do { if (it >= NS) { const u32 need = 4u * (it >> 3), wa = W_FREE + (it & 7) * 4; \
      if (cw_ld(wa) < need) { VMW(16); if (it - 1 > pub) cw_full(it - 1); VMW(0); pub = it; cw_full(pub); cw_wait(wa, need); } } } while (0)
#define LSTAMP() do { if (it == 4) STAMP(2); if (it == 8) STAMP(3); if (it == 12) STAMP(4); if (it == 16) STAMP(5); if (it == 20) STAMP(6); if (it == 24) STAMP(7); if (it == 28) STAMP(8); if (it == 32) STAMP(9); if (it == 36) STAMP(10); if (it == 40) STAMP(11); if (it == 44) STAMP(12); } while (0)
#define PACE(LW, k) do { vmw<LW>(); if ((k) == ((LW / 4) & 3) && it - LW / 16 > pub) { pub = it - LW / 16; cw_full(pub); } } while (0)
#define LOAD_W(in_, LW) do { const rsrc_t r = mkrs(a.in[in_], 4096u * 1152u); u32 wo[9]; int ln = lane; asm volatile("" : "+v"(ln)); \
      _Pragma("unroll") for (int i = 0; i < 9; ++i) { const u32 ci = i * 64 + ln; wo[i] = ci * 16 + ((ci * 3641u) >> 17) * 576u; } \
      u32 sw = ((u32)hkv * 256u + (b & 1) * 128u) * 1152u + (b >> 1) * 576u; \
      _Pragma("unroll 1") for (int t = 0; t < 8; ++t, sw += 16u * 1152u) { _Pragma("unroll") for (int i = 0; i < 9; ++i) { if (i % 3 == 0) vmw<LW>(); DMA(r, t * 9216 + i * 1024, wo[i], sw, 0, AUX_W); } } } while (0)
    if (s == 1) LOAD_W(10, 32);
    asm volatile("" : "+s"(it));
    if (s < 3) {
      const rsrc_t rk = mkrs(a.in[1], 1u << 26), rv = mkrs(a.in[2], 1u << 26), rt = mkrs((const char*)a.ws + WS_TK + ah * 128, 4u);
      u32 so = ((u32)(ab * 16 + ah) * 128u + (s == 0 ? 0 : s == 1 ? N0 : N0 + N1)) * 8192u;
      asm volatile("v_mov_b32 v254, 1" ::: "v254");
      if (s == 0 && HOLD_ST) { VMW(0); pub = 4; cw_full(4); cw_wait(W_QS, 4); }
#pragma unroll 1
      for (;; ++it) {
        const u32 sb = (it & 7) * SLOT;
        SLOT_WAIT();
        LSTAMP();
        PACE(LW_A, 0);
        if (it >= ITU) {
          u32 t;
          asm volatile("v_readlane_b32 %0, v254, %1\n\tv_writelane_b32 v254, 1, %1\n\ts_nop 4" : "=&s"(t) : "s"(it & 3) : "memory", "v254");
          if (t >= (u32)NTICK) break;
          cw_st(W_TK + (it & 15) * 4, (it << 8) | t);
          so = (((t & 3u) * 16u + ah) * 128u + NSUM + (t >> 2)) * 8192u;
        }
        if (it >= ITU - 2) {
          unsigned long long ex;
          asm volatile("s_mov_b64 %0, exec\n\ts_lshl_b64 exec, 1, %2\n\tbuffer_atomic_add v254, off, %1, 0 sc0\n\ts_mov_b64 exec, %0"
                       : "=&s"(ex) : "s"(rt), "s"((it + 2) & 3) : "memory", "scc", "v254");
        }
#pragma unroll
        for (int k = 0; k < 2; ++k) {
          if (k) PACE(LW_A, 1);
          DMA(rk, sb + k * 4096, vq[0], so + k * 4096, 0, AUX_K); DMA(rk, sb + k * 4096, vq[1], so + k * 4096, 1024, AUX_K);
          DMA(rk, sb + k * 4096, vq[2], so + k * 4096, 2048, AUX_K); DMA(rk, sb + k * 4096, vq[3], so + k * 4096, 3072, AUX_K);
        }
#pragma unroll
        for (int k = 0; k < 2; ++k) {
          PACE(LW_A, k + 2);
          DMA(rv, sb + 8192 + k * 4096, vq[0], so + k * 4096, 0, AUX_V); DMA(rv, sb + 8192 + k * 4096, vq[1], so + k * 4096, 1024, AUX_V);
          DMA(rv, sb + 8192 + k * 4096, vq[2], so + k * 4096, 2048, AUX_V); DMA(rv, sb + 8192 + k * 4096, vq[3], so + k * 4096, 3072, AUX_V);
        }
        so += 8192u;
      }
      cw_st(W_TK + ((it + (lane & 3)) & 15) * 4, ((it + (lane & 3)) << 8) | 255u);
      STAMP(13);
    } else {
#pragma unroll 1
      for (; it < IT_W; ++it) {
        const u32 sb = (it & 7) * SLOT;
        if (it == GATE1 || it == GATE2) {
          VMW(16); if (it - 1 > pub) cw_full(it - 1);
          VMW(0); pub = it; cw_full(it);
          if (it == GATE1) cw_wait(W_QS, 5);
          if (it == GATE2) cw_wait(W_GI, 4);
        }
        SLOT_WAIT();
        LSTAMP();
        if (it < IT_UV) {
          const rsrc_t r = mkrs(a.in[6], 1u << 24);
          const u32 so = ((u32)bq * 4096u + sp * 256u + (it - IT_C) * 16u) * 1024u;
#pragma unroll
          for (int k = 0; k < 4; ++k) {
            PACE(LW_B, k);
            DMA(r, sb + k * 4096, (lane ^ swz(4 * k + 0)) << 4, so + k * 4096, 0, AUX_C);
            DMA(r, sb + k * 4096, (lane ^ swz(4 * k + 1)) << 4, so + k * 4096, 1024, AUX_C);
            DMA(r, sb + k * 4096, (lane ^ swz(4 * k + 2)) << 4, so + k * 4096, 2048, AUX_C);
            DMA(r, sb + k * 4096, (lane ^ swz(4 * k + 3)) << 4, so + k * 4096, 3072, AUX_C);
          }
        } else {
          const rsrc_t r = mkrs(a.in[9], 512u * 8192u);
          const u32 so = ((u32)b * 128u + (it & 3) * 32u) * 8192u + hkv * 512u;
#pragma unroll
          for (int i = 0; i < 16; ++i) { if ((i & 3) == 0) PACE(LW_B, i >> 2); DMA(r, sb + i * 1024, vu ^ (swz((2 * i) & 15) << 4), so + i * (2 * 8192), 0, AUX_W); }
        }
      }
      VMW(0); pub = it; cw_full(it);
      cw_wait(W_GI, 8);
      STAMP(15);
      LOAD_W(4, LW_W);
    }
    VMW(16); if (it - 1 > pub) cw_full(it - 1);
    VMW(8); cw_st(W_KF, ALL);
    VMW(0); cw_full(ALL);
    STAMP(1);
    STAMP_END();
    return;
  }

  const int c = wave - 1, g4 = lane >> 4, n = lane & 15, q = n >> 2, p = lane & 3;
  const rsrc_t rws = mkrs(a.ws, WS_END);
  const char* wsp = (const char*)a.ws; asm volatile("" : "+s"(wsp));
  const u32 tag = *(const __attribute__((address_space(4))) u32*)(wsp + WS_EPOCH) + 1u;
  const u32 yso = WS_YG + ((s == 1 ? 32 : 0) + hkv * 2 + (b & 1)) * 18432 + (b >> 1) * 2304;
  if (c == 4) {
    u32x4 wf[16];
    if (s == 1) {
      cw_wait(W_FULL, 4);
      read_w(L, wf, 4, lane);
      cw_st(W_MF, 1u);
    }
    cw_wait(W_FULL, s == 3 ? POLL_B : s == 0 ? POLL_A0 : POLL_A);
    STAMP(1);
    fetch<4>(L, rws, lane * 16 + WS_QG + (s < 3 ? ah * 128 : 8192 + hkv * 128) * 8, 16384, tag, (s < 3 ? QA_OFF : OB_OFF) + lane * 8, s < 3 ? 512 : OB_STR, W_QA, bud);
    STAMP(3);
    if (s == 3) {
      cw_wait(W_QS, 5); cw_wait(W_FULL, POLL_H);
      fetch<1>(L, rws, lane * 16 + WS_HINT, 0, tag, MG_OFF + lane * 8, 0, W_HINT, bud);
      STAMP(4);
      cw_wait(W_BP, 4);
      fetch<4>(L, rws, n * 32 + (g4 & 1) * 16 + WS_SG + hkv * 2048, 512, tag, MG_OFF + lane * 8, 512, W_SGF, bud);
      STAMP(2);
      for (int i = 0; i < 64; ++i) {
        if (__builtin_amdgcn_readfirstlane(__builtin_amdgcn_raw_buffer_load_b32(rws, WS_TK + hkv * 128, 0, SC1)) >= (u32)(WTRIG)) break;
        __builtin_amdgcn_s_sleep(4); asm volatile("" ::: "memory");
      }
      cw_st(W_WT, 1u);
      cw_wait(W_FULL, ALL);
      read_w(L, wf, 4, lane);
    } else if (s == 1) {
      cw_wait(W_FULL, POLL_C);
      const u32 so = WS_CG + hkv * 32768 + (b & 1) * 1024;
      for (;;) {
        const u32x4 t = gld(rws, q * 8192 + (p & 1) * 512, so);
        if (allz(tagx(t, tag)) || !glb_retry()) break;
        __builtin_amdgcn_s_sleep(16);
      }
      u32x4 cv[16];
      for (;;) {
        u32 bad = 0;
#pragma unroll
        for (int i = 0; i < 16; ++i) cv[i] = gld(rws, lane * 16 + (i & 1) * 2048, so + (i >> 1) * 4096);
#pragma unroll
        for (int i = 0; i < 16; ++i) bad |= tagx(cv[i], tag);
        if (allz(bad) || !glb_retry()) break;
      }
#pragma unroll
      for (int i = 0; i < 4; ++i)
        *(LAS u32*)(L + OB_OFF + i * OB_STR + lane * 4) = pk((asf(cv[i][0]) + asf(cv[4 + i][0])) + (asf(cv[8 + i][0]) + asf(cv[12 + i][0])), (asf(cv[i][2]) + asf(cv[4 + i][2])) + (asf(cv[8 + i][2]) + asf(cv[12 + i][2])));
      cw_st(W_PJ, 4u);
      STAMP(2);
    }
    if (s & 1) {
      project(L, wf, rws, yso, tag, 4, lane, bud);
    } else {
      cw_wait(W_O2, 4);
      __builtin_amdgcn_s_sleep(FSLEEP);
      final_sum(rws, a.out, s == 0, hkv, b, 4, tag, lane, bud);
      if (g == 16 && lane == 0) __builtin_amdgcn_raw_buffer_store_b32(tag, rws, 0, WS_EPOCH, SC1);
      STAMP(22);
    }
    STAMP_END();
    return;
  }

  if (s < 3) {
    u32x4 wf[16];
    if (s == 1) {
      cw_wait(W_FULL, 4);
      read_w(L, wf, c, lane);
      cw_wait(W_MF, 1);
      if (lane < 4) asm volatile("ds_add_u32 %0, %1" ::"v"(W_FREE + lane * 4), "v"(1u) : "memory");
    } else {
      const u32 aw = g4 * 1024 + (c >> 1) * 512 + q * 64 + (c & 1) * 32 + 8 * p, ax = SB_OFF + p * 1152 + g4 * 16;
      const u32 vo = (n * 2048 + 2 * g4) * 8, so = WS_QG + ((s ? 0 : 8192) + hkv * 128 + b * 32 + 8 * c) * 8;
      if (j == 2 && c == 0) { __builtin_amdgcn_raw_buffer_store_b32(0u, rws, WS_TK + hkv * 128, 0, SC1); VMW(0); }
      cw_wait(W_FULL, 2);
      u32x4 xr[18], wq[9];
#pragma unroll
      for (int ks = 0; ks < 18; ++ks) xr[ks] = ld128(L, ax + ks * 64);
      f32x4 a0 = ZERO4, a1 = ZERO4;
#pragma unroll
      for (int hf = 0; hf < 2; ++hf) {
        if (hf) { cw_wait(W_FULL, 4); STAMP(1); }
#pragma unroll
        for (int ks = 0; ks < 9; ++ks) wq[ks] = ldtr2(L, aw + (hf * 9 + ks) * 4096, aw + (hf * 9 + ks) * 4096 + 256);
        if (lane < 2) asm volatile("ds_add_u32 %0, %1" ::"v"(W_FREE + hf * 8 + lane * 4), "v"(1u) : "memory");
        __builtin_amdgcn_sched_barrier(0);
#pragma unroll
        for (int ks = 0; ks < 9; ++ks) { if (ks & 1) a1 = mfma(wq[ks], xr[hf * 9 + ks], a1); else a0 = mfma(wq[ks], xr[hf * 9 + ks], a0); }
        __builtin_amdgcn_sched_barrier(0);
      }
      a0 += a1;
      if (n < 4) gst(rws, u32x4{pk(a0[0], a0[1]), tag, pk(a0[2], a0[3]), tag}, vo, so);
      cw_add(W_QS, 1, lane);
      STAMP(2);
    }
    float m = -1e30f, l = 0.f;
    f32x4 acc[8];
#pragma unroll
    for (int i = 0; i < 8; ++i) acc[i] = ZERO4;
    u32x4 qf[4], kf[8], vf[8];
    const u32 lpK = n * 256 + ((g4 ^ swz(n)) << 4), lpQ = QA_OFF + (n & 1) * 256 + g4 * 16;
    const u32 lpV = (4 * g4 + q) * 256 + (((p >> 1) ^ swz(4 * g4 + q)) << 4) + 8 * (p & 1);
#define READ_K() _Pragma("unroll") for (int kk = 0; kk < 4; ++kk) { const u32 ak = (sb + lpK) ^ (kk << 6); kf[kk] = ld128(L, ak); kf[4 + kk] = ld128(L, ak + 4096); }
#define READ_V() do { _Pragma("unroll") for (int ct = 0; ct < 8; ++ct) { const u32 av = (sb + lpV) ^ (ct << 5); vf[ct] = ldtr2(L, av + 8192, av + 12288); } \
      cw_add(W_FREE + (it & 7) * 4, 4, lane); } while (0)
#define SCORES() f32x4 s0 = ZERO4, s1 = ZERO4; \
      _Pragma("unroll") for (int kk = 0; kk < 4; ++kk) { s0 = mfma(kf[kk], qf[kk], s0); s1 = mfma(kf[4 + kk], qf[kk], s1); }
#define PICK8(e) const f32x4 e##0 = (n & 2) ? acc[1] : acc[0], e##1 = (n & 2) ? acc[3] : acc[2], e##2 = (n & 2) ? acc[5] : acc[4], e##3 = (n & 2) ? acc[7] : acc[6], \
      e##4 = (n & 4) ? e##1 : e##0, e##5 = (n & 4) ? e##3 : e##2, e = (n & 8) ? e##5 : e##4
    const u32 vrec = (n & 1) * 512 + (n >> 1) * 64 + g4 * 16;
    bool first = true;
    int it = 4 + c;
#pragma unroll 1
    for (; it < ITU; it += 4) {
      const u32 sb = (it & 7) * SLOT;
      cw_wait(W_FULL, it + 1);
      READ_K(); READ_V();
      if (first) {
        first = false;
        cw_wait(W_QA, 1);
        STAMP(3);
#pragma unroll
        for (int kk = 0; kk < 4; ++kk) qf[kk] = ld128(L, lpQ + ab * 512 + kk * 64);
      }
      SCORES();
      const u32x4 pf = soft<8>(s0, s1, m, l, acc);
#pragma unroll
      for (int ct = 0; ct < 8; ++ct) acc[ct] = mfma(vf[ct], pf, acc[ct]);
    }
    STAMP(8);
    {
      l = xg_sum(l);
      PICK8(e);
      *(LAS f32x4*)(L + MG_OFF + c * 1024 + lane * 16) = e;
      if (lane < 2) *(LAS f32x2*)(L + MS_OFF + (c * 2 + lane) * 8) = f32x2{m, l};
      if (cw_add_rtn(W_MRG, 1, lane) == 3u) {
        f32x2 sv[4]; f32x4 ov[4];
#pragma unroll
        for (int i = 0; i < 4; ++i) { sv[i] = *(LAS f32x2*)(L + MS_OFF + (i * 2 + (n & 1)) * 8); ov[i] = *(LAS f32x4*)(L + MG_OFF + i * 1024 + lane * 16); }
        const float M = fmaxf(fmaxf(sv[0][0], sv[1][0]), fmaxf(sv[2][0], sv[3][0]));
        float Ls = 0.f; f32x4 o = ZERO4;
#pragma unroll
        for (int i = 0; i < 4; ++i) { const float w = ex2((sv[i][0] - M) * CS); Ls = fmaf(w, sv[i][1], Ls); o += w * ov[i]; }
        o *= __builtin_amdgcn_rcpf(Ls);
        gst(rws, u32x4{pk(o[0], o[1]), tag, pk(o[2], o[3]), tag}, vrec, WS_AG + ((ah * 4 + ab) * 16 + as) * 1024);
        if (lane < 2) gst(rws, u32x4{asu(M), tag, asu(Ls), tag}, lane * 16, WS_AS + (ah * 4 + ab) * 2048 + as * 128);
      }
    }
    STAMP(9);
#pragma unroll 1
    for (;; it += 4) {
      const u32 t = cw_wait(W_TK + (it & 15) * 4, it << 8) & 255u, sb = (it & 7) * SLOT;
      if (t >= (u32)NTICK || bud == 0u) break;
#pragma unroll
      for (int kk = 0; kk < 4; ++kk) qf[kk] = ld128(L, lpQ + (t & 3u) * 512 + kk * 64);
      const u32 pr = ah * 4 + (t & 3u), rec = 3 + (t >> 2), va = vrec + WS_AG + (pr * 16 + rec) * 1024, vs = lane * 16 + WS_AS + pr * 2048 + rec * 128;
      cw_wait(W_FULL, it + 1);
      READ_K(); READ_V();
      SCORES();
      m = xg_max(MAX8(s0, s1));
      const float mc = -m * CS;
#pragma unroll
      for (int i = 0; i < 4; ++i) { s0[i] = ex2(fmaf(s0[i], CS, mc)); s1[i] = ex2(fmaf(s1[i], CS, mc)); }
      const u32x4 pf = {pk(s0[0], s0[1]), pk(s0[2], s0[3]), pk(s1[0], s1[1]), pk(s1[2], s1[3])};
      f32x4 e0 = ZERO4, e1 = ZERO4;
#pragma unroll
      for (int ct = 0; ct < 8; ct += 2) { e0 = mfma(vf[ct], (n >> 1) == ct ? pf : u32x4{0u, 0u, 0u, 0u}, e0); e1 = mfma(vf[ct + 1], (n >> 1) == ct + 1 ? pf : u32x4{0u, 0u, 0u, 0u}, e1); }
      s0 += s1;
      l = xg_sum((s0[0] + s0[1]) + (s0[2] + s0[3]));
      e0 = (e0 + e1) * __builtin_amdgcn_rcpf(l);
      gst(rws, u32x4{pk(e0[0], e0[1]), tag, pk(e0[2], e0[3]), tag}, va, 0);
      if (lane < 2) gst(rws, u32x4{asu(m), tag, asu(l), tag}, vs, 0);
      STAMP(10);
    }
    if (s == 1) {
      STAMP(18);
      project(L, wf, rws, yso, tag, c, lane, bud);
      STAMP(20);
    } else {
      cw_add(W_O2, 1, lane);
      cw_wait(W_O2, 4);
      __builtin_amdgcn_s_sleep(FSLEEP);
      final_sum(rws, a.out, s == 0, hkv, b, c, tag, lane, bud);
      STAMP(22);
    }
    STAMP_END();
    return;
  }

  {
    cw_wait(W_FULL, IT_UK + c + 1);
    u32x4 wu[16];
    {
      const u32 lp = (IT_UK + c) * SLOT + n * 512 + ((g4 ^ swz(n)) << 4);
#pragma unroll
      for (int i = 0; i < 16; ++i) wu[i] = ld128(L, (lp ^ ((i & 3) << 6)) + ((i >> 2) & 1) * 8192 + (i >> 3) * 256);
    }
    cw_add(W_FREE + (IT_UK + c) * 4, 4, lane);
    STAMP(15);
    cw_wait(W_QA, 1);
    STAMP(3);
    f32x4 d[4] = {ZERO4, ZERO4, ZERO4, ZERO4};
#pragma unroll
    for (int i = 0; i < 8; ++i) {
      const u32x4 qv = ld128(L, OB_OFF + p * OB_STR + i * 64 + g4 * 16);
      d[(i >> 2) * 2] = mfma(wu[(i >> 2) * 8 + (i & 3)], qv, d[(i >> 2) * 2]); d[(i >> 2) * 2 + 1] = mfma(wu[(i >> 2) * 8 + 4 + (i & 3)], qv, d[(i >> 2) * 2 + 1]);
    }
    const f32x4 e0 = (q & 1) ? d[1] : d[0], e1 = (q & 1) ? d[3] : d[2], e = (q & 2) ? e1 : e0;
    gst(rws, u32x4{pk(e[0], e[1]), tag, pk(e[2], e[3]), tag}, p * 65536 + (g4 & 1) * 128 + (q >> 1) * 64 + (q & 1) * 32 + (g4 >> 1) * 16, WS_QAG + hkv * 4096 + (b * 4 + c) * 256);
    if (cw_add_rtn(W_QS, 1, lane) == 3u) { if (lane == 0) gst(rws, u32x4{tag, tag, tag, tag}, WS_HINT + (hkv * 4 + b) * 16, 0); cw_add(W_QS, 1, lane); }
    STAMP(4);
  }
  {
    const int ch = c & 1;
    u32x4 qa[8];
    {
      const u32 so = WS_QAG + bq * 65536 + (c >> 1) * 32768 + ch * 2048;
      cw_wait(W_HINT, 1);
      STAMP(5);
      u32x4 t[16];
#pragma unroll
      for (int i = 0; i < 16; ++i) t[i] = gld0(rws, (n >> 1) * 4096 + (n & 1) * 64 + g4 * 16 + i * 128, so);
      cw_add(W_GI, 1, lane);
      for (;;) {
        u32 bad = 0;
#pragma unroll
        for (int i = 0; i < 16; ++i) bad |= tagx(t[i], tag);
        if (allz(bad) || !glb_retry()) break;
#pragma unroll
        for (int i = 0; i < 16; ++i) t[i] = gld(rws, (n >> 1) * 4096 + (n & 1) * 64 + g4 * 16 + i * 128, so);
      }
#pragma unroll
      for (int i = 0; i < 8; ++i) qa[i] = u32x4{t[2 * i][0], t[2 * i][2], t[2 * i + 1][0], t[2 * i + 1][2]};
    }
    STAMP(6);
    float mb = -1e30f, lb = 0.f;
    f32x4 ob[16];
#pragma unroll
    for (int i = 0; i < 16; ++i) ob[i] = ZERO4;
    const u32 lpR = n * 1024 + ((g4 ^ swz(n)) << 4) + ch * 512;
    const u32 lpC = (4 * g4 + q) * 1024 + (((p >> 1) ^ swz(4 * g4 + q)) << 4) + 8 * (p & 1) + ch * 512;
#pragma unroll 1
    for (int st = 0; st < 8; ++st) {
      const int it = IT_C + 2 * st;
      const u32 sb = (it & 7) * SLOT;
      const u32 xw = ((st & 1) ? (4 + c) * SLOT + 16384 : SB_OFF + c * 2048) + lane * 16, xr = ((st & 1) ? (4 + (c ^ 1)) * SLOT + 16384 : SB_OFF + (c ^ 1) * 2048) + lane * 16;
      cw_wait(W_FULL, it + 2);
      u32x4 rf[16], cf[16];
#pragma unroll
      for (int kk = 0; kk < 8; ++kk) { const u32 ak = ((sb + lpR) ^ ((kk & 3) << 6)) + (kk >> 2) * 256; rf[2 * kk] = ld128(L, ak); rf[2 * kk + 1] = ld128(L, ak + SLOT); }
      __builtin_amdgcn_sched_barrier(0);
      f32x4 s0 = ZERO4, s1 = ZERO4;
#pragma unroll
      for (int kk = 0; kk < 8; ++kk) {
        s0 = mfma(rf[2 * kk], qa[kk], s0); s1 = mfma(rf[2 * kk + 1], qa[kk], s1);
        const u32 av = (sb + lpC) ^ (kk << 5);
        cf[kk] = ldtr2(L, av, av + SLOT); cf[8 + kk] = ldtr2(L, av + 256, av + 256 + SLOT);
        __builtin_amdgcn_sched_barrier(0);
      }
      *(LAS f32x4*)(L + xw) = s0;
      *(LAS f32x4*)(L + xw + 1024) = s1;
      cw_st(W_S + c * 4, st + 1);
      if (lane < 2) asm volatile("ds_add_u32 %0, %1" ::"v"(W_FREE + (it & 7) * 4 + lane * 4), "v"(1u) : "memory");
      cw_wait(W_S + (c ^ 1) * 4, st + 1);
      s0 += *(LAS f32x4*)(L + xr); s1 += *(LAS f32x4*)(L + xr + 1024);
      const u32x4 pf = soft<16>(s0, s1, mb, lb, ob);
#pragma unroll
      for (int ct = 0; ct < 16; ++ct) ob[ct] = mfma(cf[ct], pf, ob[ct]);
      if (st == 3) STAMP(16);
      if (st == 4) STAMP(17);
    }
    STAMP(7);
#pragma unroll
    for (int i = 0; i < 8; ++i) {
      auto x = __builtin_amdgcn_permlane16_swap(pk(ob[2 * i][0], ob[2 * i][1]), pk(ob[2 * i + 1][0], ob[2 * i + 1][1]), false, false);
      auto y = __builtin_amdgcn_permlane16_swap(pk(ob[2 * i][2], ob[2 * i][3]), pk(ob[2 * i + 1][2], ob[2 * i + 1][3]), false, false);
      gst(rws, u32x4{x[0], y[0], x[1], y[1]}, (n >> 1) * 512 + (n & 1) * 64 + (g4 & 1) * 32 + (g4 >> 1) * 16 + (i & 3) * 128,
          WS_PB + ((bq * 16 + sp) * 4 + ch * 2 + (i >> 2)) * 8192 + (c >> 1) * 4096);
    }
    lb = xg_sum(lb);
    if (g4 == 0 && ch == 0) *(LAS f32x2*)(L + ST_OFF + ((c >> 1) * 16 + n) * 8) = f32x2{mb, lb};
    VMW(0);
    if (cw_add_rtn(W_BP, 1, lane) == 3u) {
      const f32x2 ml = *(LAS f32x2*)(L + ST_OFF + (lane & 31) * 8);
      if (lane < 32) gst(rws, u32x4{asu(ml[0]), tag, asu(ml[1]), tag}, (lane >> 1) * 2048 + (lane & 1) * 16, WS_SG + (bq * 16 + sp) * 32);
      cw_add(W_BP, 1, lane);
    }
    STAMP(8);
  }
  {
    cw_wait(W_SGF, 1);
    STAMP(10);
    u32x4 pv[8];
#pragma unroll
    for (int i = 0; i < 8; ++i) pv[i] = gld(rws, (lane >> 5) * 32768 + (lane & 31) * 16, WS_PB + ((c * 16 + 2 * i) * 4 + b) * 8192 + hkv * 512);
    cw_add(W_GI, 1, lane);
    const f32x2 st = *(LAS f32x2*)(L + MG_OFF + c * 512 + lane * 8);
    const float w = ex2((st[0] - row_max(st[0])) * CS), cf = w / row_sum(w * st[1]);
    f32x4 o0 = ZERO4, o1 = ZERO4;
#pragma unroll
    for (int i = 0; i < 8; ++i) {
      const float f = asf(__builtin_amdgcn_ds_bpermute((((lane >> 2) & 1) * 16 + 2 * i + (lane >> 5)) * 4, asu(cf)));
      o0 += f * f32x4{bflo(pv[i][0]), bfhi(pv[i][0]), bflo(pv[i][1]), bfhi(pv[i][1])};
      o1 += f * f32x4{bflo(pv[i][2]), bfhi(pv[i][2]), bflo(pv[i][3]), bfhi(pv[i][3])};
    }
#pragma unroll
    for (int i = 0; i < 4; ++i) {
      auto t0 = __builtin_amdgcn_permlane32_swap(asu(o0[i]), asu(o0[i]), false, false); o0[i] = asf(t0[0]) + asf(t0[1]);
      auto t1 = __builtin_amdgcn_permlane32_swap(asu(o1[i]), asu(o1[i]), false, false); o1[i] = asf(t1[0]) + asf(t1[1]);
    }
    if (lane < 32) *(LAS u32x4*)(L + OB_OFF + c * OB_STR + ((lane >> 2) & 1) * 256 + (lane >> 3) * 64 + p * 16) = u32x4{pk(o0[0], o0[1]), pk(o0[2], o0[3]), pk(o1[0], o1[1]), pk(o1[2], o1[3])};
    cw_add(W_OB, 1, lane);
    cw_wait(W_FULL, IT_UV + 4);
    u32x4 wf[16];
    {
      const u32 r0 = 8 * (g4 & 1) + q;
      const u32 lp0 = (8 * g4 + q) * 512 + ((((p >> 1) + 8 * c) ^ swz(r0)) << 4) + 8 * (p & 1);
      const u32 lp1 = (8 * g4 + q + 4) * 512 + ((((p >> 1) + 8 * c) ^ swz(r0 + 4)) << 4) + 8 * (p & 1);
#pragma unroll
      for (int i = 0; i < 16; ++i) wf[i] = ldtr2(L, (lp0 ^ ((i & 3) << 5)) + ((IT_UV + (i >> 2)) & 7) * SLOT, (lp1 ^ ((i & 3) << 5)) + ((IT_UV + (i >> 2)) & 7) * SLOT);
    }
    cw_wait(W_OB, 4);
    STAMP(11);
    f32x4 d[4] = {ZERO4, ZERO4, ZERO4, ZERO4};
#pragma unroll
    for (int ks = 0; ks < 4; ++ks) {
      const u32x4 of = ld128(L, OB_OFF + p * OB_STR + (c >> 1) * 256 + ks * 64 + g4 * 16);
#pragma unroll
      for (int t = 0; t < 4; ++t) d[t] = mfma(wf[ks * 4 + t], of, d[t]);
    }
    {
      const u32 vo = p * 2048 + (q >> 1) * 128 + g4 * 32 + (q & 1) * 16, so = WS_CG + ((hkv * 4 + b) * 1024 + c * 64) * 8;
      gst(rws, pick2(d[0], d[1], q, tag), vo, so); gst(rws, pick2(d[2], d[3], q, tag), vo + 256, so);
    }
    STAMP(12);
  }
  {
    cw_add(W_O2, 1, lane);
    cw_wait(W_O2, 4);
    const int rn = n ^ (lane >> 5);
    const u32 so = WS_AG + (hkv * 4 + c) * 16384 + (b & 1) * 512, ss = WS_AS + (hkv * 4 + c) * 2048 + (b & 1) * 16 + (rn < NREC ? rn : 0) * 128;
    const u32 lr = (4 + c) * SLOT, v0 = (lane & 31) * 16, v1 = v0 + (lane >> 5) * 1024;
    u32x4 wf[16], sv, ov[NPR];
    u32 todo = 0x5555u & ((1u << NREC) - 1u);
    {
      u32 ask = todo;
      while (cw_ld(W_FULL) < ALL && bud != 0u) {
        sv = gld(rws, ss, 0);
        const unsigned long long sb = __builtin_amdgcn_ballot_w64(tagx(sv, tag) != 0u);
        const u32 nw = ask & ~((u32)sb | (u32)(sb >> 32));
        for (u32 k = nw; k; k &= k - 1u) { const u32 i = __builtin_ctz(k); DMA(rws, lr + i * 512, i + 1 < NREC ? v1 : v0, so + i * 1024, 0, SC1); }
        ask &= ~nw;
        bud = bud < 16u ? 0u : bud - 16u;
        __builtin_amdgcn_s_sleep(CSLEEP);
      }
      todo = ask | 0x10000u;
    }
    read_w(L, wf, c, lane);
    STAMP(18);
    for (;;) {
      sv = gld(rws, ss, 0);
      for (u32 k = todo & 0xffffu; k; k &= k - 1u) { const u32 i = __builtin_ctz(k); DMA(rws, lr + i * 512, i + 1 < NREC ? v1 : v0, so + i * 1024, 0, SC1); }
      VMW(0);
      const unsigned long long sb = __builtin_amdgcn_ballot_w64(tagx(sv, tag) != 0u);
      if (todo & ~(((u32)sb | (u32)(sb >> 32)) & 0xffffu)) {
#pragma unroll
        for (int i = 0; i < NPR; ++i) ov[i] = ld128(L, lr + lane * 16 + i * 1024);
        u32 bad = 0;
#pragma unroll
        for (int i = 0; i < NPR; ++i) bad |= (tagx(ov[i], tag) != 0u) << (2 * i);
        if (allz(bad)) todo = 0u;
        else { bad |= __builtin_amdgcn_mov_dpp(bad, 0x128, 0xf, 0xf, true); bad |= __builtin_amdgcn_mov_dpp(bad, 0x124, 0xf, 0xf, true); bad |= __builtin_amdgcn_mov_dpp(bad, 0x122, 0xf, 0xf, true); bad |= __builtin_amdgcn_mov_dpp(bad, 0x121, 0xf, 0xf, true);
          todo = __builtin_amdgcn_readlane(bad, 0) | __builtin_amdgcn_readlane(bad, 16) | __builtin_amdgcn_readlane(bad, 32) | __builtin_amdgcn_readlane(bad, 48); }
      }
      if (todo == 0u && sb == 0ull) break;
      bud = bud < 16u ? 0u : bud - 16u;
      if (bud == 0u) break;
      asm volatile("" ::: "memory");
    }
    STAMP(19);
    const float ms = asf(sv[0]), w0 = ex2((ms - row_max(ms)) * CS) * asf(sv[2]), w = rn < NREC ? w0 : 0.f;
    f32x4 o[2] = {ZERO4, ZERO4};
    merge_rec<0>(o, ov, w);
    o[0] += o[1];
    const float rs = __builtin_amdgcn_rcpf(asf(__builtin_amdgcn_readfirstlane(asu(row_sum(w)))));
#pragma unroll
    for (int i = 0; i < 4; ++i) { auto t = __builtin_amdgcn_permlane32_swap(asu(o[0][i]), asu(o[0][i]), false, false); o[0][i] = (asf(t[0]) + asf(t[1])) * rs; }
    *(LAS u32x2*)(L + OB_OFF + c * OB_STR + (lane & 31) * 8) = u32x2{pk(o[0][0], o[0][1]), pk(o[0][2], o[0][3])};
    cw_add(W_PJ, 1, lane);
    project(L, wf, rws, yso, tag, c, lane, bud);
    STAMP(20);
  }
  STAMP_END();
}

extern "C" void kernel_launch(void* const* d_in, const int* in_sizes, int n_in, void* d_out, int out_size, void* d_ws, size_t ws_size, hipStream_t stream) {
  Args a;
  for (int i = 0; i < 11; ++i) a.in[i] = d_in[i];
  a.out = d_out;
  a.ws = d_ws;
  mk_kernel<<<256, 384, 0, stream>>>(a);
}
```

```cpp
#include <hip/hip_runtime.h>
#include <stdint.h>

#define LAS __attribute__((address_space(3)))
typedef unsigned u32;
typedef u32 u32x2 __attribute__((ext_vector_type(2)));
typedef u32 u32x4 __attribute__((ext_vector_type(4)));
typedef float f32x2 __attribute__((ext_vector_type(2)));
typedef float f32x4 __attribute__((ext_vector_type(4)));
typedef short s16x4 __attribute__((ext_vector_type(4)));
typedef __bf16 bf16x2 __attribute__((ext_vector_type(2)));
typedef __bf16 bf16x8 __attribute__((ext_vector_type(8)));
typedef __amdgpu_buffer_rsrc_t rsrc_t;

#define N0 37
#define N1 38
#define N2 40
constexpr int NSUM = N0 + N1 + N2, NU = 128 - NSUM, NREC = 3 + NU, NTICK = 4 * NU;
static_assert(NU >= 1 && NREC <= 16, "split");
#define LW_A 32
#define LW_B 28
#define HOLD_ST 1
#define POLL_A 9
#define POLL_A0 7
#define GATE1 8
#define GATE2 12
#define POLL_C (N1 + 2)
#define OUT_AUX 16
#define CODE_PF 1
#define LW_W 44
#define FSLEEP 16
#define POLL_B GATE1
#define POLL_H GATE2
#define CSLEEP 2
#define EARLY 0
#define AUX_K 2
#define AUX_V 2
#define AUX_C 2
#define AUX_W 2
static_assert(LW_A <= 32, "a ticket is read two items after it was drawn");

constexpr int SLOT = 18432, NS = 8, RING = SLOT * NS;
constexpr int CTL = RING;
constexpr int W_FULL = CTL, W_WF = CTL + 4, W_QA = CTL + 8, W_MRG = CTL + 12, W_HINT = CTL + 16, W_SGF = CTL + 20, W_OB = CTL + 24, W_BP = CTL + 28,
              W_FREE = CTL + 32, W_S = CTL + 64, W_GI = CTL + 80, W_QS = CTL + 84, W_PJ = CTL + 88, W_WI = CTL + 92, W_MF = CTL + 96, W_O2 = CTL + 100, W_TK = CTL + 128;
constexpr int ST_OFF = CTL + 256;
constexpr int OB_OFF = ST_OFF + 512, OB_STR = 576;
constexpr int SB_OFF = OB_OFF + 4 * OB_STR;
constexpr int QA_OFF = SB_OFF + 5120;
constexpr int MG_OFF = SB_OFF + 8192;
constexpr int MS_OFF = MG_OFF + 4096;
constexpr int LDS_BYTES = MS_OFF + 64;
static_assert(LDS_BYTES <= 163840 && (SB_OFF & 15) == 0 && (MG_OFF & 15) == 0, "lds");

constexpr u32 WS_EPOCH = 0, WS_HINT = 1024;
constexpr u32 WS_TK = 2048;
constexpr u32 WS_QG = 4096;
constexpr u32 WS_QAG = WS_QG + 2 * 4 * 2048 * 8;
constexpr u32 WS_AG = WS_QAG + 4 * 32 * 256 * 8;
constexpr u32 WS_AS = WS_AG + 64 * 16 * 1024;
constexpr u32 WS_SG = WS_AS + 64 * 2048;
constexpr u32 WS_CG = WS_SG + 4 * 16 * 32 * 16;
constexpr u32 WS_YG = WS_CG + 16 * 4 * 4 * 256 * 8;
constexpr u32 WS_PB = WS_YG + 2 * 32 * 18432;
constexpr u32 WS_END = WS_PB + 4 * 16 * 4 * 16 * 512;
constexpr u32 WS_DIAG = 8u << 20;
static_assert(WS_END <= WS_DIAG && (WS_PB & 127) == 0, "ws");

constexpr float CS = 0.08838834764831845f * 1.4426950408889634f;
constexpr int SC1 = 16;
constexpr u32 CODE_BYTES = 25600;
constexpr u32 SPIN_BUDGET = 1u << 19;

struct Args { const void* in[11]; void* out; void* ws; };

__device__ __forceinline__ rsrc_t mkrs(const void* p, u32 n) { return __builtin_amdgcn_make_buffer_rsrc((void*)p, 0, n, 0x00020000); }
constexpr __device__ __forceinline__ u32 swz(u32 r) { return (((r & 7u) ^ ((r >> 1) & 4u)) << 1) | ((r >> 3) & 1u); }
__device__ __forceinline__ f32x4 mfma(u32x4 a, u32x4 b, f32x4 c) {
  return __builtin_amdgcn_mfma_f32_16x16x32_bf16(__builtin_bit_cast(bf16x8, a), __builtin_bit_cast(bf16x8, b), c, 0, 0, 0);
}
__device__ __forceinline__ u32 pk(float lo, float hi) { f32x2 v = {lo, hi}; return __builtin_bit_cast(u32, __builtin_convertvector(v, bf16x2)); }
__device__ __forceinline__ float bflo(u32 v) { return __builtin_bit_cast(float, v << 16); }
__device__ __forceinline__ float bfhi(u32 v) { return __builtin_bit_cast(float, v & 0xffff0000u); }
__device__ __forceinline__ float asf(u32 v) { return __builtin_bit_cast(float, v); }
__device__ __forceinline__ u32 asu(float v) { return __builtin_bit_cast(u32, v); }
__device__ __forceinline__ float ex2(float x) { return __builtin_amdgcn_exp2f(x); }
__device__ __forceinline__ u32x4 ld128(LAS unsigned char* L, u32 a) { return *(LAS u32x4*)(L + a); }
__device__ __forceinline__ u32x4 ldtr2(LAS unsigned char* L, u32 a0, u32 a1) {
  const u32x2 x = __builtin_bit_cast(u32x2, __builtin_amdgcn_ds_read_tr16_b64_v4i16((LAS s16x4*)(L + a0)));
  const u32x2 y = __builtin_bit_cast(u32x2, __builtin_amdgcn_ds_read_tr16_b64_v4i16((LAS s16x4*)(L + a1)));
  return u32x4{x[0], x[1], y[0], y[1]};
}
__device__ __forceinline__ float xg_max(float x) {
  auto s = __builtin_amdgcn_permlane16_swap(asu(x), asu(x), false, false);
  x = fmaxf(asf(s[0]), asf(s[1]));
  auto t = __builtin_amdgcn_permlane32_swap(asu(x), asu(x), false, false);
  return fmaxf(asf(t[0]), asf(t[1]));
}
__device__ __forceinline__ float xg_sum(float x) {
  auto s = __builtin_amdgcn_permlane16_swap(asu(x), asu(x), false, false);
  x = asf(s[0]) + asf(s[1]);
  auto t = __builtin_amdgcn_permlane32_swap(asu(x), asu(x), false, false);
  return asf(t[0]) + asf(t[1]);
}
template <int CTRL> __device__ __forceinline__ float dpp(float x) { return asf(__builtin_amdgcn_mov_dpp(asu(x), CTRL, 0xf, 0xf, true)); }
__device__ __forceinline__ float row_sum(float x) { x += dpp<0x128>(x); x += dpp<0x124>(x); x += dpp<0x122>(x); x += dpp<0x121>(x); return x; }
__device__ __forceinline__ float row_max(float x) { x = fmaxf(x, dpp<0x128>(x)); x = fmaxf(x, dpp<0x124>(x)); x = fmaxf(x, dpp<0x122>(x)); x = fmaxf(x, dpp<0x121>(x)); return x; }

__device__ __forceinline__ u32 cw_ld(u32 a) { u32 v; asm volatile("ds_read_b32 %0, %1\n\ts_waitcnt lgkmcnt(0)" : "=v"(v) : "v"(a) : "memory"); return __builtin_amdgcn_readfirstlane(v); }
__device__ __forceinline__ void cw_st(u32 a, u32 v) { asm volatile("ds_write_b32 %0, %1" ::"v"(a), "v"(v) : "memory"); }
#define cw_full(v) cw_st(W_FULL, (v))
__device__ __forceinline__ void cw_add(u32 a, u32 v, int lane) { if (lane == 0) asm volatile("ds_add_u32 %0, %1" ::"v"(a), "v"(v) : "memory"); asm volatile("" ::: "memory"); }
__device__ __forceinline__ u32 cw_add_rtn(u32 a, u32 v, int lane) {
  u32 r = 0;
  if (lane == 0) asm volatile("ds_add_rtn_u32 %0, %1, %2\n\ts_waitcnt lgkmcnt(0)" : "=v"(r) : "v"(a), "v"(v) : "memory");
  asm volatile("" ::: "memory");
  return __builtin_amdgcn_readfirstlane(r);
}
__device__ __forceinline__ u32 cw_wait_(u32 a, u32 want, u32& bud) {
  u32 t;
  bud = __builtin_amdgcn_readfirstlane(bud); want = __builtin_amdgcn_readfirstlane(want);
  asm volatile("1:\n\tds_read_b32 v255, %2\n\ts_waitcnt lgkmcnt(0)\n\tv_readfirstlane_b32 %0, v255\n\ts_cmp_ge_u32 %0, %3\n\ts_cbranch_scc1 2f\n\t"
               "s_sleep 1\n\ts_sub_u32 %1, %1, 1\n\ts_cbranch_scc0 1b\n\ts_mov_b32 %1, 0\n2:\n\ts_nop 1"
               : "=&s"(t), "+s"(bud) : "v"(a), "s"(want) : "memory", "scc", "v255");
  return t;
}
#define cw_wait(a, want) cw_wait_((a), (want), bud)
__device__ __forceinline__ bool glb_retry_(u32& bud) {
  bud = __builtin_amdgcn_readfirstlane(bud);
  asm volatile("s_sleep 1\n\ts_sub_u32 %0, %0, 16\n\ts_cselect_b32 %0, 0, %0" : "+s"(bud) :: "memory", "scc");
  return bud != 0u;
}
#define glb_retry() glb_retry_(bud)
__device__ __forceinline__ u32x4 gld(rsrc_t r, u32 voff, u32 soff) { return __builtin_amdgcn_raw_buffer_load_b128(r, voff, soff, SC1); }
__device__ __forceinline__ u32x4 gld0(rsrc_t r, u32 voff, u32 soff) { return __builtin_amdgcn_raw_buffer_load_b128(r, voff, soff, 0); }
__device__ __forceinline__ void gst(rsrc_t r, u32x4 v, u32 voff, u32 soff) { __builtin_amdgcn_raw_buffer_store_b128(v, r, voff + soff, 0, SC1); }
__device__ __forceinline__ u32 tagx(u32x4 v, u32 tag) { return (v[1] ^ tag) | (v[3] ^ tag); }
__device__ __forceinline__ bool allz(u32 x) { return __builtin_amdgcn_ballot_w64(x != 0u) == 0ull; }
template <int N> __device__ __forceinline__ void fetch(LAS unsigned char* L, rsrc_t r, u32 voff, u32 ss, u32 tag, u32 dst, u32 ds, u32 flag, u32& bud) {
  u32x4 v[N];
  for (;;) {
    u32 bad = 0;
#pragma unroll
    for (int i = 0; i < N; ++i) v[i] = gld(r, voff + i * ss, 0);
#pragma unroll
    for (int i = 0; i < N; ++i) bad |= tagx(v[i], tag);
    if (allz(bad) || !glb_retry()) break;
  }
#pragma unroll
  for (int i = 0; i < N; ++i) *(LAS u32x2*)(L + dst + i * ds) = u32x2{v[i][0], v[i][2]};
  cw_st(flag, 1u);
}
#define DMA(r, ldsoff, voff, soff, imm, aux) __builtin_amdgcn_raw_ptr_buffer_load_lds((r), (LAS void*)(L + (ldsoff)), 16, (voff), (soff), (imm), (aux))

#define STAMP(i) do {} while (0)
#define STAMP_BEGIN() do {} while (0)
#define STAMP_END() do {} while (0)

#define VMW(n) asm volatile("s_waitcnt vmcnt(" #n ")" ::: "memory")
#define ZERO4 f32x4{0.f, 0.f, 0.f, 0.f}
#define MAX8(a, b) fmaxf(fmaxf(fmaxf(a[0], a[1]), fmaxf(a[2], a[3])), fmaxf(fmaxf(b[0], b[1]), fmaxf(b[2], b[3])))
constexpr float LAZY = 192.f;
template <int NA> __device__ __forceinline__ u32x4 soft(const f32x4 s0, const f32x4 s1, float& m, float& l, f32x4* acc) {
  const float mx = MAX8(s0, s1);
  if (__builtin_amdgcn_ballot_w64(mx > m + LAZY) != 0ull) {
    const float mn = fmaxf(m, xg_max(mx)), al = ex2((m - mn) * CS);
    m = mn; l *= al;
#pragma unroll
    for (int i = 0; i < NA; ++i) acc[i] *= al;
  }
  const float mc = -m * CS;
  float pr[8];
#pragma unroll
  for (int i = 0; i < 4; ++i) { pr[i] = ex2(fmaf(s0[i], CS, mc)); pr[4 + i] = ex2(fmaf(s1[i], CS, mc)); }
  l += ((pr[0] + pr[1]) + (pr[2] + pr[3])) + ((pr[4] + pr[5]) + (pr[6] + pr[7]));
  return u32x4{pk(pr[0], pr[1]), pk(pr[2], pr[3]), pk(pr[4], pr[5]), pk(pr[6], pr[7])};
}
__device__ __forceinline__ u32x4 pick2(const f32x4 d0, const f32x4 d1, int q, u32 tag) {
  const float x0 = (q & 2) ? d1[0] : d0[0], x1 = (q & 2) ? d1[1] : d0[1], x2 = (q & 2) ? d1[2] : d0[2], x3 = (q & 2) ? d1[3] : d0[3];
  return u32x4{asu((q & 1) ? x2 : x0), tag, asu((q & 1) ? x3 : x1), tag};
}

constexpr int NPR = (NREC + 1) / 2;
template <int I> __device__ __forceinline__ void merge_rec(f32x4* o, const u32x4* t, float w) {
  if constexpr (I < NPR) { o[I & 1] += dpp<0x150 + 2 * I>(w) * f32x4{bflo(t[I][0]), bfhi(t[I][0]), bflo(t[I][2]), bfhi(t[I][2])}; merge_rec<I + 1>(o, t, w); }
}

constexpr int IT_UK = 0, IT_C = 4, IT_UV = 20, IT_W = 24;
constexpr u32 ALL = 1u << 20;
template <int N> __device__ __forceinline__ void vmw() { asm volatile("s_waitcnt vmcnt(%0)" ::"i"(N) : "memory"); }

__device__ __forceinline__ void read_w(LAS unsigned char* L, u32x4* wf, int c, int lane) {
  const u32 lp = (8 * (lane >> 4) + ((lane >> 2) & 3)) * 576 + 8 * (lane & 3) + 128 * c, t2 = c < 4 ? 64 : 0;
#pragma unroll
  for (int i = 0; i < 16; ++i) { const u32 ad = (i >> 2) * SLOT + lp + (i & 1) * 32 + ((i & 2) ? t2 : 0); wf[i] = ldtr2(L, ad, ad + 2304); }
}
__device__ __forceinline__ void project(LAS unsigned char* L, const u32x4* wf, rsrc_t rws, u32 so, u32 tag, int c, int lane, u32& bud) {
  const int g4 = lane >> 4, q = (lane >> 2) & 3, p = lane & 3;
  u32 ao = OB_OFF + p * OB_STR + g4 * 16, vo = so + c * 512 + p * 4608 + (q >> 1) * 128 + g4 * 32 + (q & 1) * 16;
  asm volatile("" : "+v"(ao), "+v"(vo));
  cw_wait(W_PJ, 4);
  f32x4 d[4] = {ZERO4, ZERO4, ZERO4, ZERO4};
  u32x4 of[4];
#pragma unroll
  for (int ks = 0; ks < 4; ++ks) of[ks] = ld128(L, ao + ks * 64);
  __builtin_amdgcn_sched_barrier(0);
#pragma unroll
  for (int ks = 0; ks < 4; ++ks) { d[0] = mfma(wf[ks * 4], of[ks], d[0]); d[1] = mfma(wf[ks * 4 + 1], of[ks], d[1]); }
#pragma unroll
  for (int ks = 0; ks < 4; ++ks) { d[2] = mfma(wf[ks * 4 + 2], of[ks], d[2]); d[3] = mfma(wf[ks * 4 + 3], of[ks], d[3]); }
  gst(rws, pick2(d[0], d[1], q, tag), vo, 0);
  if (c < 4) gst(rws, pick2(d[2], d[3], q, tag), vo + 256, 0);
}
__device__ __forceinline__ void final_sum(rsrc_t rws, void* out, int br, int hkv, int b, int i, u32 tag, int lane, u32& bud) {
  const int c0 = 36 * hkv + 8 * i + 2 * (lane >> 5);
  u32 vo = WS_YG + (br * 32 + (lane & 31)) * 18432 + (b * 576 + c0) * 8, oo = (b * 1152 + br * 576 + c0 + (lane & 1) * 4) * 2;
  const u32 v2 = i < 4 ? 32 : 0;
  const rsrc_t ro = mkrs(out, 4u * 1152u * 2u);
  asm volatile("" : "+v"(vo), "+v"(oo));
  u32x4 y, z;
  for (;;) { y = gld(rws, vo, 0); z = gld(rws, vo, v2); if (allz(tagx(y, tag) | tagx(z, tag)) || !glb_retry()) break; }
  float r[4] = {asf(y[0]), asf(y[2]), asf(z[0]), asf(z[2])};
#pragma unroll
  for (int k = 0; k < 4; ++k) { auto t = __builtin_amdgcn_permlane16_swap(asu(r[k]), asu(r[k]), false, false); r[k] = row_sum(asf(t[0]) + asf(t[1])); }
  if ((lane & 31) < (i < 4 ? 2 : 1)) __builtin_amdgcn_raw_buffer_store_b32((lane & 1) ? pk(r[2], r[3]) : pk(r[0], r[1]), ro, oo, 0, OUT_AUX);
}

__global__ void __launch_bounds__(384, 2) mk_kernel(Args a) {
  __shared__ __attribute__((aligned(16))) unsigned char smem[LDS_BYTES];
  LAS unsigned char* L = (LAS unsigned char*)smem;
  const int lane = threadIdx.x & 63, wave = __builtin_amdgcn_readfirstlane(threadIdx.x >> 6);
  const int g = blockIdx.x, gi = g >> 3, hkv = 2 * (g & 7) + (gi >> 4), j = gi & 15, b = j >> 2, s = j & 3;
  STAMP_BEGIN();
  if (wave == 1) {
    cw_st(CTL + lane * 4, 0u);
    unsigned long long pc; asm volatile("s_getpc_b64 %0" : "=s"(pc));
    DMA(mkrs((const void*)(pc & ~255ull), CODE_BYTES), MG_OFF, lane * 16, gi * 1024, 0, 0);
    asm volatile("s_waitcnt lgkmcnt(0)" ::: "memory");
  }
  __builtin_amdgcn_s_barrier();
  const u32 vu = (lane >> 5) * 8192 + (((lane & 31) ^ ((lane >> 5) << 1)) << 4);
#define LOAD_W(in_, LW) do { const rsrc_t r = mkrs(a.in[in_], 4096u * 1152u); u32 wo[9]; int ln = lane; asm volatile("" : "+v"(ln)); \
      _Pragma("unroll") for (int i = 0; i < 9; ++i) { const u32 ci = i * 64 + ln; wo[i] = ci * 16 + ((ci * 3641u) >> 17) * 576u; } \
      u32 sw = ((u32)hkv * 256u + (b & 1) * 128u) * 1152u + (b >> 1) * 576u; \
      _Pragma("unroll 1") for (int t = 0; t < 8; ++t, sw += 16u * 1152u) { _Pragma("unroll") for (int i = 0; i < 9; ++i) { if (i % 3 == 0) vmw<LW>(); DMA(r, t * 9216 + i * 1024, wo[i], sw, 0, AUX_W); } } } while (0)
  if (wave == 5) {
    if (s != 1) {
      const rsrc_t rw = mkrs(a.in[s == 0 ? 7 : s == 2 ? 3 : 8], s < 3 ? 576u * 8192u : 512u * 8192u), rx = mkrs(a.in[s ? 0 : 5], s < 3 ? 4u * 576u * 2u : 0u);
      const u32 vo = s < 3 ? ((lane >> 2) & 7) * 8192 + (lane & 3) * 16 + (lane >> 5) * 64 : vu, st = s < 3 ? 8 * 8192 : 2 * 8192, xm = s == 3 ? 0xf0u : 0u;
      u32 so = hkv * 512 + (s < 3 ? b * 128 : b * (128u * 8192u)), lo = 0;
#pragma unroll 1
      for (int t = 0; t < (s < 3 ? 9 : 8); ++t) {
        vmw<40>();
        if (t == 7) cw_st(W_WF, 1u);
#pragma unroll
        for (int i = 0; i < 8; ++i, so += st) DMA(rw, lo + i * 1024, vo ^ ((swz(2 * i) << 4) & xm), so, 0, AUX_W);
        lo += s == 3 && (t & 1) ? SLOT - 8192 : 8192;
        if (t == 1 && s < 3) {
#pragma unroll
          for (int i = 0; i < 5; ++i) DMA(rx, SB_OFF + i * 1024, lane * 16, i * 1024, 0, 0);
        }
      }
    } else {
      LOAD_W(10, 40);
    }
    STAMP(14);
    VMW(32); cw_st(W_WF, 2u); cw_st(W_WI, 1u);
    VMW(16); cw_st(W_WF, 3u);
    VMW(0); cw_st(W_WF, 4u);
  }
  const int ab = gi >> 3, ah = ((g & 7) + 2 * (gi & 4) + 3 * (ab + 4 * s)) & 15, as = s;
  const int ITU = 4 + (s == 0 ? N0 : s == 1 ? N1 : N2);
  const int bq = (g & 7) >> 1, sp = (g & 1) * 8 + (gi >> 2);
  u32 bud = SPIN_BUDGET;

  if (wave == 0) {
    __builtin_amdgcn_s_setprio(3);
    u32 vq[4];
#pragma unroll
    for (int q = 0; q < 4; ++q) vq[q] = (lane >> 4) * 256 + (((lane & 15) ^ swz(4 * q + (lane >> 4))) << 4);
    int pub = 0, it = 4;
#define SLOT_WAIT() do { if (it >= NS) { const u32 need = 4u * (it >> 3), wa = W_FREE + (it & 7) * 4; \
      if (cw_ld(wa) < need) { VMW(16); if (it - 1 > pub) cw_full(it - 1); VMW(0); pub = it; cw_full(pub); cw_wait(wa, need); } } } while (0)
#define LSTAMP() do { if (it == 4) STAMP(2); if (it == 8) STAMP(3); if (it == 12) STAMP(4); if (it == 16) STAMP(5); if (it == 20) STAMP(6); if (it == 24) STAMP(7); if (it == 28) STAMP(8); if (it == 32) STAMP(9); if (it == 36) STAMP(10); if (it == 40) STAMP(11); if (it == 44) STAMP(12); } while (0)
#define PACE(LW, k) do { vmw<LW>(); if ((k) == ((LW / 4) & 3) && it - LW / 16 > pub) { pub = it - LW / 16; cw_full(pub); } } while (0)
    asm volatile("" : "+s"(it));
    if (s < 3) {
      const rsrc_t rk = mkrs(a.in[1], 1u << 26), rv = mkrs(a.in[2], 1u << 26), rt = mkrs((const char*)a.ws + WS_TK + ah * 128, 4u);
      u32 so = ((u32)(ab * 16 + ah) * 128u + (s == 0 ? 0 : s == 1 ? N0 : N0 + N1)) * 8192u;
      asm volatile("v_mov_b32 v254, 1" ::: "v254");
#pragma unroll 1
      for (;; ++it) {
        const u32 sb = (it & 7) * SLOT;
        if (it == 4 + EARLY) { if (s == 0 && HOLD_ST) cw_wait(W_QS, 4); else cw_wait(W_WI, 1); }
        SLOT_WAIT();
        LSTAMP();
        PACE(LW_A, 0);
        if (it >= ITU) {
          u32 t;
          asm volatile("v_readlane_b32 %0, v254, %1\n\tv_writelane_b32 v254, 1, %1\n\ts_nop 4" : "=&s"(t) : "s"(it & 3) : "memory", "v254");
          if (t >= (u32)NTICK) break;
          cw_st(W_TK + (it & 15) * 4, (it << 8) | t);
          so = (((t & 3u) * 16u + ah) * 128u + NSUM + (t >> 2)) * 8192u;
        }
        if (it >= ITU - 2) {
          unsigned long long ex;
          asm volatile("s_mov_b64 %0, exec\n\ts_lshl_b64 exec, 1, %2\n\tbuffer_atomic_add v254, off, %1, 0 sc0\n\ts_mov_b64 exec, %0"
                       : "=&s"(ex) : "s"(rt), "s"((it + 2) & 3) : "memory", "scc", "v254");
        }
#pragma unroll
        for (int k = 0; k < 2; ++k) {
          if (k) PACE(LW_A, 1);
          DMA(rk, sb + k * 4096, vq[0], so + k * 4096, 0, AUX_K); DMA(rk, sb + k * 4096, vq[1], so + k * 4096, 1024, AUX_K);
          DMA(rk, sb + k * 4096, vq[2], so + k * 4096, 2048, AUX_K); DMA(rk, sb + k * 4096, vq[3], so + k * 4096, 3072, AUX_K);
        }
#pragma unroll
        for (int k = 0; k < 2; ++k) {
          PACE(LW_A, k + 2);
          DMA(rv, sb + 8192 + k * 4096, vq[0], so + k * 4096, 0, AUX_V); DMA(rv, sb + 8192 + k * 4096, vq[1], so + k * 4096, 1024, AUX_V);
          DMA(rv, sb + 8192 + k * 4096, vq[2], so + k * 4096, 2048, AUX_V); DMA(rv, sb + 8192 + k * 4096, vq[3], so + k * 4096, 3072, AUX_V);
        }
        so += 8192u;
      }
      cw_st(W_TK + ((it + (lane & 3)) & 15) * 4, ((it + (lane & 3)) << 8) | 255u);
      STAMP(13);
    } else {
#pragma unroll 1
      for (; it < IT_W; ++it) {
        const u32 sb = (it & 7) * SLOT;
        if (it == 4 + EARLY) cw_wait(W_WI, 1);
        if (it == GATE1 || it == GATE2) {
          VMW(16); if (it - 1 > pub) cw_full(it - 1);
          VMW(0); pub = it; cw_full(it);
          if (it == GATE1) cw_wait(W_QS, 5);
          if (it == GATE2) cw_wait(W_GI, 4);
        }
        SLOT_WAIT();
        LSTAMP();
        if (it < IT_UV) {
          const rsrc_t r = mkrs(a.in[6], 1u << 24);
          const u32 so = ((u32)bq * 4096u + sp * 256u + (it - IT_C) * 16u) * 1024u;
#pragma unroll
          for (int k = 0; k < 4; ++k) {
            PACE(LW_B, k);
            DMA(r, sb + k * 4096, (lane ^ swz(4 * k + 0)) << 4, so + k * 4096, 0, AUX_C);
            DMA(r, sb + k * 4096, (lane ^ swz(4 * k + 1)) << 4, so + k * 4096, 1024, AUX_C);
            DMA(r, sb + k * 4096, (lane ^ swz(4 * k + 2)) << 4, so + k * 4096, 2048, AUX_C);
            DMA(r, sb + k * 4096, (lane ^ swz(4 * k + 3)) << 4, so + k * 4096, 3072, AUX_C);
          }
        } else {
          const rsrc_t r = mkrs(a.in[9], 512u * 8192u);
          const u32 so = ((u32)b * 128u + (it & 3) * 32u) * 8192u + hkv * 512u;
#pragma unroll
          for (int i = 0; i < 16; ++i) { if ((i & 3) == 0) PACE(LW_B, i >> 2); DMA(r, sb + i * 1024, vu ^ (swz((2 * i) & 15) << 4), so + i * (2 * 8192), 0, AUX_W); }
        }
      }
      VMW(0); pub = it; cw_full(it);
      cw_wait(W_GI, 8);
      STAMP(15);
      LOAD_W(4, LW_W);
    }
    VMW(16); if (it - 1 > pub) cw_full(it - 1);
    VMW(0); cw_full(ALL);
    STAMP(1);
    STAMP_END();
    return;
  }

  const int c = wave - 1, g4 = lane >> 4, n = lane & 15, q = n >> 2, p = lane & 3;
  const rsrc_t rws = mkrs(a.ws, WS_END);
  const char* wsp = (const char*)a.ws; asm volatile("" : "+s"(wsp));
  const u32 tag = *(const __attribute__((address_space(4))) u32*)(wsp + WS_EPOCH) + 1u;
  asm volatile("" ::"s"(tag));
  VMW(0);
  const u32 yso = WS_YG + ((s == 1 ? 32 : 0) + hkv * 2 + (b & 1)) * 18432 + (b >> 1) * 2304;
  if (c == 4) {
    u32x4 wf[16];
    if (s == 1) {
      read_w(L, wf, 4, lane);
      cw_st(W_MF, 1u);
    }
    cw_wait(W_FULL, s == 3 ? POLL_B : s == 0 ? POLL_A0 : POLL_A);
    STAMP(1);
    fetch<4>(L, rws, lane * 16 + WS_QG + (s < 3 ? ah * 128 : 8192 + hkv * 128) * 8, 16384, tag, (s < 3 ? QA_OFF : OB_OFF) + lane * 8, s < 3 ? 512 : OB_STR, W_QA, bud);
    STAMP(3);
    if (s == 3) {
      cw_wait(W_QS, 5); cw_wait(W_FULL, POLL_H);
      fetch<1>(L, rws, lane * 16 + WS_HINT, 0, tag, MG_OFF + lane * 8, 0, W_HINT, bud);
      STAMP(4);
      cw_wait(W_BP, 4);
      fetch<4>(L, rws, n * 32 + (g4 & 1) * 16 + WS_SG + hkv * 2048, 512, tag, MG_OFF + lane * 8, 512, W_SGF, bud);
      STAMP(2);
      cw_wait(W_FULL, ALL);
      read_w(L, wf, 4, lane);
    } else if (s == 1) {
      cw_wait(W_FULL, POLL_C);
      const u32 so = WS_CG + hkv * 32768 + (b & 1) * 1024;
      for (;;) {
        const u32x4 t = gld(rws, q * 8192 + (p & 1) * 512, so);
        if (allz(tagx(t, tag)) || !glb_retry()) break;
        __builtin_amdgcn_s_sleep(16);
      }
      u32x4 cv[16];
      for (;;) {
        u32 bad = 0;
#pragma unroll
        for (int i = 0; i < 16; ++i) cv[i] = gld(rws, lane * 16 + (i & 1) * 2048, so + (i >> 1) * 4096);
#pragma unroll
        for (int i = 0; i < 16; ++i) bad |= tagx(cv[i], tag);
        if (allz(bad) || !glb_retry()) break;
      }
#pragma unroll
      for (int i = 0; i < 4; ++i)
        *(LAS u32*)(L + OB_OFF + i * OB_STR + lane * 4) = pk((asf(cv[i][0]) + asf(cv[4 + i][0])) + (asf(cv[8 + i][0]) + asf(cv[12 + i][0])), (asf(cv[i][2]) + asf(cv[4 + i][2])) + (asf(cv[8 + i][2]) + asf(cv[12 + i][2])));
      cw_st(W_PJ, 4u);
      STAMP(2);
    }
    if (s & 1) {
      project(L, wf, rws, yso, tag, 4, lane, bud);
    } else {
      cw_wait(W_O2, 4);
      __builtin_amdgcn_s_sleep(FSLEEP);
      final_sum(rws, a.out, s == 0, hkv, b, 4, tag, lane, bud);
      if (g == 16 && lane == 0) __builtin_amdgcn_raw_buffer_store_b32(tag, rws, 0, WS_EPOCH, SC1);
      STAMP(22);
    }
    STAMP_END();
    return;
  }

  if (s < 3) {
    u32x4 wf[16];
    if (s == 1) {
      cw_wait(W_WF, 4);
      read_w(L, wf, c, lane);
      cw_wait(W_MF, 1);
      if (lane < 4) asm volatile("ds_add_u32 %0, %1" ::"v"(W_FREE + lane * 4), "v"(1u) : "memory");
    } else {
      const u32 aw = g4 * 1024 + (c >> 1) * 512 + q * 64 + (c & 1) * 32 + 8 * p, ax = SB_OFF + p * 1152 + g4 * 16;
      const u32 vo = (n * 2048 + 2 * g4) * 8, so = WS_QG + ((s ? 0 : 8192) + hkv * 128 + b * 32 + 8 * c) * 8;
      if (j == 2 && c == 0) { __builtin_amdgcn_raw_buffer_store_b32(0u, rws, WS_TK + hkv * 128, 0, SC1); VMW(0); }
      cw_wait(W_WF, 2);
      u32x4 xr[18], wq[9];
#pragma unroll
      for (int ks = 0; ks < 18; ++ks) xr[ks] = ld128(L, ax + ks * 64);
      f32x4 a0 = ZERO4, a1 = ZERO4;
#pragma unroll
      for (int hf = 0; hf < 2; ++hf) {
        if (hf) { cw_wait(W_WF, 4); STAMP(1); }
#pragma unroll
        for (int ks = 0; ks < 9; ++ks) wq[ks] = ldtr2(L, aw + (hf * 9 + ks) * 4096, aw + (hf * 9 + ks) * 4096 + 256);
        if (lane < 2) asm volatile("ds_add_u32 %0, %1" ::"v"(W_FREE + hf * 8 + lane * 4), "v"(1u) : "memory");
        __builtin_amdgcn_sched_barrier(0);
#pragma unroll
        for (int ks = 0; ks < 9; ++ks) { if (ks & 1) a1 = mfma(wq[ks], xr[hf * 9 + ks], a1); else a0 = mfma(wq[ks], xr[hf * 9 + ks], a0); }
        __builtin_amdgcn_sched_barrier(0);
      }
      a0 += a1;
      if (n < 4) gst(rws, u32x4{pk(a0[0], a0[1]), tag, pk(a0[2], a0[3]), tag}, vo, so);
      cw_add(W_QS, 1, lane);
      STAMP(2);
    }
    float m = -1e30f, l = 0.f;
    f32x4 acc[8];
#pragma unroll
    for (int i = 0; i < 8; ++i) acc[i] = ZERO4;
    u32x4 qf[4], kf[8], vf[8];
    const u32 lpK = n * 256 + ((g4 ^ swz(n)) << 4), lpQ = QA_OFF + (n & 1) * 256 + g4 * 16;
    const u32 lpV = (4 * g4 + q) * 256 + (((p >> 1) ^ swz(4 * g4 + q)) << 4) + 8 * (p & 1);
#define READ_K() _Pragma("unroll") for (int kk = 0; kk < 4; ++kk) { const u32 ak = (sb + lpK) ^ (kk << 6); kf[kk] = ld128(L, ak); kf[4 + kk] = ld128(L, ak + 4096); }
#define READ_V() do { _Pragma("unroll") for (int ct = 0; ct < 8; ++ct) { const u32 av = (sb + lpV) ^ (ct << 5); vf[ct] = ldtr2(L, av + 8192, av + 12288); } \
      cw_add(W_FREE + (it & 7) * 4, 4, lane); } while (0)
#define SCORES() f32x4 s0 = ZERO4, s1 = ZERO4; \
      _Pragma("unroll") for (int kk = 0; kk < 4; ++kk) { s0 = mfma(kf[kk], qf[kk], s0); s1 = mfma(kf[4 + kk], qf[kk], s1); }
#define PICK8(e) const f32x4 e##0 = (n & 2) ? acc[1] : acc[0], e##1 = (n & 2) ? acc[3] : acc[2], e##2 = (n & 2) ? acc[5] : acc[4], e##3 = (n & 2) ? acc[7] : acc[6], \
      e##4 = (n & 4) ? e##1 : e##0, e##5 = (n & 4) ? e##3 : e##2, e = (n & 8) ? e##5 : e##4
    const u32 vrec = (n & 1) * 512 + (n >> 1) * 64 + g4 * 16;
    bool first = true;
    int it = 4 + c;
#pragma unroll 1
    for (; it < ITU; it += 4) {
      const u32 sb = (it & 7) * SLOT;
      cw_wait(W_FULL, it + 1);
      READ_K(); READ_V();
      if (first) {
        first = false;
        cw_wait(W_QA, 1);
        STAMP(3);
#pragma unroll
        for (int kk = 0; kk < 4; ++kk) qf[kk] = ld128(L, lpQ + ab * 512 + kk * 64);
      }
      SCORES();
      const u32x4 pf = soft<8>(s0, s1, m, l, acc);
#pragma unroll
      for (int ct = 0; ct < 8; ++ct) acc[ct] = mfma(vf[ct], pf, acc[ct]);
    }
    STAMP(8);
    {
      l = xg_sum(l);
      PICK8(e);
      *(LAS f32x4*)(L + MG_OFF + c * 1024 + lane * 16) = e;
      if (lane < 2) *(LAS f32x2*)(L + MS_OFF + (c * 2 + lane) * 8) = f32x2{m, l};
      if (cw_add_rtn(W_MRG, 1, lane) == 3u) {
        f32x2 sv[4]; f32x4 ov[4];
#pragma unroll
        for (int i = 0; i < 4; ++i) { sv[i] = *(LAS f32x2*)(L + MS_OFF + (i * 2 + (n & 1)) * 8); ov[i] = *(LAS f32x4*)(L + MG_OFF + i * 1024 + lane * 16); }
        const float M = fmaxf(fmaxf(sv[0][0], sv[1][0]), fmaxf(sv[2][0], sv[3][0]));
        float Ls = 0.f; f32x4 o = ZERO4;
#pragma unroll
        for (int i = 0; i < 4; ++i) { const float w = ex2((sv[i][0] - M) * CS); Ls = fmaf(w, sv[i][1], Ls); o += w * ov[i]; }
        o *= __builtin_amdgcn_rcpf(Ls);
        gst(rws, u32x4{pk(o[0], o[1]), tag, pk(o[2], o[3]), tag}, vrec, WS_AG + ((ah * 4 + ab) * 16 + as) * 1024);
        if (lane < 2) gst(rws, u32x4{asu(M), tag, asu(Ls), tag}, lane * 16, WS_AS + (ah * 4 + ab) * 2048 + as * 128);
      }
    }
    STAMP(9);
#pragma unroll 1
    for (;; it += 4) {
      const u32 t = cw_wait(W_TK + (it & 15) * 4, it << 8) & 255u, sb = (it & 7) * SLOT;
      if (t >= (u32)NTICK || bud == 0u) break;
#pragma unroll
      for (int kk = 0; kk < 4; ++kk) qf[kk] = ld128(L, lpQ + (t & 3u) * 512 + kk * 64);
      const u32 pr = ah * 4 + (t & 3u), rec = 3 + (t >> 2), va = vrec + WS_AG + (pr * 16 + rec) * 1024, vs = lane * 16 + WS_AS + pr * 2048 + rec * 128;
      cw_wait(W_FULL, it + 1);
      READ_K(); READ_V();
      SCORES();
      m = xg_max(MAX8(s0, s1));
      const float mc = -m * CS;
#pragma unroll
      for (int i = 0; i < 4; ++i) { s0[i] = ex2(fmaf(s0[i], CS, mc)); s1[i] = ex2(fmaf(s1[i], CS, mc)); }
      const u32x4 pf = {pk(s0[0], s0[1]), pk(s0[2], s0[3]), pk(s1[0], s1[1]), pk(s1[2], s1[3])};
      f32x4 e0 = ZERO4, e1 = ZERO4;
#pragma unroll
      for (int ct = 0; ct < 8; ct += 2) { e0 = mfma(vf[ct], (n >> 1) == ct ? pf : u32x4{0u, 0u, 0u, 0u}, e0); e1 = mfma(vf[ct + 1], (n >> 1) == ct + 1 ? pf : u32x4{0u, 0u, 0u, 0u}, e1); }
      s0 += s1;
      l = xg_sum((s0[0] + s0[1]) + (s0[2] + s0[3]));
      e0 = (e0 + e1) * __builtin_amdgcn_rcpf(l);
      gst(rws, u32x4{pk(e0[0], e0[1]), tag, pk(e0[2], e0[3]), tag}, va, 0);
      if (lane < 2) gst(rws, u32x4{asu(m), tag, asu(l), tag}, vs, 0);
      STAMP(10);
    }
    if (s == 1) {
      STAMP(18);
      project(L, wf, rws, yso, tag, c, lane, bud);
      STAMP(20);
    } else {
      cw_add(W_O2, 1, lane);
      cw_wait(W_O2, 4);
      __builtin_amdgcn_s_sleep(FSLEEP);
      final_sum(rws, a.out, s == 0, hkv, b, c, tag, lane, bud);
      STAMP(22);
    }
    STAMP_END();
    return;
  }

  {
    cw_wait(W_WF, c + 1);
    u32x4 wu[16];
    {
      const u32 lp = (IT_UK + c) * SLOT + n * 512 + ((g4 ^ swz(n)) << 4);
#pragma unroll
      for (int i = 0; i < 16; ++i) wu[i] = ld128(L, (lp ^ ((i & 3) << 6)) + ((i >> 2) & 1) * 8192 + (i >> 3) * 256);
    }
    cw_add(W_FREE + (IT_UK + c) * 4, 4, lane);
    STAMP(15);
    cw_wait(W_QA, 1);
    STAMP(3);
    f32x4 d[4] = {ZERO4, ZERO4, ZERO4, ZERO4};
#pragma unroll
    for (int i = 0; i < 8; ++i) {
      const u32x4 qv = ld128(L, OB_OFF + p * OB_STR + i * 64 + g4 * 16);
      d[(i >> 2) * 2] = mfma(wu[(i >> 2) * 8 + (i & 3)], qv, d[(i >> 2) * 2]); d[(i >> 2) * 2 + 1] = mfma(wu[(i >> 2) * 8 + 4 + (i & 3)], qv, d[(i >> 2) * 2 + 1]);
    }
    const f32x4 e0 = (q & 1) ? d[1] : d[0], e1 = (q & 1) ? d[3] : d[2], e = (q & 2) ? e1 : e0;
    gst(rws, u32x4{pk(e[0], e[1]), tag, pk(e[2], e[3]), tag}, p * 65536 + (g4 & 1) * 128 + (q >> 1) * 64 + (q & 1) * 32 + (g4 >> 1) * 16, WS_QAG + hkv * 4096 + (b * 4 + c) * 256);
    if (cw_add_rtn(W_QS, 1, lane) == 3u) { if (lane == 0) gst(rws, u32x4{tag, tag, tag, tag}, WS_HINT + (hkv * 4 + b) * 16, 0); cw_add(W_QS, 1, lane); }
    STAMP(4);
  }
  {
    const int ch = c & 1;
    u32x4 qa[8];
    {
      const u32 so = WS_QAG + bq * 65536 + (c >> 1) * 32768 + ch * 2048;
      cw_wait(W_HINT, 1);
      STAMP(5);
      u32x4 t[16];
#pragma unroll
      for (int i = 0; i < 16; ++i) t[i] = gld0(rws, (n >> 1) * 4096 + (n & 1) * 64 + g4 * 16 + i * 128, so);
      cw_add(W_GI, 1, lane);
      for (;;) {
        u32 bad = 0;
#pragma unroll
        for (int i = 0; i < 16; ++i) bad |= tagx(t[i], tag);
        if (allz(bad) || !glb_retry()) break;
#pragma unroll
        for (int i = 0; i < 16; ++i) t[i] = gld(rws, (n >> 1) * 4096 + (n & 1) * 64 + g4 * 16 + i * 128, so);
      }
#pragma unroll
      for (int i = 0; i < 8; ++i) qa[i] = u32x4{t[2 * i][0], t[2 * i][2], t[2 * i + 1][0], t[2 * i + 1][2]};
    }
    STAMP(6);
    float mb = -1e30f, lb = 0.f;
    f32x4 ob[16];
#pragma unroll
    for (int i = 0; i < 16; ++i) ob[i] = ZERO4;
    const u32 lpR = n * 1024 + ((g4 ^ swz(n)) << 4) + ch * 512;
    const u32 lpC = (4 * g4 + q) * 1024 + (((p >> 1) ^ swz(4 * g4 + q)) << 4) + 8 * (p & 1) + ch * 512;
#pragma unroll 1
    for (int st = 0; st < 8; ++st) {
      const int it = IT_C + 2 * st;
      const u32 sb = (it & 7) * SLOT;
      const u32 xw = ((st & 1) ? (4 + c) * SLOT + 16384 : SB_OFF + c * 2048) + lane * 16, xr = ((st & 1) ? (4 + (c ^ 1)) * SLOT + 16384 : SB_OFF + (c ^ 1) * 2048) + lane * 16;
      cw_wait(W_FULL, it + 2);
      u32x4 rf[16], cf[16];
#pragma unroll
      for (int kk = 0; kk < 8; ++kk) { const u32 ak = ((sb + lpR) ^ ((kk & 3) << 6)) + (kk >> 2) * 256; rf[2 * kk] = ld128(L, ak); rf[2 * kk + 1] = ld128(L, ak + SLOT); }
      __builtin_amdgcn_sched_barrier(0);
      f32x4 s0 = ZERO4, s1 = ZERO4;
#pragma unroll
      for (int kk = 0; kk < 8; ++kk) {
        s0 = mfma(rf[2 * kk], qa[kk], s0); s1 = mfma(rf[2 * kk + 1], qa[kk], s1);
        const u32 av = (sb + lpC) ^ (kk << 5);
        cf[kk] = ldtr2(L, av, av + SLOT); cf[8 + kk] = ldtr2(L, av + 256, av + 256 + SLOT);
        __builtin_amdgcn_sched_barrier(0);
      }
      *(LAS f32x4*)(L + xw) = s0;
      *(LAS f32x4*)(L + xw + 1024) = s1;
      cw_st(W_S + c * 4, st + 1);
      if (lane < 2) asm volatile("ds_add_u32 %0, %1" ::"v"(W_FREE + (it & 7) * 4 + lane * 4), "v"(1u) : "memory");
      cw_wait(W_S + (c ^ 1) * 4, st + 1);
      s0 += *(LAS f32x4*)(L + xr); s1 += *(LAS f32x4*)(L + xr + 1024);
      const u32x4 pf = soft<16>(s0, s1, mb, lb, ob);
#pragma unroll
      for (int ct = 0; ct < 16; ++ct) ob[ct] = mfma(cf[ct], pf, ob[ct]);
      if (st == 3) STAMP(16);
      if (st == 4) STAMP(17);
    }
    STAMP(7);
#pragma unroll
    for (int i = 0; i < 8; ++i) {
      auto x = __builtin_amdgcn_permlane16_swap(pk(ob[2 * i][0], ob[2 * i][1]), pk(ob[2 * i + 1][0], ob[2 * i + 1][1]), false, false);
      auto y = __builtin_amdgcn_permlane16_swap(pk(ob[2 * i][2], ob[2 * i][3]), pk(ob[2 * i + 1][2], ob[2 * i + 1][3]), false, false);
      gst(rws, u32x4{x[0], y[0], x[1], y[1]}, (n >> 1) * 512 + (n & 1) * 64 + (g4 & 1) * 32 + (g4 >> 1) * 16 + (i & 3) * 128,
          WS_PB + ((bq * 16 + sp) * 4 + ch * 2 + (i >> 2)) * 8192 + (c >> 1) * 4096);
    }
    lb = xg_sum(lb);
    if (g4 == 0 && ch == 0) *(LAS f32x2*)(L + ST_OFF + ((c >> 1) * 16 + n) * 8) = f32x2{mb, lb};
    VMW(0);
    if (cw_add_rtn(W_BP, 1, lane) == 3u) {
      const f32x2 ml = *(LAS f32x2*)(L + ST_OFF + (lane & 31) * 8);
      if (lane < 32) gst(rws, u32x4{asu(ml[0]), tag, asu(ml[1]), tag}, (lane >> 1) * 2048 + (lane & 1) * 16, WS_SG + (bq * 16 + sp) * 32);
    }
    STAMP(8);
  }
  {
    cw_wait(W_SGF, 1);
    STAMP(10);
    u32x4 pv[8];
#pragma unroll
    for (int i = 0; i < 8; ++i) pv[i] = gld(rws, (lane >> 5) * 32768 + (lane & 31) * 16, WS_PB + ((c * 16 + 2 * i) * 4 + b) * 8192 + hkv * 512);
    cw_add(W_GI, 1, lane);
    const f32x2 st = *(LAS f32x2*)(L + MG_OFF + c * 512 + lane * 8);
    const float w = ex2((st[0] - row_max(st[0])) * CS), cf = w / row_sum(w * st[1]);
    f32x4 o0 = ZERO4, o1 = ZERO4;
#pragma unroll
    for (int i = 0; i < 8; ++i) {
      const float f = asf(__builtin_amdgcn_ds_bpermute((((lane >> 2) & 1) * 16 + 2 * i + (lane >> 5)) * 4, asu(cf)));
      o0 += f * f32x4{bflo(pv[i][0]), bfhi(pv[i][0]), bflo(pv[i][1]), bfhi(pv[i][1])};
      o1 += f * f32x4{bflo(pv[i][2]), bfhi(pv[i][2]), bflo(pv[i][3]), bfhi(pv[i][3])};
    }
#pragma unroll
    for (int i = 0; i < 4; ++i) {
      auto t0 = __builtin_amdgcn_permlane32_swap(asu(o0[i]), asu(o0[i]), false, false); o0[i] = asf(t0[0]) + asf(t0[1]);
      auto t1 = __builtin_amdgcn_permlane32_swap(asu(o1[i]), asu(o1[i]), false, false); o1[i] = asf(t1[0]) + asf(t1[1]);
    }
    if (lane < 32) *(LAS u32x4*)(L + OB_OFF + c * OB_STR + ((lane >> 2) & 1) * 256 + (lane >> 3) * 64 + p * 16) = u32x4{pk(o0[0], o0[1]), pk(o0[2], o0[3]), pk(o1[0], o1[1]), pk(o1[2], o1[3])};
    cw_add(W_OB, 1, lane);
    cw_wait(W_FULL, IT_UV + 4);
    u32x4 wf[16];
    {
      const u32 r0 = 8 * (g4 & 1) + q;
      const u32 lp0 = (8 * g4 + q) * 512 + ((((p >> 1) + 8 * c) ^ swz(r0)) << 4) + 8 * (p & 1);
      const u32 lp1 = (8 * g4 + q + 4) * 512 + ((((p >> 1) + 8 * c) ^ swz(r0 + 4)) << 4) + 8 * (p & 1);
#pragma unroll
      for (int i = 0; i < 16; ++i) wf[i] = ldtr2(L, (lp0 ^ ((i & 3) << 5)) + ((IT_UV + (i >> 2)) & 7) * SLOT, (lp1 ^ ((i & 3) << 5)) + ((IT_UV + (i >> 2)) & 7) * SLOT);
    }
    cw_wait(W_OB, 4);
    STAMP(11);
    f32x4 d[4] = {ZERO4, ZERO4, ZERO4, ZERO4};
#pragma unroll
    for (int ks = 0; ks < 4; ++ks) {
      const u32x4 of = ld128(L, OB_OFF + p * OB_STR + (c >> 1) * 256 + ks * 64 + g4 * 16);
#pragma unroll
      for (int t = 0; t < 4; ++t) d[t] = mfma(wf[ks * 4 + t], of, d[t]);
    }
    {
      const u32 vo = p * 2048 + (q >> 1) * 128 + g4 * 32 + (q & 1) * 16, so = WS_CG + ((hkv * 4 + b) * 1024 + c * 64) * 8;
      gst(rws, pick2(d[0], d[1], q, tag), vo, so); gst(rws, pick2(d[2], d[3], q, tag), vo + 256, so);
    }
    STAMP(12);
  }
  {
    cw_add(W_O2, 1, lane);
    cw_wait(W_O2, 4);
    const int rn = n ^ (lane >> 5);
    const u32 so = WS_AG + (hkv * 4 + c) * 16384 + (b & 1) * 512, ss = WS_AS + (hkv * 4 + c) * 2048 + (b & 1) * 16 + (rn < NREC ? rn : 0) * 128;
    const u32 lr = (4 + c) * SLOT, v0 = (lane & 31) * 16, v1 = v0 + (lane >> 5) * 1024;
    u32x4 wf[16], sv, ov[NPR];
    u32 todo = 0x5555u & ((1u << NREC) - 1u);
    {
      u32 ask = todo;
      while (cw_ld(W_FULL) < ALL && bud != 0u) {
        sv = gld(rws, ss, 0);
        const unsigned long long sb = __builtin_amdgcn_ballot_w64(tagx(sv, tag) != 0u);
        const u32 nw = ask & ~((u32)sb | (u32)(sb >> 32));
        for (u32 k = nw; k; k &= k - 1u) { const u32 i = __builtin_ctz(k); DMA(rws, lr + i * 512, i + 1 < NREC ? v1 : v0, so + i * 1024, 0, SC1); }
        ask &= ~nw;
        bud = bud < 16u ? 0u : bud - 16u;
        __builtin_amdgcn_s_sleep(CSLEEP);
      }
      todo = ask | 0x10000u;
    }
    read_w(L, wf, c, lane);
    STAMP(18);
    for (;;) {
      sv = gld(rws, ss, 0);
      for (u32 k = todo & 0xffffu; k; k &= k - 1u) { const u32 i = __builtin_ctz(k); DMA(rws, lr + i * 512, i + 1 < NREC ? v1 : v0, so + i * 1024, 0, SC1); }
      VMW(0);
      const unsigned long long sb = __builtin_amdgcn_ballot_w64(tagx(sv, tag) != 0u);
      if (todo & ~(((u32)sb | (u32)(sb >> 32)) & 0xffffu)) {
#pragma unroll
        for (int i = 0; i < NPR; ++i) ov[i] = ld128(L, lr + lane * 16 + i * 1024);
        u32 bad = 0;
#pragma unroll
        for (int i = 0; i < NPR; ++i) bad |= (tagx(ov[i], tag) != 0u) << (2 * i);
        if (allz(bad)) todo = 0u;
        else { bad |= __builtin_amdgcn_mov_dpp(bad, 0x128, 0xf, 0xf, true); bad |= __builtin_amdgcn_mov_dpp(bad, 0x124, 0xf, 0xf, true); bad |= __builtin_amdgcn_mov_dpp(bad, 0x122, 0xf, 0xf, true); bad |= __builtin_amdgcn_mov_dpp(bad, 0x121, 0xf, 0xf, true);
          todo = __builtin_amdgcn_readlane(bad, 0) | __builtin_amdgcn_readlane(bad, 16) | __builtin_amdgcn_readlane(bad, 32) | __builtin_amdgcn_readlane(bad, 48); }
      }
      if (todo == 0u && sb == 0ull) break;
      bud = bud < 16u ? 0u : bud - 16u;
      if (bud == 0u) break;
      asm volatile("" ::: "memory");
    }
    STAMP(19);
    const float ms = asf(sv[0]), w0 = ex2((ms - row_max(ms)) * CS) * asf(sv[2]), w = rn < NREC ? w0 : 0.f;
    f32x4 o[2] = {ZERO4, ZERO4};
    merge_rec<0>(o, ov, w);
    o[0] += o[1];
    const float rs = __builtin_amdgcn_rcpf(asf(__builtin_amdgcn_readfirstlane(asu(row_sum(w)))));
#pragma unroll
    for (int i = 0; i < 4; ++i) { auto t = __builtin_amdgcn_permlane32_swap(asu(o[0][i]), asu(o[0][i]), false, false); o[0][i] = (asf(t[0]) + asf(t[1])) * rs; }
    *(LAS u32x2*)(L + OB_OFF + c * OB_STR + (lane & 31) * 8) = u32x2{pk(o[0][0], o[0][1]), pk(o[0][2], o[0][3])};
    cw_add(W_PJ, 1, lane);
    project(L, wf, rws, yso, tag, c, lane, bud);
    STAMP(20);
  }
  STAMP_END();
}

extern "C" void kernel_launch(void* const* d_in, const int* in_sizes, int n_in, void* d_out, int out_size, void* d_ws, size_t ws_size, hipStream_t stream) {
  Args a;
  for (int i = 0; i < 11; ++i) a.in[i] = d_in[i];
  a.out = d_out;
  a.ws = d_ws;
  mk_kernel<<<256, 384, 0, stream>>>(a);
}
```

```cpp
#define DIAG 1
#include <hip/hip_runtime.h>
#include <stdint.h>

#define LAS __attribute__((address_space(3)))
typedef unsigned u32;
typedef u32 u32x2 __attribute__((ext_vector_type(2)));
typedef u32 u32x4 __attribute__((ext_vector_type(4)));
typedef float f32x2 __attribute__((ext_vector_type(2)));
typedef float f32x4 __attribute__((ext_vector_type(4)));
typedef short s16x4 __attribute__((ext_vector_type(4)));
typedef __bf16 bf16x2 __attribute__((ext_vector_type(2)));
typedef __bf16 bf16x8 __attribute__((ext_vector_type(8)));
typedef __amdgpu_buffer_rsrc_t rsrc_t;

#define N0 37
#define N1 38
#define N2 40
constexpr int NSUM = N0 + N1 + N2, NU = 128 - NSUM, NREC = 3 + NU, NTICK = 4 * NU;
static_assert(NU >= 1 && NREC <= 16, "split");
#define LW_A 32
#define LW_B 28
#define HOLD_ST 1
#define POLL_A 9
#define POLL_A0 7
#define GATE1 8
#define GATE2 12
#define POLL_C (N1 + 2)
#define OUT_AUX 16
#define CODE_PF 1
#define LW_W 44
#define FSLEEP 16
#define POLL_B GATE1
#define POLL_H GATE2
#define CSLEEP 2
#define EARLY 1
#define AUX_K 2
#define AUX_V 2
#define AUX_C 2
#define AUX_W 2
static_assert(LW_A <= 32, "a ticket is read two items after it was drawn");

constexpr int SLOT = 18432, NS = 8, RING = SLOT * NS;
constexpr int CTL = RING;
constexpr int W_FULL = CTL, W_WF = CTL + 4, W_QA = CTL + 8, W_MRG = CTL + 12, W_HINT = CTL + 16, W_SGF = CTL + 20, W_OB = CTL + 24, W_BP = CTL + 28,
              W_FREE = CTL + 32, W_S = CTL + 64, W_GI = CTL + 80, W_QS = CTL + 84, W_PJ = CTL + 88, W_WI = CTL + 92, W_MF = CTL + 96, W_O2 = CTL + 100, W_TK = CTL + 128;
constexpr int ST_OFF = CTL + 256;
constexpr int OB_OFF = ST_OFF + 512, OB_STR = 576;
constexpr int SB_OFF = OB_OFF + 4 * OB_STR;
constexpr int QA_OFF = SB_OFF + 5120;
constexpr int MG_OFF = SB_OFF + 8192;
constexpr int MS_OFF = MG_OFF + 4096;
constexpr int LDS_BYTES = MS_OFF + 64;
static_assert(LDS_BYTES <= 163840 && (SB_OFF & 15) == 0 && (MG_OFF & 15) == 0, "lds");

constexpr u32 WS_EPOCH = 0, WS_HINT = 1024;
constexpr u32 WS_TK = 2048;
constexpr u32 WS_QG = 4096;
constexpr u32 WS_QAG = WS_QG + 2 * 4 * 2048 * 8;
constexpr u32 WS_AG = WS_QAG + 4 * 32 * 256 * 8;
constexpr u32 WS_AS = WS_AG + 64 * 16 * 1024;
constexpr u32 WS_SG = WS_AS + 64 * 2048;
constexpr u32 WS_CG = WS_SG + 4 * 16 * 32 * 16;
constexpr u32 WS_YG = WS_CG + 16 * 4 * 4 * 256 * 8;
constexpr u32 WS_PB = WS_YG + 2 * 32 * 18432;
constexpr u32 WS_END = WS_PB + 4 * 16 * 4 * 16 * 512;
constexpr u32 WS_DIAG = 8u << 20;
static_assert(WS_END <= WS_DIAG && (WS_PB & 127) == 0, "ws");

constexpr float CS = 0.08838834764831845f * 1.4426950408889634f;
constexpr int SC1 = 16;
constexpr u32 CODE_BYTES = 25600;
constexpr u32 SPIN_BUDGET = 1u << 19;

struct Args { const void* in[11]; void* out; void* ws; };

__device__ __forceinline__ rsrc_t mkrs(const void* p, u32 n) { return __builtin_amdgcn_make_buffer_rsrc((void*)p, 0, n, 0x00020000); }
constexpr __device__ __forceinline__ u32 swz(u32 r) { return (((r & 7u) ^ ((r >> 1) & 4u)) << 1) | ((r >> 3) & 1u); }
__device__ __forceinline__ f32x4 mfma(u32x4 a, u32x4 b, f32x4 c) {
  return __builtin_amdgcn_mfma_f32_16x16x32_bf16(__builtin_bit_cast(bf16x8, a), __builtin_bit_cast(bf16x8, b), c, 0, 0, 0);
}
__device__ __forceinline__ u32 pk(float lo, float hi) { f32x2 v = {lo, hi}; return __builtin_bit_cast(u32, __builtin_convertvector(v, bf16x2)); }
__device__ __forceinline__ float bflo(u32 v) { return __builtin_bit_cast(float, v << 16); }
__device__ __forceinline__ float bfhi(u32 v) { return __builtin_bit_cast(float, v & 0xffff0000u); }
__device__ __forceinline__ float asf(u32 v) { return __builtin_bit_cast(float, v); }
__device__ __forceinline__ u32 asu(float v) { return __builtin_bit_cast(u32, v); }
__device__ __forceinline__ float ex2(float x) { return __builtin_amdgcn_exp2f(x); }
__device__ __forceinline__ u32x4 ld128(LAS unsigned char* L, u32 a) { return *(LAS u32x4*)(L + a); }
__device__ __forceinline__ u32x4 ldtr2(LAS unsigned char* L, u32 a0, u32 a1) {
  const u32x2 x = __builtin_bit_cast(u32x2, __builtin_amdgcn_ds_read_tr16_b64_v4i16((LAS s16x4*)(L + a0)));
  const u32x2 y = __builtin_bit_cast(u32x2, __builtin_amdgcn_ds_read_tr16_b64_v4i16((LAS s16x4*)(L + a1)));
  return u32x4{x[0], x[1], y[0], y[1]};
}
__device__ __forceinline__ float xg_max(float x) {
  auto s = __builtin_amdgcn_permlane16_swap(asu(x), asu(x), false, false);
  x = fmaxf(asf(s[0]), asf(s[1]));
  auto t = __builtin_amdgcn_permlane32_swap(asu(x), asu(x), false, false);
  return fmaxf(asf(t[0]), asf(t[1]));
}
__device__ __forceinline__ float xg_sum(float x) {
  auto s = __builtin_amdgcn_permlane16_swap(asu(x), asu(x), false, false);
  x = asf(s[0]) + asf(s[1]);
  auto t = __builtin_amdgcn_permlane32_swap(asu(x), asu(x), false, false);
  return asf(t[0]) + asf(t[1]);
}
template <int CTRL> __device__ __forceinline__ float dpp(float x) { return asf(__builtin_amdgcn_mov_dpp(asu(x), CTRL, 0xf, 0xf, true)); }
__device__ __forceinline__ float row_sum(float x) { x += dpp<0x128>(x); x += dpp<0x124>(x); x += dpp<0x122>(x); x += dpp<0x121>(x); return x; }
__device__ __forceinline__ float row_max(float x) { x = fmaxf(x, dpp<0x128>(x)); x = fmaxf(x, dpp<0x124>(x)); x = fmaxf(x, dpp<0x122>(x)); x = fmaxf(x, dpp<0x121>(x)); return x; }

__device__ __forceinline__ u32 cw_ld(u32 a) { u32 v; asm volatile("ds_read_b32 %0, %1\n\ts_waitcnt lgkmcnt(0)" : "=v"(v) : "v"(a) : "memory"); return __builtin_amdgcn_readfirstlane(v); }
__device__ __forceinline__ void cw_st(u32 a, u32 v) { asm volatile("ds_write_b32 %0, %1" ::"v"(a), "v"(v) : "memory"); }
#define cw_full(v) cw_st(W_FULL, (v))
__device__ __forceinline__ void cw_add(u32 a, u32 v, int lane) { if (lane == 0) asm volatile("ds_add_u32 %0, %1" ::"v"(a), "v"(v) : "memory"); asm volatile("" ::: "memory"); }
__device__ __forceinline__ u32 cw_add_rtn(u32 a, u32 v, int lane) {
  u32 r = 0;
  if (lane == 0) asm volatile("ds_add_rtn_u32 %0, %1, %2\n\ts_waitcnt lgkmcnt(0)" : "=v"(r) : "v"(a), "v"(v) : "memory");
  asm volatile("" ::: "memory");
  return __builtin_amdgcn_readfirstlane(r);
}
__device__ __forceinline__ u32 cw_wait_(u32 a, u32 want, u32& bud) {
  u32 t;
  bud = __builtin_amdgcn_readfirstlane(bud); want = __builtin_amdgcn_readfirstlane(want);
  asm volatile("1:\n\tds_read_b32 v255, %2\n\ts_waitcnt lgkmcnt(0)\n\tv_readfirstlane_b32 %0, v255\n\ts_cmp_ge_u32 %0, %3\n\ts_cbranch_scc1 2f\n\t"
               "s_sleep 1\n\ts_sub_u32 %1, %1, 1\n\ts_cbranch_scc0 1b\n\ts_mov_b32 %1, 0\n2:\n\ts_nop 1"
               : "=&s"(t), "+s"(bud) : "v"(a), "s"(want) : "memory", "scc", "v255");
  return t;
}
#define cw_wait(a, want) cw_wait_((a), (want), bud)
__device__ __forceinline__ bool glb_retry_(u32& bud) {
  bud = __builtin_amdgcn_readfirstlane(bud);
  asm volatile("s_sleep 1\n\ts_sub_u32 %0, %0, 16\n\ts_cselect_b32 %0, 0, %0" : "+s"(bud) :: "memory", "scc");
  return bud != 0u;
}
#define glb_retry() glb_retry_(bud)
__device__ __forceinline__ u32x4 gld(rsrc_t r, u32 voff, u32 soff) { return __builtin_amdgcn_raw_buffer_load_b128(r, voff, soff, SC1); }
__device__ __forceinline__ u32x4 gld0(rsrc_t r, u32 voff, u32 soff) { return __builtin_amdgcn_raw_buffer_load_b128(r, voff, soff, 0); }
__device__ __forceinline__ void gst(rsrc_t r, u32x4 v, u32 voff, u32 soff) { __builtin_amdgcn_raw_buffer_store_b128(v, r, voff + soff, 0, SC1); }
__device__ __forceinline__ u32 tagx(u32x4 v, u32 tag) { return (v[1] ^ tag) | (v[3] ^ tag); }
__device__ __forceinline__ bool allz(u32 x) { return __builtin_amdgcn_ballot_w64(x != 0u) == 0ull; }
template <int N> __device__ __forceinline__ void fetch(LAS unsigned char* L, rsrc_t r, u32 voff, u32 ss, u32 tag, u32 dst, u32 ds, u32 flag, u32& bud) {
  u32x4 v[N];
  for (;;) {
    u32 bad = 0;
#pragma unroll
    for (int i = 0; i < N; ++i) v[i] = gld(r, voff + i * ss, 0);
#pragma unroll
    for (int i = 0; i < N; ++i) bad |= tagx(v[i], tag);
    if (allz(bad) || !glb_retry()) break;
  }
#pragma unroll
  for (int i = 0; i < N; ++i) *(LAS u32x2*)(L + dst + i * ds) = u32x2{v[i][0], v[i][2]};
  cw_st(flag, 1u);
}
#define DMA(r, ldsoff, voff, soff, imm, aux) __builtin_amdgcn_raw_ptr_buffer_load_lds((r), (LAS void*)(L + (ldsoff)), 16, (voff), (soff), (imm), (aux))

#define WLANE(x, i) asm volatile("v_writelane_b32 %0, %1, %2" : "+v"(stv) : "s"((u32)(x)), "i"(i))
#define SKIPST(i) false
#define STAMP(i) do { if (SKIPST(i)) break; unsigned long long t_; asm volatile("s_memtime %0\n\ts_waitcnt lgkmcnt(0)" : "=s"(t_) :: "memory"); WLANE(t_, (i)); } while (0)
#define STAMP_BEGIN() u32 stv = 0; do { unsigned long long r_ = __builtin_amdgcn_s_memrealtime(); WLANE(r_, 30); STAMP(0); } while (0)
#define STAMP_END() do { STAMP(29); unsigned long long r_ = __builtin_amdgcn_s_memrealtime(); WLANE(r_, 31); \
    if ((threadIdx.x & 63) < 32) ((u32*)((char*)a.ws + WS_DIAG))[(blockIdx.x * 6 + wave) * 32 + (threadIdx.x & 31)] = stv; } while (0)

#define VMW(n) asm volatile("s_waitcnt vmcnt(" #n ")" ::: "memory")
#define ZERO4 f32x4{0.f, 0.f, 0.f, 0.f}
#define MAX8(a, b) fmaxf(fmaxf(fmaxf(a[0], a[1]), fmaxf(a[2], a[3])), fmaxf(fmaxf(b[0], b[1]), fmaxf(b[2], b[3])))
constexpr float LAZY = 192.f;
template <int NA> __device__ __forceinline__ u32x4 soft(const f32x4 s0, const f32x4 s1, float& m, float& l, f32x4* acc) {
  const float mx = MAX8(s0, s1);
  if (__builtin_amdgcn_ballot_w64(mx > m + LAZY) != 0ull) {
    const float mn = fmaxf(m, xg_max(mx)), al = ex2((m - mn) * CS);
    m = mn; l *= al;
#pragma unroll
    for (int i = 0; i < NA; ++i) acc[i] *= al;
  }
  const float mc = -m * CS;
  float pr[8];
#pragma unroll
  for (int i = 0; i < 4; ++i) { pr[i] = ex2(fmaf(s0[i], CS, mc)); pr[4 + i] = ex2(fmaf(s1[i], CS, mc)); }
  l += ((pr[0] + pr[1]) + (pr[2] + pr[3])) + ((pr[4] + pr[5]) + (pr[6] + pr[7]));
  return u32x4{pk(pr[0], pr[1]), pk(pr[2], pr[3]), pk(pr[4], pr[5]), pk(pr[6], pr[7])};
}
__device__ __forceinline__ u32x4 pick2(const f32x4 d0, const f32x4 d1, int q, u32 tag) {
  const float x0 = (q & 2) ? d1[0] : d0[0], x1 = (q & 2) ? d1[1] : d0[1], x2 = (q & 2) ? d1[2] : d0[2], x3 = (q & 2) ? d1[3] : d0[3];
  return u32x4{asu((q & 1) ? x2 : x0), tag, asu((q & 1) ? x3 : x1), tag};
}

constexpr int NPR = (NREC + 1) / 2;
template <int I> __device__ __forceinline__ void merge_rec(f32x4* o, const u32x4* t, float w) {
  if constexpr (I < NPR) { o[I & 1] += dpp<0x150 + 2 * I>(w) * f32x4{bflo(t[I][0]), bfhi(t[I][0]), bflo(t[I][2]), bfhi(t[I][2])}; merge_rec<I + 1>(o, t, w); }
}

constexpr int IT_UK = 0, IT_C = 4, IT_UV = 20, IT_W = 24;
constexpr u32 ALL = 1u << 20;
template <int N> __device__ __forceinline__ void vmw() { asm volatile("s_waitcnt vmcnt(%0)" ::"i"(N) : "memory"); }

__device__ __forceinline__ void read_w(LAS unsigned char* L, u32x4* wf, int c, int lane) {
  const u32 lp = (8 * (lane >> 4) + ((lane >> 2) & 3)) * 576 + 8 * (lane & 3) + 128 * c, t2 = c < 4 ? 64 : 0;
#pragma unroll
  for (int i = 0; i < 16; ++i) { const u32 ad = (i >> 2) * SLOT + lp + (i & 1) * 32 + ((i & 2) ? t2 : 0); wf[i] = ldtr2(L, ad, ad + 2304); }
}
__device__ __forceinline__ void project(LAS unsigned char* L, const u32x4* wf, rsrc_t rws, u32 so, u32 tag, int c, int lane, u32& bud) {
  const int g4 = lane >> 4, q = (lane >> 2) & 3, p = lane & 3;
  u32 ao = OB_OFF + p * OB_STR + g4 * 16, vo = so + c * 512 + p * 4608 + (q >> 1) * 128 + g4 * 32 + (q & 1) * 16;
  asm volatile("" : "+v"(ao), "+v"(vo));
  cw_wait(W_PJ, 4);
  f32x4 d[4] = {ZERO4, ZERO4, ZERO4, ZERO4};
  u32x4 of[4];
#pragma unroll
  for (int ks = 0; ks < 4; ++ks) of[ks] = ld128(L, ao + ks * 64);
  __builtin_amdgcn_sched_barrier(0);
#pragma unroll
  for (int ks = 0; ks < 4; ++ks) { d[0] = mfma(wf[ks * 4], of[ks], d[0]); d[1] = mfma(wf[ks * 4 + 1], of[ks], d[1]); }
#pragma unroll
  for (int ks = 0; ks < 4; ++ks) { d[2] = mfma(wf[ks * 4 + 2], of[ks], d[2]); d[3] = mfma(wf[ks * 4 + 3], of[ks], d[3]); }
  gst(rws, pick2(d[0], d[1], q, tag), vo, 0);
  if (c < 4) gst(rws, pick2(d[2], d[3], q, tag), vo + 256, 0);
}
__device__ __forceinline__ void final_sum(rsrc_t rws, void* out, int br, int hkv, int b, int i, u32 tag, int lane, u32& bud) {
  const int c0 = 36 * hkv + 8 * i + 2 * (lane >> 5);
  u32 vo = WS_YG + (br * 32 + (lane & 31)) * 18432 + (b * 576 + c0) * 8, oo = (b * 1152 + br * 576 + c0 + (lane & 1) * 4) * 2;
  const u32 v2 = i < 4 ? 32 : 0;
  const rsrc_t ro = mkrs(out, 4u * 1152u * 2u);
  asm volatile("" : "+v"(vo), "+v"(oo));
  u32x4 y, z;
  for (;;) { y = gld(rws, vo, 0); z = gld(rws, vo, v2); if (allz(tagx(y, tag) | tagx(z, tag)) || !glb_retry()) break; }
  float r[4] = {asf(y[0]), asf(y[2]), asf(z[0]), asf(z[2])};
#pragma unroll
  for (int k = 0; k < 4; ++k) { auto t = __builtin_amdgcn_permlane16_swap(asu(r[k]), asu(r[k]), false, false); r[k] = row_sum(asf(t[0]) + asf(t[1])); }
  if ((lane & 31) < (i < 4 ? 2 : 1)) __builtin_amdgcn_raw_buffer_store_b32((lane & 1) ? pk(r[2], r[3]) : pk(r[0], r[1]), ro, oo, 0, OUT_AUX);
}

__global__ void __launch_bounds__(384, 2) mk_kernel(Args a) {
  __shared__ __attribute__((aligned(16))) unsigned char smem[LDS_BYTES];
  LAS unsigned char* L = (LAS unsigned char*)smem;
  const int lane = threadIdx.x & 63, wave = __builtin_amdgcn_readfirstlane(threadIdx.x >> 6);
  const int g = blockIdx.x, gi = g >> 3, hkv = 2 * (g & 7) + (gi >> 4), j = gi & 15, b = j >> 2, s = j & 3;
  STAMP_BEGIN();
  if (wave == 1) {
    cw_st(CTL + lane * 4, 0u);
    unsigned long long pc; asm volatile("s_getpc_b64 %0" : "=s"(pc));
    DMA(mkrs((const void*)(pc & ~255ull), CODE_BYTES), MG_OFF, lane * 16, gi * 1024, 0, 0);
    asm volatile("s_waitcnt lgkmcnt(0)" ::: "memory");
  }
  __builtin_amdgcn_s_barrier();
  const u32 vu = (lane >> 5) * 8192 + (((lane & 31) ^ ((lane >> 5) << 1)) << 4);
#define LOAD_W(in_, LW) do { const rsrc_t r = mkrs(a.in[in_], 4096u * 1152u); u32 wo[9]; int ln = lane; asm volatile("" : "+v"(ln)); \
      _Pragma("unroll") for (int i = 0; i < 9; ++i) { const u32 ci = i * 64 + ln; wo[i] = ci * 16 + ((ci * 3641u) >> 17) * 576u; } \
      u32 sw = ((u32)hkv * 256u + (b & 1) * 128u) * 1152u + (b >> 1) * 576u; \
      _Pragma("unroll 1") for (int t = 0; t < 8; ++t, sw += 16u * 1152u) { _Pragma("unroll") for (int i = 0; i < 9; ++i) { if (i % 3 == 0) vmw<LW>(); DMA(r, t * 9216 + i * 1024, wo[i], sw, 0, AUX_W); } } } while (0)
  if (wave == 5) {
    if (s != 1) {
      const rsrc_t rw = mkrs(a.in[s == 0 ? 7 : s == 2 ? 3 : 8], s < 3 ? 576u * 8192u : 512u * 8192u), rx = mkrs(a.in[s ? 0 : 5], s < 3 ? 4u * 576u * 2u : 0u);
      const u32 vo = s < 3 ? ((lane >> 2) & 7) * 8192 + (lane & 3) * 16 + (lane >> 5) * 64 : vu, st = s < 3 ? 8 * 8192 : 2 * 8192, xm = s == 3 ? 0xf0u : 0u;
      u32 so = hkv * 512 + (s < 3 ? b * 128 : b * (128u * 8192u)), lo = 0;
#pragma unroll 1
      for (int t = 0; t < (s < 3 ? 9 : 8); ++t) {
        vmw<40>();
        if (t == 7) cw_st(W_WF, 1u);
#pragma unroll
        for (int i = 0; i < 8; ++i, so += st) DMA(rw, lo + i * 1024, vo ^ ((swz(2 * i) << 4) & xm), so, 0, AUX_W);
        lo += s == 3 && (t & 1) ? SLOT - 8192 : 8192;
        if (t == 1 && s < 3) {
#pragma unroll
          for (int i = 0; i < 5; ++i) DMA(rx, SB_OFF + i * 1024, lane * 16, i * 1024, 0, 0);
        }
      }
    } else {
      LOAD_W(10, 40);
    }
    STAMP(14);
    VMW(32); cw_st(W_WF, 2u); cw_st(W_WI, 1u);
    VMW(16); cw_st(W_WF, 3u);
    VMW(0); cw_st(W_WF, 4u);
  }
  const int ab = gi >> 3, ah = ((g & 7) + 2 * (gi & 4) + 3 * (ab + 4 * s)) & 15, as = s;
  const int ITU = 4 + (s == 0 ? N0 : s == 1 ? N1 : N2);
  const int bq = (g & 7) >> 1, sp = (g & 1) * 8 + (gi >> 2);
  u32 bud = SPIN_BUDGET;

  if (wave == 0) {
    __builtin_amdgcn_s_setprio(3);
    u32 vq[4];
#pragma unroll
    for (int q = 0; q < 4; ++q) vq[q] = (lane >> 4) * 256 + (((lane & 15) ^ swz(4 * q + (lane >> 4))) << 4);
    int pub = 0, it = 4;
#define SLOT_WAIT() do { if (it >= NS) { const u32 need = 4u * (it >> 3), wa = W_FREE + (it & 7) * 4; \
      if (cw_ld(wa) < need) { VMW(16); if (it - 1 > pub) cw_full(it - 1); VMW(0); pub = it; cw_full(pub); cw_wait(wa, need); } } } while (0)
#define LSTAMP() do { if (it == 4) STAMP(2); if (it == 8) STAMP(3); if (it == 12) STAMP(4); if (it == 16) STAMP(5); if (it == 20) STAMP(6); if (it == 24) STAMP(7); if (it == 28) STAMP(8); if (it == 32) STAMP(9); if (it == 36) STAMP(10); if (it == 40) STAMP(11); if (it == 44) STAMP(12); } while (0)
#define PACE(LW, k) do { vmw<LW>(); if ((k) == ((LW / 4) & 3) && it - LW / 16 > pub) { pub = it - LW / 16; cw_full(pub); } } while (0)
    asm volatile("" : "+s"(it));
    if (s < 3) {
      const rsrc_t rk = mkrs(a.in[1], 1u << 26), rv = mkrs(a.in[2], 1u << 26), rt = mkrs((const char*)a.ws + WS_TK + ah * 128, 4u);
      u32 so = ((u32)(ab * 16 + ah) * 128u + (s == 0 ? 0 : s == 1 ? N0 : N0 + N1)) * 8192u;
      asm volatile("v_mov_b32 v254, 1" ::: "v254");
#pragma unroll 1
      for (;; ++it) {
        const u32 sb = (it & 7) * SLOT;
        if (it == 4 + EARLY) { if (s == 0 && HOLD_ST) cw_wait(W_QS, 4); else cw_wait(W_WI, 1); }
        SLOT_WAIT();
        LSTAMP();
        PACE(LW_A, 0);
        if (it >= ITU) {
          u32 t;
          asm volatile("v_readlane_b32 %0, v254, %1\n\tv_writelane_b32 v254, 1, %1\n\ts_nop 4" : "=&s"(t) : "s"(it & 3) : "memory", "v254");
          if (t >= (u32)NTICK) break;
          cw_st(W_TK + (it & 15) * 4, (it << 8) | t);
          so = (((t & 3u) * 16u + ah) * 128u + NSUM + (t >> 2)) * 8192u;
        }
        if (it >= ITU - 2) {
          unsigned long long ex;
          asm volatile("s_mov_b64 %0, exec\n\ts_lshl_b64 exec, 1, %2\n\tbuffer_atomic_add v254, off, %1, 0 sc0\n\ts_mov_b64 exec, %0"
                       : "=&s"(ex) : "s"(rt), "s"((it + 2) & 3) : "memory", "scc", "v254");
        }
#pragma unroll
        for (int k = 0; k < 2; ++k) {
          if (k) PACE(LW_A, 1);
          DMA(rk, sb + k * 4096, vq[0], so + k * 4096, 0, AUX_K); DMA(rk, sb + k * 4096, vq[1], so + k * 4096, 1024, AUX_K);
          DMA(rk, sb + k * 4096, vq[2], so + k * 4096, 2048, AUX_K); DMA(rk, sb + k * 4096, vq[3], so + k * 4096, 3072, AUX_K);
        }
#pragma unroll
        for (int k = 0; k < 2; ++k) {
          PACE(LW_A, k + 2);
          DMA(rv, sb + 8192 + k * 4096, vq[0], so + k * 4096, 0, AUX_V); DMA(rv, sb + 8192 + k * 4096, vq[1], so + k * 4096, 1024, AUX_V);
          DMA(rv, sb + 8192 + k * 4096, vq[2], so + k * 4096, 2048, AUX_V); DMA(rv, sb + 8192 + k * 4096, vq[3], so + k * 4096, 3072, AUX_V);
        }
        so += 8192u;
      }
      cw_st(W_TK + ((it + (lane & 3)) & 15) * 4, ((it + (lane & 3)) << 8) | 255u);
      STAMP(13);
    } else {
#pragma unroll 1
      for (; it < IT_W; ++it) {
        const u32 sb = (it & 7) * SLOT;
        if (it == 4 + EARLY) cw_wait(W_WI, 1);
        if (it == GATE1 || it == GATE2) {
          VMW(16); if (it - 1 > pub) cw_full(it - 1);
          VMW(0); pub = it; cw_full(it);
          if (it == GATE1) cw_wait(W_QS, 5);
          if (it == GATE2) cw_wait(W_GI, 4);
        }
        SLOT_WAIT();
        LSTAMP();
        if (it < IT_UV) {
          const rsrc_t r = mkrs(a.in[6], 1u << 24);
          const u32 so = ((u32)bq * 4096u + sp * 256u + (it - IT_C) * 16u) * 1024u;
#pragma unroll
          for (int k = 0; k < 4; ++k) {
            PACE(LW_B, k);
            DMA(r, sb + k * 4096, (lane ^ swz(4 * k + 0)) << 4, so + k * 4096, 0, AUX_C);
            DMA(r, sb + k * 4096, (lane ^ swz(4 * k + 1)) << 4, so + k * 4096, 1024, AUX_C);
            DMA(r, sb + k * 4096, (lane ^ swz(4 * k + 2)) << 4, so + k * 4096, 2048, AUX_C);
            DMA(r, sb + k * 4096, (lane ^ swz(4 * k + 3)) << 4, so + k * 4096, 3072, AUX_C);
          }
        } else {
          const rsrc_t r = mkrs(a.in[9], 512u * 8192u);
          const u32 so = ((u32)b * 128u + (it & 3) * 32u) * 8192u + hkv * 512u;
#pragma unroll
          for (int i = 0; i < 16; ++i) { if ((i & 3) == 0) PACE(LW_B, i >> 2); DMA(r, sb + i * 1024, vu ^ (swz((2 * i) & 15) << 4), so + i * (2 * 8192), 0, AUX_W); }
        }
      }
      VMW(0); pub = it; cw_full(it);
      cw_wait(W_GI, 8);
      STAMP(15);
      LOAD_W(4, LW_W);
    }
    VMW(16); if (it - 1 > pub) cw_full(it - 1);
    VMW(0); cw_full(ALL);
    STAMP(1);
    STAMP_END();
    return;
  }

  const int c = wave - 1, g4 = lane >> 4, n = lane & 15, q = n >> 2, p = lane & 3;
  const rsrc_t rws = mkrs(a.ws, WS_END);
  const char* wsp = (const char*)a.ws; asm volatile("" : "+s"(wsp));
  const u32 tag = *(const __attribute__((address_space(4))) u32*)(wsp + WS_EPOCH) + 1u;
  asm volatile("" ::"s"(tag));
  VMW(0);
  const u32 yso = WS_YG + ((s == 1 ? 32 : 0) + hkv * 2 + (b & 1)) * 18432 + (b >> 1) * 2304;
  if (c == 4) {
    u32x4 wf[16];
    if (s == 1) {
      read_w(L, wf, 4, lane);
      cw_st(W_MF, 1u);
    }
    cw_wait(W_FULL, s == 3 ? POLL_B : s == 0 ? POLL_A0 : POLL_A);
    STAMP(1);
    fetch<4>(L, rws, lane * 16 + WS_QG + (s < 3 ? ah * 128 : 8192 + hkv * 128) * 8, 16384, tag, (s < 3 ? QA_OFF : OB_OFF) + lane * 8, s < 3 ? 512 : OB_STR, W_QA, bud);
    STAMP(3);
    if (s == 3) {
      cw_wait(W_QS, 5); cw_wait(W_FULL, POLL_H);
      fetch<1>(L, rws, lane * 16 + WS_HINT, 0, tag, MG_OFF + lane * 8, 0, W_HINT, bud);
      STAMP(4);
      cw_wait(W_BP, 4);
      fetch<4>(L, rws, n * 32 + (g4 & 1) * 16 + WS_SG + hkv * 2048, 512, tag, MG_OFF + lane * 8, 512, W_SGF, bud);
      STAMP(2);
      cw_wait(W_FULL, ALL);
      read_w(L, wf, 4, lane);
    } else if (s == 1) {
      cw_wait(W_FULL, POLL_C);
      const u32 so = WS_CG + hkv * 32768 + (b & 1) * 1024;
      for (;;) {
        const u32x4 t = gld(rws, q * 8192 + (p & 1) * 512, so);
        if (allz(tagx(t, tag)) || !glb_retry()) break;
        __builtin_amdgcn_s_sleep(16);
      }
      u32x4 cv[16];
      for (;;) {
        u32 bad = 0;
#pragma unroll
        for (int i = 0; i < 16; ++i) cv[i] = gld(rws, lane * 16 + (i & 1) * 2048, so + (i >> 1) * 4096);
#pragma unroll
        for (int i = 0; i < 16; ++i) bad |= tagx(cv[i], tag);
        if (allz(bad) || !glb_retry()) break;
      }
#pragma unroll
      for (int i = 0; i < 4; ++i)
        *(LAS u32*)(L + OB_OFF + i * OB_STR + lane * 4) = pk((asf(cv[i][0]) + asf(cv[4 + i][0])) + (asf(cv[8 + i][0]) + asf(cv[12 + i][0])), (asf(cv[i][2]) + asf(cv[4 + i][2])) + (asf(cv[8 + i][2]) + asf(cv[12 + i][2])));
      cw_st(W_PJ, 4u);
      STAMP(2);
    }
    if (s & 1) {
      project(L, wf, rws, yso, tag, 4, lane, bud);
    } else {
      cw_wait(W_O2, 4);
      __builtin_amdgcn_s_sleep(FSLEEP);
      final_sum(rws, a.out, s == 0, hkv, b, 4, tag, lane, bud);
      if (g == 16 && lane == 0) __builtin_amdgcn_raw_buffer_store_b32(tag, rws, 0, WS_EPOCH, SC1);
      STAMP(22);
    }
    STAMP_END();
    return;
  }

  if (s < 3) {
    u32x4 wf[16];
    if (s == 1) {
      cw_wait(W_WF, 4);
      read_w(L, wf, c, lane);
      cw_wait(W_MF, 1);
      if (lane < 4) asm volatile("ds_add_u32 %0, %1" ::"v"(W_FREE + lane * 4), "v"(1u) : "memory");
    } else {
      const u32 aw = g4 * 1024 + (c >> 1) * 512 + q * 64 + (c & 1) * 32 + 8 * p, ax = SB_OFF + p * 1152 + g4 * 16;
      const u32 vo = (n * 2048 + 2 * g4) * 8, so = WS_QG + ((s ? 0 : 8192) + hkv * 128 + b * 32 + 8 * c) * 8;
      if (j == 2 && c == 0) { __builtin_amdgcn_raw_buffer_store_b32(0u, rws, WS_TK + hkv * 128, 0, SC1); VMW(0); }
      cw_wait(W_WF, 2);
      u32x4 xr[18], wq[9];
#pragma unroll
      for (int ks = 0; ks < 18; ++ks) xr[ks] = ld128(L, ax + ks * 64);
      f32x4 a0 = ZERO4, a1 = ZERO4;
#pragma unroll
      for (int hf = 0; hf < 2; ++hf) {
        if (hf) { cw_wait(W_WF, 4); STAMP(1); }
#pragma unroll
        for (int ks = 0; ks < 9; ++ks) wq[ks] = ldtr2(L, aw + (hf * 9 + ks) * 4096, aw + (hf * 9 + ks) * 4096 + 256);
        if (lane < 2) asm volatile("ds_add_u32 %0, %1" ::"v"(W_FREE + hf * 8 + lane * 4), "v"(1u) : "memory");
        __builtin_amdgcn_sched_barrier(0);
#pragma unroll
        for (int ks = 0; ks < 9; ++ks) { if (ks & 1) a1 = mfma(wq[ks], xr[hf * 9 + ks], a1); else a0 = mfma(wq[ks], xr[hf * 9 + ks], a0); }
        __builtin_amdgcn_sched_barrier(0);
      }
      a0 += a1;
      if (n < 4) gst(rws, u32x4{pk(a0[0], a0[1]), tag, pk(a0[2], a0[3]), tag}, vo, so);
      cw_add(W_QS, 1, lane);
      STAMP(2);
    }
    float m = -1e30f, l = 0.f;
    f32x4 acc[8];
#pragma unroll
    for (int i = 0; i < 8; ++i) acc[i] = ZERO4;
    u32x4 qf[4], kf[8], vf[8];
    const u32 lpK = n * 256 + ((g4 ^ swz(n)) << 4), lpQ = QA_OFF + (n & 1) * 256 + g4 * 16;
    const u32 lpV = (4 * g4 + q) * 256 + (((p >> 1) ^ swz(4 * g4 + q)) << 4) + 8 * (p & 1);
#define READ_K() _Pragma("unroll") for (int kk = 0; kk < 4; ++kk) { const u32 ak = (sb + lpK) ^ (kk << 6); kf[kk] = ld128(L, ak); kf[4 + kk] = ld128(L, ak + 4096); }
#define READ_V() do { _Pragma("unroll") for (int ct = 0; ct < 8; ++ct) { const u32 av = (sb + lpV) ^ (ct << 5); vf[ct] = ldtr2(L, av + 8192, av + 12288); } \
      cw_add(W_FREE + (it & 7) * 4, 4, lane); } while (0)
#define SCORES() f32x4 s0 = ZERO4, s1 = ZERO4; \
      _Pragma("unroll") for (int kk = 0; kk < 4; ++kk) { s0 = mfma(kf[kk], qf[kk], s0); s1 = mfma(kf[4 + kk], qf[kk], s1); }
#define PICK8(e) const f32x4 e##0 = (n & 2) ? acc[1] : acc[0], e##1 = (n & 2) ? acc[3] : acc[2], e##2 = (n & 2) ? acc[5] : acc[4], e##3 = (n & 2) ? acc[7] : acc[6], \
      e##4 = (n & 4) ? e##1 : e##0, e##5 = (n & 4) ? e##3 : e##2, e = (n & 8) ? e##5 : e##4
    const u32 vrec = (n & 1) * 512 + (n >> 1) * 64 + g4 * 16;
    bool first = true;
    int it = 4 + c;
#pragma unroll 1
    for (; it < ITU; it += 4) {
      const u32 sb = (it & 7) * SLOT;
      cw_wait(W_FULL, it + 1);
      READ_K(); READ_V();
      if (first) {
        first = false;
        cw_wait(W_QA, 1);
        STAMP(3);
#pragma unroll
        for (int kk = 0; kk < 4; ++kk) qf[kk] = ld128(L, lpQ + ab * 512 + kk * 64);
      }
      SCORES();
      const u32x4 pf = soft<8>(s0, s1, m, l, acc);
#pragma unroll
      for (int ct = 0; ct < 8; ++ct) acc[ct] = mfma(vf[ct], pf, acc[ct]);
    }
    STAMP(8);
    {
      l = xg_sum(l);
      PICK8(e);
      *(LAS f32x4*)(L + MG_OFF + c * 1024 + lane * 16) = e;
      if (lane < 2) *(LAS f32x2*)(L + MS_OFF + (c * 2 + lane) * 8) = f32x2{m, l};
      if (cw_add_rtn(W_MRG, 1, lane) == 3u) {
        f32x2 sv[4]; f32x4 ov[4];
#pragma unroll
        for (int i = 0; i < 4; ++i) { sv[i] = *(LAS f32x2*)(L + MS_OFF + (i * 2 + (n & 1)) * 8); ov[i] = *(LAS f32x4*)(L + MG_OFF + i * 1024 + lane * 16); }
        const float M = fmaxf(fmaxf(sv[0][0], sv[1][0]), fmaxf(sv[2][0], sv[3][0]));
        float Ls = 0.f; f32x4 o = ZERO4;
#pragma unroll
        for (int i = 0; i < 4; ++i) { const float w = ex2((sv[i][0] - M) * CS); Ls = fmaf(w, sv[i][1], Ls); o += w * ov[i]; }
        o *= __builtin_amdgcn_rcpf(Ls);
        gst(rws, u32x4{pk(o[0], o[1]), tag, pk(o[2], o[3]), tag}, vrec, WS_AG + ((ah * 4 + ab) * 16 + as) * 1024);
        if (lane < 2) gst(rws, u32x4{asu(M), tag, asu(Ls), tag}, lane * 16, WS_AS + (ah * 4 + ab) * 2048 + as * 128);
      }
    }
    STAMP(9);
#pragma unroll 1
    for (;; it += 4) {
      const u32 t = cw_wait(W_TK + (it & 15) * 4, it << 8) & 255u, sb = (it & 7) * SLOT;
      if (t >= (u32)NTICK || bud == 0u) break;
#pragma unroll
      for (int kk = 0; kk < 4; ++kk) qf[kk] = ld128(L, lpQ + (t & 3u) * 512 + kk * 64);
      const u32 pr = ah * 4 + (t & 3u), rec = 3 + (t >> 2), va = vrec + WS_AG + (pr * 16 + rec) * 1024, vs = lane * 16 + WS_AS + pr * 2048 + rec * 128;
      cw_wait(W_FULL, it + 1);
      READ_K(); READ_V();
      SCORES();
      m = xg_max(MAX8(s0, s1));
      const float mc = -m * CS;
#pragma unroll
      for (int i = 0; i < 4; ++i) { s0[i] = ex2(fmaf(s0[i], CS, mc)); s1[i] = ex2(fmaf(s1[i], CS, mc)); }
      const u32x4 pf = {pk(s0[0], s0[1]), pk(s0[2], s0[3]), pk(s1[0], s1[1]), pk(s1[2], s1[3])};
      f32x4 e0 = ZERO4, e1 = ZERO4;
#pragma unroll
      for (int ct = 0; ct < 8; ct += 2) { e0 = mfma(vf[ct], (n >> 1) == ct ? pf : u32x4{0u, 0u, 0u, 0u}, e0); e1 = mfma(vf[ct + 1], (n >> 1) == ct + 1 ? pf : u32x4{0u, 0u, 0u, 0u}, e1); }
      s0 += s1;
      l = xg_sum((s0[0] + s0[1]) + (s0[2] + s0[3]));
      e0 = (e0 + e1) * __builtin_amdgcn_rcpf(l);
      gst(rws, u32x4{pk(e0[0], e0[1]), tag, pk(e0[2], e0[3]), tag}, va, 0);
      if (lane < 2) gst(rws, u32x4{asu(m), tag, asu(l), tag}, vs, 0);
      STAMP(10);
    }
    if (s == 1) {
      STAMP(18);
      project(L, wf, rws, yso, tag, c, lane, bud);
      STAMP(20);
    } else {
      cw_add(W_O2, 1, lane);
      cw_wait(W_O2, 4);
      __builtin_amdgcn_s_sleep(FSLEEP);
      final_sum(rws, a.out, s == 0, hkv, b, c, tag, lane, bud);
      STAMP(22);
    }
    STAMP_END();
    return;
  }

  {
    cw_wait(W_WF, c + 1);
    u32x4 wu[16];
    {
      const u32 lp = (IT_UK + c) * SLOT + n * 512 + ((g4 ^ swz(n)) << 4);
#pragma unroll
      for (int i = 0; i < 16; ++i) wu[i] = ld128(L, (lp ^ ((i & 3) << 6)) + ((i >> 2) & 1) * 8192 + (i >> 3) * 256);
    }
    cw_add(W_FREE + (IT_UK + c) * 4, 4, lane);
    STAMP(15);
    cw_wait(W_QA, 1);
    STAMP(3);
    f32x4 d[4] = {ZERO4, ZERO4, ZERO4, ZERO4};
#pragma unroll
    for (int i = 0; i < 8; ++i) {
      const u32x4 qv = ld128(L, OB_OFF + p * OB_STR + i * 64 + g4 * 16);
      d[(i >> 2) * 2] = mfma(wu[(i >> 2) * 8 + (i & 3)], qv, d[(i >> 2) * 2]); d[(i >> 2) * 2 + 1] = mfma(wu[(i >> 2) * 8 + 4 + (i & 3)], qv, d[(i >> 2) * 2 + 1]);
    }
    const f32x4 e0 = (q & 1) ? d[1] : d[0], e1 = (q & 1) ? d[3] : d[2], e = (q & 2) ? e1 : e0;
    gst(rws, u32x4{pk(e[0], e[1]), tag, pk(e[2], e[3]), tag}, p * 65536 + (g4 & 1) * 128 + (q >> 1) * 64 + (q & 1) * 32 + (g4 >> 1) * 16, WS_QAG + hkv * 4096 + (b * 4 + c) * 256);
    if (cw_add_rtn(W_QS, 1, lane) == 3u) { if (lane == 0) gst(rws, u32x4{tag, tag, tag, tag}, WS_HINT + (hkv * 4 + b) * 16, 0); cw_add(W_QS, 1, lane); }
    STAMP(4);
  }
  {
    const int ch = c & 1;
    u32x4 qa[8];
    {
      const u32 so = WS_QAG + bq * 65536 + (c >> 1) * 32768 + ch * 2048;
      cw_wait(W_HINT, 1);
      STAMP(5);
      u32x4 t[16];
#pragma unroll
      for (int i = 0; i < 16; ++i) t[i] = gld0(rws, (n >> 1) * 4096 + (n & 1) * 64 + g4 * 16 + i * 128, so);
      cw_add(W_GI, 1, lane);
      for (;;) {
        u32 bad = 0;
#pragma unroll
        for (int i = 0; i < 16; ++i) bad |= tagx(t[i], tag);
        if (allz(bad) || !glb_retry()) break;
#pragma unroll
        for (int i = 0; i < 16; ++i) t[i] = gld(rws, (n >> 1) * 4096 + (n & 1) * 64 + g4 * 16 + i * 128, so);
      }
#pragma unroll
      for (int i = 0; i < 8; ++i) qa[i] = u32x4{t[2 * i][0], t[2 * i][2], t[2 * i + 1][0], t[2 * i + 1][2]};
    }
    STAMP(6);
    float mb = -1e30f, lb = 0.f;
    f32x4 ob[16];
#pragma unroll
    for (int i = 0; i < 16; ++i) ob[i] = ZERO4;
    const u32 lpR = n * 1024 + ((g4 ^ swz(n)) << 4) + ch * 512;
    const u32 lpC = (4 * g4 + q) * 1024 + (((p >> 1) ^ swz(4 * g4 + q)) << 4) + 8 * (p & 1) + ch * 512;
#pragma unroll 1
    for (int st = 0; st < 8; ++st) {
      const int it = IT_C + 2 * st;
      const u32 sb = (it & 7) * SLOT;
      const u32 xw = ((st & 1) ? (4 + c) * SLOT + 16384 : SB_OFF + c * 2048) + lane * 16, xr = ((st & 1) ? (4 + (c ^ 1)) * SLOT + 16384 : SB_OFF + (c ^ 1) * 2048) + lane * 16;
      cw_wait(W_FULL, it + 2);
      u32x4 rf[16], cf[16];
#pragma unroll
      for (int kk = 0; kk < 8; ++kk) { const u32 ak = ((sb + lpR) ^ ((kk & 3) << 6)) + (kk >> 2) * 256; rf[2 * kk] = ld128(L, ak); rf[2 * kk + 1] = ld128(L, ak + SLOT); }
      __builtin_amdgcn_sched_barrier(0);
      f32x4 s0 = ZERO4, s1 = ZERO4;
#pragma unroll
      for (int kk = 0; kk < 8; ++kk) {
        s0 = mfma(rf[2 * kk], qa[kk], s0); s1 = mfma(rf[2 * kk + 1], qa[kk], s1);
        const u32 av = (sb + lpC) ^ (kk << 5);
        cf[kk] = ldtr2(L, av, av + SLOT); cf[8 + kk] = ldtr2(L, av + 256, av + 256 + SLOT);
        __builtin_amdgcn_sched_barrier(0);
      }
      *(LAS f32x4*)(L + xw) = s0;
      *(LAS f32x4*)(L + xw + 1024) = s1;
      cw_st(W_S + c * 4, st + 1);
      if (lane < 2) asm volatile("ds_add_u32 %0, %1" ::"v"(W_FREE + (it & 7) * 4 + lane * 4), "v"(1u) : "memory");
      cw_wait(W_S + (c ^ 1) * 4, st + 1);
      s0 += *(LAS f32x4*)(L + xr); s1 += *(LAS f32x4*)(L + xr + 1024);
      const u32x4 pf = soft<16>(s0, s1, mb, lb, ob);
#pragma unroll
      for (int ct = 0; ct < 16; ++ct) ob[ct] = mfma(cf[ct], pf, ob[ct]);
      if (st == 3) STAMP(16);
      if (st == 4) STAMP(17);
    }
    STAMP(7);
#pragma unroll
    for (int i = 0; i < 8; ++i) {
      auto x = __builtin_amdgcn_permlane16_swap(pk(ob[2 * i][0], ob[2 * i][1]), pk(ob[2 * i + 1][0], ob[2 * i + 1][1]), false, false);
      auto y = __builtin_amdgcn_permlane16_swap(pk(ob[2 * i][2], ob[2 * i][3]), pk(ob[2 * i + 1][2], ob[2 * i + 1][3]), false, false);
      gst(rws, u32x4{x[0], y[0], x[1], y[1]}, (n >> 1) * 512 + (n & 1) * 64 + (g4 & 1) * 32 + (g4 >> 1) * 16 + (i & 3) * 128,
          WS_PB + ((bq * 16 + sp) * 4 + ch * 2 + (i >> 2)) * 8192 + (c >> 1) * 4096);
    }
    lb = xg_sum(lb);
    if (g4 == 0 && ch == 0) *(LAS f32x2*)(L + ST_OFF + ((c >> 1) * 16 + n) * 8) = f32x2{mb, lb};
    VMW(0);
    if (cw_add_rtn(W_BP, 1, lane) == 3u) {
      const f32x2 ml = *(LAS f32x2*)(L + ST_OFF + (lane & 31) * 8);
      if (lane < 32) gst(rws, u32x4{asu(ml[0]), tag, asu(ml[1]), tag}, (lane >> 1) * 2048 + (lane & 1) * 16, WS_SG + (bq * 16 + sp) * 32);
    }
    STAMP(8);
  }
  {
    cw_wait(W_SGF, 1);
    STAMP(10);
    u32x4 pv[8];
#pragma unroll
    for (int i = 0; i < 8; ++i) pv[i] = gld(rws, (lane >> 5) * 32768 + (lane & 31) * 16, WS_PB + ((c * 16 + 2 * i) * 4 + b) * 8192 + hkv * 512);
    cw_add(W_GI, 1, lane);
    const f32x2 st = *(LAS f32x2*)(L + MG_OFF + c * 512 + lane * 8);
    const float w = ex2((st[0] - row_max(st[0])) * CS), cf = w / row_sum(w * st[1]);
    f32x4 o0 = ZERO4, o1 = ZERO4;
#pragma unroll
    for (int i = 0; i < 8; ++i) {
      const float f = asf(__builtin_amdgcn_ds_bpermute((((lane >> 2) & 1) * 16 + 2 * i + (lane >> 5)) * 4, asu(cf)));
      o0 += f * f32x4{bflo(pv[i][0]), bfhi(pv[i][0]), bflo(pv[i][1]), bfhi(pv[i][1])};
      o1 += f * f32x4{bflo(pv[i][2]), bfhi(pv[i][2]), bflo(pv[i][3]), bfhi(pv[i][3])};
    }
#pragma unroll
    for (int i = 0; i < 4; ++i) {
      auto t0 = __builtin_amdgcn_permlane32_swap(asu(o0[i]), asu(o0[i]), false, false); o0[i] = asf(t0[0]) + asf(t0[1]);
      auto t1 = __builtin_amdgcn_permlane32_swap(asu(o1[i]), asu(o1[i]), false, false); o1[i] = asf(t1[0]) + asf(t1[1]);
    }
    if (lane < 32) *(LAS u32x4*)(L + OB_OFF + c * OB_STR + ((lane >> 2) & 1) * 256 + (lane >> 3) * 64 + p * 16) = u32x4{pk(o0[0], o0[1]), pk(o0[2], o0[3]), pk(o1[0], o1[1]), pk(o1[2], o1[3])};
    cw_add(W_OB, 1, lane);
    cw_wait(W_FULL, IT_UV + 4);
    u32x4 wf[16];
    {
      const u32 r0 = 8 * (g4 & 1) + q;
      const u32 lp0 = (8 * g4 + q) * 512 + ((((p >> 1) + 8 * c) ^ swz(r0)) << 4) + 8 * (p & 1);
      const u32 lp1 = (8 * g4 + q + 4) * 512 + ((((p >> 1) + 8 * c) ^ swz(r0 + 4)) << 4) + 8 * (p & 1);
#pragma unroll
      for (int i = 0; i < 16; ++i) wf[i] = ldtr2(L, (lp0 ^ ((i & 3) << 5)) + ((IT_UV + (i >> 2)) & 7) * SLOT, (lp1 ^ ((i & 3) << 5)) + ((IT_UV + (i >> 2)) & 7) * SLOT);
    }
    cw_wait(W_OB, 4);
    STAMP(11);
    f32x4 d[4] = {ZERO4, ZERO4, ZERO4, ZERO4};
#pragma unroll
    for (int ks = 0; ks < 4; ++ks) {
      const u32x4 of = ld128(L, OB_OFF + p * OB_STR + (c >> 1) * 256 + ks * 64 + g4 * 16);
#pragma unroll
      for (int t = 0; t < 4; ++t) d[t] = mfma(wf[ks * 4 + t], of, d[t]);
    }
    {
      const u32 vo = p * 2048 + (q >> 1) * 128 + g4 * 32 + (q & 1) * 16, so = WS_CG + ((hkv * 4 + b) * 1024 + c * 64) * 8;
      gst(rws, pick2(d[0], d[1], q, tag), vo, so); gst(rws, pick2(d[2], d[3], q, tag), vo + 256, so);
    }
    STAMP(12);
  }
  {
    cw_add(W_O2, 1, lane);
    cw_wait(W_O2, 4);
    const int rn = n ^ (lane >> 5);
    const u32 so = WS_AG + (hkv * 4 + c) * 16384 + (b & 1) * 512, ss = WS_AS + (hkv * 4 + c) * 2048 + (b & 1) * 16 + (rn < NREC ? rn : 0) * 128;
    const u32 lr = (4 + c) * SLOT, v0 = (lane & 31) * 16, v1 = v0 + (lane >> 5) * 1024;
    u32x4 wf[16], sv, ov[NPR];
    u32 todo = 0x5555u & ((1u << NREC) - 1u);
    {
      u32 ask = todo;
      while (cw_ld(W_FULL) < ALL && bud != 0u) {
        sv = gld(rws, ss, 0);
        const unsigned long long sb = __builtin_amdgcn_ballot_w64(tagx(sv, tag) != 0u);
        const u32 nw = ask & ~((u32)sb | (u32)(sb >> 32));
        for (u32 k = nw; k; k &= k - 1u) { const u32 i = __builtin_ctz(k); DMA(rws, lr + i * 512, i + 1 < NREC ? v1 : v0, so + i * 1024, 0, SC1); }
        ask &= ~nw;
        bud = bud < 16u ? 0u : bud - 16u;
        __builtin_amdgcn_s_sleep(CSLEEP);
      }
      todo = ask | 0x10000u;
    }
    read_w(L, wf, c, lane);
    STAMP(18);
    for (;;) {
      sv = gld(rws, ss, 0);
      for (u32 k = todo & 0xffffu; k; k &= k - 1u) { const u32 i = __builtin_ctz(k); DMA(rws, lr + i * 512, i + 1 < NREC ? v1 : v0, so + i * 1024, 0, SC1); }
      VMW(0);
      const unsigned long long sb = __builtin_amdgcn_ballot_w64(tagx(sv, tag) != 0u);
      if (todo & ~(((u32)sb | (u32)(sb >> 32)) & 0xffffu)) {
#pragma unroll
        for (int i = 0; i < NPR; ++i) ov[i] = ld128(L, lr + lane * 16 + i * 1024);
        u32 bad = 0;
#pragma unroll
        for (int i = 0; i < NPR; ++i) bad |= (tagx(ov[i], tag) != 0u) << (2 * i);
        if (allz(bad)) todo = 0u;
        else { bad |= __builtin_amdgcn_mov_dpp(bad, 0x128, 0xf, 0xf, true); bad |= __builtin_amdgcn_mov_dpp(bad, 0x124, 0xf, 0xf, true); bad |= __builtin_amdgcn_mov_dpp(bad, 0x122, 0xf, 0xf, true); bad |= __builtin_amdgcn_mov_dpp(bad, 0x121, 0xf, 0xf, true);
          todo = __builtin_amdgcn_readlane(bad, 0) | __builtin_amdgcn_readlane(bad, 16) | __builtin_amdgcn_readlane(bad, 32) | __builtin_amdgcn_readlane(bad, 48); }
      }
      if (todo == 0u && sb == 0ull) break;
      bud = bud < 16u ? 0u : bud - 16u;
      if (bud == 0u) break;
      asm volatile("" ::: "memory");
    }
    STAMP(19);
    const float ms = asf(sv[0]), w0 = ex2((ms - row_max(ms)) * CS) * asf(sv[2]), w = rn < NREC ? w0 : 0.f;
    f32x4 o[2] = {ZERO4, ZERO4};
    merge_rec<0>(o, ov, w);
    o[0] += o[1];
    const float rs = __builtin_amdgcn_rcpf(asf(__builtin_amdgcn_readfirstlane(asu(row_sum(w)))));
#pragma unroll
    for (int i = 0; i < 4; ++i) { auto t = __builtin_amdgcn_permlane32_swap(asu(o[0][i]), asu(o[0][i]), false, false); o[0][i] = (asf(t[0]) + asf(t[1])) * rs; }
    *(LAS u32x2*)(L + OB_OFF + c * OB_STR + (lane & 31) * 8) = u32x2{pk(o[0][0], o[0][1]), pk(o[0][2], o[0][3])};
    cw_add(W_PJ, 1, lane);
    project(L, wf, rws, yso, tag, c, lane, bud);
    STAMP(20);
  }
  STAMP_END();
}

__global__ void probe_kernel(const u32* d) {
  __shared__ float red[256]; __shared__ u32 t0s[256]; __shared__ float cnt[256];
  const unsigned long long st = __builtin_amdgcn_s_memrealtime();
  const int g = threadIdx.x;
  u32 t0 = d[g * 6 * 32 + 30];
  t0s[g] = t0; __syncthreads();
  for (int i = 0; i < 256; ++i) if ((int)(t0s[i] - t0) < 0) t0 = t0s[i];
  float v = 0, c = 0;
  for (int w = 0; w < 6; ++w) {
    if (5 >= 0 && w != 5) continue;
    if (2 >= 0 && ((g >> 3) & 3) != 2) continue;
    const u32* e = d + (g * 6 + w) * 32; const float f = (float)(e[29] - e[0]) / ((e[31] - e[30]) * 0.01f);
    if (e[14]) { v = fmaxf(v, (e[30] - t0) * 0.01f + (e[14] - e[0]) / f); c = 1; }
  }
  red[g] = v; cnt[g] = c; __syncthreads();
  float mx = 0, sum = 0, n = 0;
  for (int i = 0; i < 256; ++i) { mx = fmaxf(mx, red[i]); sum += red[i]; n += cnt[i]; }
  float t = sum / n;
  if (!(t >= 0.f && t < 200.f)) t = 200.f;
  const unsigned long long ticks = (unsigned long long)(t * 2000.f) + 5000ull;
  while (__builtin_amdgcn_s_memrealtime() - st < ticks) __builtin_amdgcn_s_sleep(1);
}
extern "C" void kernel_launch(void* const* d_in, const int* in_sizes, int n_in, void* d_out, int out_size, void* d_ws, size_t ws_size, hipStream_t stream) {
  Args a;
  for (int i = 0; i < 11; ++i) a.in[i] = d_in[i];
  a.out = d_out;
  a.ws = d_ws;
  mk_kernel<<<256, 384, 0, stream>>>(a);
  probe_kernel<<<1, 256, 0, stream>>>((const u32*)((char*)d_ws + WS_DIAG));
}
```

```cpp
#include <hip/hip_runtime.h>
#include <stdint.h>

#define LAS __attribute__((address_space(3)))
typedef unsigned u32;
typedef u32 u32x2 __attribute__((ext_vector_type(2)));
typedef u32 u32x4 __attribute__((ext_vector_type(4)));
typedef float f32x2 __attribute__((ext_vector_type(2)));
typedef float f32x4 __attribute__((ext_vector_type(4)));
typedef short s16x4 __attribute__((ext_vector_type(4)));
typedef __bf16 bf16x2 __attribute__((ext_vector_type(2)));
typedef __bf16 bf16x8 __attribute__((ext_vector_type(8)));
typedef __amdgpu_buffer_rsrc_t rsrc_t;

#define N0 37
#define N1 38
#define N2 40
constexpr int NSUM = N0 + N1 + N2, NU = 128 - NSUM, NREC = 3 + NU, NTICK = 4 * NU;
static_assert(NU >= 1 && NREC <= 16, "split");
#define LW_A 32
#define LW_B 28
#define HOLD_ST 1
#define POLL_A 9
#define POLL_A0 7
#define GATE1 8
#define GATE2 12
#define POLL_C (N1 + 2)
#define OUT_AUX 16
#define CODE_PF 1
#define LW_W 44
#define FSLEEP 16
#define POLL_B GATE1
#define POLL_H GATE2
#define CSLEEP 2
#define EARLY 0
#define STOUCH 1
#define AUX_K 2
#define AUX_V 2
#define AUX_C 2
#define AUX_W 2
static_assert(LW_A <= 32, "a ticket is read two items after it was drawn");

constexpr int SLOT = 18432, NS = 8, RING = SLOT * NS;
constexpr int CTL = RING;
constexpr int W_FULL = CTL, W_WF = CTL + 4, W_QA = CTL + 8, W_MRG = CTL + 12, W_HINT = CTL + 16, W_SGF = CTL + 20, W_OB = CTL + 24, W_BP = CTL + 28,
              W_FREE = CTL + 32, W_S = CTL + 64, W_GI = CTL + 80, W_QS = CTL + 84, W_PJ = CTL + 88, W_WI = CTL + 92, W_MF = CTL + 96, W_O2 = CTL + 100, W_TK = CTL + 128;
constexpr int ST_OFF = CTL + 256;
constexpr int OB_OFF = ST_OFF + 512, OB_STR = 576;
constexpr int SB_OFF = OB_OFF + 4 * OB_STR;
constexpr int QA_OFF = SB_OFF + 5120;
constexpr int MG_OFF = SB_OFF + 8192;
constexpr int MS_OFF = MG_OFF + 4096;
constexpr int LDS_BYTES = MS_OFF + 64;
static_assert(LDS_BYTES <= 163840 && (SB_OFF & 15) == 0 && (MG_OFF & 15) == 0, "lds");

constexpr u32 WS_EPOCH = 0, WS_HINT = 1024;
constexpr u32 WS_TK = 2048;
constexpr u32 WS_QG = 4096;
constexpr u32 WS_QAG = WS_QG + 2 * 4 * 2048 * 8;
constexpr u32 WS_AG = WS_QAG + 4 * 32 * 256 * 8;
constexpr u32 WS_AS = WS_AG + 64 * 16 * 1024;
constexpr u32 WS_SG = WS_AS + 64 * 2048;
constexpr u32 WS_CG = (2u << 20) + 4096;
constexpr u32 WS_YG = WS_CG + 16 * 4 * 4 * 256 * 8;
constexpr u32 WS_PB = (4u << 20) + 4096;
constexpr u32 WS_END = WS_PB + 4 * 16 * 4 * 16 * 512;
constexpr u32 WS_DIAG = 8u << 20;
static_assert(WS_SG + 32768 <= (2u << 20) && WS_YG + 2 * 32 * 18432 <= (4u << 20) && WS_END <= WS_DIAG, "ws");

constexpr float CS = 0.08838834764831845f * 1.4426950408889634f;
constexpr int SC1 = 16;
constexpr u32 CODE_BYTES = 25600;
constexpr u32 SPIN_BUDGET = 1u << 19;

struct Args { const void* in[11]; void* out; void* ws; };

__device__ __forceinline__ rsrc_t mkrs(const void* p, u32 n) { return __builtin_amdgcn_make_buffer_rsrc((void*)p, 0, n, 0x00020000); }
constexpr __device__ __forceinline__ u32 swz(u32 r) { return (((r & 7u) ^ ((r >> 1) & 4u)) << 1) | ((r >> 3) & 1u); }
__device__ __forceinline__ f32x4 mfma(u32x4 a, u32x4 b, f32x4 c) {
  return __builtin_amdgcn_mfma_f32_16x16x32_bf16(__builtin_bit_cast(bf16x8, a), __builtin_bit_cast(bf16x8, b), c, 0, 0, 0);
}
__device__ __forceinline__ u32 pk(float lo, float hi) { f32x2 v = {lo, hi}; return __builtin_bit_cast(u32, __builtin_convertvector(v, bf16x2)); }
__device__ __forceinline__ float bflo(u32 v) { return __builtin_bit_cast(float, v << 16); }
__device__ __forceinline__ float bfhi(u32 v) { return __builtin_bit_cast(float, v & 0xffff0000u); }
__device__ __forceinline__ float asf(u32 v) { return __builtin_bit_cast(float, v); }
__device__ __forceinline__ u32 asu(float v) { return __builtin_bit_cast(u32, v); }
__device__ __forceinline__ float ex2(float x) { return __builtin_amdgcn_exp2f(x); }
__device__ __forceinline__ u32x4 ld128(LAS unsigned char* L, u32 a) { return *(LAS u32x4*)(L + a); }
__device__ __forceinline__ u32x4 ldtr2(LAS unsigned char* L, u32 a0, u32 a1) {
  const u32x2 x = __builtin_bit_cast(u32x2, __builtin_amdgcn_ds_read_tr16_b64_v4i16((LAS s16x4*)(L + a0)));
  const u32x2 y = __builtin_bit_cast(u32x2, __builtin_amdgcn_ds_read_tr16_b64_v4i16((LAS s16x4*)(L + a1)));
  return u32x4{x[0], x[1], y[0], y[1]};
}
__device__ __forceinline__ float xg_max(float x) {
  auto s = __builtin_amdgcn_permlane16_swap(asu(x), asu(x), false, false);
  x = fmaxf(asf(s[0]), asf(s[1]));
  auto t = __builtin_amdgcn_permlane32_swap(asu(x), asu(x), false, false);
  return fmaxf(asf(t[0]), asf(t[1]));
}
__device__ __forceinline__ float xg_sum(float x) {
  auto s = __builtin_amdgcn_permlane16_swap(asu(x), asu(x), false, false);
  x = asf(s[0]) + asf(s[1]);
  auto t = __builtin_amdgcn_permlane32_swap(asu(x), asu(x), false, false);
  return asf(t[0]) + asf(t[1]);
}
template <int CTRL> __device__ __forceinline__ float dpp(float x) { return asf(__builtin_amdgcn_mov_dpp(asu(x), CTRL, 0xf, 0xf, true)); }
__device__ __forceinline__ float row_sum(float x) { x += dpp<0x128>(x); x += dpp<0x124>(x); x += dpp<0x122>(x); x += dpp<0x121>(x); return x; }
__device__ __forceinline__ float row_max(float x) { x = fmaxf(x, dpp<0x128>(x)); x = fmaxf(x, dpp<0x124>(x)); x = fmaxf(x, dpp<0x122>(x)); x = fmaxf(x, dpp<0x121>(x)); return x; }

__device__ __forceinline__ u32 cw_ld(u32 a) { u32 v; asm volatile("ds_read_b32 %0, %1\n\ts_waitcnt lgkmcnt(0)" : "=v"(v) : "v"(a) : "memory"); return __builtin_amdgcn_readfirstlane(v); }
__device__ __forceinline__ void cw_st(u32 a, u32 v) { asm volatile("ds_write_b32 %0, %1" ::"v"(a), "v"(v) : "memory"); }
#define cw_full(v) cw_st(W_FULL, (v))
__device__ __forceinline__ void cw_add(u32 a, u32 v, int lane) { if (lane == 0) asm volatile("ds_add_u32 %0, %1" ::"v"(a), "v"(v) : "memory"); asm volatile("" ::: "memory"); }
__device__ __forceinline__ u32 cw_add_rtn(u32 a, u32 v, int lane) {
  u32 r = 0;
  if (lane == 0) asm volatile("ds_add_rtn_u32 %0, %1, %2\n\ts_waitcnt lgkmcnt(0)" : "=v"(r) : "v"(a), "v"(v) : "memory");
  asm volatile("" ::: "memory");
  return __builtin_amdgcn_readfirstlane(r);
}
__device__ __forceinline__ u32 cw_wait_(u32 a, u32 want, u32& bud) {
  u32 t;
  bud = __builtin_amdgcn_readfirstlane(bud); want = __builtin_amdgcn_readfirstlane(want);
  asm volatile("1:\n\tds_read_b32 v255, %2\n\ts_waitcnt lgkmcnt(0)\n\tv_readfirstlane_b32 %0, v255\n\ts_cmp_ge_u32 %0, %3\n\ts_cbranch_scc1 2f\n\t"
               "s_sleep 1\n\ts_sub_u32 %1, %1, 1\n\ts_cbranch_scc0 1b\n\ts_mov_b32 %1, 0\n2:\n\ts_nop 1"
               : "=&s"(t), "+s"(bud) : "v"(a), "s"(want) : "memory", "scc", "v255");
  return t;
}
#define cw_wait(a, want) cw_wait_((a), (want), bud)
__device__ __forceinline__ bool glb_retry_(u32& bud) {
  bud = __builtin_amdgcn_readfirstlane(bud);
  asm volatile("s_sleep 1\n\ts_sub_u32 %0, %0, 16\n\ts_cselect_b32 %0, 0, %0" : "+s"(bud) :: "memory", "scc");
  return bud != 0u;
}
#define glb_retry() glb_retry_(bud)
__device__ __forceinline__ u32x4 gld(rsrc_t r, u32 voff, u32 soff) { return __builtin_amdgcn_raw_buffer_load_b128(r, voff, soff, SC1); }
__device__ __forceinline__ u32x4 gld0(rsrc_t r, u32 voff, u32 soff) { return __builtin_amdgcn_raw_buffer_load_b128(r, voff, soff, 0); }
__device__ __forceinline__ void gst(rsrc_t r, u32x4 v, u32 voff, u32 soff) { __builtin_amdgcn_raw_buffer_store_b128(v, r, voff + soff, 0, SC1); }
__device__ __forceinline__ u32 tagx(u32x4 v, u32 tag) { return (v[1] ^ tag) | (v[3] ^ tag); }
__device__ __forceinline__ bool allz(u32 x) { return __builtin_amdgcn_ballot_w64(x != 0u) == 0ull; }
template <int N> __device__ __forceinline__ void fetch(LAS unsigned char* L, rsrc_t r, u32 voff, u32 ss, u32 tag, u32 dst, u32 ds, u32 flag, u32& bud) {
  u32x4 v[N];
  for (;;) {
    u32 bad = 0;
#pragma unroll
    for (int i = 0; i < N; ++i) v[i] = gld(r, voff + i * ss, 0);
#pragma unroll
    for (int i = 0; i < N; ++i) bad |= tagx(v[i], tag);
    if (allz(bad) || !glb_retry()) break;
  }
#pragma unroll
  for (int i = 0; i < N; ++i) *(LAS u32x2*)(L + dst + i * ds) = u32x2{v[i][0], v[i][2]};
  cw_st(flag, 1u);
}
#define DMA(r, ldsoff, voff, soff, imm, aux) __builtin_amdgcn_raw_ptr_buffer_load_lds((r), (LAS void*)(L + (ldsoff)), 16, (voff), (soff), (imm), (aux))

#define STAMP(i) do {} while (0)
#define STAMP_BEGIN() do {} while (0)
#define STAMP_END() do {} while (0)

#define VMW(n) asm volatile("s_waitcnt vmcnt(" #n ")" ::: "memory")
#define ZERO4 f32x4{0.f, 0.f, 0.f, 0.f}
#define MAX8(a, b) fmaxf(fmaxf(fmaxf(a[0], a[1]), fmaxf(a[2], a[3])), fmaxf(fmaxf(b[0], b[1]), fmaxf(b[2], b[3])))
constexpr float LAZY = 192.f;
template <int NA> __device__ __forceinline__ u32x4 soft(const f32x4 s0, const f32x4 s1, float& m, float& l, f32x4* acc) {
  const float mx = MAX8(s0, s1);
  if (__builtin_amdgcn_ballot_w64(mx > m + LAZY) != 0ull) {
    const float mn = fmaxf(m, xg_max(mx)), al = ex2((m - mn) * CS);
    m = mn; l *= al;
#pragma unroll
    for (int i = 0; i < NA; ++i) acc[i] *= al;
  }
  const float mc = -m * CS;
  float pr[8];
#pragma unroll
  for (int i = 0; i < 4; ++i) { pr[i] = ex2(fmaf(s0[i], CS, mc)); pr[4 + i] = ex2(fmaf(s1[i], CS, mc)); }
  l += ((pr[0] + pr[1]) + (pr[2] + pr[3])) + ((pr[4] + pr[5]) + (pr[6] + pr[7]));
  return u32x4{pk(pr[0], pr[1]), pk(pr[2], pr[3]), pk(pr[4], pr[5]), pk(pr[6], pr[7])};
}
__device__ __forceinline__ u32x4 pick2(const f32x4 d0, const f32x4 d1, int q, u32 tag) {
  const float x0 = (q & 2) ? d1[0] : d0[0], x1 = (q & 2) ? d1[1] : d0[1], x2 = (q & 2) ? d1[2] : d0[2], x3 = (q & 2) ? d1[3] : d0[3];
  return u32x4{asu((q & 1) ? x2 : x0), tag, asu((q & 1) ? x3 : x1), tag};
}

constexpr int NPR = (NREC + 1) / 2;
template <int I> __device__ __forceinline__ void merge_rec(f32x4* o, const u32x4* t, float w) {
  if constexpr (I < NPR) { o[I & 1] += dpp<0x150 + 2 * I>(w) * f32x4{bflo(t[I][0]), bfhi(t[I][0]), bflo(t[I][2]), bfhi(t[I][2])}; merge_rec<I + 1>(o, t, w); }
}

constexpr int IT_UK = 0, IT_C = 4, IT_UV = 20, IT_W = 24;
constexpr u32 ALL = 1u << 20;
template <int N> __device__ __forceinline__ void vmw() { asm volatile("s_waitcnt vmcnt(%0)" ::"i"(N) : "memory"); }

__device__ __forceinline__ void read_w(LAS unsigned char* L, u32x4* wf, int c, int lane) {
  const u32 lp = (8 * (lane >> 4) + ((lane >> 2) & 3)) * 576 + 8 * (lane & 3) + 128 * c, t2 = c < 4 ? 64 : 0;
#pragma unroll
  for (int i = 0; i < 16; ++i) { const u32 ad = (i >> 2) * SLOT + lp + (i & 1) * 32 + ((i & 2) ? t2 : 0); wf[i] = ldtr2(L, ad, ad + 2304); }
}
__device__ __forceinline__ void project(LAS unsigned char* L, const u32x4* wf, rsrc_t rws, u32 so, u32 tag, int c, int lane, u32& bud) {
  const int g4 = lane >> 4, q = (lane >> 2) & 3, p = lane & 3;
  u32 ao = OB_OFF + p * OB_STR + g4 * 16, vo = so + c * 512 + p * 4608 + (q >> 1) * 128 + g4 * 32 + (q & 1) * 16;
  asm volatile("" : "+v"(ao), "+v"(vo));
  cw_wait(W_PJ, 4);
  f32x4 d[4] = {ZERO4, ZERO4, ZERO4, ZERO4};
  u32x4 of[4];
#pragma unroll
  for (int ks = 0; ks < 4; ++ks) of[ks] = ld128(L, ao + ks * 64);
  __builtin_amdgcn_sched_barrier(0);
#pragma unroll
  for (int ks = 0; ks < 4; ++ks) { d[0] = mfma(wf[ks * 4], of[ks], d[0]); d[1] = mfma(wf[ks * 4 + 1], of[ks], d[1]); }
#pragma unroll
  for (int ks = 0; ks < 4; ++ks) { d[2] = mfma(wf[ks * 4 + 2], of[ks], d[2]); d[3] = mfma(wf[ks * 4 + 3], of[ks], d[3]); }
  gst(rws, pick2(d[0], d[1], q, tag), vo, 0);
  if (c < 4) gst(rws, pick2(d[2], d[3], q, tag), vo + 256, 0);
}
__device__ __forceinline__ void final_sum(rsrc_t rws, void* out, int br, int hkv, int b, int i, u32 tag, int lane, u32& bud) {
  const int c0 = 36 * hkv + 8 * i + 2 * (lane >> 5);
  u32 vo = WS_YG + (br * 32 + (lane & 31)) * 18432 + (b * 576 + c0) * 8, oo = (b * 1152 + br * 576 + c0 + (lane & 1) * 4) * 2;
  const u32 v2 = i < 4 ? 32 : 0;
  const rsrc_t ro = mkrs(out, 4u * 1152u * 2u);
  asm volatile("" : "+v"(vo), "+v"(oo));
  u32x4 y, z;
  for (;;) { y = gld(rws, vo, 0); z = gld(rws, vo, v2); if (allz(tagx(y, tag) | tagx(z, tag)) || !glb_retry()) break; }
  float r[4] = {asf(y[0]), asf(y[2]), asf(z[0]), asf(z[2])};
#pragma unroll
  for (int k = 0; k < 4; ++k) { auto t = __builtin_amdgcn_permlane16_swap(asu(r[k]), asu(r[k]), false, false); r[k] = row_sum(asf(t[0]) + asf(t[1])); }
  if ((lane & 31) < (i < 4 ? 2 : 1)) __builtin_amdgcn_raw_buffer_store_b32((lane & 1) ? pk(r[2], r[3]) : pk(r[0], r[1]), ro, oo, 0, OUT_AUX);
}

__global__ void __launch_bounds__(384, 2) mk_kernel(Args a) {
  __shared__ __attribute__((aligned(16))) unsigned char smem[LDS_BYTES];
  LAS unsigned char* L = (LAS unsigned char*)smem;
  const int lane = threadIdx.x & 63, wave = __builtin_amdgcn_readfirstlane(threadIdx.x >> 6);
  const int g = blockIdx.x, gi = g >> 3, hkv = 2 * (g & 7) + (gi >> 4), j = gi & 15, b = j >> 2, s = j & 3;
  STAMP_BEGIN();
  if (wave == 1) {
    cw_st(CTL + lane * 4, 0u);
    unsigned long long pc; asm volatile("s_getpc_b64 %0" : "=s"(pc));
    DMA(mkrs((const void*)(pc & ~255ull), CODE_BYTES), MG_OFF, lane * 16, gi * 1024, 0, 0);
    asm volatile("s_waitcnt lgkmcnt(0)" ::: "memory");
  }
  __builtin_amdgcn_s_barrier();
  const u32 vu = (lane >> 5) * 8192 + (((lane & 31) ^ ((lane >> 5) << 1)) << 4);
#define LOAD_W(in_, LW) do { const rsrc_t r = mkrs(a.in[in_], 4096u * 1152u); u32 wo[9]; int ln = lane; asm volatile("" : "+v"(ln)); \
      _Pragma("unroll") for (int i = 0; i < 9; ++i) { const u32 ci = i * 64 + ln; wo[i] = ci * 16 + ((ci * 3641u) >> 17) * 576u; } \
      u32 sw = ((u32)hkv * 256u + (b & 1) * 128u) * 1152u + (b >> 1) * 576u; \
      _Pragma("unroll 1") for (int t = 0; t < 8; ++t, sw += 16u * 1152u) { _Pragma("unroll") for (int i = 0; i < 9; ++i) { if (i % 3 == 0) vmw<LW>(); DMA(r, t * 9216 + i * 1024, wo[i], sw, 0, AUX_W); } } } while (0)
  if (wave == 5) {
    if (s != 1) {
      const rsrc_t rw = mkrs(a.in[s == 0 ? 7 : s == 2 ? 3 : 8], s < 3 ? 576u * 8192u : 512u * 8192u), rx = mkrs(a.in[s ? 0 : 5], s < 3 ? 4u * 576u * 2u : 0u);
      const u32 vo = s < 3 ? ((lane >> 2) & 7) * 8192 + (lane & 3) * 16 + (lane >> 5) * 64 : vu, st = s < 3 ? 8 * 8192 : 2 * 8192, xm = s == 3 ? 0xf0u : 0u;
      u32 so = hkv * 512 + (s < 3 ? b * 128 : b * (128u * 8192u)), lo = 0;
#pragma unroll 1
      for (int t = 0; t < (s < 3 ? 9 : 8); ++t) {
        vmw<40>();
        if (t == 7) cw_st(W_WF, 1u);
#pragma unroll
        for (int i = 0; i < 8; ++i, so += st) DMA(rw, lo + i * 1024, vo ^ ((swz(2 * i) << 4) & xm), so, 0, AUX_W);
        lo += s == 3 && (t & 1) ? SLOT - 8192 : 8192;
        if (t == 1 && s < 3) {
#pragma unroll
          for (int i = 0; i < 5; ++i) DMA(rx, SB_OFF + i * 1024, lane * 16, i * 1024, 0, 0);
        }
      }
    } else {
      LOAD_W(10, 40);
    }
    STAMP(14);
    VMW(32); cw_st(W_WF, 2u); cw_st(W_WI, 1u);
    VMW(16); cw_st(W_WF, 3u);
    VMW(0); cw_st(W_WF, 4u);
  }
  const int ab = gi >> 3, ah = ((g & 7) + 2 * (gi & 4) + 3 * (ab + 4 * s)) & 15, as = s;
  const int ITU = 4 + (s == 0 ? N0 : s == 1 ? N1 : N2);
  const int bq = (g & 7) >> 1, sp = (g & 1) * 8 + (gi >> 2);
  u32 bud = SPIN_BUDGET;

  if (wave == 0) {
    __builtin_amdgcn_s_setprio(3);
    {
      const char* p0 = s < 3 ? (const char*)a.in[1] + ((size_t)(ab * 16 + ah) << 20) + s * 344064 : (const char*)a.in[6] + ((size_t)(bq * 16 + sp) << 18);
      const char* p1 = s < 3 ? (const char*)a.in[2] + ((size_t)(ab * 16 + ah) << 20) + s * 344064 : (const char*)a.in[9] + ((size_t)b << 20);
      u32 d0, d1;
      asm volatile("s_load_dword %0, %2, 0x0\n\ts_load_dword %1, %3, 0x0\n\ts_waitcnt lgkmcnt(0)" : "=&s"(d0), "=&s"(d1) : "s"(p0), "s"(p1) : "memory");
    }
    u32 vq[4];
#pragma unroll
    for (int q = 0; q < 4; ++q) vq[q] = (lane >> 4) * 256 + (((lane & 15) ^ swz(4 * q + (lane >> 4))) << 4);
    int pub = 0, it = 4;
#define SLOT_WAIT() do { if (it >= NS) { const u32 need = 4u * (it >> 3), wa = W_FREE + (it & 7) * 4; \
      if (cw_ld(wa) < need) { VMW(16); if (it - 1 > pub) cw_full(it - 1); VMW(0); pub = it; cw_full(pub); cw_wait(wa, need); } } } while (0)
#define LSTAMP() do { if (it == 4) STAMP(2); if (it == 8) STAMP(3); if (it == 12) STAMP(4); if (it == 16) STAMP(5); if (it == 20) STAMP(6); if (it == 24) STAMP(7); if (it == 28) STAMP(8); if (it == 32) STAMP(9); if (it == 36) STAMP(10); if (it == 40) STAMP(11); if (it == 44) STAMP(12); } while (0)
#define PACE(LW, k) do { vmw<LW>(); if ((k) == ((LW / 4) & 3) && it - LW / 16 > pub) { pub = it - LW / 16; cw_full(pub); } } while (0)
    asm volatile("" : "+s"(it));
    if (s < 3) {
      const rsrc_t rk = mkrs(a.in[1], 1u << 26), rv = mkrs(a.in[2], 1u << 26), rt = mkrs((const char*)a.ws + WS_TK + ah * 128, 4u);
      u32 so = ((u32)(ab * 16 + ah) * 128u + (s == 0 ? 0 : s == 1 ? N0 : N0 + N1)) * 8192u;
      asm volatile("v_mov_b32 v254, 1" ::: "v254");
#pragma unroll 1
      for (;; ++it) {
        const u32 sb = (it & 7) * SLOT;
        if (it == 4 + EARLY) { if (s == 0 && HOLD_ST) cw_wait(W_QS, 4); else cw_wait(W_WI, 1); }
        SLOT_WAIT();
        LSTAMP();
        PACE(LW_A, 0);
        if (it >= ITU) {
          u32 t;
          asm volatile("v_readlane_b32 %0, v254, %1\n\tv_writelane_b32 v254, 1, %1\n\ts_nop 4" : "=&s"(t) : "s"(it & 3) : "memory", "v254");
          if (t >= (u32)NTICK) break;
          cw_st(W_TK + (it & 15) * 4, (it << 8) | t);
          so = (((t & 3u) * 16u + ah) * 128u + NSUM + (t >> 2)) * 8192u;
        }
        if (it >= ITU - 2) {
          unsigned long long ex;
          asm volatile("s_mov_b64 %0, exec\n\ts_lshl_b64 exec, 1, %2\n\tbuffer_atomic_add v254, off, %1, 0 sc0\n\ts_mov_b64 exec, %0"
                       : "=&s"(ex) : "s"(rt), "s"((it + 2) & 3) : "memory", "scc", "v254");
        }
#pragma unroll
        for (int k = 0; k < 2; ++k) {
          if (k) PACE(LW_A, 1);
          DMA(rk, sb + k * 4096, vq[0], so + k * 4096, 0, AUX_K); DMA(rk, sb + k * 4096, vq[1], so + k * 4096, 1024, AUX_K);
          DMA(rk, sb + k * 4096, vq[2], so + k * 4096, 2048, AUX_K); DMA(rk, sb + k * 4096, vq[3], so + k * 4096, 3072, AUX_K);
        }
#pragma unroll
        for (int k = 0; k < 2; ++k) {
          PACE(LW_A, k + 2);
          DMA(rv, sb + 8192 + k * 4096, vq[0], so + k * 4096, 0, AUX_V); DMA(rv, sb + 8192 + k * 4096, vq[1], so + k * 4096, 1024, AUX_V);
          DMA(rv, sb + 8192 + k * 4096, vq[2], so + k * 4096, 2048, AUX_V); DMA(rv, sb + 8192 + k * 4096, vq[3], so + k * 4096, 3072, AUX_V);
        }
        so += 8192u;
      }
      cw_st(W_TK + ((it + (lane & 3)) & 15) * 4, ((it + (lane & 3)) << 8) | 255u);
      STAMP(13);
    } else {
#pragma unroll 1
      for (; it < IT_W; ++it) {
        const u32 sb = (it & 7) * SLOT;
        if (it == 4 + EARLY) cw_wait(W_WI, 1);
        if (it == GATE1 || it == GATE2) {
          VMW(16); if (it - 1 > pub) cw_full(it - 1);
          VMW(0); pub = it; cw_full(it);
          if (it == GATE1) cw_wait(W_QS, 5);
          if (it == GATE2) cw_wait(W_GI, 4);
        }
        SLOT_WAIT();
        LSTAMP();
        if (it < IT_UV) {
          const rsrc_t r = mkrs(a.in[6], 1u << 24);
          const u32 so = ((u32)bq * 4096u + sp * 256u + (it - IT_C) * 16u) * 1024u;
#pragma unroll
          for (int k = 0; k < 4; ++k) {
            PACE(LW_B, k);
            DMA(r, sb + k * 4096, (lane ^ swz(4 * k + 0)) << 4, so + k * 4096, 0, AUX_C);
            DMA(r, sb + k * 4096, (lane ^ swz(4 * k + 1)) << 4, so + k * 4096, 1024, AUX_C);
            DMA(r, sb + k * 4096, (lane ^ swz(4 * k + 2)) << 4, so + k * 4096, 2048, AUX_C);
            DMA(r, sb + k * 4096, (lane ^ swz(4 * k + 3)) << 4, so + k * 4096, 3072, AUX_C);
          }
        } else {
          const rsrc_t r = mkrs(a.in[9], 512u * 8192u);
          const u32 so = ((u32)b * 128u + (it & 3) * 32u) * 8192u + hkv * 512u;
#pragma unroll
          for (int i = 0; i < 16; ++i) { if ((i & 3) == 0) PACE(LW_B, i >> 2); DMA(r, sb + i * 1024, vu ^ (swz((2 * i) & 15) << 4), so + i * (2 * 8192), 0, AUX_W); }
        }
      }
      VMW(0); pub = it; cw_full(it);
      cw_wait(W_GI, 8);
      STAMP(15);
      LOAD_W(4, LW_W);
    }
    VMW(16); if (it - 1 > pub) cw_full(it - 1);
    VMW(0); cw_full(ALL);
    STAMP(1);
    STAMP_END();
    return;
  }

  const int c = wave - 1, g4 = lane >> 4, n = lane & 15, q = n >> 2, p = lane & 3;
  const rsrc_t rws = mkrs(a.ws, WS_END);
  const char* wsp = (const char*)a.ws; asm volatile("" : "+s"(wsp));
  const u32 tag = *(const __attribute__((address_space(4))) u32*)(wsp + WS_EPOCH) + 1u;
  asm volatile("" ::"s"(tag));
  VMW(0);
  const u32 yso = WS_YG + ((s == 1 ? 32 : 0) + hkv * 2 + (b & 1)) * 18432 + (b >> 1) * 2304;
  if (c == 4) {
    u32x4 wf[16];
    if (s == 1) {
      read_w(L, wf, 4, lane);
      cw_st(W_MF, 1u);
    }
    cw_wait(W_FULL, s == 3 ? POLL_B : s == 0 ? POLL_A0 : POLL_A);
    STAMP(1);
    fetch<4>(L, rws, lane * 16 + WS_QG + (s < 3 ? ah * 128 : 8192 + hkv * 128) * 8, 16384, tag, (s < 3 ? QA_OFF : OB_OFF) + lane * 8, s < 3 ? 512 : OB_STR, W_QA, bud);
    STAMP(3);
    if (s == 3) {
      cw_wait(W_QS, 5); cw_wait(W_FULL, POLL_H);
      fetch<1>(L, rws, lane * 16 + WS_HINT, 0, tag, MG_OFF + lane * 8, 0, W_HINT, bud);
      STAMP(4);
      cw_wait(W_BP, 4);
      fetch<4>(L, rws, n * 32 + (g4 & 1) * 16 + WS_SG + hkv * 2048, 512, tag, MG_OFF + lane * 8, 512, W_SGF, bud);
      STAMP(2);
      cw_wait(W_FULL, ALL);
      read_w(L, wf, 4, lane);
    } else if (s == 1) {
      cw_wait(W_FULL, POLL_C);
      const u32 so = WS_CG + hkv * 32768 + (b & 1) * 1024;
      for (;;) {
        const u32x4 t = gld(rws, q * 8192 + (p & 1) * 512, so);
        if (allz(tagx(t, tag)) || !glb_retry()) break;
        __builtin_amdgcn_s_sleep(16);
      }
      u32x4 cv[16];
      for (;;) {
        u32 bad = 0;
#pragma unroll
        for (int i = 0; i < 16; ++i) cv[i] = gld(rws, lane * 16 + (i & 1) * 2048, so + (i >> 1) * 4096);
#pragma unroll
        for (int i = 0; i < 16; ++i) bad |= tagx(cv[i], tag);
        if (allz(bad) || !glb_retry()) break;
      }
#pragma unroll
      for (int i = 0; i < 4; ++i)
        *(LAS u32*)(L + OB_OFF + i * OB_STR + lane * 4) = pk((asf(cv[i][0]) + asf(cv[4 + i][0])) + (asf(cv[8 + i][0]) + asf(cv[12 + i][0])), (asf(cv[i][2]) + asf(cv[4 + i][2])) + (asf(cv[8 + i][2]) + asf(cv[12 + i][2])));
      cw_st(W_PJ, 4u);
      STAMP(2);
    }
    if (s & 1) {
      project(L, wf, rws, yso, tag, 4, lane, bud);
    } else {
      cw_wait(W_O2, 4);
      __builtin_amdgcn_s_sleep(FSLEEP);
      final_sum(rws, a.out, s == 0, hkv, b, 4, tag, lane, bud);
      if (g == 16 && lane == 0) __builtin_amdgcn_raw_buffer_store_b32(tag, rws, 0, WS_EPOCH, SC1);
      STAMP(22);
    }
    STAMP_END();
    return;
  }

  if (s < 3) {
    u32x4 wf[16];
    if (s == 1) {
      cw_wait(W_WF, 4);
      read_w(L, wf, c, lane);
      cw_wait(W_MF, 1);
      if (lane < 4) asm volatile("ds_add_u32 %0, %1" ::"v"(W_FREE + lane * 4), "v"(1u) : "memory");
    } else {
      const u32 aw = g4 * 1024 + (c >> 1) * 512 + q * 64 + (c & 1) * 32 + 8 * p, ax = SB_OFF + p * 1152 + g4 * 16;
      const u32 vo = (n * 2048 + 2 * g4) * 8, so = WS_QG + ((s ? 0 : 8192) + hkv * 128 + b * 32 + 8 * c) * 8;
      if (j == 2 && c == 0) { __builtin_amdgcn_raw_buffer_store_b32(0u, rws, WS_TK + hkv * 128, 0, SC1); VMW(0); }
      cw_wait(W_WF, 2);
      u32x4 xr[18], wq[9];
#pragma unroll
      for (int ks = 0; ks < 18; ++ks) xr[ks] = ld128(L, ax + ks * 64);
      f32x4 a0 = ZERO4, a1 = ZERO4;
#pragma unroll
      for (int hf = 0; hf < 2; ++hf) {
        if (hf) { cw_wait(W_WF, 4); STAMP(1); }
#pragma unroll
        for (int ks = 0; ks < 9; ++ks) wq[ks] = ldtr2(L, aw + (hf * 9 + ks) * 4096, aw + (hf * 9 + ks) * 4096 + 256);
        if (lane < 2) asm volatile("ds_add_u32 %0, %1" ::"v"(W_FREE + hf * 8 + lane * 4), "v"(1u) : "memory");
        __builtin_amdgcn_sched_barrier(0);
#pragma unroll
        for (int ks = 0; ks < 9; ++ks) { if (ks & 1) a1 = mfma(wq[ks], xr[hf * 9 + ks], a1); else a0 = mfma(wq[ks], xr[hf * 9 + ks], a0); }
        __builtin_amdgcn_sched_barrier(0);
      }
      a0 += a1;
      if (n < 4) gst(rws, u32x4{pk(a0[0], a0[1]), tag, pk(a0[2], a0[3]), tag}, vo, so);
      cw_add(W_QS, 1, lane);
      STAMP(2);
    }
    float m = -1e30f, l = 0.f;
    f32x4 acc[8];
#pragma unroll
    for (int i = 0; i < 8; ++i) acc[i] = ZERO4;
    u32x4 qf[4], kf[8], vf[8];
    const u32 lpK = n * 256 + ((g4 ^ swz(n)) << 4), lpQ = QA_OFF + (n & 1) * 256 + g4 * 16;
    const u32 lpV = (4 * g4 + q) * 256 + (((p >> 1) ^ swz(4 * g4 + q)) << 4) + 8 * (p & 1);
#define READ_K() _Pragma("unroll") for (int kk = 0; kk < 4; ++kk) { const u32 ak = (sb + lpK) ^ (kk << 6); kf[kk] = ld128(L, ak); kf[4 + kk] = ld128(L, ak + 4096); }
#define READ_V() do { _Pragma("unroll") for (int ct = 0; ct < 8; ++ct) { const u32 av = (sb + lpV) ^ (ct << 5); vf[ct] = ldtr2(L, av + 8192, av + 12288); } \
      cw_add(W_FREE + (it & 7) * 4, 4, lane); } while (0)
#define SCORES() f32x4 s0 = ZERO4, s1 = ZERO4; \
      _Pragma("unroll") for (int kk = 0; kk < 4; ++kk) { s0 = mfma(kf[kk], qf[kk], s0); s1 = mfma(kf[4 + kk], qf[kk], s1); }
#define PICK8(e) const f32x4 e##0 = (n & 2) ? acc[1] : acc[0], e##1 = (n & 2) ? acc[3] : acc[2], e##2 = (n & 2) ? acc[5] : acc[4], e##3 = (n & 2) ? acc[7] : acc[6], \
      e##4 = (n & 4) ? e##1 : e##0, e##5 = (n & 4) ? e##3 : e##2, e = (n & 8) ? e##5 : e##4
    const u32 vrec = (n & 1) * 512 + (n >> 1) * 64 + g4 * 16;
    bool first = true;
    int it = 4 + c;
#pragma unroll 1
    for (; it < ITU; it += 4) {
      const u32 sb = (it & 7) * SLOT;
      cw_wait(W_FULL, it + 1);
      READ_K(); READ_V();
      if (first) {
        first = false;
        cw_wait(W_QA, 1);
        STAMP(3);
#pragma unroll
        for (int kk = 0; kk < 4; ++kk) qf[kk] = ld128(L, lpQ + ab * 512 + kk * 64);
      }
      SCORES();
      const u32x4 pf = soft<8>(s0, s1, m, l, acc);
#pragma unroll
      for (int ct = 0; ct < 8; ++ct) acc[ct] = mfma(vf[ct], pf, acc[ct]);
    }
    STAMP(8);
    {
      l = xg_sum(l);
      PICK8(e);
      *(LAS f32x4*)(L + MG_OFF + c * 1024 + lane * 16) = e;
      if (lane < 2) *(LAS f32x2*)(L + MS_OFF + (c * 2 + lane) * 8) = f32x2{m, l};
      if (cw_add_rtn(W_MRG, 1, lane) == 3u) {
        f32x2 sv[4]; f32x4 ov[4];
#pragma unroll
        for (int i = 0; i < 4; ++i) { sv[i] = *(LAS f32x2*)(L + MS_OFF + (i * 2 + (n & 1)) * 8); ov[i] = *(LAS f32x4*)(L + MG_OFF + i * 1024 + lane * 16); }
        const float M = fmaxf(fmaxf(sv[0][0], sv[1][0]), fmaxf(sv[2][0], sv[3][0]));
        float Ls = 0.f; f32x4 o = ZERO4;
#pragma unroll
        for (int i = 0; i < 4; ++i) { const float w = ex2((sv[i][0] - M) * CS); Ls = fmaf(w, sv[i][1], Ls); o += w * ov[i]; }
        o *= __builtin_amdgcn_rcpf(Ls);
        gst(rws, u32x4{pk(o[0], o[1]), tag, pk(o[2], o[3]), tag}, vrec, WS_AG + ((ah * 4 + ab) * 16 + as) * 1024);
        if (lane < 2) gst(rws, u32x4{asu(M), tag, asu(Ls), tag}, lane * 16, WS_AS + (ah * 4 + ab) * 2048 + as * 128);
      }
    }
    STAMP(9);
#pragma unroll 1
    for (;; it += 4) {
      const u32 t = cw_wait(W_TK + (it & 15) * 4, it << 8) & 255u, sb = (it & 7) * SLOT;
      if (t >= (u32)NTICK || bud == 0u) break;
#pragma unroll
      for (int kk = 0; kk < 4; ++kk) qf[kk] = ld128(L, lpQ + (t & 3u) * 512 + kk * 64);
      const u32 pr = ah * 4 + (t & 3u), rec = 3 + (t >> 2), va = vrec + WS_AG + (pr * 16 + rec) * 1024, vs = lane * 16 + WS_AS + pr * 2048 + rec * 128;
      cw_wait(W_FULL, it + 1);
      READ_K(); READ_V();
      SCORES();
      m = xg_max(MAX8(s0, s1));
      const float mc = -m * CS;
#pragma unroll
      for (int i = 0; i < 4; ++i) { s0[i] = ex2(fmaf(s0[i], CS, mc)); s1[i] = ex2(fmaf(s1[i], CS, mc)); }
      const u32x4 pf = {pk(s0[0], s0[1]), pk(s0[2], s0[3]), pk(s1[0], s1[1]), pk(s1[2], s1[3])};
      f32x4 e0 = ZERO4, e1 = ZERO4;
#pragma unroll
      for (int ct = 0; ct < 8; ct += 2) { e0 = mfma(vf[ct], (n >> 1) == ct ? pf : u32x4{0u, 0u, 0u, 0u}, e0); e1 = mfma(vf[ct + 1], (n >> 1) == ct + 1 ? pf : u32x4{0u, 0u, 0u, 0u}, e1); }
      s0 += s1;
      l = xg_sum((s0[0] + s0[1]) + (s0[2] + s0[3]));
      e0 = (e0 + e1) * __builtin_amdgcn_rcpf(l);
      gst(rws, u32x4{pk(e0[0], e0[1]), tag, pk(e0[2], e0[3]), tag}, va, 0);
      if (lane < 2) gst(rws, u32x4{asu(m), tag, asu(l), tag}, vs, 0);
      STAMP(10);
    }
    if (s == 1) {
      STAMP(18);
      project(L, wf, rws, yso, tag, c, lane, bud);
      STAMP(20);
    } else {
      cw_add(W_O2, 1, lane);
      cw_wait(W_O2, 4);
      __builtin_amdgcn_s_sleep(FSLEEP);
      final_sum(rws, a.out, s == 0, hkv, b, c, tag, lane, bud);
      STAMP(22);
    }
    STAMP_END();
    return;
  }

  {
    cw_wait(W_WF, c + 1);
    u32x4 wu[16];
    {
      const u32 lp = (IT_UK + c) * SLOT + n * 512 + ((g4 ^ swz(n)) << 4);
#pragma unroll
      for (int i = 0; i < 16; ++i) wu[i] = ld128(L, (lp ^ ((i & 3) << 6)) + ((i >> 2) & 1) * 8192 + (i >> 3) * 256);
    }
    cw_add(W_FREE + (IT_UK + c) * 4, 4, lane);
    STAMP(15);
    cw_wait(W_QA, 1);
    STAMP(3);
    f32x4 d[4] = {ZERO4, ZERO4, ZERO4, ZERO4};
#pragma unroll
    for (int i = 0; i < 8; ++i) {
      const u32x4 qv = ld128(L, OB_OFF + p * OB_STR + i * 64 + g4 * 16);
      d[(i >> 2) * 2] = mfma(wu[(i >> 2) * 8 + (i & 3)], qv, d[(i >> 2) * 2]); d[(i >> 2) * 2 + 1] = mfma(wu[(i >> 2) * 8 + 4 + (i & 3)], qv, d[(i >> 2) * 2 + 1]);
    }
    const f32x4 e0 = (q & 1) ? d[1] : d[0], e1 = (q & 1) ? d[3] : d[2], e = (q & 2) ? e1 : e0;
    gst(rws, u32x4{pk(e[0], e[1]), tag, pk(e[2], e[3]), tag}, p * 65536 + (g4 & 1) * 128 + (q >> 1) * 64 + (q & 1) * 32 + (g4 >> 1) * 16, WS_QAG + hkv * 4096 + (b * 4 + c) * 256);
    if (cw_add_rtn(W_QS, 1, lane) == 3u) { if (lane == 0) gst(rws, u32x4{tag, tag, tag, tag}, WS_HINT + (hkv * 4 + b) * 16, 0); cw_add(W_QS, 1, lane); }
    STAMP(4);
  }
  {
    const int ch = c & 1;
    u32x4 qa[8];
    {
      const u32 so = WS_QAG + bq * 65536 + (c >> 1) * 32768 + ch * 2048;
      cw_wait(W_HINT, 1);
      STAMP(5);
      u32x4 t[16];
#pragma unroll
      for (int i = 0; i < 16; ++i) t[i] = gld0(rws, (n >> 1) * 4096 + (n & 1) * 64 + g4 * 16 + i * 128, so);
      cw_add(W_GI, 1, lane);
      for (;;) {
        u32 bad = 0;
#pragma unroll
        for (int i = 0; i < 16; ++i) bad |= tagx(t[i], tag);
        if (allz(bad) || !glb_retry()) break;
#pragma unroll
        for (int i = 0; i < 16; ++i) t[i] = gld(rws, (n >> 1) * 4096 + (n & 1) * 64 + g4 * 16 + i * 128, so);
      }
#pragma unroll
      for (int i = 0; i < 8; ++i) qa[i] = u32x4{t[2 * i][0], t[2 * i][2], t[2 * i + 1][0], t[2 * i + 1][2]};
    }
    STAMP(6);
    float mb = -1e30f, lb = 0.f;
    f32x4 ob[16];
#pragma unroll
    for (int i = 0; i < 16; ++i) ob[i] = ZERO4;
    const u32 lpR = n * 1024 + ((g4 ^ swz(n)) << 4) + ch * 512;
    const u32 lpC = (4 * g4 + q) * 1024 + (((p >> 1) ^ swz(4 * g4 + q)) << 4) + 8 * (p & 1) + ch * 512;
#pragma unroll 1
    for (int st = 0; st < 8; ++st) {
      const int it = IT_C + 2 * st;
      const u32 sb = (it & 7) * SLOT;
      const u32 xw = ((st & 1) ? (4 + c) * SLOT + 16384 : SB_OFF + c * 2048) + lane * 16, xr = ((st & 1) ? (4 + (c ^ 1)) * SLOT + 16384 : SB_OFF + (c ^ 1) * 2048) + lane * 16;
      cw_wait(W_FULL, it + 2);
      u32x4 rf[16], cf[16];
#pragma unroll
      for (int kk = 0; kk < 8; ++kk) { const u32 ak = ((sb + lpR) ^ ((kk & 3) << 6)) + (kk >> 2) * 256; rf[2 * kk] = ld128(L, ak); rf[2 * kk + 1] = ld128(L, ak + SLOT); }
      __builtin_amdgcn_sched_barrier(0);
      f32x4 s0 = ZERO4, s1 = ZERO4;
#pragma unroll
      for (int kk = 0; kk < 8; ++kk) {
        s0 = mfma(rf[2 * kk], qa[kk], s0); s1 = mfma(rf[2 * kk + 1], qa[kk], s1);
        const u32 av = (sb + lpC) ^ (kk << 5);
        cf[kk] = ldtr2(L, av, av + SLOT); cf[8 + kk] = ldtr2(L, av + 256, av + 256 + SLOT);
        __builtin_amdgcn_sched_barrier(0);
      }
      *(LAS f32x4*)(L + xw) = s0;
      *(LAS f32x4*)(L + xw + 1024) = s1;
      cw_st(W_S + c * 4, st + 1);
      if (lane < 2) asm volatile("ds_add_u32 %0, %1" ::"v"(W_FREE + (it & 7) * 4 + lane * 4), "v"(1u) : "memory");
      cw_wait(W_S + (c ^ 1) * 4, st + 1);
      s0 += *(LAS f32x4*)(L + xr); s1 += *(LAS f32x4*)(L + xr + 1024);
      const u32x4 pf = soft<16>(s0, s1, mb, lb, ob);
#pragma unroll
      for (int ct = 0; ct < 16; ++ct) ob[ct] = mfma(cf[ct], pf, ob[ct]);
      if (st == 3) STAMP(16);
      if (st == 4) STAMP(17);
    }
    STAMP(7);
#pragma unroll
    for (int i = 0; i < 8; ++i) {
      auto x = __builtin_amdgcn_permlane16_swap(pk(ob[2 * i][0], ob[2 * i][1]), pk(ob[2 * i + 1][0], ob[2 * i + 1][1]), false, false);
      auto y = __builtin_amdgcn_permlane16_swap(pk(ob[2 * i][2], ob[2 * i][3]), pk(ob[2 * i + 1][2], ob[2 * i + 1][3]), false, false);
      gst(rws, u32x4{x[0], y[0], x[1], y[1]}, (n >> 1) * 512 + (n & 1) * 64 + (g4 & 1) * 32 + (g4 >> 1) * 16 + (i & 3) * 128,
          WS_PB + ((bq * 16 + sp) * 4 + ch * 2 + (i >> 2)) * 8192 + (c >> 1) * 4096);
    }
    lb = xg_sum(lb);
    if (g4 == 0 && ch == 0) *(LAS f32x2*)(L + ST_OFF + ((c >> 1) * 16 + n) * 8) = f32x2{mb, lb};
    VMW(0);
    if (cw_add_rtn(W_BP, 1, lane) == 3u) {
      const f32x2 ml = *(LAS f32x2*)(L + ST_OFF + (lane & 31) * 8);
      if (lane < 32) gst(rws, u32x4{asu(ml[0]), tag, asu(ml[1]), tag}, (lane >> 1) * 2048 + (lane & 1) * 16, WS_SG + (bq * 16 + sp) * 32);
    }
    STAMP(8);
  }
  {
    cw_wait(W_SGF, 1);
    STAMP(10);
    u32x4 pv[8];
#pragma unroll
    for (int i = 0; i < 8; ++i) pv[i] = gld(rws, (lane >> 5) * 32768 + (lane & 31) * 16, WS_PB + ((c * 16 + 2 * i) * 4 + b) * 8192 + hkv * 512);
    cw_add(W_GI, 1, lane);
    const f32x2 st = *(LAS f32x2*)(L + MG_OFF + c * 512 + lane * 8);
    const float w = ex2((st[0] - row_max(st[0])) * CS), cf = w / row_sum(w * st[1]);
    f32x4 o0 = ZERO4, o1 = ZERO4;
#pragma unroll
    for (int i = 0; i < 8; ++i) {
      const float f = asf(__builtin_amdgcn_ds_bpermute((((lane >> 2) & 1) * 16 + 2 * i + (lane >> 5)) * 4, asu(cf)));
      o0 += f * f32x4{bflo(pv[i][0]), bfhi(pv[i][0]), bflo(pv[i][1]), bfhi(pv[i][1])};
      o1 += f * f32x4{bflo(pv[i][2]), bfhi(pv[i][2]), bflo(pv[i][3]), bfhi(pv[i][3])};
    }
#pragma unroll
    for (int i = 0; i < 4; ++i) {
      auto t0 = __builtin_amdgcn_permlane32_swap(asu(o0[i]), asu(o0[i]), false, false); o0[i] = asf(t0[0]) + asf(t0[1]);
      auto t1 = __builtin_amdgcn_permlane32_swap(asu(o1[i]), asu(o1[i]), false, false); o1[i] = asf(t1[0]) + asf(t1[1]);
    }
    if (lane < 32) *(LAS u32x4*)(L + OB_OFF + c * OB_STR + ((lane >> 2) & 1) * 256 + (lane >> 3) * 64 + p * 16) = u32x4{pk(o0[0], o0[1]), pk(o0[2], o0[3]), pk(o1[0], o1[1]), pk(o1[2], o1[3])};
    cw_add(W_OB, 1, lane);
    cw_wait(W_FULL, IT_UV + 4);
    u32x4 wf[16];
    {
      const u32 r0 = 8 * (g4 & 1) + q;
      const u32 lp0 = (8 * g4 + q) * 512 + ((((p >> 1) + 8 * c) ^ swz(r0)) << 4) + 8 * (p & 1);
      const u32 lp1 = (8 * g4 + q + 4) * 512 + ((((p >> 1) + 8 * c) ^ swz(r0 + 4)) << 4) + 8 * (p & 1);
#pragma unroll
      for (int i = 0; i < 16; ++i) wf[i] = ldtr2(L, (lp0 ^ ((i & 3) << 5)) + ((IT_UV + (i >> 2)) & 7) * SLOT, (lp1 ^ ((i & 3) << 5)) + ((IT_UV + (i >> 2)) & 7) * SLOT);
    }
    cw_wait(W_OB, 4);
    STAMP(11);
    f32x4 d[4] = {ZERO4, ZERO4, ZERO4, ZERO4};
#pragma unroll
    for (int ks = 0; ks < 4; ++ks) {
      const u32x4 of = ld128(L, OB_OFF + p * OB_STR + (c >> 1) * 256 + ks * 64 + g4 * 16);
#pragma unroll
      for (int t = 0; t < 4; ++t) d[t] = mfma(wf[ks * 4 + t], of, d[t]);
    }
    {
      const u32 vo = p * 2048 + (q >> 1) * 128 + g4 * 32 + (q & 1) * 16, so = WS_CG + ((hkv * 4 + b) * 1024 + c * 64) * 8;
      gst(rws, pick2(d[0], d[1], q, tag), vo, so); gst(rws, pick2(d[2], d[3], q, tag), vo + 256, so);
    }
    STAMP(12);
  }
  {
    cw_add(W_O2, 1, lane);
    cw_wait(W_O2, 4);
    const int rn = n ^ (lane >> 5);
    const u32 so = WS_AG + (hkv * 4 + c) * 16384 + (b & 1) * 512, ss = WS_AS + (hkv * 4 + c) * 2048 + (b & 1) * 16 + (rn < NREC ? rn : 0) * 128;
    const u32 lr = (4 + c) * SLOT, v0 = (lane & 31) * 16, v1 = v0 + (lane >> 5) * 1024;
    u32x4 wf[16], sv, ov[NPR];
    u32 todo = 0x5555u & ((1u << NREC) - 1u);
    {
      u32 ask = todo;
      while (cw_ld(W_FULL) < ALL && bud != 0u) {
        sv = gld(rws, ss, 0);
        const unsigned long long sb = __builtin_amdgcn_ballot_w64(tagx(sv, tag) != 0u);
        const u32 nw = ask & ~((u32)sb | (u32)(sb >> 32));
        for (u32 k = nw; k; k &= k - 1u) { const u32 i = __builtin_ctz(k); DMA(rws, lr + i * 512, i + 1 < NREC ? v1 : v0, so + i * 1024, 0, SC1); }
        ask &= ~nw;
        bud = bud < 16u ? 0u : bud - 16u;
        __builtin_amdgcn_s_sleep(CSLEEP);
      }
      todo = ask | 0x10000u;
    }
    read_w(L, wf, c, lane);
    STAMP(18);
    for (;;) {
      sv = gld(rws, ss, 0);
      for (u32 k = todo & 0xffffu; k; k &= k - 1u) { const u32 i = __builtin_ctz(k); DMA(rws, lr + i * 512, i + 1 < NREC ? v1 : v0, so + i * 1024, 0, SC1); }
      VMW(0);
      const unsigned long long sb = __builtin_amdgcn_ballot_w64(tagx(sv, tag) != 0u);
      if (todo & ~(((u32)sb | (u32)(sb >> 32)) & 0xffffu)) {
#pragma unroll
        for (int i = 0; i < NPR; ++i) ov[i] = ld128(L, lr + lane * 16 + i * 1024);
        u32 bad = 0;
#pragma unroll
        for (int i = 0; i < NPR; ++i) bad |= (tagx(ov[i], tag) != 0u) << (2 * i);
        if (allz(bad)) todo = 0u;
        else { bad |= __builtin_amdgcn_mov_dpp(bad, 0x128, 0xf, 0xf, true); bad |= __builtin_amdgcn_mov_dpp(bad, 0x124, 0xf, 0xf, true); bad |= __builtin_amdgcn_mov_dpp(bad, 0x122, 0xf, 0xf, true); bad |= __builtin_amdgcn_mov_dpp(bad, 0x121, 0xf, 0xf, true);
          todo = __builtin_amdgcn_readlane(bad, 0) | __builtin_amdgcn_readlane(bad, 16) | __builtin_amdgcn_readlane(bad, 32) | __builtin_amdgcn_readlane(bad, 48); }
      }
      if (todo == 0u && sb == 0ull) break;
      bud = bud < 16u ? 0u : bud - 16u;
      if (bud == 0u) break;
      asm volatile("" ::: "memory");
    }
    STAMP(19);
    const float ms = asf(sv[0]), w0 = ex2((ms - row_max(ms)) * CS) * asf(sv[2]), w = rn < NREC ? w0 : 0.f;
    f32x4 o[2] = {ZERO4, ZERO4};
    merge_rec<0>(o, ov, w);
    o[0] += o[1];
    const float rs = __builtin_amdgcn_rcpf(asf(__builtin_amdgcn_readfirstlane(asu(row_sum(w)))));
#pragma unroll
    for (int i = 0; i < 4; ++i) { auto t = __builtin_amdgcn_permlane32_swap(asu(o[0][i]), asu(o[0][i]), false, false); o[0][i] = (asf(t[0]) + asf(t[1])) * rs; }
    *(LAS u32x2*)(L + OB_OFF + c * OB_STR + (lane & 31) * 8) = u32x2{pk(o[0][0], o[0][1]), pk(o[0][2], o[0][3])};
    cw_add(W_PJ, 1, lane);
    project(L, wf, rws, yso, tag, c, lane, bud);
    STAMP(20);
  }
  STAMP_END();
}

extern "C" void kernel_launch(void* const* d_in, const int* in_sizes, int n_in, void* d_out, int out_size, void* d_ws, size_t ws_size, hipStream_t stream) {
  Args a;
  for (int i = 0; i < 11; ++i) a.in[i] = d_in[i];
  a.out = d_out;
  a.ws = d_ws;
  mk_kernel<<<256, 384, 0, stream>>>(a);
}
```

```cpp
#include <hip/hip_runtime.h>
#include <stdint.h>

#define LAS __attribute__((address_space(3)))
typedef unsigned u32;
typedef u32 u32x2 __attribute__((ext_vector_type(2)));
typedef u32 u32x4 __attribute__((ext_vector_type(4)));
typedef float f32x2 __attribute__((ext_vector_type(2)));
typedef float f32x4 __attribute__((ext_vector_type(4)));
typedef short s16x4 __attribute__((ext_vector_type(4)));
typedef __bf16 bf16x2 __attribute__((ext_vector_type(2)));
typedef __bf16 bf16x8 __attribute__((ext_vector_type(8)));
typedef __amdgpu_buffer_rsrc_t rsrc_t;

constexpr int N0 = 37, N1 = 38, N2 = 40, SLOWK = 2;
constexpr int NSUM = N0 + N1 + N2, NU = 128 - NSUM, NREC = 3 + NU, NTICK = 4 * NU;
static_assert(NU >= 1 && NREC <= 16, "split");
constexpr int LW_A = 32, LW_B = 28, LW_W = 44;
constexpr int POLL_A = 9, POLL_A0 = 7, POLL_C = N1 + 2;
constexpr int GATE1 = 8, GATE2 = 12;
constexpr int NT = 2;
static_assert(LW_A <= 32, "a ticket is read two items after it was drawn");

constexpr int SLOT = 18432, NS = 8, RING = SLOT * NS;
constexpr int CTL = RING;
constexpr int W_FULL = CTL, W_WF = CTL + 4, W_QA = CTL + 8, W_MRG = CTL + 12, W_HINT = CTL + 16, W_SGF = CTL + 20, W_OB = CTL + 24, W_BP = CTL + 28,
              W_FREE = CTL + 32, W_S = CTL + 64, W_GI = CTL + 80, W_QS = CTL + 84, W_PJ = CTL + 88, W_WI = CTL + 92, W_MF = CTL + 96, W_O2 = CTL + 100, W_TK = CTL + 128;
constexpr int ST_OFF = CTL + 256;
constexpr int OB_OFF = ST_OFF + 512, OB_STR = 576;
constexpr int SB_OFF = OB_OFF + 4 * OB_STR;
constexpr int QA_OFF = SB_OFF + 5120;
constexpr int MG_OFF = SB_OFF + 8192;
constexpr int MS_OFF = MG_OFF + 4096;
constexpr int LDS_BYTES = MS_OFF + 64;
static_assert(LDS_BYTES <= 163840 && (SB_OFF & 15) == 0 && (MG_OFF & 15) == 0, "lds");

constexpr u32 WS_EPOCH = 0, WS_HINT = 1024;
constexpr u32 WS_TK = 2048;
constexpr u32 WS_QG = 4096;
constexpr u32 WS_QAG = WS_QG + 2 * 4 * 2048 * 8;
constexpr u32 WS_AG = WS_QAG + 4 * 32 * 256 * 8;
constexpr u32 WS_AS = WS_AG + 64 * 16 * 1024;
constexpr u32 WS_SG = WS_AS + 64 * 2048;
constexpr u32 WS_CG = WS_SG + 4 * 16 * 32 * 16;
constexpr u32 WS_YG = WS_CG + 16 * 4 * 4 * 256 * 8;
constexpr u32 WS_PB = WS_YG + 2 * 32 * 18432;
constexpr u32 WS_END = WS_PB + 4 * 16 * 4 * 16 * 512;
static_assert((WS_PB & 127) == 0, "ws");

constexpr float CS = 0.08838834764831845f * 1.4426950408889634f;
constexpr int SC1 = 16;
constexpr u32 CODE_BYTES = 25600;
constexpr u32 SPIN_BUDGET = 1u << 19;

struct Args { const void* in[11]; void* out; void* ws; };

__device__ __forceinline__ rsrc_t mkrs(const void* p, u32 n) { return __builtin_amdgcn_make_buffer_rsrc((void*)p, 0, n, 0x00020000); }
constexpr __device__ __forceinline__ u32 swz(u32 r) { return (((r & 7u) ^ ((r >> 1) & 4u)) << 1) | ((r >> 3) & 1u); }
__device__ __forceinline__ f32x4 mfma(u32x4 a, u32x4 b, f32x4 c) {
  return __builtin_amdgcn_mfma_f32_16x16x32_bf16(__builtin_bit_cast(bf16x8, a), __builtin_bit_cast(bf16x8, b), c, 0, 0, 0);
}
__device__ __forceinline__ u32 pk(float lo, float hi) { f32x2 v = {lo, hi}; return __builtin_bit_cast(u32, __builtin_convertvector(v, bf16x2)); }
__device__ __forceinline__ float bflo(u32 v) { return __builtin_bit_cast(float, v << 16); }
__device__ __forceinline__ float bfhi(u32 v) { return __builtin_bit_cast(float, v & 0xffff0000u); }
__device__ __forceinline__ float asf(u32 v) { return __builtin_bit_cast(float, v); }
__device__ __forceinline__ u32 asu(float v) { return __builtin_bit_cast(u32, v); }
__device__ __forceinline__ float ex2(float x) { return __builtin_amdgcn_exp2f(x); }
__device__ __forceinline__ u32x4 ld128(LAS unsigned char* L, u32 a) { return *(LAS u32x4*)(L + a); }
__device__ __forceinline__ u32x4 ldtr2(LAS unsigned char* L, u32 a0, u32 a1) {
  const u32x2 x = __builtin_bit_cast(u32x2, __builtin_amdgcn_ds_read_tr16_b64_v4i16((LAS s16x4*)(L + a0)));
  const u32x2 y = __builtin_bit_cast(u32x2, __builtin_amdgcn_ds_read_tr16_b64_v4i16((LAS s16x4*)(L + a1)));
  return u32x4{x[0], x[1], y[0], y[1]};
}
__device__ __forceinline__ float xg_max(float x) {
  auto s = __builtin_amdgcn_permlane16_swap(asu(x), asu(x), false, false);
  x = fmaxf(asf(s[0]), asf(s[1]));
  auto t = __builtin_amdgcn_permlane32_swap(asu(x), asu(x), false, false);
  return fmaxf(asf(t[0]), asf(t[1]));
}
__device__ __forceinline__ float xg_sum(float x) {
  auto s = __builtin_amdgcn_permlane16_swap(asu(x), asu(x), false, false);
  x = asf(s[0]) + asf(s[1]);
  auto t = __builtin_amdgcn_permlane32_swap(asu(x), asu(x), false, false);
  return asf(t[0]) + asf(t[1]);
}
template <int CTRL> __device__ __forceinline__ float dpp(float x) { return asf(__builtin_amdgcn_mov_dpp(asu(x), CTRL, 0xf, 0xf, true)); }
__device__ __forceinline__ float row_sum(float x) { x += dpp<0x128>(x); x += dpp<0x124>(x); x += dpp<0x122>(x); x += dpp<0x121>(x); return x; }
__device__ __forceinline__ float row_max(float x) { x = fmaxf(x, dpp<0x128>(x)); x = fmaxf(x, dpp<0x124>(x)); x = fmaxf(x, dpp<0x122>(x)); x = fmaxf(x, dpp<0x121>(x)); return x; }

__device__ __forceinline__ u32 cw_ld(u32 a) { u32 v; asm volatile("ds_read_b32 %0, %1\n\ts_waitcnt lgkmcnt(0)" : "=v"(v) : "v"(a) : "memory"); return __builtin_amdgcn_readfirstlane(v); }
__device__ __forceinline__ void cw_st(u32 a, u32 v) { asm volatile("ds_write_b32 %0, %1" ::"v"(a), "v"(v) : "memory"); }
#define cw_full(v) cw_st(W_FULL, (v))
__device__ __forceinline__ void cw_add(u32 a, u32 v, int lane) { if (lane == 0) asm volatile("ds_add_u32 %0, %1" ::"v"(a), "v"(v) : "memory"); asm volatile("" ::: "memory"); }
__device__ __forceinline__ u32 cw_add_rtn(u32 a, u32 v, int lane) {
  u32 r = 0;
  if (lane == 0) asm volatile("ds_add_rtn_u32 %0, %1, %2\n\ts_waitcnt lgkmcnt(0)" : "=v"(r) : "v"(a), "v"(v) : "memory");
  asm volatile("" ::: "memory");
  return __builtin_amdgcn_readfirstlane(r);
}
__device__ __forceinline__ u32 cw_wait_(u32 a, u32 want, u32& bud) {
  u32 t;
  bud = __builtin_amdgcn_readfirstlane(bud); want = __builtin_amdgcn_readfirstlane(want);
  asm volatile("1:\n\tds_read_b32 v255, %2\n\ts_waitcnt lgkmcnt(0)\n\tv_readfirstlane_b32 %0, v255\n\ts_cmp_ge_u32 %0, %3\n\ts_cbranch_scc1 2f\n\t"
               "s_sleep 1\n\ts_sub_u32 %1, %1, 1\n\ts_cbranch_scc0 1b\n\ts_mov_b32 %1, 0\n2:\n\ts_nop 1"
               : "=&s"(t), "+s"(bud) : "v"(a), "s"(want) : "memory", "scc", "v255");
  return t;
}
#define cw_wait(a, want) cw_wait_((a), (want), bud)
__device__ __forceinline__ bool glb_retry_(u32& bud) {
  bud = __builtin_amdgcn_readfirstlane(bud);
  asm volatile("s_sleep 1\n\ts_sub_u32 %0, %0, 16\n\ts_cselect_b32 %0, 0, %0" : "+s"(bud) :: "memory", "scc");
  return bud != 0u;
}
#define glb_retry() glb_retry_(bud)
__device__ __forceinline__ u32x4 gld(rsrc_t r, u32 voff, u32 soff) { return __builtin_amdgcn_raw_buffer_load_b128(r, voff, soff, SC1); }
__device__ __forceinline__ void gst(rsrc_t r, u32x4 v, u32 voff, u32 soff) { __builtin_amdgcn_raw_buffer_store_b128(v, r, voff + soff, 0, SC1); }
__device__ __forceinline__ u32 tagx(u32x4 v, u32 tag) { return (v[1] ^ tag) | (v[3] ^ tag); }
__device__ __forceinline__ bool allz(u32 x) { return __builtin_amdgcn_ballot_w64(x != 0u) == 0ull; }
template <int N> __device__ __forceinline__ void fetch(LAS unsigned char* L, rsrc_t r, u32 voff, u32 ss, u32 tag, u32 dst, u32 ds, u32 flag, u32& bud) {
  u32x4 v[N];
  for (;;) {
    u32 bad = 0;
#pragma unroll
    for (int i = 0; i < N; ++i) v[i] = gld(r, voff + i * ss, 0);
#pragma unroll
    for (int i = 0; i < N; ++i) bad |= tagx(v[i], tag);
    if (allz(bad) || !glb_retry()) break;
  }
#pragma unroll
  for (int i = 0; i < N; ++i) *(LAS u32x2*)(L + dst + i * ds) = u32x2{v[i][0], v[i][2]};
  cw_st(flag, 1u);
}
__device__ __forceinline__ void dma_sc1(rsrc_t r, u32 lds, u32 voff, u32 soff) {
  u32 keep;
  asm volatile("s_mov_b32 %0, m0\n\ts_mov_b32 m0, %1\n\ts_nop 4\n\tbuffer_load_dwordx4 %2, %3, %4 offen sc1 lds\n\ts_mov_b32 m0, %0"
               : "=&s"(keep) : "s"(__builtin_amdgcn_readfirstlane(lds)), "v"(voff), "s"(r), "s"(__builtin_amdgcn_readfirstlane(soff)) : "memory");
}
#define DMA(r, ldsoff, voff, soff, imm, aux) __builtin_amdgcn_raw_ptr_buffer_load_lds((r), (LAS void*)(L + (ldsoff)), 16, (voff), (soff), (imm), (aux))

#define VMW(n) asm volatile("s_waitcnt vmcnt(" #n ")" ::: "memory")
#define ZERO4 f32x4{0.f, 0.f, 0.f, 0.f}
#define MAX8(a, b) fmaxf(fmaxf(fmaxf(a[0], a[1]), fmaxf(a[2], a[3])), fmaxf(fmaxf(b[0], b[1]), fmaxf(b[2], b[3])))
constexpr float LAZY = 192.f;
template <int NA> __device__ __forceinline__ u32x4 soft(const f32x4 s0, const f32x4 s1, float& m, float& l, f32x4* acc) {
  const float mx = MAX8(s0, s1);
  if (__builtin_amdgcn_ballot_w64(mx > m + LAZY) != 0ull) {
    const float mn = fmaxf(m, xg_max(mx)), al = ex2((m - mn) * CS);
    m = mn; l *= al;
#pragma unroll
    for (int i = 0; i < NA; ++i) acc[i] *= al;
  }
  const float mc = -m * CS;
  float pr[8];
#pragma unroll
  for (int i = 0; i < 4; ++i) { pr[i] = ex2(fmaf(s0[i], CS, mc)); pr[4 + i] = ex2(fmaf(s1[i], CS, mc)); }
  l += ((pr[0] + pr[1]) + (pr[2] + pr[3])) + ((pr[4] + pr[5]) + (pr[6] + pr[7]));
  return u32x4{pk(pr[0], pr[1]), pk(pr[2], pr[3]), pk(pr[4], pr[5]), pk(pr[6], pr[7])};
}
__device__ __forceinline__ u32x4 pick2(const f32x4 d0, const f32x4 d1, int q, u32 tag) {
  const float x0 = (q & 2) ? d1[0] : d0[0], x1 = (q & 2) ? d1[1] : d0[1], x2 = (q & 2) ? d1[2] : d0[2], x3 = (q & 2) ? d1[3] : d0[3];
  return u32x4{asu((q & 1) ? x2 : x0), tag, asu((q & 1) ? x3 : x1), tag};
}

constexpr int NPR = (NREC + 1) / 2;
template <int I> __device__ __forceinline__ void merge_rec(f32x4* o, const u32x4* t, float w) {
  if constexpr (I < NPR) { o[I & 1] += dpp<0x150 + 2 * I>(w) * f32x4{bflo(t[I][0]), bfhi(t[I][0]), bflo(t[I][2]), bfhi(t[I][2])}; merge_rec<I + 1>(o, t, w); }
}

constexpr int IT_UK = 0, IT_C = 4, IT_UV = 20, IT_W = 24;
constexpr u32 ALL = 1u << 20;
template <int N> __device__ __forceinline__ void vmw() { asm volatile("s_waitcnt vmcnt(%0)" ::"i"(N) : "memory"); }

__device__ __forceinline__ void read_w(LAS unsigned char* L, u32x4* wf, int c, int lane) {
  const u32 lp = (8 * (lane >> 4) + ((lane >> 2) & 3)) * 576 + 8 * (lane & 3) + 128 * c, t2 = c < 4 ? 64 : 0;
#pragma unroll
  for (int i = 0; i < 16; ++i) { const u32 ad = (i >> 2) * SLOT + lp + (i & 1) * 32 + ((i & 2) ? t2 : 0); wf[i] = ldtr2(L, ad, ad + 2304); }
}
__device__ __forceinline__ void project(LAS unsigned char* L, const u32x4* wf, rsrc_t rws, u32 so, u32 tag, int c, int lane, u32& bud) {
  const int g4 = lane >> 4, q = (lane >> 2) & 3, p = lane & 3;
  u32 ao = OB_OFF + p * OB_STR + g4 * 16, vo = so + c * 16384 + p * (72 * 2048) + ((q >> 1) * 2 + (g4 >> 1)) * 2048 + (g4 & 1) * 32 + (q & 1) * 16;
  asm volatile("" : "+v"(ao), "+v"(vo));
  cw_wait(W_PJ, 4);
  f32x4 d[4] = {ZERO4, ZERO4, ZERO4, ZERO4};
  u32x4 of[4];
#pragma unroll
  for (int ks = 0; ks < 4; ++ks) of[ks] = ld128(L, ao + ks * 64);
  __builtin_amdgcn_sched_barrier(0);
#pragma unroll
  for (int ks = 0; ks < 4; ++ks) { d[0] = mfma(wf[ks * 4], of[ks], d[0]); d[1] = mfma(wf[ks * 4 + 1], of[ks], d[1]); }
#pragma unroll
  for (int ks = 0; ks < 4; ++ks) { d[2] = mfma(wf[ks * 4 + 2], of[ks], d[2]); d[3] = mfma(wf[ks * 4 + 3], of[ks], d[3]); }
  gst(rws, pick2(d[0], d[1], q, tag), vo, 0);
  if (c < 4) gst(rws, pick2(d[2], d[3], q, tag), vo + 8192, 0);
}
__device__ __forceinline__ void final_sum(rsrc_t rws, void* out, int br, int t, u32 tag, int lane, u32& bud) {
  if (t >= 288) return;
  u32 vo = WS_YG + (br * 288 + t) * 2048 + lane * 16, oo = ((t / 72) * 1152 + br * 576 + (t % 72) * 8) * 2 + lane * 4;
  const rsrc_t ro = mkrs(out, 4u * 1152u * 2u);
  asm volatile("" : "+v"(vo), "+v"(oo));
  u32x4 y, z;
  for (;;) { y = gld(rws, vo, 0); z = gld(rws, vo + 1024, 0); if (allz(tagx(y, tag) | tagx(z, tag)) || !glb_retry()) break; }
  float r0 = asf(y[0]) + asf(z[0]), r1 = asf(y[2]) + asf(z[2]);
  r0 += dpp<0x128>(r0); r1 += dpp<0x128>(r1); r0 += dpp<0x124>(r0); r1 += dpp<0x124>(r1);
  const u32 r = pk(xg_sum(r0), xg_sum(r1));
  if (lane < 4) __builtin_amdgcn_raw_buffer_store_b32(r, ro, oo, 0, SC1);
}

__global__ void __launch_bounds__(384, 2) concat_heads_decode_kernel(Args a) {
  __shared__ __attribute__((aligned(16))) unsigned char smem[LDS_BYTES];
  LAS unsigned char* L = (LAS unsigned char*)smem;
  const int lane = threadIdx.x & 63, wave = __builtin_amdgcn_readfirstlane(threadIdx.x >> 6);
  const int g = blockIdx.x, gi = g >> 3, hkv = 2 * (g & 7) + (gi >> 4), j = gi & 15, b = j >> 2, s = j & 3;
  if (wave == 1) {
    cw_st(CTL + lane * 4, 0u);
    unsigned long long pc; asm volatile("s_getpc_b64 %0" : "=s"(pc));
    DMA(mkrs((const void*)(pc & ~255ull), CODE_BYTES), MG_OFF, lane * 16, gi * 1024, 0, 0);
    asm volatile("s_waitcnt lgkmcnt(0)" ::: "memory");
  }
  __builtin_amdgcn_s_barrier();
  const u32 vu = (lane >> 5) * 8192 + (((lane & 31) ^ ((lane >> 5) << 1)) << 4);
#define LOAD_W(in_, LW) do { const rsrc_t r = mkrs(a.in[in_], 4096u * 1152u); u32 wo[9]; int ln = lane; asm volatile("" : "+v"(ln)); \
      _Pragma("unroll") for (int i = 0; i < 9; ++i) { const u32 ci = i * 64 + ln; wo[i] = ci * 16 + ((ci * 3641u) >> 17) * 576u; } \
      u32 sw = ((u32)hkv * 256u + (b & 1) * 128u) * 1152u + (b >> 1) * 576u; \
      _Pragma("unroll 1") for (int t = 0; t < 8; ++t, sw += 16u * 1152u) { _Pragma("unroll") for (int i = 0; i < 9; ++i) { if (i % 3 == 0) vmw<LW>(); DMA(r, t * 9216 + i * 1024, wo[i], sw, 0, NT); } } } while (0)
  if (wave == 5) {
    if (s != 1) {
      const rsrc_t rw = mkrs(a.in[s == 0 ? 7 : s == 2 ? 3 : 8], s < 3 ? 576u * 8192u : 512u * 8192u), rx = mkrs(a.in[s ? 0 : 5], s < 3 ? 4u * 576u * 2u : 0u);
      const u32 vo = s < 3 ? ((lane >> 2) & 7) * 8192 + (lane & 3) * 16 + (lane >> 5) * 64 : vu, st = s < 3 ? 8 * 8192 : 2 * 8192, xm = s == 3 ? 0xf0u : 0u;
      u32 so = hkv * 512 + (s < 3 ? b * 128 : b * (128u * 8192u)), lo = 0;
#pragma unroll 1
      for (int t = 0; t < (s < 3 ? 9 : 8); ++t) {
        vmw<40>();
        if (t == 7) cw_st(W_WF, 1u);
#pragma unroll
        for (int i = 0; i < 8; ++i, so += st) DMA(rw, lo + i * 1024, vo ^ ((swz(2 * i) << 4) & xm), so, 0, NT);
        lo += s == 3 && (t & 1) ? SLOT - 8192 : 8192;
        if (t == 1 && s < 3) {
#pragma unroll
          for (int i = 0; i < 5; ++i) DMA(rx, SB_OFF + i * 1024, lane * 16, i * 1024, 0, 0);
        }
      }
    } else {
      LOAD_W(10, 40);
    }
    VMW(32); cw_st(W_WF, 2u); cw_st(W_WI, 1u);
    VMW(16); cw_st(W_WF, 3u);
    VMW(0); cw_st(W_WF, 4u);
  }
  const int ab = gi >> 3, ah = ((g & 7) + 2 * (gi & 4) + 3 * (ab + 4 * s)) & 15, as = s;
  const int z0 = ab == 1 && ((ah - 3) & 8) ? SLOWK : 0, z2 = ab == 1 && ((ah - 27) & 8) ? SLOWK : 0;
  const int n0 = N0 - 2 * z0 + z2, n1 = N1 + z0 + z2;
  const int ITU = 4 + (s == 0 ? n0 : s == 1 ? n1 : NSUM - n0 - n1);
  const int bq = (g & 7) >> 1, sp = (g & 1) * 8 + (gi >> 2);
  u32 bud = SPIN_BUDGET;

  if (wave == 0) {
    __builtin_amdgcn_s_setprio(3);
    u32 vq[4];
#pragma unroll
    for (int q = 0; q < 4; ++q) vq[q] = (lane >> 4) * 256 + (((lane & 15) ^ swz(4 * q + (lane >> 4))) << 4);
    int pub = 0, it = 4;
#define SLOT_WAIT() do { if (it >= NS) { const u32 need = 4u * (it >> 3), wa = W_FREE + (it & 7) * 4; \
      if (cw_ld(wa) < need) { VMW(16); if (it - 1 > pub) cw_full(it - 1); VMW(0); pub = it; cw_full(pub); cw_wait(wa, need); } } } while (0)
#define PACE(LW, k) do { vmw<LW>(); if ((k) == ((LW / 4) & 3) && it - LW / 16 > pub) { pub = it - LW / 16; cw_full(pub); } } while (0)
    asm volatile("" : "+s"(it));
    if (s < 3) {
      const rsrc_t rk = mkrs(a.in[1], 1u << 26), rv = mkrs(a.in[2], 1u << 26), rt = mkrs((const char*)a.ws + WS_TK + ah * 128, 4u);
      u32 so = ((u32)(ab * 16 + ah) * 128u + (s == 0 ? 0 : s == 1 ? n0 : n0 + n1)) * 8192u;
      asm volatile("v_mov_b32 v254, 1" ::: "v254");
#pragma unroll 1
      for (;; ++it) {
        const u32 sb = (it & 7) * SLOT;
        if (it == 5) { if (s == 0) cw_wait(W_QS, 4); else cw_wait(W_WI, 1); }
        SLOT_WAIT();
        PACE(LW_A, 0);
        if (it >= ITU) {
          u32 t;
          asm volatile("v_readlane_b32 %0, v254, %1\n\ts_cmp_lg_u32 %0, 1\n\ts_cbranch_scc1 1f\n\ts_waitcnt vmcnt(0)\n\tv_readlane_b32 %0, v254, %1\n1:\n\t"
                       "v_writelane_b32 v254, 1, %1\n\ts_nop 4" : "=&s"(t) : "s"(it & 3) : "memory", "scc", "v254");
          t -= 2u;
          if (t >= (u32)NTICK) break;
          cw_st(W_TK + (it & 15) * 4, (it << 8) | t);
          so = (((t & 3u) * 16u + ah) * 128u + NSUM + (t >> 2)) * 8192u;
        }
        if (it >= ITU - 2) {
          unsigned long long ex;
          asm volatile("s_mov_b64 %0, exec\n\ts_lshl_b64 exec, 1, %2\n\tbuffer_atomic_add v254, off, %1, 0 sc0\n\ts_mov_b64 exec, %0"
                       : "=&s"(ex) : "s"(rt), "s"((it + 2) & 3) : "memory", "scc", "v254");
        }
#pragma unroll
        for (int k = 0; k < 2; ++k) {
          if (k) PACE(LW_A, 1);
          DMA(rk, sb + k * 4096, vq[0], so + k * 4096, 0, NT); DMA(rk, sb + k * 4096, vq[1], so + k * 4096, 1024, NT);
          DMA(rk, sb + k * 4096, vq[2], so + k * 4096, 2048, NT); DMA(rk, sb + k * 4096, vq[3], so + k * 4096, 3072, NT);
        }
#pragma unroll
        for (int k = 0; k < 2; ++k) {
          PACE(LW_A, k + 2);
          DMA(rv, sb + 8192 + k * 4096, vq[0], so + k * 4096, 0, NT); DMA(rv, sb + 8192 + k * 4096, vq[1], so + k * 4096, 1024, NT);
          DMA(rv, sb + 8192 + k * 4096, vq[2], so + k * 4096, 2048, NT); DMA(rv, sb + 8192 + k * 4096, vq[3], so + k * 4096, 3072, NT);
        }
        so += 8192u;
      }
      cw_st(W_TK + ((it + (lane & 3)) & 15) * 4, ((it + (lane & 3)) << 8) | 255u);
    } else {
#pragma unroll 1
      for (; it < IT_W; ++it) {
        const u32 sb = (it & 7) * SLOT;
        if (it == 5) cw_wait(W_WI, 1);
        if (it == GATE1 || it == GATE2) {
          VMW(16); if (it - 1 > pub) cw_full(it - 1);
          VMW(0); pub = it; cw_full(it);
          if (it == GATE1) cw_wait(W_QS, 5);
          if (it == GATE2) cw_wait(W_GI, 4);
        }
        SLOT_WAIT();
        if (it < IT_UV) {
          const rsrc_t r = mkrs(a.in[6], 1u << 24);
          const u32 so = ((u32)bq * 4096u + sp * 256u + (it - IT_C) * 16u) * 1024u;
#pragma unroll
          for (int k = 0; k < 4; ++k) {
            PACE(LW_B, k);
            DMA(r, sb + k * 4096, (lane ^ swz(4 * k + 0)) << 4, so + k * 4096, 0, NT);
            DMA(r, sb + k * 4096, (lane ^ swz(4 * k + 1)) << 4, so + k * 4096, 1024, NT);
            DMA(r, sb + k * 4096, (lane ^ swz(4 * k + 2)) << 4, so + k * 4096, 2048, NT);
            DMA(r, sb + k * 4096, (lane ^ swz(4 * k + 3)) << 4, so + k * 4096, 3072, NT);
          }
        } else {
          const rsrc_t r = mkrs(a.in[9], 512u * 8192u);
          const u32 so = ((u32)b * 128u + (it & 3) * 32u) * 8192u + hkv * 512u;
#pragma unroll
          for (int i = 0; i < 16; ++i) { if ((i & 3) == 0) PACE(LW_B, i >> 2); DMA(r, sb + i * 1024, vu ^ (swz((2 * i) & 15) << 4), so + i * (2 * 8192), 0, NT); }
        }
      }
      VMW(0); pub = it; cw_full(it);
      cw_wait(W_GI, 8);
      LOAD_W(4, LW_W);
      vmw<32>(); cw_st(W_WI, 2u);
    }
    VMW(16); if (it - 1 > pub) cw_full(it - 1);
    VMW(0); cw_full(ALL);
    return;
  }

  const int c = wave - 1, g4 = lane >> 4, n = lane & 15, q = n >> 2, p = lane & 3;
  const rsrc_t rws = mkrs(a.ws, WS_END);
  const char* wsp = (const char*)a.ws; asm volatile("" : "+s"(wsp));
  const u32 tag = *(const __attribute__((address_space(4))) u32*)(wsp + WS_EPOCH) + 1u;
  asm volatile("" ::"s"(tag));
  VMW(0);
  const u32 yso = WS_YG + (s == 1 ? 288 * 2048 : 0) + (b >> 1) * (36 * 2048) + (hkv * 2 + (b & 1)) * 64;
  if (c == 4) {
    u32x4 wf[16];
    if (s == 1) {
      read_w(L, wf, 4, lane);
      cw_st(W_MF, 1u);
    }
    cw_wait(W_FULL, s == 3 ? GATE1 : s == 0 ? POLL_A0 : POLL_A);
    fetch<4>(L, rws, lane * 16 + WS_QG + (s < 3 ? ah * 128 : 8192 + hkv * 128) * 8, 16384, tag, (s < 3 ? QA_OFF : OB_OFF) + lane * 8, s < 3 ? 512 : OB_STR, W_QA, bud);
    if (s == 3) {
      cw_wait(W_QS, 5); cw_wait(W_FULL, GATE2);
      fetch<1>(L, rws, lane * 16 + WS_HINT, 0, tag, MG_OFF + lane * 8, 0, W_HINT, bud);
      cw_wait(W_BP, 4);
      fetch<4>(L, rws, n * 32 + (g4 & 1) * 16 + WS_SG + hkv * 2048, 512, tag, MG_OFF + lane * 8, 512, W_SGF, bud);
      cw_wait(W_FULL, ALL);
      read_w(L, wf, 4, lane);
    } else if (s == 1) {
      cw_wait(W_FULL, POLL_C);
      const u32 so = WS_CG + hkv * 32768 + (b & 1) * 1024;
      for (;;) {
        const u32x4 t = gld(rws, q * 8192 + (p & 1) * 512, so);
        if (allz(tagx(t, tag)) || !glb_retry()) break;
        __builtin_amdgcn_s_sleep(16);
      }
      u32x4 cv[16];
      for (;;) {
        u32 bad = 0;
#pragma unroll
        for (int i = 0; i < 16; ++i) cv[i] = gld(rws, lane * 16 + (i & 1) * 2048, so + (i >> 1) * 4096);
#pragma unroll
        for (int i = 0; i < 16; ++i) bad |= tagx(cv[i], tag);
        if (allz(bad) || !glb_retry()) break;
      }
#pragma unroll
      for (int i = 0; i < 4; ++i)
        *(LAS u32*)(L + OB_OFF + i * OB_STR + lane * 4) = pk((asf(cv[i][0]) + asf(cv[4 + i][0])) + (asf(cv[8 + i][0]) + asf(cv[12 + i][0])), (asf(cv[i][2]) + asf(cv[4 + i][2])) + (asf(cv[8 + i][2]) + asf(cv[12 + i][2])));
      cw_st(W_PJ, 4u);
    }
    if (s & 1) {
      project(L, wf, rws, yso, tag, 4, lane, bud);
    } else {
      cw_wait(W_O2, 4);
      __builtin_amdgcn_s_sleep(16);
      final_sum(rws, a.out, s == 0, 256 + hkv * 4 + b, tag, lane, bud);
      if (g == 16 && lane == 0) __builtin_amdgcn_raw_buffer_store_b32(tag, rws, 0, WS_EPOCH, SC1);
    }
    return;
  }

  if (s < 3) {
    u32x4 wf[16];
    if (s == 1) {
      cw_wait(W_WF, 4);
      read_w(L, wf, c, lane);
      cw_wait(W_MF, 1);
      if (lane < 4) asm volatile("ds_add_u32 %0, %1" ::"v"(W_FREE + lane * 4), "v"(1u) : "memory");
    } else {
      const u32 aw = g4 * 1024 + (c >> 1) * 512 + q * 64 + (c & 1) * 32 + 8 * p, ax = SB_OFF + p * 1152 + g4 * 16;
      const u32 vo = (n * 2048 + 2 * g4) * 8, so = WS_QG + ((s ? 0 : 8192) + hkv * 128 + b * 32 + 8 * c) * 8;
      if (j == 2 && c == 0) { __builtin_amdgcn_raw_buffer_store_b32(2u, rws, WS_TK + hkv * 128, 0, SC1); VMW(0); }
      cw_wait(W_WF, 2);
      u32x4 xr[18], wq[9];
#pragma unroll
      for (int ks = 0; ks < 18; ++ks) xr[ks] = ld128(L, ax + ks * 64);
      f32x4 a0 = ZERO4, a1 = ZERO4;
#pragma unroll
      for (int hf = 0; hf < 2; ++hf) {
        if (hf) { cw_wait(W_WF, 4); }
#pragma unroll
        for (int ks = 0; ks < 9; ++ks) wq[ks] = ldtr2(L, aw + (hf * 9 + ks) * 4096, aw + (hf * 9 + ks) * 4096 + 256);
        if (lane < 2) asm volatile("ds_add_u32 %0, %1" ::"v"(W_FREE + hf * 8 + lane * 4), "v"(1u) : "memory");
        __builtin_amdgcn_sched_barrier(0);
#pragma unroll
        for (int ks = 0; ks < 9; ++ks) { if (ks & 1) a1 = mfma(wq[ks], xr[hf * 9 + ks], a1); else a0 = mfma(wq[ks], xr[hf * 9 + ks], a0); }
        __builtin_amdgcn_sched_barrier(0);
      }
      a0 += a1;
      if (n < 4) gst(rws, u32x4{pk(a0[0], a0[1]), tag, pk(a0[2], a0[3]), tag}, vo, so);
      cw_add(W_QS, 1, lane);
    }
    float m = -1e30f, l = 0.f;
    f32x4 acc[8];
#pragma unroll
    for (int i = 0; i < 8; ++i) acc[i] = ZERO4;
    u32x4 qf[4], kf[8], vf[8];
    const u32 lpK = n * 256 + ((g4 ^ swz(n)) << 4), lpQ = QA_OFF + (n & 1) * 256 + g4 * 16;
    const u32 lpV = (4 * g4 + q) * 256 + (((p >> 1) ^ swz(4 * g4 + q)) << 4) + 8 * (p & 1);
#define READ_K() _Pragma("unroll") for (int kk = 0; kk < 4; ++kk) { const u32 ak = (sb + lpK) ^ (kk << 6); kf[kk] = ld128(L, ak); kf[4 + kk] = ld128(L, ak + 4096); }
#define READ_V() do { _Pragma("unroll") for (int ct = 0; ct < 8; ++ct) { const u32 av = (sb + lpV) ^ (ct << 5); vf[ct] = ldtr2(L, av + 8192, av + 12288); } \
      cw_add(W_FREE + (it & 7) * 4, 4, lane); } while (0)
#define SCORES() f32x4 s0 = ZERO4, s1 = ZERO4; \
      _Pragma("unroll") for (int kk = 0; kk < 4; ++kk) { s0 = mfma(kf[kk], qf[kk], s0); s1 = mfma(kf[4 + kk], qf[kk], s1); }
#define PICK8(e) const f32x4 e##0 = (n & 2) ? acc[1] : acc[0], e##1 = (n & 2) ? acc[3] : acc[2], e##2 = (n & 2) ? acc[5] : acc[4], e##3 = (n & 2) ? acc[7] : acc[6], \
      e##4 = (n & 4) ? e##1 : e##0, e##5 = (n & 4) ? e##3 : e##2, e = (n & 8) ? e##5 : e##4
    const u32 vrec = (n & 1) * 512 + (n >> 1) * 64 + g4 * 16;
    bool first = true;
    int it = 4 + c;
#pragma unroll 1
    for (; it < ITU; it += 4) {
      const u32 sb = (it & 7) * SLOT;
      cw_wait(W_FULL, it + 1);
      READ_K(); READ_V();
      if (first) {
        first = false;
        cw_wait(W_QA, 1);
#pragma unroll
        for (int kk = 0; kk < 4; ++kk) qf[kk] = ld128(L, lpQ + ab * 512 + kk * 64);
      }
      SCORES();
      const u32x4 pf = soft<8>(s0, s1, m, l, acc);
#pragma unroll
      for (int ct = 0; ct < 8; ++ct) acc[ct] = mfma(vf[ct], pf, acc[ct]);
    }
    {
      l = xg_sum(l);
      PICK8(e);
      *(LAS f32x4*)(L + MG_OFF + c * 1024 + lane * 16) = e;
      if (lane < 2) *(LAS f32x2*)(L + MS_OFF + (c * 2 + lane) * 8) = f32x2{m, l};
      if (cw_add_rtn(W_MRG, 1, lane) == 3u) {
        f32x2 sv[4]; f32x4 ov[4];
#pragma unroll
        for (int i = 0; i < 4; ++i) { sv[i] = *(LAS f32x2*)(L + MS_OFF + (i * 2 + (n & 1)) * 8); ov[i] = *(LAS f32x4*)(L + MG_OFF + i * 1024 + lane * 16); }
        const float M = fmaxf(fmaxf(sv[0][0], sv[1][0]), fmaxf(sv[2][0], sv[3][0]));
        float Ls = 0.f; f32x4 o = ZERO4;
#pragma unroll
        for (int i = 0; i < 4; ++i) { const float w = ex2((sv[i][0] - M) * CS); Ls = fmaf(w, sv[i][1], Ls); o += w * ov[i]; }
        o *= __builtin_amdgcn_rcpf(Ls);
        gst(rws, u32x4{pk(o[0], o[1]), tag, pk(o[2], o[3]), tag}, vrec, WS_AG + ((ah * 4 + ab) * 16 + as) * 1024);
        if (lane < 2) gst(rws, u32x4{asu(M), tag, asu(Ls), tag}, lane * 16, WS_AS + (ah * 4 + ab) * 2048 + as * 128);
      }
    }
#pragma unroll 1
    for (;; it += 4) {
      const u32 t = cw_wait(W_TK + (it & 15) * 4, it << 8) & 255u, sb = (it & 7) * SLOT;
      if (t >= (u32)NTICK || bud == 0u) break;
#pragma unroll
      for (int kk = 0; kk < 4; ++kk) qf[kk] = ld128(L, lpQ + (t & 3u) * 512 + kk * 64);
      const u32 pr = ah * 4 + (t & 3u), rec = 3 + (t >> 2), va = vrec + WS_AG + (pr * 16 + rec) * 1024, vs = lane * 16 + WS_AS + pr * 2048 + rec * 128;
      cw_wait(W_FULL, it + 1);
      READ_K(); READ_V();
      SCORES();
      m = xg_max(MAX8(s0, s1));
      const float mc = -m * CS;
#pragma unroll
      for (int i = 0; i < 4; ++i) { s0[i] = ex2(fmaf(s0[i], CS, mc)); s1[i] = ex2(fmaf(s1[i], CS, mc)); }
      const u32x4 pf = {pk(s0[0], s0[1]), pk(s0[2], s0[3]), pk(s1[0], s1[1]), pk(s1[2], s1[3])};
      f32x4 e0 = ZERO4, e1 = ZERO4;
#pragma unroll
      for (int ct = 0; ct < 8; ct += 2) { e0 = mfma(vf[ct], (n >> 1) == ct ? pf : u32x4{0u, 0u, 0u, 0u}, e0); e1 = mfma(vf[ct + 1], (n >> 1) == ct + 1 ? pf : u32x4{0u, 0u, 0u, 0u}, e1); }
      s0 += s1;
      l = xg_sum((s0[0] + s0[1]) + (s0[2] + s0[3]));
      e0 = (e0 + e1) * __builtin_amdgcn_rcpf(l);
      gst(rws, u32x4{pk(e0[0], e0[1]), tag, pk(e0[2], e0[3]), tag}, va, 0);
      if (lane < 2) gst(rws, u32x4{asu(m), tag, asu(l), tag}, vs, 0);
    }
    if (s == 1) {
      project(L, wf, rws, yso, tag, c, lane, bud);
    } else {
      cw_add(W_O2, 1, lane);
      cw_wait(W_O2, 4);
      __builtin_amdgcn_s_sleep(16);
      final_sum(rws, a.out, s == 0, (hkv * 4 + b) * 4 + c, tag, lane, bud);
    }
    return;
  }

  {
    cw_wait(W_WF, c + 1);
    u32x4 wu[16];
    {
      const u32 lp = (IT_UK + c) * SLOT + n * 512 + ((g4 ^ swz(n)) << 4);
#pragma unroll
      for (int i = 0; i < 16; ++i) wu[i] = ld128(L, (lp ^ ((i & 3) << 6)) + ((i >> 2) & 1) * 8192 + (i >> 3) * 256);
    }
    cw_add(W_FREE + (IT_UK + c) * 4, 4, lane);
    cw_wait(W_QA, 1);
    f32x4 d[4] = {ZERO4, ZERO4, ZERO4, ZERO4};
#pragma unroll
    for (int i = 0; i < 8; ++i) {
      const u32x4 qv = ld128(L, OB_OFF + p * OB_STR + i * 64 + g4 * 16);
      d[(i >> 2) * 2] = mfma(wu[(i >> 2) * 8 + (i & 3)], qv, d[(i >> 2) * 2]); d[(i >> 2) * 2 + 1] = mfma(wu[(i >> 2) * 8 + 4 + (i & 3)], qv, d[(i >> 2) * 2 + 1]);
    }
    const f32x4 e0 = (q & 1) ? d[1] : d[0], e1 = (q & 1) ? d[3] : d[2], e = (q & 2) ? e1 : e0;
    gst(rws, u32x4{pk(e[0], e[1]), tag, pk(e[2], e[3]), tag}, p * 65536 + (g4 & 1) * 128 + (q >> 1) * 64 + (q & 1) * 32 + (g4 >> 1) * 16, WS_QAG + hkv * 4096 + (b * 4 + c) * 256);
    if (cw_add_rtn(W_QS, 1, lane) == 3u) { if (lane == 0) gst(rws, u32x4{tag, tag, tag, tag}, WS_HINT + (hkv * 4 + b) * 16, 0); cw_add(W_QS, 1, lane); }
  }
  {
    const int ch = c & 1;
    u32x4 qa[8];
    {
      const u32 so = WS_QAG + bq * 65536 + (c >> 1) * 32768 + ch * 2048;
      cw_wait(W_HINT, 1);
      u32x4 t[16];
#pragma unroll
      for (int i = 0; i < 16; ++i) t[i] = gld(rws, (n >> 1) * 4096 + (n & 1) * 64 + g4 * 16 + i * 128, so);
      cw_add(W_GI, 1, lane);
      for (;;) {
        u32 bad = 0;
#pragma unroll
        for (int i = 0; i < 16; ++i) bad |= tagx(t[i], tag);
        if (allz(bad) || !glb_retry()) break;
#pragma unroll
        for (int i = 0; i < 16; ++i) t[i] = gld(rws, (n >> 1) * 4096 + (n & 1) * 64 + g4 * 16 + i * 128, so);
      }
#pragma unroll
      for (int i = 0; i < 8; ++i) qa[i] = u32x4{t[2 * i][0], t[2 * i][2], t[2 * i + 1][0], t[2 * i + 1][2]};
    }
    float mb = -1e30f, lb = 0.f;
    f32x4 ob[16];
#pragma unroll
    for (int i = 0; i < 16; ++i) ob[i] = ZERO4;
    const u32 lpR = n * 1024 + ((g4 ^ swz(n)) << 4) + ch * 512;
    const u32 lpC = (4 * g4 + q) * 1024 + (((p >> 1) ^ swz(4 * g4 + q)) << 4) + 8 * (p & 1) + ch * 512;
#pragma unroll 1
    for (int st = 0; st < 8; ++st) {
      const int it = IT_C + 2 * st;
      const u32 sb = (it & 7) * SLOT;
      const u32 xw = ((st & 1) ? (4 + c) * SLOT + 16384 : SB_OFF + c * 2048) + lane * 16, xr = ((st & 1) ? (4 + (c ^ 1)) * SLOT + 16384 : SB_OFF + (c ^ 1) * 2048) + lane * 16;
      cw_wait(W_FULL, it + 2);
      u32x4 rf[16], cf[16];
#pragma unroll
      for (int kk = 0; kk < 8; ++kk) { const u32 ak = ((sb + lpR) ^ ((kk & 3) << 6)) + (kk >> 2) * 256; rf[2 * kk] = ld128(L, ak); rf[2 * kk + 1] = ld128(L, ak + SLOT); }
      __builtin_amdgcn_sched_barrier(0);
      f32x4 s0 = ZERO4, s1 = ZERO4;
#pragma unroll
      for (int kk = 0; kk < 8; ++kk) {
        s0 = mfma(rf[2 * kk], qa[kk], s0); s1 = mfma(rf[2 * kk + 1], qa[kk], s1);
        const u32 av = (sb + lpC) ^ (kk << 5);
        cf[kk] = ldtr2(L, av, av + SLOT); cf[8 + kk] = ldtr2(L, av + 256, av + 256 + SLOT);
        __builtin_amdgcn_sched_barrier(0);
      }
      *(LAS f32x4*)(L + xw) = s0;
      *(LAS f32x4*)(L + xw + 1024) = s1;
      cw_st(W_S + c * 4, st + 1);
      if (lane < 2) asm volatile("ds_add_u32 %0, %1" ::"v"(W_FREE + (it & 7) * 4 + lane * 4), "v"(1u) : "memory");
      cw_wait(W_S + (c ^ 1) * 4, st + 1);
      s0 += *(LAS f32x4*)(L + xr); s1 += *(LAS f32x4*)(L + xr + 1024);
      const u32x4 pf = soft<16>(s0, s1, mb, lb, ob);
#pragma unroll
      for (int ct = 0; ct < 16; ++ct) ob[ct] = mfma(cf[ct], pf, ob[ct]);
    }
#pragma unroll
    for (int i = 0; i < 8; ++i) {
      auto x = __builtin_amdgcn_permlane16_swap(pk(ob[2 * i][0], ob[2 * i][1]), pk(ob[2 * i + 1][0], ob[2 * i + 1][1]), false, false);
      auto y = __builtin_amdgcn_permlane16_swap(pk(ob[2 * i][2], ob[2 * i][3]), pk(ob[2 * i + 1][2], ob[2 * i + 1][3]), false, false);
      gst(rws, u32x4{x[0], y[0], x[1], y[1]}, (n >> 1) * 512 + (n & 1) * 64 + (g4 & 1) * 32 + (g4 >> 1) * 16 + (i & 3) * 128,
          WS_PB + ((bq * 16 + sp) * 4 + ch * 2 + (i >> 2)) * 8192 + (c >> 1) * 4096);
    }
    lb = xg_sum(lb);
    if (g4 == 0 && ch == 0) *(LAS f32x2*)(L + ST_OFF + ((c >> 1) * 16 + n) * 8) = f32x2{mb, lb};
    VMW(0);
    if (cw_add_rtn(W_BP, 1, lane) == 3u) {
      const f32x2 ml = *(LAS f32x2*)(L + ST_OFF + (lane & 31) * 8);
      if (lane < 32) gst(rws, u32x4{asu(ml[0]), tag, asu(ml[1]), tag}, (lane >> 1) * 2048 + (lane & 1) * 16, WS_SG + (bq * 16 + sp) * 32);
    }
  }
  {
    cw_wait(W_SGF, 1);
    u32x4 pv[8];
#pragma unroll
    for (int i = 0; i < 8; ++i) pv[i] = gld(rws, (lane >> 5) * 32768 + (lane & 31) * 16, WS_PB + ((c * 16 + 2 * i) * 4 + b) * 8192 + hkv * 512);
    cw_add(W_GI, 1, lane);
    const f32x2 st = *(LAS f32x2*)(L + MG_OFF + c * 512 + lane * 8);
    const float w = ex2((st[0] - row_max(st[0])) * CS), cf = w / row_sum(w * st[1]);
    f32x4 o0 = ZERO4, o1 = ZERO4;
#pragma unroll
    for (int i = 0; i < 8; ++i) {
      const float f = asf(__builtin_amdgcn_ds_bpermute((((lane >> 2) & 1) * 16 + 2 * i + (lane >> 5)) * 4, asu(cf)));
      o0 += f * f32x4{bflo(pv[i][0]), bfhi(pv[i][0]), bflo(pv[i][1]), bfhi(pv[i][1])};
      o1 += f * f32x4{bflo(pv[i][2]), bfhi(pv[i][2]), bflo(pv[i][3]), bfhi(pv[i][3])};
    }
#pragma unroll
    for (int i = 0; i < 4; ++i) {
      auto t0 = __builtin_amdgcn_permlane32_swap(asu(o0[i]), asu(o0[i]), false, false); o0[i] = asf(t0[0]) + asf(t0[1]);
      auto t1 = __builtin_amdgcn_permlane32_swap(asu(o1[i]), asu(o1[i]), false, false); o1[i] = asf(t1[0]) + asf(t1[1]);
    }
    if (lane < 32) *(LAS u32x4*)(L + OB_OFF + c * OB_STR + ((lane >> 2) & 1) * 256 + (lane >> 3) * 64 + p * 16) = u32x4{pk(o0[0], o0[1]), pk(o0[2], o0[3]), pk(o1[0], o1[1]), pk(o1[2], o1[3])};
    cw_add(W_OB, 1, lane);
    cw_wait(W_FULL, IT_UV + 4);
    u32x4 wf[16];
    {
      const u32 r0 = 8 * (g4 & 1) + q;
      const u32 lp0 = (8 * g4 + q) * 512 + ((((p >> 1) + 8 * c) ^ swz(r0)) << 4) + 8 * (p & 1);
      const u32 lp1 = (8 * g4 + q + 4) * 512 + ((((p >> 1) + 8 * c) ^ swz(r0 + 4)) << 4) + 8 * (p & 1);
#pragma unroll
      for (int i = 0; i < 16; ++i) wf[i] = ldtr2(L, (lp0 ^ ((i & 3) << 5)) + ((IT_UV + (i >> 2)) & 7) * SLOT, (lp1 ^ ((i & 3) << 5)) + ((IT_UV + (i >> 2)) & 7) * SLOT);
    }
    cw_wait(W_OB, 4);
    f32x4 d[4] = {ZERO4, ZERO4, ZERO4, ZERO4};
#pragma unroll
    for (int ks = 0; ks < 4; ++ks) {
      const u32x4 of = ld128(L, OB_OFF + p * OB_STR + (c >> 1) * 256 + ks * 64 + g4 * 16);
#pragma unroll
      for (int t = 0; t < 4; ++t) d[t] = mfma(wf[ks * 4 + t], of, d[t]);
    }
    {
      const u32 vo = p * 2048 + (q >> 1) * 128 + g4 * 32 + (q & 1) * 16, so = WS_CG + ((hkv * 4 + b) * 1024 + c * 64) * 8;
      gst(rws, pick2(d[0], d[1], q, tag), vo, so); gst(rws, pick2(d[2], d[3], q, tag), vo + 256, so);
    }
  }
  {
    cw_add(W_O2, 1, lane);
    cw_wait(W_O2, 4);
    const int rn = n ^ (lane >> 5);
    const u32 so = WS_AG + (hkv * 4 + c) * 16384 + (b & 1) * 512, ss = WS_AS + (hkv * 4 + c) * 2048 + (b & 1) * 16 + (rn < NREC ? rn : 0) * 128;
    const u32 lr = (4 + c) * SLOT, v0 = (lane & 31) * 16, v1 = v0 + (lane >> 5) * 1024;
    u32x4 wf[16], sv, ov[NPR];
    u32 todo = 0x5555u & ((1u << NREC) - 1u);
    {
      u32 ask = todo;
      while (cw_ld(W_WI) < 2u && bud != 0u) {
        sv = gld(rws, ss, 0);
        const unsigned long long sb = __builtin_amdgcn_ballot_w64(tagx(sv, tag) != 0u);
        const u32 nw = ask & ~((u32)sb | (u32)(sb >> 32));
        for (u32 k = nw; k; k &= k - 1u) { const u32 i = __builtin_ctz(k); dma_sc1(rws, lr + i * 512, i + 1 < NREC ? v1 : v0, so + i * 1024); }
        ask &= ~nw;
        bud = bud < 16u ? 0u : bud - 16u;
        __builtin_amdgcn_s_sleep(2);
      }
      todo = ask | 0x10000u;
    }
    for (;;) {
      sv = gld(rws, ss, 0);
      for (u32 k = todo & 0xffffu; k; k &= k - 1u) { const u32 i = __builtin_ctz(k); dma_sc1(rws, lr + i * 512, i + 1 < NREC ? v1 : v0, so + i * 1024); }
      if (todo >> 16) { cw_wait(W_FULL, ALL); read_w(L, wf, c, lane); }
      VMW(0);
      const unsigned long long sb = __builtin_amdgcn_ballot_w64(tagx(sv, tag) != 0u);
      if (todo & ~(((u32)sb | (u32)(sb >> 32)) & 0xffffu)) {
#pragma unroll
        for (int i = 0; i < NPR; ++i) ov[i] = ld128(L, lr + lane * 16 + i * 1024);
        u32 bad = 0;
#pragma unroll
        for (int i = 0; i < NPR; ++i) bad |= (tagx(ov[i], tag) != 0u) << (2 * i);
        if (allz(bad)) todo = 0u;
        else { bad |= __builtin_amdgcn_mov_dpp(bad, 0x128, 0xf, 0xf, true); bad |= __builtin_amdgcn_mov_dpp(bad, 0x124, 0xf, 0xf, true); bad |= __builtin_amdgcn_mov_dpp(bad, 0x122, 0xf, 0xf, true); bad |= __builtin_amdgcn_mov_dpp(bad, 0x121, 0xf, 0xf, true);
          todo = __builtin_amdgcn_readlane(bad, 0) | __builtin_amdgcn_readlane(bad, 16) | __builtin_amdgcn_readlane(bad, 32) | __builtin_amdgcn_readlane(bad, 48); }
      }
      if (todo == 0u && sb == 0ull) break;
      bud = bud < 16u ? 0u : bud - 16u;
      if (bud == 0u) break;
      asm volatile("" ::: "memory");
    }
    const float ms = asf(sv[0]), w0 = ex2((ms - row_max(ms)) * CS) * asf(sv[2]), w = rn < NREC ? w0 : 0.f;
    f32x4 o[2] = {ZERO4, ZERO4};
    merge_rec<0>(o, ov, w);
    o[0] += o[1];
    const float rs = __builtin_amdgcn_rcpf(asf(__builtin_amdgcn_readfirstlane(asu(row_sum(w)))));
#pragma unroll
    for (int i = 0; i < 4; ++i) { auto t = __builtin_amdgcn_permlane32_swap(asu(o[0][i]), asu(o[0][i]), false, false); o[0][i] = (asf(t[0]) + asf(t[1])) * rs; }
    *(LAS u32x2*)(L + OB_OFF + c * OB_STR + (lane & 31) * 8) = u32x2{pk(o[0][0], o[0][1]), pk(o[0][2], o[0][3])};
    cw_add(W_PJ, 1, lane);
    project(L, wf, rws, yso, tag, c, lane, bud);
  }
}

extern "C" void kernel_launch(void* const* d_in, const int* in_sizes, int n_in, void* d_out, int out_size, void* d_ws, size_t ws_size, hipStream_t stream) {
  Args a;
  for (int i = 0; i < 11; ++i) a.in[i] = d_in[i];
  a.out = d_out;
  a.ws = d_ws;
  concat_heads_decode_kernel<<<256, 384, 0, stream>>>(a);
}
```

```cpp
#include <hip/hip_runtime.h>
#include <stdint.h>

#define LAS __attribute__((address_space(3)))
typedef unsigned u32;
typedef u32 u32x2 __attribute__((ext_vector_type(2)));
typedef u32 u32x4 __attribute__((ext_vector_type(4)));
typedef float f32x2 __attribute__((ext_vector_type(2)));
typedef float f32x4 __attribute__((ext_vector_type(4)));
typedef short s16x4 __attribute__((ext_vector_type(4)));
typedef __bf16 bf16x2 __attribute__((ext_vector_type(2)));
typedef __bf16 bf16x8 __attribute__((ext_vector_type(8)));
typedef __amdgpu_buffer_rsrc_t rsrc_t;

constexpr int N0 = 38, N1 = 38, N2 = 39, SLOWK = 2;
constexpr int NSUM = N0 + N1 + N2, NU = 128 - NSUM, NREC = 3 + NU, NTICK = 4 * NU;
static_assert(NU >= 1 && NREC <= 16, "split");
constexpr int LW_A = 32, LW_B = 32, LW_W = 44;
constexpr int POLL_A = 9, POLL_A0 = 7, POLL_C = N1 + 2;
constexpr int GATE1 = 10, GATE2 = 14;
constexpr int NT = 2;
static_assert(LW_A <= 32, "a ticket is read two items after it was drawn");

constexpr int SLOT = 18432, NS = 8, RING = SLOT * NS;
constexpr int CTL = RING;
constexpr int W_FULL = CTL, W_WF = CTL + 4, W_QA = CTL + 8, W_MRG = CTL + 12, W_HINT = CTL + 16, W_SGF = CTL + 20, W_OB = CTL + 24, W_BP = CTL + 28,
              W_FREE = CTL + 32, W_S = CTL + 64, W_GI = CTL + 80, W_QS = CTL + 84, W_PJ = CTL + 88, W_WI = CTL + 92, W_MF = CTL + 96, W_O2 = CTL + 100, W_G1 = CTL + 104, W_TK = CTL + 128;
constexpr int ST_OFF = CTL + 256;
constexpr int OB_OFF = ST_OFF + 512, OB_STR = 576;
constexpr int SB_OFF = OB_OFF + 4 * OB_STR;
constexpr int QA_OFF = SB_OFF + 5120;
constexpr int MG_OFF = SB_OFF + 8192;
constexpr int MS_OFF = MG_OFF + 4096;
constexpr int LDS_BYTES = MS_OFF + 64;
static_assert(LDS_BYTES <= 163840 && (SB_OFF & 15) == 0 && (MG_OFF & 15) == 0, "lds");

constexpr u32 WS_EPOCH = 0, WS_HINT = 1024;
constexpr u32 WS_TK = 2048;
constexpr u32 WS_QG = 4096;
constexpr u32 WS_QAG = WS_QG + 2 * 4 * 2048 * 8;
constexpr u32 WS_AG = WS_QAG + 4 * 32 * 256 * 8;
constexpr u32 WS_AS = WS_AG + 64 * 16 * 1024;
constexpr u32 WS_SG = WS_AS + 64 * 2048;
constexpr u32 WS_CG = WS_SG + 4 * 16 * 32 * 16;
constexpr u32 WS_YG = WS_CG + 16 * 4 * 4 * 256 * 8;
constexpr u32 WS_PB = WS_YG + 2 * 32 * 18432;
constexpr u32 WS_END = WS_PB + 4 * 16 * 4 * 16 * 512;
static_assert((WS_PB & 127) == 0, "ws");

constexpr float CS = 0.08838834764831845f * 1.4426950408889634f;
constexpr int SC1 = 16;
constexpr u32 CODE_BYTES = 25600;
constexpr u32 SPIN_BUDGET = 1u << 19;

struct Args { const void* in[11]; void* out; void* ws; };

__device__ __forceinline__ rsrc_t mkrs(const void* p, u32 n) { return __builtin_amdgcn_make_buffer_rsrc((void*)p, 0, n, 0x00020000); }
constexpr __device__ __forceinline__ u32 swz(u32 r) { return (((r & 7u) ^ ((r >> 1) & 4u)) << 1) | ((r >> 3) & 1u); }
__device__ __forceinline__ f32x4 mfma(u32x4 a, u32x4 b, f32x4 c) {
  return __builtin_amdgcn_mfma_f32_16x16x32_bf16(__builtin_bit_cast(bf16x8, a), __builtin_bit_cast(bf16x8, b), c, 0, 0, 0);
}
__device__ __forceinline__ u32 pk(float lo, float hi) { f32x2 v = {lo, hi}; return __builtin_bit_cast(u32, __builtin_convertvector(v, bf16x2)); }
__device__ __forceinline__ float bflo(u32 v) { return __builtin_bit_cast(float, v << 16); }
__device__ __forceinline__ float bfhi(u32 v) { return __builtin_bit_cast(float, v & 0xffff0000u); }
__device__ __forceinline__ float asf(u32 v) { return __builtin_bit_cast(float, v); }
__device__ __forceinline__ u32 asu(float v) { return __builtin_bit_cast(u32, v); }
__device__ __forceinline__ float ex2(float x) { return __builtin_amdgcn_exp2f(x); }
__device__ __forceinline__ u32x4 ld128(LAS unsigned char* L, u32 a) { return *(LAS u32x4*)(L + a); }
__device__ __forceinline__ u32x4 ldtr2(LAS unsigned char* L, u32 a0, u32 a1) {
  const u32x2 x = __builtin_bit_cast(u32x2, __builtin_amdgcn_ds_read_tr16_b64_v4i16((LAS s16x4*)(L + a0)));
  const u32x2 y = __builtin_bit_cast(u32x2, __builtin_amdgcn_ds_read_tr16_b64_v4i16((LAS s16x4*)(L + a1)));
  return u32x4{x[0], x[1], y[0], y[1]};
}
__device__ __forceinline__ float xg_max(float x) {
  auto s = __builtin_amdgcn_permlane16_swap(asu(x), asu(x), false, false);
  x = fmaxf(asf(s[0]), asf(s[1]));
  auto t = __builtin_amdgcn_permlane32_swap(asu(x), asu(x), false, false);
  return fmaxf(asf(t[0]), asf(t[1]));
}
__device__ __forceinline__ float xg_sum(float x) {
  auto s = __builtin_amdgcn_permlane16_swap(asu(x), asu(x), false, false);
  x = asf(s[0]) + asf(s[1]);
  auto t = __builtin_amdgcn_permlane32_swap(asu(x), asu(x), false, false);
  return asf(t[0]) + asf(t[1]);
}
template <int CTRL> __device__ __forceinline__ float dpp(float x) { return asf(__builtin_amdgcn_mov_dpp(asu(x), CTRL, 0xf, 0xf, true)); }
__device__ __forceinline__ float row_sum(float x) { x += dpp<0x128>(x); x += dpp<0x124>(x); x += dpp<0x122>(x); x += dpp<0x121>(x); return x; }
__device__ __forceinline__ float row_max(float x) { x = fmaxf(x, dpp<0x128>(x)); x = fmaxf(x, dpp<0x124>(x)); x = fmaxf(x, dpp<0x122>(x)); x = fmaxf(x, dpp<0x121>(x)); return x; }

__device__ __forceinline__ u32 cw_ld(u32 a) { u32 v; asm volatile("ds_read_b32 %0, %1\n\ts_waitcnt lgkmcnt(0)" : "=v"(v) : "v"(a) : "memory"); return __builtin_amdgcn_readfirstlane(v); }
__device__ __forceinline__ void cw_st(u32 a, u32 v) { asm volatile("ds_write_b32 %0, %1" ::"v"(a), "v"(v) : "memory"); }
#define cw_full(v) cw_st(W_FULL, (v))
__device__ __forceinline__ void cw_add(u32 a, u32 v, int lane) { if (lane == 0) asm volatile("ds_add_u32 %0, %1" ::"v"(a), "v"(v) : "memory"); asm volatile("" ::: "memory"); }
__device__ __forceinline__ u32 cw_add_rtn(u32 a, u32 v, int lane) {
  u32 r = 0;
  if (lane == 0) asm volatile("ds_add_rtn_u32 %0, %1, %2\n\ts_waitcnt lgkmcnt(0)" : "=v"(r) : "v"(a), "v"(v) : "memory");
  asm volatile("" ::: "memory");
  return __builtin_amdgcn_readfirstlane(r);
}
__device__ __forceinline__ u32 cw_wait_(u32 a, u32 want, u32& bud) {
  u32 t;
  bud = __builtin_amdgcn_readfirstlane(bud); want = __builtin_amdgcn_readfirstlane(want);
  asm volatile("1:\n\tds_read_b32 v255, %2\n\ts_waitcnt lgkmcnt(0)\n\tv_readfirstlane_b32 %0, v255\n\ts_cmp_ge_u32 %0, %3\n\ts_cbranch_scc1 2f\n\t"
               "s_sleep 1\n\ts_sub_u32 %1, %1, 1\n\ts_cbranch_scc0 1b\n\ts_mov_b32 %1, 0\n2:\n\ts_nop 1"
               : "=&s"(t), "+s"(bud) : "v"(a), "s"(want) : "memory", "scc", "v255");
  return t;
}
#define cw_wait(a, want) cw_wait_((a), (want), bud)
__device__ __forceinline__ bool glb_retry_(u32& bud) {
  bud = __builtin_amdgcn_readfirstlane(bud);
  asm volatile("s_sleep 1\n\ts_sub_u32 %0, %0, 16\n\ts_cselect_b32 %0, 0, %0" : "+s"(bud) :: "memory", "scc");
  return bud != 0u;
}
#define glb_retry() glb_retry_(bud)
__device__ __forceinline__ u32x4 gld(rsrc_t r, u32 voff, u32 soff) { return __builtin_amdgcn_raw_buffer_load_b128(r, voff, soff, SC1); }
__device__ __forceinline__ void gst(rsrc_t r, u32x4 v, u32 voff, u32 soff) { __builtin_amdgcn_raw_buffer_store_b128(v, r, voff + soff, 0, SC1); }
__device__ __forceinline__ u32 tagx(u32x4 v, u32 tag) { return (v[1] ^ tag) | (v[3] ^ tag); }
__device__ __forceinline__ bool allz(u32 x) { return __builtin_amdgcn_ballot_w64(x != 0u) == 0ull; }
template <int N> __device__ __forceinline__ void fetch(LAS unsigned char* L, rsrc_t r, u32 voff, u32 ss, u32 tag, u32 dst, u32 ds, u32 flag, u32& bud) {
  u32x4 v[N];
  for (;;) {
    u32 bad = 0;
#pragma unroll
    for (int i = 0; i < N; ++i) v[i] = gld(r, voff + i * ss, 0);
#pragma unroll
    for (int i = 0; i < N; ++i) bad |= tagx(v[i], tag);
    if (allz(bad) || !glb_retry()) break;
    if (flag == W_QA) cw_st(W_G1, 1u);
  }
#pragma unroll
  for (int i = 0; i < N; ++i) *(LAS u32x2*)(L + dst + i * ds) = u32x2{v[i][0], v[i][2]};
  cw_st(flag, 1u);
}
__device__ __forceinline__ void dma_sc1(rsrc_t r, u32 lds, u32 voff, u32 soff) {
  u32 keep;
  asm volatile("s_mov_b32 %0, m0\n\ts_mov_b32 m0, %1\n\ts_nop 4\n\tbuffer_load_dwordx4 %2, %3, %4 offen sc1 lds\n\ts_mov_b32 m0, %0"
               : "=&s"(keep) : "s"(__builtin_amdgcn_readfirstlane(lds)), "v"(voff), "s"(r), "s"(__builtin_amdgcn_readfirstlane(soff)) : "memory");
}
#define DMA(r, ldsoff, voff, soff, imm, aux) __builtin_amdgcn_raw_ptr_buffer_load_lds((r), (LAS void*)(L + (ldsoff)), 16, (voff), (soff), (imm), (aux))

#define VMW(n) asm volatile("s_waitcnt vmcnt(" #n ")" ::: "memory")
#define ZERO4 f32x4{0.f, 0.f, 0.f, 0.f}
#define MAX8(a, b) fmaxf(fmaxf(fmaxf(a[0], a[1]), fmaxf(a[2], a[3])), fmaxf(fmaxf(b[0], b[1]), fmaxf(b[2], b[3])))
constexpr float LAZY = 192.f;
template <int NA> __device__ __forceinline__ u32x4 soft(const f32x4 s0, const f32x4 s1, float& m, float& l, f32x4* acc) {
  const float mx = MAX8(s0, s1);
  if (__builtin_amdgcn_ballot_w64(mx > m + LAZY) != 0ull) {
    const float mn = fmaxf(m, xg_max(mx)), al = ex2((m - mn) * CS);
    m = mn; l *= al;
#pragma unroll
    for (int i = 0; i < NA; ++i) acc[i] *= al;
  }
  const float mc = -m * CS;
  float pr[8];
#pragma unroll
  for (int i = 0; i < 4; ++i) { pr[i] = ex2(fmaf(s0[i], CS, mc)); pr[4 + i] = ex2(fmaf(s1[i], CS, mc)); }
  l += ((pr[0] + pr[1]) + (pr[2] + pr[3])) + ((pr[4] + pr[5]) + (pr[6] + pr[7]));
  return u32x4{pk(pr[0], pr[1]), pk(pr[2], pr[3]), pk(pr[4], pr[5]), pk(pr[6], pr[7])};
}
__device__ __forceinline__ u32x4 pick2(const f32x4 d0, const f32x4 d1, int q, u32 tag) {
  const float x0 = (q & 2) ? d1[0] : d0[0], x1 = (q & 2) ? d1[1] : d0[1], x2 = (q & 2) ? d1[2] : d0[2], x3 = (q & 2) ? d1[3] : d0[3];
  return u32x4{asu((q & 1) ? x2 : x0), tag, asu((q & 1) ? x3 : x1), tag};
}

constexpr int NPR = (NREC + 1) / 2;
template <int I> __device__ __forceinline__ void merge_rec(f32x4* o, const u32x4* t, float w) {
  if constexpr (I < NPR) { o[I & 1] += dpp<0x150 + 2 * I>(w) * f32x4{bflo(t[I][0]), bfhi(t[I][0]), bflo(t[I][2]), bfhi(t[I][2])}; merge_rec<I + 1>(o, t, w); }
}

constexpr int IT_UK = 0, IT_C = 4, IT_UV = 20, IT_W = 24;
constexpr u32 ALL = 1u << 20;
template <int N> __device__ __forceinline__ void vmw() { asm volatile("s_waitcnt vmcnt(%0)" ::"i"(N) : "memory"); }

__device__ __forceinline__ void read_w(LAS unsigned char* L, u32x4* wf, int c, int lane) {
  const u32 lp = (8 * (lane >> 4) + ((lane >> 2) & 3)) * 576 + 8 * (lane & 3) + 128 * c, t2 = c < 4 ? 64 : 0;
#pragma unroll
  for (int i = 0; i < 16; ++i) { const u32 ad = (i >> 2) * SLOT + lp + (i & 1) * 32 + ((i & 2) ? t2 : 0); wf[i] = ldtr2(L, ad, ad + 2304); }
}
__device__ __forceinline__ void project(LAS unsigned char* L, const u32x4* wf, rsrc_t rws, u32 so, u32 tag, int c, int lane, u32& bud) {
  const int g4 = lane >> 4, q = (lane >> 2) & 3, p = lane & 3;
  u32 ao = OB_OFF + p * OB_STR + g4 * 16, vo = so + c * 16384 + p * (72 * 2048) + ((q >> 1) * 2 + (g4 >> 1)) * 2048 + (g4 & 1) * 32 + (q & 1) * 16;
  asm volatile("" : "+v"(ao), "+v"(vo));
  cw_wait(W_PJ, 4);
  f32x4 d[4] = {ZERO4, ZERO4, ZERO4, ZERO4};
  u32x4 of[4];
#pragma unroll
  for (int ks = 0; ks < 4; ++ks) of[ks] = ld128(L, ao + ks * 64);
  __builtin_amdgcn_sched_barrier(0);
#pragma unroll
  for (int ks = 0; ks < 4; ++ks) { d[0] = mfma(wf[ks * 4], of[ks], d[0]); d[1] = mfma(wf[ks * 4 + 1], of[ks], d[1]); }
#pragma unroll
  for (int ks = 0; ks < 4; ++ks) { d[2] = mfma(wf[ks * 4 + 2], of[ks], d[2]); d[3] = mfma(wf[ks * 4 + 3], of[ks], d[3]); }
  gst(rws, pick2(d[0], d[1], q, tag), vo, 0);
  if (c < 4) gst(rws, pick2(d[2], d[3], q, tag), vo + 8192, 0);
}
__device__ __forceinline__ void final_sum(rsrc_t rws, void* out, int br, int t, u32 tag, int lane, u32& bud) {
  if (t >= 288) return;
  u32 vo = WS_YG + (br * 288 + t) * 2048 + lane * 16, oo = ((t / 72) * 1152 + br * 576 + (t % 72) * 8) * 2 + lane * 4;
  const rsrc_t ro = mkrs(out, 4u * 1152u * 2u);
  asm volatile("" : "+v"(vo), "+v"(oo));
  u32x4 y, z;
  for (;;) { y = gld(rws, vo, 0); z = gld(rws, vo + 1024, 0); if (allz(tagx(y, tag) | tagx(z, tag)) || !glb_retry()) break; }
  float r0 = asf(y[0]) + asf(z[0]), r1 = asf(y[2]) + asf(z[2]);
  r0 += dpp<0x128>(r0); r1 += dpp<0x128>(r1); r0 += dpp<0x124>(r0); r1 += dpp<0x124>(r1);
  const u32 r = pk(xg_sum(r0), xg_sum(r1));
  if (lane < 4) __builtin_amdgcn_raw_buffer_store_b32(r, ro, oo, 0, SC1);
}

__global__ void __launch_bounds__(384, 2) concat_heads_decode_kernel(Args a) {
  __shared__ __attribute__((aligned(16))) unsigned char smem[LDS_BYTES];
  LAS unsigned char* L = (LAS unsigned char*)smem;
  const int lane = threadIdx.x & 63, wave = __builtin_amdgcn_readfirstlane(threadIdx.x >> 6);
  const int g = blockIdx.x, gi = g >> 3, hkv = 2 * (g & 7) + (gi >> 4), j = gi & 15, b = j >> 2, s = j & 3;
  if (wave == 1) {
    cw_st(CTL + lane * 4, 0u);
    unsigned long long pc; asm volatile("s_getpc_b64 %0" : "=s"(pc));
    DMA(mkrs((const void*)(pc & ~255ull), CODE_BYTES), MG_OFF, lane * 16, gi * 1024, 0, 0);
    asm volatile("s_waitcnt lgkmcnt(0)" ::: "memory");
  }
  __builtin_amdgcn_s_barrier();
  const u32 vu = (lane >> 5) * 8192 + (((lane & 31) ^ ((lane >> 5) << 1)) << 4);
#define LOAD_W(in_, LW) do { const rsrc_t r = mkrs(a.in[in_], 4096u * 1152u); u32 wo[9]; int ln = lane; asm volatile("" : "+v"(ln)); \
      _Pragma("unroll") for (int i = 0; i < 9; ++i) { const u32 ci = i * 64 + ln; wo[i] = ci * 16 + ((ci * 3641u) >> 17) * 576u; } \
      u32 sw = ((u32)hkv * 256u + (b & 1) * 128u) * 1152u + (b >> 1) * 576u; \
      _Pragma("unroll 1") for (int t = 0; t < 8; ++t, sw += 16u * 1152u) { _Pragma("unroll") for (int i = 0; i < 9; ++i) { if (i % 3 == 0) vmw<LW>(); DMA(r, t * 9216 + i * 1024, wo[i], sw, 0, NT); } } } while (0)
  if (wave == 5) {
    if (s != 1) {
      const rsrc_t rw = mkrs(a.in[s == 0 ? 7 : s == 2 ? 3 : 8], s < 3 ? 576u * 8192u : 512u * 8192u), rx = mkrs(a.in[s ? 0 : 5], s < 3 ? 4u * 576u * 2u : 0u);
      const u32 vo = s < 3 ? ((lane >> 2) & 7) * 8192 + (lane & 3) * 16 + (lane >> 5) * 64 : vu, st = s < 3 ? 8 * 8192 : 2 * 8192, xm = s == 3 ? 0xf0u : 0u;
      u32 so = hkv * 512 + (s < 3 ? b * 128 : b * (128u * 8192u)), lo = 0;
#pragma unroll 1
      for (int t = 0; t < (s < 3 ? 9 : 8); ++t) {
        vmw<40>();
        if (t == 7) cw_st(W_WF, 1u);
#pragma unroll
        for (int i = 0; i < 8; ++i, so += st) DMA(rw, lo + i * 1024, vo ^ ((swz(2 * i) << 4) & xm), so, 0, NT);
        lo += s == 3 && (t & 1) ? SLOT - 8192 : 8192;
        if (t == 1 && s < 3) {
#pragma unroll
          for (int i = 0; i < 5; ++i) DMA(rx, SB_OFF + i * 1024, lane * 16, i * 1024, 0, 0);
        }
      }
    } else {
      LOAD_W(10, 40);
    }
    VMW(32); cw_st(W_WF, 2u); cw_st(W_WI, 1u);
    VMW(16); cw_st(W_WF, 3u);
    VMW(0); cw_st(W_WF, 4u);
  }
  const int ab = gi >> 3, ah = ((g & 7) + 2 * (gi & 4) + 3 * (ab + 4 * s)) & 15, as = s;
  const int z0 = ab == 1 && ((ah - 3) & 8) ? SLOWK : 0, z2 = ab == 1 && ((ah - 27) & 8) ? SLOWK : 0;
  const int n0 = N0 - 2 * z0 + z2, n1 = N1 + z0 + z2;
  const int ITU = 4 + (s == 0 ? n0 : s == 1 ? n1 : NSUM - n0 - n1);
  const int bq = (g & 7) >> 1, sp = (g & 1) * 8 + (gi >> 2);
  u32 bud = SPIN_BUDGET;

  if (wave == 0) {
    __builtin_amdgcn_s_setprio(3);
    u32 vq[4];
#pragma unroll
    for (int q = 0; q < 4; ++q) vq[q] = (lane >> 4) * 256 + (((lane & 15) ^ swz(4 * q + (lane >> 4))) << 4);
    int pub = 0, it = 4;
#define SLOT_WAIT() do { if (it >= NS) { const u32 need = 4u * (it >> 3), wa = W_FREE + (it & 7) * 4; \
      if (cw_ld(wa) < need) { VMW(16); if (it - 1 > pub) cw_full(it - 1); VMW(0); pub = it; cw_full(pub); cw_wait(wa, need); } } } while (0)
#define PACE(LW, k) do { vmw<LW>(); if ((k) == ((LW / 4) & 3) && it - LW / 16 > pub) { pub = it - LW / 16; cw_full(pub); } } while (0)
    asm volatile("" : "+s"(it));
    if (s < 3) {
      const rsrc_t rk = mkrs(a.in[1], 1u << 26), rv = mkrs(a.in[2], 1u << 26), rt = mkrs((const char*)a.ws + WS_TK + ah * 128, 4u);
      u32 so = ((u32)(ab * 16 + ah) * 128u + (s == 0 ? 0 : s == 1 ? n0 : n0 + n1)) * 8192u;
      asm volatile("v_mov_b32 v254, 1" ::: "v254");
#pragma unroll 1
      for (;; ++it) {
        const u32 sb = (it & 7) * SLOT;
        if (it == 5) cw_wait(W_WI, 1);
        SLOT_WAIT();
        PACE(LW_A, 0);
        if (it >= ITU) {
          u32 t;
          asm volatile("v_readlane_b32 %0, v254, %1\n\ts_cmp_lg_u32 %0, 1\n\ts_cbranch_scc1 1f\n\ts_waitcnt vmcnt(0)\n\tv_readlane_b32 %0, v254, %1\n1:\n\t"
                       "v_writelane_b32 v254, 1, %1\n\ts_nop 4" : "=&s"(t) : "s"(it & 3) : "memory", "scc", "v254");
          t -= 2u;
          if (t >= (u32)NTICK) break;
          cw_st(W_TK + (it & 15) * 4, (it << 8) | t);
          so = (((t & 3u) * 16u + ah) * 128u + NSUM + (t >> 2)) * 8192u;
        }
        if (it >= ITU - 2) {
          unsigned long long ex;
          asm volatile("s_mov_b64 %0, exec\n\ts_lshl_b64 exec, 1, %2\n\tbuffer_atomic_add v254, off, %1, 0 sc0\n\ts_mov_b64 exec, %0"
                       : "=&s"(ex) : "s"(rt), "s"((it + 2) & 3) : "memory", "scc", "v254");
        }
#pragma unroll
        for (int k = 0; k < 2; ++k) {
          if (k) PACE(LW_A, 1);
          DMA(rk, sb + k * 4096, vq[0], so + k * 4096, 0, NT); DMA(rk, sb + k * 4096, vq[1], so + k * 4096, 1024, NT);
          DMA(rk, sb + k * 4096, vq[2], so + k * 4096, 2048, NT); DMA(rk, sb + k * 4096, vq[3], so + k * 4096, 3072, NT);
        }
#pragma unroll
        for (int k = 0; k < 2; ++k) {
          PACE(LW_A, k + 2);
          DMA(rv, sb + 8192 + k * 4096, vq[0], so + k * 4096, 0, NT); DMA(rv, sb + 8192 + k * 4096, vq[1], so + k * 4096, 1024, NT);
          DMA(rv, sb + 8192 + k * 4096, vq[2], so + k * 4096, 2048, NT); DMA(rv, sb + 8192 + k * 4096, vq[3], so + k * 4096, 3072, NT);
        }
        so += 8192u;
      }
      cw_st(W_TK + ((it + (lane & 3)) & 15) * 4, ((it + (lane & 3)) << 8) | 255u);
    } else {
#pragma unroll 1
      for (; it < IT_W; ++it) {
        const u32 sb = (it & 7) * SLOT;
        if (it == 5) cw_wait(W_WI, 1);
        if (it == GATE1 || it == GATE2) {
          VMW(16); if (it - 1 > pub) cw_full(it - 1);
          VMW(0); pub = it; cw_full(it);
          if (it == GATE1) cw_wait(W_G1, 1);
          if (it == GATE2) cw_wait(W_GI, 4);
        }
        SLOT_WAIT();
        if (it < IT_UV) {
          const rsrc_t r = mkrs(a.in[6], 1u << 24);
          const u32 so = ((u32)bq * 4096u + sp * 256u + (it - IT_C) * 16u) * 1024u;
#pragma unroll
          for (int k = 0; k < 4; ++k) {
            PACE(LW_B, k);
            DMA(r, sb + k * 4096, (lane ^ swz(4 * k + 0)) << 4, so + k * 4096, 0, NT);
            DMA(r, sb + k * 4096, (lane ^ swz(4 * k + 1)) << 4, so + k * 4096, 1024, NT);
            DMA(r, sb + k * 4096, (lane ^ swz(4 * k + 2)) << 4, so + k * 4096, 2048, NT);
            DMA(r, sb + k * 4096, (lane ^ swz(4 * k + 3)) << 4, so + k * 4096, 3072, NT);
          }
        } else {
          const rsrc_t r = mkrs(a.in[9], 512u * 8192u);
          const u32 so = ((u32)b * 128u + (it & 3) * 32u) * 8192u + hkv * 512u;
#pragma unroll
          for (int i = 0; i < 16; ++i) { if ((i & 3) == 0) PACE(LW_B, i >> 2); DMA(r, sb + i * 1024, vu ^ (swz((2 * i) & 15) << 4), so + i * (2 * 8192), 0, NT); }
        }
      }
      VMW(0); pub = it; cw_full(it);
      cw_wait(W_GI, 8);
      LOAD_W(4, LW_W);
      vmw<32>(); cw_st(W_WI, 2u);
    }
    VMW(16); if (it - 1 > pub) cw_full(it - 1);
    VMW(0); cw_full(ALL);
    return;
  }

  const int c = wave - 1, g4 = lane >> 4, n = lane & 15, q = n >> 2, p = lane & 3;
  const rsrc_t rws = mkrs(a.ws, WS_END);
  const char* wsp = (const char*)a.ws; asm volatile("" : "+s"(wsp));
  const u32 tag = *(const __attribute__((address_space(4))) u32*)(wsp + WS_EPOCH) + 1u;
  asm volatile("" ::"s"(tag));
  VMW(0);
  const u32 yso = WS_YG + (s == 1 ? 288 * 2048 : 0) + (b >> 1) * (36 * 2048) + (hkv * 2 + (b & 1)) * 64;
  if (c == 4) {
    u32x4 wf[16];
    if (s == 1) {
      read_w(L, wf, 4, lane);
      cw_st(W_MF, 1u);
    }
    cw_wait(W_FULL, s == 3 ? GATE1 : s == 0 ? POLL_A0 : POLL_A);
    fetch<4>(L, rws, lane * 16 + WS_QG + (s < 3 ? ah * 128 : 8192 + hkv * 128) * 8, 16384, tag, (s < 3 ? QA_OFF : OB_OFF) + lane * 8, s < 3 ? 512 : OB_STR, W_QA, bud);
    if (s == 3) {
      cw_wait(W_QS, 5); cw_wait(W_FULL, GATE2);
      fetch<1>(L, rws, lane * 16 + WS_HINT, 0, tag, MG_OFF + lane * 8, 0, W_HINT, bud);
      cw_wait(W_BP, 4);
      fetch<4>(L, rws, n * 32 + (g4 & 1) * 16 + WS_SG + hkv * 2048, 512, tag, MG_OFF + lane * 8, 512, W_SGF, bud);
      cw_wait(W_FULL, ALL);
      read_w(L, wf, 4, lane);
    } else if (s == 1) {
      cw_wait(W_FULL, POLL_C);
      const u32 so = WS_CG + hkv * 32768 + (b & 1) * 1024;
      for (;;) {
        const u32x4 t = gld(rws, q * 8192 + (p & 1) * 512, so);
        if (allz(tagx(t, tag)) || !glb_retry()) break;
        __builtin_amdgcn_s_sleep(16);
      }
      u32x4 cv[16];
      for (;;) {
        u32 bad = 0;
#pragma unroll
        for (int i = 0; i < 16; ++i) cv[i] = gld(rws, lane * 16 + (i & 1) * 2048, so + (i >> 1) * 4096);
#pragma unroll
        for (int i = 0; i < 16; ++i) bad |= tagx(cv[i], tag);
        if (allz(bad) || !glb_retry()) break;
      }
#pragma unroll
      for (int i = 0; i < 4; ++i)
        *(LAS u32*)(L + OB_OFF + i * OB_STR + lane * 4) = pk((asf(cv[i][0]) + asf(cv[4 + i][0])) + (asf(cv[8 + i][0]) + asf(cv[12 + i][0])), (asf(cv[i][2]) + asf(cv[4 + i][2])) + (asf(cv[8 + i][2]) + asf(cv[12 + i][2])));
      cw_st(W_PJ, 4u);
    }
    if (s & 1) {
      project(L, wf, rws, yso, tag, 4, lane, bud);
    } else {
      cw_wait(W_O2, 4);
      __builtin_amdgcn_s_sleep(16);
      final_sum(rws, a.out, s == 0, 256 + hkv * 4 + b, tag, lane, bud);
      if (g == 16 && lane == 0) __builtin_amdgcn_raw_buffer_store_b32(tag, rws, 0, WS_EPOCH, SC1);
    }
    return;
  }

  if (s < 3) {
    u32x4 wf[16];
    if (s == 1) {
      cw_wait(W_WF, 4);
      read_w(L, wf, c, lane);
      cw_wait(W_MF, 1);
      if (lane < 4) asm volatile("ds_add_u32 %0, %1" ::"v"(W_FREE + lane * 4), "v"(1u) : "memory");
    } else {
      const u32 aw = g4 * 1024 + (c >> 1) * 512 + q * 64 + (c & 1) * 32 + 8 * p, ax = SB_OFF + p * 1152 + g4 * 16;
      const u32 vo = (n * 2048 + 2 * g4) * 8, so = WS_QG + ((s ? 0 : 8192) + hkv * 128 + b * 32 + 8 * c) * 8;
      if (j == 2 && c == 0) { __builtin_amdgcn_raw_buffer_store_b32(2u, rws, WS_TK + hkv * 128, 0, SC1); VMW(0); }
      cw_wait(W_WF, 2);
      u32x4 xr[18], wq[9];
#pragma unroll
      for (int ks = 0; ks < 18; ++ks) xr[ks] = ld128(L, ax + ks * 64);
      f32x4 a0 = ZERO4, a1 = ZERO4;
#pragma unroll
      for (int hf = 0; hf < 2; ++hf) {
        if (hf) { cw_wait(W_WF, 4); }
#pragma unroll
        for (int ks = 0; ks < 9; ++ks) wq[ks] = ldtr2(L, aw + (hf * 9 + ks) * 4096, aw + (hf * 9 + ks) * 4096 + 256);
        if (lane < 2) asm volatile("ds_add_u32 %0, %1" ::"v"(W_FREE + hf * 8 + lane * 4), "v"(1u) : "memory");
        __builtin_amdgcn_sched_barrier(0);
#pragma unroll
        for (int ks = 0; ks < 9; ++ks) { if (ks & 1) a1 = mfma(wq[ks], xr[hf * 9 + ks], a1); else a0 = mfma(wq[ks], xr[hf * 9 + ks], a0); }
        __builtin_amdgcn_sched_barrier(0);
      }
      a0 += a1;
      if (n < 4) gst(rws, u32x4{pk(a0[0], a0[1]), tag, pk(a0[2], a0[3]), tag}, vo, so);
    }
    float m = -1e30f, l = 0.f;
    f32x4 acc[8];
#pragma unroll
    for (int i = 0; i < 8; ++i) acc[i] = ZERO4;
    u32x4 qf[4], kf[8], vf[8];
    const u32 lpK = n * 256 + ((g4 ^ swz(n)) << 4), lpQ = QA_OFF + (n & 1) * 256 + g4 * 16;
    const u32 lpV = (4 * g4 + q) * 256 + (((p >> 1) ^ swz(4 * g4 + q)) << 4) + 8 * (p & 1);
#define READ_K() _Pragma("unroll") for (int kk = 0; kk < 4; ++kk) { const u32 ak = (sb + lpK) ^ (kk << 6); kf[kk] = ld128(L, ak); kf[4 + kk] = ld128(L, ak + 4096); }
#define READ_V() do { _Pragma("unroll") for (int ct = 0; ct < 8; ++ct) { const u32 av = (sb + lpV) ^ (ct << 5); vf[ct] = ldtr2(L, av + 8192, av + 12288); } \
      cw_add(W_FREE + (it & 7) * 4, 4, lane); } while (0)
#define SCORES() f32x4 s0 = ZERO4, s1 = ZERO4; \
      _Pragma("unroll") for (int kk = 0; kk < 4; ++kk) { s0 = mfma(kf[kk], qf[kk], s0); s1 = mfma(kf[4 + kk], qf[kk], s1); }
#define PICK8(e) const f32x4 e##0 = (n & 2) ? acc[1] : acc[0], e##1 = (n & 2) ? acc[3] : acc[2], e##2 = (n & 2) ? acc[5] : acc[4], e##3 = (n & 2) ? acc[7] : acc[6], \
      e##4 = (n & 4) ? e##1 : e##0, e##5 = (n & 4) ? e##3 : e##2, e = (n & 8) ? e##5 : e##4
    const u32 vrec = (n & 1) * 512 + (n >> 1) * 64 + g4 * 16;
    bool first = true;
    int it = 4 + c;
#pragma unroll 1
    for (; it < ITU; it += 4) {
      const u32 sb = (it & 7) * SLOT;
      cw_wait(W_FULL, it + 1);
      READ_K(); READ_V();
      if (first) {
        first = false;
        cw_wait(W_QA, 1);
#pragma unroll
        for (int kk = 0; kk < 4; ++kk) qf[kk] = ld128(L, lpQ + ab * 512 + kk * 64);
      }
      SCORES();
      const u32x4 pf = soft<8>(s0, s1, m, l, acc);
#pragma unroll
      for (int ct = 0; ct < 8; ++ct) acc[ct] = mfma(vf[ct], pf, acc[ct]);
    }
    {
      l = xg_sum(l);
      PICK8(e);
      *(LAS f32x4*)(L + MG_OFF + c * 1024 + lane * 16) = e;
      if (lane < 2) *(LAS f32x2*)(L + MS_OFF + (c * 2 + lane) * 8) = f32x2{m, l};
      if (cw_add_rtn(W_MRG, 1, lane) == 3u) {
        f32x2 sv[4]; f32x4 ov[4];
#pragma unroll
        for (int i = 0; i < 4; ++i) { sv[i] = *(LAS f32x2*)(L + MS_OFF + (i * 2 + (n & 1)) * 8); ov[i] = *(LAS f32x4*)(L + MG_OFF + i * 1024 + lane * 16); }
        const float M = fmaxf(fmaxf(sv[0][0], sv[1][0]), fmaxf(sv[2][0], sv[3][0]));
        float Ls = 0.f; f32x4 o = ZERO4;
#pragma unroll
        for (int i = 0; i < 4; ++i) { const float w = ex2((sv[i][0] - M) * CS); Ls = fmaf(w, sv[i][1], Ls); o += w * ov[i]; }
        o *= __builtin_amdgcn_rcpf(Ls);
        gst(rws, u32x4{pk(o[0], o[1]), tag, pk(o[2], o[3]), tag}, vrec, WS_AG + ((ah * 4 + ab) * 16 + as) * 1024);
        if (lane < 2) gst(rws, u32x4{asu(M), tag, asu(Ls), tag}, lane * 16, WS_AS + (ah * 4 + ab) * 2048 + as * 128);
      }
    }
#pragma unroll 1
    for (;; it += 4) {
      const u32 t = cw_wait(W_TK + (it & 15) * 4, it << 8) & 255u, sb = (it & 7) * SLOT;
      if (t >= (u32)NTICK || bud == 0u) break;
#pragma unroll
      for (int kk = 0; kk < 4; ++kk) qf[kk] = ld128(L, lpQ + (t & 3u) * 512 + kk * 64);
      const u32 pr = ah * 4 + (t & 3u), rec = 3 + (t >> 2), va = vrec + WS_AG + (pr * 16 + rec) * 1024, vs = lane * 16 + WS_AS + pr * 2048 + rec * 128;
      cw_wait(W_FULL, it + 1);
      READ_K(); READ_V();
      SCORES();
      m = xg_max(MAX8(s0, s1));
      const float mc = -m * CS;
#pragma unroll
      for (int i = 0; i < 4; ++i) { s0[i] = ex2(fmaf(s0[i], CS, mc)); s1[i] = ex2(fmaf(s1[i], CS, mc)); }
      const u32x4 pf = {pk(s0[0], s0[1]), pk(s0[2], s0[3]), pk(s1[0], s1[1]), pk(s1[2], s1[3])};
      f32x4 e0 = ZERO4, e1 = ZERO4;
#pragma unroll
      for (int ct = 0; ct < 8; ct += 2) { e0 = mfma(vf[ct], (n >> 1) == ct ? pf : u32x4{0u, 0u, 0u, 0u}, e0); e1 = mfma(vf[ct + 1], (n >> 1) == ct + 1 ? pf : u32x4{0u, 0u, 0u, 0u}, e1); }
      s0 += s1;
      l = xg_sum((s0[0] + s0[1]) + (s0[2] + s0[3]));
      e0 = (e0 + e1) * __builtin_amdgcn_rcpf(l);
      gst(rws, u32x4{pk(e0[0], e0[1]), tag, pk(e0[2], e0[3]), tag}, va, 0);
      if (lane < 2) gst(rws, u32x4{asu(m), tag, asu(l), tag}, vs, 0);
    }
    if (s == 1) {
      project(L, wf, rws, yso, tag, c, lane, bud);
    } else {
      cw_add(W_O2, 1, lane);
      cw_wait(W_O2, 4);
      __builtin_amdgcn_s_sleep(16);
      final_sum(rws, a.out, s == 0, (hkv * 4 + b) * 4 + c, tag, lane, bud);
    }
    return;
  }

  {
    cw_wait(W_WF, c + 1);
    u32x4 wu[16];
    {
      const u32 lp = (IT_UK + c) * SLOT + n * 512 + ((g4 ^ swz(n)) << 4);
#pragma unroll
      for (int i = 0; i < 16; ++i) wu[i] = ld128(L, (lp ^ ((i & 3) << 6)) + ((i >> 2) & 1) * 8192 + (i >> 3) * 256);
    }
    cw_add(W_FREE + (IT_UK + c) * 4, 4, lane);
    cw_wait(W_QA, 1);
    f32x4 d[4] = {ZERO4, ZERO4, ZERO4, ZERO4};
#pragma unroll
    for (int i = 0; i < 8; ++i) {
      const u32x4 qv = ld128(L, OB_OFF + p * OB_STR + i * 64 + g4 * 16);
      d[(i >> 2) * 2] = mfma(wu[(i >> 2) * 8 + (i & 3)], qv, d[(i >> 2) * 2]); d[(i >> 2) * 2 + 1] = mfma(wu[(i >> 2) * 8 + 4 + (i & 3)], qv, d[(i >> 2) * 2 + 1]);
    }
    const f32x4 e0 = (q & 1) ? d[1] : d[0], e1 = (q & 1) ? d[3] : d[2], e = (q & 2) ? e1 : e0;
    gst(rws, u32x4{pk(e[0], e[1]), tag, pk(e[2], e[3]), tag}, p * 65536 + (g4 & 1) * 128 + (q >> 1) * 64 + (q & 1) * 32 + (g4 >> 1) * 16, WS_QAG + hkv * 4096 + (b * 4 + c) * 256);
    if (cw_add_rtn(W_QS, 1, lane) == 3u) { if (lane == 0) gst(rws, u32x4{tag, tag, tag, tag}, WS_HINT + (hkv * 4 + b) * 16, 0); cw_add(W_QS, 1, lane); cw_st(W_G1, 1u); }
  }
  {
    const int ch = c & 1;
    const u32 lpR = n * 1024 + ((g4 ^ swz(n)) << 4) + ch * 512;
    const u32 lpC = (4 * g4 + q) * 1024 + (((p >> 1) ^ swz(4 * g4 + q)) << 4) + 8 * (p & 1) + ch * 512;
    u32x4 qa[8], rf[16], cf[16];
#define READ_C(it) do { const u32 sb = ((it) & 7) * SLOT; cw_wait(W_FULL, (it) + 2); \
      _Pragma("unroll") for (int kk = 0; kk < 8; ++kk) { const u32 ak = ((sb + lpR) ^ ((kk & 3) << 6)) + (kk >> 2) * 256; rf[2 * kk] = ld128(L, ak); rf[2 * kk + 1] = ld128(L, ak + SLOT); } \
      _Pragma("unroll") for (int kk = 0; kk < 8; ++kk) { const u32 av = (sb + lpC) ^ (kk << 5); cf[kk] = ldtr2(L, av, av + SLOT); cf[8 + kk] = ldtr2(L, av + 256, av + 256 + SLOT); } \
      if (lane < 2) asm volatile("ds_add_u32 %0, %1" ::"v"(W_FREE + ((it) & 7) * 4 + lane * 4), "v"(1u) : "memory"); __builtin_amdgcn_sched_barrier(0); } while (0)
    READ_C(IT_C);
    {
      const u32 so = WS_QAG + bq * 65536 + (c >> 1) * 32768 + ch * 2048;
      cw_wait(W_HINT, 1);
      u32x4 t[16];
#pragma unroll
      for (int i = 0; i < 16; ++i) t[i] = gld(rws, (n >> 1) * 4096 + (n & 1) * 64 + g4 * 16 + i * 128, so);
      cw_add(W_GI, 1, lane);
      for (;;) {
        u32 bad = 0;
#pragma unroll
        for (int i = 0; i < 16; ++i) bad |= tagx(t[i], tag);
        if (allz(bad) || !glb_retry()) break;
#pragma unroll
        for (int i = 0; i < 16; ++i) t[i] = gld(rws, (n >> 1) * 4096 + (n & 1) * 64 + g4 * 16 + i * 128, so);
      }
#pragma unroll
      for (int i = 0; i < 8; ++i) qa[i] = u32x4{t[2 * i][0], t[2 * i][2], t[2 * i + 1][0], t[2 * i + 1][2]};
    }
    float mb = -1e30f, lb = 0.f;
    f32x4 ob[16];
#pragma unroll
    for (int i = 0; i < 16; ++i) ob[i] = ZERO4;
#pragma unroll 1
    for (int st = 0;; ++st) {
      const u32 xw = ((st & 1) ? (4 + c) * SLOT + 16384 : SB_OFF + c * 2048) + lane * 16, xr = ((st & 1) ? (4 + (c ^ 1)) * SLOT + 16384 : SB_OFF + (c ^ 1) * 2048) + lane * 16;
      f32x4 s0 = ZERO4, s1 = ZERO4;
#pragma unroll
      for (int kk = 0; kk < 8; ++kk) { s0 = mfma(rf[2 * kk], qa[kk], s0); s1 = mfma(rf[2 * kk + 1], qa[kk], s1); }
      *(LAS f32x4*)(L + xw) = s0;
      *(LAS f32x4*)(L + xw + 1024) = s1;
      cw_st(W_S + c * 4, st + 1);
      cw_wait(W_S + (c ^ 1) * 4, st + 1);
      s0 += *(LAS f32x4*)(L + xr); s1 += *(LAS f32x4*)(L + xr + 1024);
      const u32x4 pf = soft<16>(s0, s1, mb, lb, ob);
#pragma unroll
      for (int ct = 0; ct < 16; ++ct) ob[ct] = mfma(cf[ct], pf, ob[ct]);
      if (st == 7) break;
      __builtin_amdgcn_sched_barrier(0);
      READ_C(IT_C + 2 * st + 2);
    }
#pragma unroll
    for (int i = 0; i < 8; ++i) {
      auto x = __builtin_amdgcn_permlane16_swap(pk(ob[2 * i][0], ob[2 * i][1]), pk(ob[2 * i + 1][0], ob[2 * i + 1][1]), false, false);
      auto y = __builtin_amdgcn_permlane16_swap(pk(ob[2 * i][2], ob[2 * i][3]), pk(ob[2 * i + 1][2], ob[2 * i + 1][3]), false, false);
      gst(rws, u32x4{x[0], y[0], x[1], y[1]}, (n >> 1) * 512 + (n & 1) * 64 + (g4 & 1) * 32 + (g4 >> 1) * 16 + (i & 3) * 128,
          WS_PB + ((bq * 16 + sp) * 4 + ch * 2 + (i >> 2)) * 8192 + (c >> 1) * 4096);
    }
    lb = xg_sum(lb);
    if (g4 == 0 && ch == 0) *(LAS f32x2*)(L + ST_OFF + ((c >> 1) * 16 + n) * 8) = f32x2{mb, lb};
    VMW(0);
    if (cw_add_rtn(W_BP, 1, lane) == 3u) {
      const f32x2 ml = *(LAS f32x2*)(L + ST_OFF + (lane & 31) * 8);
      if (lane < 32) gst(rws, u32x4{asu(ml[0]), tag, asu(ml[1]), tag}, (lane >> 1) * 2048 + (lane & 1) * 16, WS_SG + (bq * 16 + sp) * 32);
    }
  }
  {
    cw_wait(W_SGF, 1);
    u32x4 pv[8];
#pragma unroll
    for (int i = 0; i < 8; ++i) pv[i] = gld(rws, (lane >> 5) * 32768 + (lane & 31) * 16, WS_PB + ((c * 16 + 2 * i) * 4 + b) * 8192 + hkv * 512);
    cw_add(W_GI, 1, lane);
    const f32x2 st = *(LAS f32x2*)(L + MG_OFF + c * 512 + lane * 8);
    const float w = ex2((st[0] - row_max(st[0])) * CS), cf = w / row_sum(w * st[1]);
    f32x4 o0 = ZERO4, o1 = ZERO4;
#pragma unroll
    for (int i = 0; i < 8; ++i) {
      const float f = asf(__builtin_amdgcn_ds_bpermute((((lane >> 2) & 1) * 16 + 2 * i + (lane >> 5)) * 4, asu(cf)));
      o0 += f * f32x4{bflo(pv[i][0]), bfhi(pv[i][0]), bflo(pv[i][1]), bfhi(pv[i][1])};
      o1 += f * f32x4{bflo(pv[i][2]), bfhi(pv[i][2]), bflo(pv[i][3]), bfhi(pv[i][3])};
    }
#pragma unroll
    for (int i = 0; i < 4; ++i) {
      auto t0 = __builtin_amdgcn_permlane32_swap(asu(o0[i]), asu(o0[i]), false, false); o0[i] = asf(t0[0]) + asf(t0[1]);
      auto t1 = __builtin_amdgcn_permlane32_swap(asu(o1[i]), asu(o1[i]), false, false); o1[i] = asf(t1[0]) + asf(t1[1]);
    }
    if (lane < 32) *(LAS u32x4*)(L + OB_OFF + c * OB_STR + ((lane >> 2) & 1) * 256 + (lane >> 3) * 64 + p * 16) = u32x4{pk(o0[0], o0[1]), pk(o0[2], o0[3]), pk(o1[0], o1[1]), pk(o1[2], o1[3])};
    cw_add(W_OB, 1, lane);
    cw_wait(W_FULL, IT_UV + 4);
    u32x4 wf[16];
    {
      const u32 r0 = 8 * (g4 & 1) + q;
      const u32 lp0 = (8 * g4 + q) * 512 + ((((p >> 1) + 8 * c) ^ swz(r0)) << 4) + 8 * (p & 1);
      const u32 lp1 = (8 * g4 + q + 4) * 512 + ((((p >> 1) + 8 * c) ^ swz(r0 + 4)) << 4) + 8 * (p & 1);
#pragma unroll
      for (int i = 0; i < 16; ++i) wf[i] = ldtr2(L, (lp0 ^ ((i & 3) << 5)) + ((IT_UV + (i >> 2)) & 7) * SLOT, (lp1 ^ ((i & 3) << 5)) + ((IT_UV + (i >> 2)) & 7) * SLOT);
    }
    cw_wait(W_OB, 4);
    f32x4 d[4] = {ZERO4, ZERO4, ZERO4, ZERO4};
#pragma unroll
    for (int ks = 0; ks < 4; ++ks) {
      const u32x4 of = ld128(L, OB_OFF + p * OB_STR + (c >> 1) * 256 + ks * 64 + g4 * 16);
#pragma unroll
      for (int t = 0; t < 4; ++t) d[t] = mfma(wf[ks * 4 + t], of, d[t]);
    }
    {
      const u32 vo = p * 2048 + (q >> 1) * 128 + g4 * 32 + (q & 1) * 16, so = WS_CG + ((hkv * 4 + b) * 1024 + c * 64) * 8;
      gst(rws, pick2(d[0], d[1], q, tag), vo, so); gst(rws, pick2(d[2], d[3], q, tag), vo + 256, so);
    }
  }
  {
    cw_add(W_O2, 1, lane);
    cw_wait(W_O2, 4);
    const int rn = n ^ (lane >> 5);
    const u32 so = WS_AG + (hkv * 4 + c) * 16384 + (b & 1) * 512, ss = WS_AS + (hkv * 4 + c) * 2048 + (b & 1) * 16 + (rn < NREC ? rn : 0) * 128;
    const u32 lr = (4 + c) * SLOT, v0 = (lane & 31) * 16, v1 = v0 + (lane >> 5) * 1024;
    u32x4 wf[16], sv, ov[NPR];
    u32 todo = 0x5555u & ((1u << NREC) - 1u);
    {
      u32 ask = todo;
      while (cw_ld(W_WI) < 2u && bud != 0u) {
        sv = gld(rws, ss, 0);
        const unsigned long long sb = __builtin_amdgcn_ballot_w64(tagx(sv, tag) != 0u);
        const u32 nw = ask & ~((u32)sb | (u32)(sb >> 32));
        for (u32 k = nw; k; k &= k - 1u) { const u32 i = __builtin_ctz(k); dma_sc1(rws, lr + i * 512, i + 1 < NREC ? v1 : v0, so + i * 1024); }
        ask &= ~nw;
        bud = bud < 16u ? 0u : bud - 16u;
        __builtin_amdgcn_s_sleep(2);
      }
      todo = ask | 0x10000u;
    }
    for (;;) {
      sv = gld(rws, ss, 0);
      for (u32 k = todo & 0xffffu; k; k &= k - 1u) { const u32 i = __builtin_ctz(k); dma_sc1(rws, lr + i * 512, i + 1 < NREC ? v1 : v0, so + i * 1024); }
      if (todo >> 16) { cw_wait(W_FULL, ALL); read_w(L, wf, c, lane); }
      VMW(0);
      const unsigned long long sb = __builtin_amdgcn_ballot_w64(tagx(sv, tag) != 0u);
      if (todo & ~(((u32)sb | (u32)(sb >> 32)) & 0xffffu)) {
#pragma unroll
        for (int i = 0; i < NPR; ++i) ov[i] = ld128(L, lr + lane * 16 + i * 1024);
        u32 bad = 0;
#pragma unroll
        for (int i = 0; i < NPR; ++i) bad |= (tagx(ov[i], tag) != 0u) << (2 * i);
        if (allz(bad)) todo = 0u;
        else { bad |= __builtin_amdgcn_mov_dpp(bad, 0x128, 0xf, 0xf, true); bad |= __builtin_amdgcn_mov_dpp(bad, 0x124, 0xf, 0xf, true); bad |= __builtin_amdgcn_mov_dpp(bad, 0x122, 0xf, 0xf, true); bad |= __builtin_amdgcn_mov_dpp(bad, 0x121, 0xf, 0xf, true);
          todo = __builtin_amdgcn_readlane(bad, 0) | __builtin_amdgcn_readlane(bad, 16) | __builtin_amdgcn_readlane(bad, 32) | __builtin_amdgcn_readlane(bad, 48); }
      }
      if (todo == 0u && sb == 0ull) break;
      bud = bud < 16u ? 0u : bud - 16u;
      if (bud == 0u) break;
      asm volatile("" ::: "memory");
    }
    const float ms = asf(sv[0]), w0 = ex2((ms - row_max(ms)) * CS) * asf(sv[2]), w = rn < NREC ? w0 : 0.f;
    f32x4 o[2] = {ZERO4, ZERO4};
    merge_rec<0>(o, ov, w);
    o[0] += o[1];
    const float rs = __builtin_amdgcn_rcpf(asf(__builtin_amdgcn_readfirstlane(asu(row_sum(w)))));
#pragma unroll
    for (int i = 0; i < 4; ++i) { auto t = __builtin_amdgcn_permlane32_swap(asu(o[0][i]), asu(o[0][i]), false, false); o[0][i] = (asf(t[0]) + asf(t[1])) * rs; }
    *(LAS u32x2*)(L + OB_OFF + c * OB_STR + (lane & 31) * 8) = u32x2{pk(o[0][0], o[0][1]), pk(o[0][2], o[0][3])};
    cw_add(W_PJ, 1, lane);
    project(L, wf, rws, yso, tag, c, lane, bud);
  }
}

extern "C" void kernel_launch(void* const* d_in, const int* in_sizes, int n_in, void* d_out, int out_size, void* d_ws, size_t ws_size, hipStream_t stream) {
  Args a;
  for (int i = 0; i < 11; ++i) a.in[i] = d_in[i];
  a.out = d_out;
  a.ws = d_ws;
  concat_heads_decode_kernel<<<256, 384, 0, stream>>>(a);
}
```

```cpp
#include <hip/hip_runtime.h>
#include <stdint.h>

#define LAS __attribute__((address_space(3)))
typedef unsigned u32;
typedef u32 u32x2 __attribute__((ext_vector_type(2)));
typedef u32 u32x4 __attribute__((ext_vector_type(4)));
typedef float f32x2 __attribute__((ext_vector_type(2)));
typedef float f32x4 __attribute__((ext_vector_type(4)));
typedef short s16x4 __attribute__((ext_vector_type(4)));
typedef __bf16 bf16x2 __attribute__((ext_vector_type(2)));
typedef __bf16 bf16x8 __attribute__((ext_vector_type(8)));
typedef __amdgpu_buffer_rsrc_t rsrc_t;

constexpr int N0 = 38, N1 = 38, N2 = 39, SLOWK = 2;
constexpr int NSUM = N0 + N1 + N2, NU = 128 - NSUM, NREC = 3 + NU, NTICK = 4 * NU;
static_assert(NU >= 1 && NREC <= 16, "split");
constexpr int LW_A = 32, LW_B = 32, LW_W = 44;
constexpr int POLL_A = 9, POLL_A0 = 7, POLL_C = N1 + 2;
constexpr int GATE1 = 10, GATE2 = 14;
constexpr int NT = 2;
static_assert(LW_A <= 32, "a ticket is read two items after it was drawn");

constexpr int SLOT = 18432, NS = 8, RING = SLOT * NS;
constexpr int CTL = RING;
constexpr int W_FULL = CTL, W_WF = CTL + 4, W_QA = CTL + 8, W_MRG = CTL + 12, W_HINT = CTL + 16, W_SGF = CTL + 20, W_OB = CTL + 24, W_BP = CTL + 28,
              W_FREE = CTL + 32, W_S = CTL + 64, W_GI = CTL + 80, W_QS = CTL + 84, W_PJ = CTL + 88, W_WI = CTL + 92, W_MF = CTL + 96, W_O2 = CTL + 100, W_G1 = CTL + 104, W_TK = CTL + 128;
constexpr int ST_OFF = CTL + 256;
constexpr int OB_OFF = ST_OFF + 512, OB_STR = 576;
constexpr int SB_OFF = OB_OFF + 4 * OB_STR;
constexpr int QA_OFF = SB_OFF + 5120;
constexpr int MG_OFF = SB_OFF + 8192;
constexpr int MS_OFF = MG_OFF + 4096;
constexpr int LDS_BYTES = MS_OFF + 64;
static_assert(LDS_BYTES <= 163840 && (SB_OFF & 15) == 0 && (MG_OFF & 15) == 0, "lds");

constexpr u32 WS_EPOCH = 0, WS_HINT = 1024;
constexpr u32 WS_TK = 2048;
constexpr u32 WS_QG = 4096;
constexpr u32 WS_QAG = WS_QG + 2 * 4 * 2048 * 8;
constexpr u32 WS_AG = WS_QAG + 4 * 32 * 512 * 2;
constexpr u32 WS_AS = WS_AG + 64 * 16 * 1024;
constexpr u32 WS_SG = WS_AS + 64 * 2048;
constexpr u32 WS_CG = WS_SG + 4 * 16 * 32 * 16;
constexpr u32 WS_YG = WS_CG + 16 * 4 * 4 * 256 * 8;
constexpr u32 WS_PB = WS_YG + 2 * 32 * 18432;
constexpr u32 WS_END = WS_PB + 4 * 16 * 4 * 16 * 512;
static_assert((WS_PB & 127) == 0, "ws");

constexpr float CS = 0.08838834764831845f * 1.4426950408889634f;
constexpr int SC1 = 16;
constexpr u32 CODE_BYTES = 24576;
constexpr u32 SPIN_BUDGET = 1u << 19;

struct Args { const void* in[11]; void* out; void* ws; };

__device__ __forceinline__ rsrc_t mkrs(const void* p, u32 n) { return __builtin_amdgcn_make_buffer_rsrc((void*)p, 0, n, 0x00020000); }
constexpr __device__ __forceinline__ u32 swz(u32 r) { return (((r & 7u) ^ ((r >> 1) & 4u)) << 1) | ((r >> 3) & 1u); }
__device__ __forceinline__ f32x4 mfma(u32x4 a, u32x4 b, f32x4 c) {
  return __builtin_amdgcn_mfma_f32_16x16x32_bf16(__builtin_bit_cast(bf16x8, a), __builtin_bit_cast(bf16x8, b), c, 0, 0, 0);
}
__device__ __forceinline__ u32 pk(float lo, float hi) { f32x2 v = {lo, hi}; return __builtin_bit_cast(u32, __builtin_convertvector(v, bf16x2)); }
__device__ __forceinline__ float bflo(u32 v) { return __builtin_bit_cast(float, v << 16); }
__device__ __forceinline__ float bfhi(u32 v) { return __builtin_bit_cast(float, v & 0xffff0000u); }
__device__ __forceinline__ float asf(u32 v) { return __builtin_bit_cast(float, v); }
__device__ __forceinline__ u32 asu(float v) { return __builtin_bit_cast(u32, v); }
__device__ __forceinline__ float ex2(float x) { return __builtin_amdgcn_exp2f(x); }
__device__ __forceinline__ u32x4 ld128(LAS unsigned char* L, u32 a) { return *(LAS u32x4*)(L + a); }
__device__ __forceinline__ u32x4 ldtr2(LAS unsigned char* L, u32 a0, u32 a1) {
  const u32x2 x = __builtin_bit_cast(u32x2, __builtin_amdgcn_ds_read_tr16_b64_v4i16((LAS s16x4*)(L + a0)));
  const u32x2 y = __builtin_bit_cast(u32x2, __builtin_amdgcn_ds_read_tr16_b64_v4i16((LAS s16x4*)(L + a1)));
  return u32x4{x[0], x[1], y[0], y[1]};
}
__device__ __forceinline__ float xg_max(float x) {
  auto s = __builtin_amdgcn_permlane16_swap(asu(x), asu(x), false, false);
  x = fmaxf(asf(s[0]), asf(s[1]));
  auto t = __builtin_amdgcn_permlane32_swap(asu(x), asu(x), false, false);
  return fmaxf(asf(t[0]), asf(t[1]));
}
__device__ __forceinline__ float xg_sum(float x) {
  auto s = __builtin_amdgcn_permlane16_swap(asu(x), asu(x), false, false);
  x = asf(s[0]) + asf(s[1]);
  auto t = __builtin_amdgcn_permlane32_swap(asu(x), asu(x), false, false);
  return asf(t[0]) + asf(t[1]);
}
template <int CTRL> __device__ __forceinline__ float dpp(float x) { return asf(__builtin_amdgcn_mov_dpp(asu(x), CTRL, 0xf, 0xf, true)); }
__device__ __forceinline__ float row_sum(float x) { x += dpp<0x128>(x); x += dpp<0x124>(x); x += dpp<0x122>(x); x += dpp<0x121>(x); return x; }
__device__ __forceinline__ float row_max(float x) { x = fmaxf(x, dpp<0x128>(x)); x = fmaxf(x, dpp<0x124>(x)); x = fmaxf(x, dpp<0x122>(x)); x = fmaxf(x, dpp<0x121>(x)); return x; }

__device__ __forceinline__ u32 cw_ld(u32 a) { u32 v; asm volatile("ds_read_b32 %0, %1\n\ts_waitcnt lgkmcnt(0)" : "=v"(v) : "v"(a) : "memory"); return __builtin_amdgcn_readfirstlane(v); }
__device__ __forceinline__ void cw_st(u32 a, u32 v) { asm volatile("ds_write_b32 %0, %1" ::"v"(a), "v"(v) : "memory"); }
#define cw_full(v) cw_st(W_FULL, (v))
__device__ __forceinline__ void cw_add(u32 a, u32 v, int lane) { if (lane == 0) asm volatile("ds_add_u32 %0, %1" ::"v"(a), "v"(v) : "memory"); asm volatile("" ::: "memory"); }
__device__ __forceinline__ u32 cw_add_rtn(u32 a, u32 v, int lane) {
  u32 r = 0;
  if (lane == 0) asm volatile("ds_add_rtn_u32 %0, %1, %2\n\ts_waitcnt lgkmcnt(0)" : "=v"(r) : "v"(a), "v"(v) : "memory");
  asm volatile("" ::: "memory");
  return __builtin_amdgcn_readfirstlane(r);
}
__device__ __forceinline__ u32 cw_wait_(u32 a, u32 want, u32& bud) {
  u32 t;
  bud = __builtin_amdgcn_readfirstlane(bud); want = __builtin_amdgcn_readfirstlane(want);
  asm volatile("1:\n\tds_read_b32 v255, %2\n\ts_waitcnt lgkmcnt(0)\n\tv_readfirstlane_b32 %0, v255\n\ts_cmp_ge_u32 %0, %3\n\ts_cbranch_scc1 2f\n\t"
               "s_sleep 1\n\ts_sub_u32 %1, %1, 1\n\ts_cbranch_scc0 1b\n\ts_mov_b32 %1, 0\n2:\n\ts_nop 1"
               : "=&s"(t), "+s"(bud) : "v"(a), "s"(want) : "memory", "scc", "v255");
  return t;
}
#define cw_wait(a, want) cw_wait_((a), (want), bud)
__device__ __forceinline__ bool glb_retry_(u32& bud) {
  bud = __builtin_amdgcn_readfirstlane(bud);
  asm volatile("s_sleep 1\n\ts_sub_u32 %0, %0, 16\n\ts_cselect_b32 %0, 0, %0" : "+s"(bud) :: "memory", "scc");
  return bud != 0u;
}
#define glb_retry() glb_retry_(bud)
__device__ __forceinline__ u32x4 gld(rsrc_t r, u32 voff, u32 soff) { return __builtin_amdgcn_raw_buffer_load_b128(r, voff, soff, SC1); }
__device__ __forceinline__ void gst(rsrc_t r, u32x4 v, u32 voff, u32 soff) { __builtin_amdgcn_raw_buffer_store_b128(v, r, voff + soff, 0, SC1); }
__device__ __forceinline__ u32 tagx(u32x4 v, u32 tag) { return (v[1] ^ tag) | (v[3] ^ tag); }
__device__ __forceinline__ bool allz(u32 x) { return __builtin_amdgcn_ballot_w64(x != 0u) == 0ull; }
template <int N> __device__ __forceinline__ void fetch(LAS unsigned char* L, rsrc_t r, u32 voff, u32 ss, u32 tag, u32 dst, u32 ds, u32 flag, u32& bud) {
  u32x4 v[N];
  for (;;) {
    u32 bad = 0;
#pragma unroll
    for (int i = 0; i < N; ++i) v[i] = gld(r, voff + i * ss, 0);
#pragma unroll
    for (int i = 0; i < N; ++i) bad |= tagx(v[i], tag);
    if (allz(bad) || !glb_retry()) break;
    if (flag == W_QA) cw_st(W_G1, 1u);
  }
#pragma unroll
  for (int i = 0; i < N; ++i) *(LAS u32x2*)(L + dst + i * ds) = u32x2{v[i][0], v[i][2]};
  cw_st(flag, 1u);
}
__device__ __forceinline__ void dma_sc1(rsrc_t r, u32 lds, u32 voff, u32 soff) {
  u32 keep;
  asm volatile("s_mov_b32 %0, m0\n\ts_mov_b32 m0, %1\n\ts_nop 4\n\tbuffer_load_dwordx4 %2, %3, %4 offen sc1 lds\n\ts_mov_b32 m0, %0"
               : "=&s"(keep) : "s"(__builtin_amdgcn_readfirstlane(lds)), "v"(voff), "s"(r), "s"(__builtin_amdgcn_readfirstlane(soff)) : "memory");
}
#define DMA(r, ldsoff, voff, soff, imm, aux) __builtin_amdgcn_raw_ptr_buffer_load_lds((r), (LAS void*)(L + (ldsoff)), 16, (voff), (soff), (imm), (aux))

#define VMW(n) asm volatile("s_waitcnt vmcnt(" #n ")" ::: "memory")
#define ZERO4 f32x4{0.f, 0.f, 0.f, 0.f}
#define MAX8(a, b) fmaxf(fmaxf(fmaxf(a[0], a[1]), fmaxf(a[2], a[3])), fmaxf(fmaxf(b[0], b[1]), fmaxf(b[2], b[3])))
constexpr float LAZY = 192.f;
template <int NA> __device__ __forceinline__ u32x4 soft(const f32x4 s0, const f32x4 s1, float& m, float& l, f32x4* acc) {
  const float mx = MAX8(s0, s1);
  if (__builtin_amdgcn_ballot_w64(mx > m + LAZY) != 0ull) {
    const float mn = fmaxf(m, xg_max(mx)), al = ex2((m - mn) * CS);
    m = mn; l *= al;
#pragma unroll
    for (int i = 0; i < NA; ++i) acc[i] *= al;
  }
  const float mc = -m * CS;
  float pr[8];
#pragma unroll
  for (int i = 0; i < 4; ++i) { pr[i] = ex2(fmaf(s0[i], CS, mc)); pr[4 + i] = ex2(fmaf(s1[i], CS, mc)); }
  l += ((pr[0] + pr[1]) + (pr[2] + pr[3])) + ((pr[4] + pr[5]) + (pr[6] + pr[7]));
  return u32x4{pk(pr[0], pr[1]), pk(pr[2], pr[3]), pk(pr[4], pr[5]), pk(pr[6], pr[7])};
}
__device__ __forceinline__ u32x4 pick2(const f32x4 d0, const f32x4 d1, int q, u32 tag) {
  const float x0 = (q & 2) ? d1[0] : d0[0], x1 = (q & 2) ? d1[1] : d0[1], x2 = (q & 2) ? d1[2] : d0[2], x3 = (q & 2) ? d1[3] : d0[3];
  return u32x4{asu((q & 1) ? x2 : x0), tag, asu((q & 1) ? x3 : x1), tag};
}

constexpr int NPR = (NREC + 1) / 2;
template <int I> __device__ __forceinline__ void merge_rec(f32x4* o, const u32x4* t, float w) {
  if constexpr (I < NPR) { o[I & 1] += dpp<0x150 + 2 * I>(w) * f32x4{bflo(t[I][0]), bfhi(t[I][0]), bflo(t[I][2]), bfhi(t[I][2])}; merge_rec<I + 1>(o, t, w); }
}

constexpr int IT_UK = 0, IT_C = 4, IT_UV = 20, IT_W = 24;
constexpr u32 ALL = 1u << 20;
template <int N> __device__ __forceinline__ void vmw() { asm volatile("s_waitcnt vmcnt(%0)" ::"i"(N) : "memory"); }

__device__ __forceinline__ void read_w(LAS unsigned char* L, u32x4* wf, int c, int lane) {
  const u32 lp = (8 * (lane >> 4) + ((lane >> 2) & 3)) * 576 + 8 * (lane & 3) + 128 * c, t2 = c < 4 ? 64 : 0;
#pragma unroll
  for (int i = 0; i < 16; ++i) { const u32 ad = (i >> 2) * SLOT + lp + (i & 1) * 32 + ((i & 2) ? t2 : 0); wf[i] = ldtr2(L, ad, ad + 2304); }
}
__device__ __forceinline__ void project(LAS unsigned char* L, const u32x4* wf, rsrc_t rws, u32 so, u32 tag, int c, int lane, u32& bud) {
  const int g4 = lane >> 4, q = (lane >> 2) & 3, p = lane & 3;
  u32 ao = OB_OFF + p * OB_STR + g4 * 16, vo = so + c * 16384 + p * (72 * 2048) + ((q >> 1) * 2 + (g4 >> 1)) * 2048 + (g4 & 1) * 32 + (q & 1) * 16;
  asm volatile("" : "+v"(ao), "+v"(vo));
  cw_wait(W_PJ, 4);
  f32x4 d[4] = {ZERO4, ZERO4, ZERO4, ZERO4};
  u32x4 of[4];
#pragma unroll
  for (int ks = 0; ks < 4; ++ks) of[ks] = ld128(L, ao + ks * 64);
  __builtin_amdgcn_sched_barrier(0);
#pragma unroll
  for (int ks = 0; ks < 4; ++ks) { d[0] = mfma(wf[ks * 4], of[ks], d[0]); d[1] = mfma(wf[ks * 4 + 1], of[ks], d[1]); }
#pragma unroll
  for (int ks = 0; ks < 4; ++ks) { d[2] = mfma(wf[ks * 4 + 2], of[ks], d[2]); d[3] = mfma(wf[ks * 4 + 3], of[ks], d[3]); }
  gst(rws, pick2(d[0], d[1], q, tag), vo, 0);
  if (c < 4) gst(rws, pick2(d[2], d[3], q, tag), vo + 8192, 0);
}
__device__ __forceinline__ void final_sum(rsrc_t rws, void* out, int br, int t, u32 tag, int lane, u32& bud) {
  if (t >= 288) return;
  u32 vo = WS_YG + (br * 288 + t) * 2048 + lane * 16, oo = ((t / 72) * 1152 + br * 576 + (t % 72) * 8) * 2 + lane * 4;
  const rsrc_t ro = mkrs(out, 4u * 1152u * 2u);
  asm volatile("" : "+v"(vo), "+v"(oo));
  u32x4 y, z;
  for (;;) { y = gld(rws, vo, 0); z = gld(rws, vo + 1024, 0); if (allz(tagx(y, tag) | tagx(z, tag)) || !glb_retry()) break; }
  float r0 = asf(y[0]) + asf(z[0]), r1 = asf(y[2]) + asf(z[2]);
  r0 += dpp<0x128>(r0); r1 += dpp<0x128>(r1); r0 += dpp<0x124>(r0); r1 += dpp<0x124>(r1);
  const u32 r = pk(xg_sum(r0), xg_sum(r1));
  if (lane < 4) __builtin_amdgcn_raw_buffer_store_b32(r, ro, oo, 0, SC1);
}

__global__ void __launch_bounds__(384, 2) concat_heads_decode_kernel(Args a) {
  __shared__ __attribute__((aligned(16))) unsigned char smem[LDS_BYTES];
  LAS unsigned char* L = (LAS unsigned char*)smem;
  const int lane = threadIdx.x & 63, wave = __builtin_amdgcn_readfirstlane(threadIdx.x >> 6);
  const int g = blockIdx.x, gi = g >> 3, hkv = 2 * (g & 7) + (gi >> 4), j = gi & 15, b = j >> 2, s = j & 3;
  if (wave == 1) {
    cw_st(CTL + lane * 4, 0u);
    unsigned long long pc; asm volatile("s_getpc_b64 %0" : "=s"(pc));
    DMA(mkrs((const void*)(pc & ~255ull), CODE_BYTES), MG_OFF, lane * 16, gi * 1024, 0, 0);
    asm volatile("s_waitcnt lgkmcnt(0)" ::: "memory");
  }
  __builtin_amdgcn_s_barrier();
  const u32 vu = (lane >> 5) * 8192 + (((lane & 31) ^ ((lane >> 5) << 1)) << 4);
#define LOAD_W(in_, LW) do { const rsrc_t r = mkrs(a.in[in_], 4096u * 1152u); u32 wo[9]; int ln = lane; asm volatile("" : "+v"(ln)); \
      _Pragma("unroll") for (int i = 0; i < 9; ++i) { const u32 ci = i * 64 + ln; wo[i] = ci * 16 + ((ci * 3641u) >> 17) * 576u; } \
      u32 sw = ((u32)hkv * 256u + (b & 1) * 128u) * 1152u + (b >> 1) * 576u; \
      _Pragma("unroll 1") for (int t = 0; t < 8; ++t, sw += 16u * 1152u) { _Pragma("unroll") for (int i = 0; i < 9; ++i) { if (i % 3 == 0) vmw<LW>(); DMA(r, t * 9216 + i * 1024, wo[i], sw, 0, NT); } } } while (0)
  if (wave == 5) {
    if (s != 1) {
      const rsrc_t rw = mkrs(a.in[s == 0 ? 7 : s == 2 ? 3 : 8], s < 3 ? 576u * 8192u : 512u * 8192u), rx = mkrs(a.in[s ? 0 : 5], s < 3 ? 4u * 576u * 2u : 0u);
      const u32 vo = s < 3 ? ((lane >> 2) & 7) * 8192 + (lane & 3) * 16 + (lane >> 5) * 64 : vu, st = s < 3 ? 8 * 8192 : 2 * 8192, xm = s == 3 ? 0xf0u : 0u;
      u32 so = hkv * 512 + (s < 3 ? b * 128 : b * (128u * 8192u)), lo = 0;
#pragma unroll 1
      for (int t = 0; t < (s < 3 ? 9 : 8); ++t) {
        vmw<40>();
        if (t == 7) cw_st(W_WF, 1u);
#pragma unroll
        for (int i = 0; i < 8; ++i, so += st) DMA(rw, lo + i * 1024, vo ^ ((swz(2 * i) << 4) & xm), so, 0, NT);
        lo += s == 3 && (t & 1) ? SLOT - 8192 : 8192;
        if (t == 1 && s < 3) {
#pragma unroll
          for (int i = 0; i < 5; ++i) DMA(rx, SB_OFF + i * 1024, lane * 16, i * 1024, 0, 0);
        }
      }
    } else {
      LOAD_W(10, 40);
    }
    VMW(32); cw_st(W_WF, 2u); cw_st(W_WI, 1u);
    VMW(16); cw_st(W_WF, 3u);
    VMW(0); cw_st(W_WF, 4u);
  }
  const int ab = gi >> 3, ah = ((g & 7) + 2 * (gi & 4) + 3 * (ab + 4 * s)) & 15, as = s;
  const int z0 = ab == 1 && ((ah - 3) & 8) ? SLOWK : 0, z2 = ab == 1 && ((ah - 27) & 8) ? SLOWK : 0;
  const int n0 = N0 - 2 * z0 + z2, n1 = N1 + z0 + z2;
  const int ITU = 4 + (s == 0 ? n0 : s == 1 ? n1 : NSUM - n0 - n1);
  const int bq = (g & 7) >> 1, sp = (g & 1) * 8 + (gi >> 2);
  u32 bud = SPIN_BUDGET;

  if (wave == 0) {
    __builtin_amdgcn_s_setprio(3);
    u32 vq[4];
#pragma unroll
    for (int q = 0; q < 4; ++q) vq[q] = (lane >> 4) * 256 + (((lane & 15) ^ swz(4 * q + (lane >> 4))) << 4);
    int pub = 0, it = 4;
#define SLOT_WAIT() do { if (it >= NS) { const u32 need = 4u * (it >> 3), wa = W_FREE + (it & 7) * 4; \
      if (cw_ld(wa) < need) { VMW(16); if (it - 1 > pub) cw_full(it - 1); VMW(0); pub = it; cw_full(pub); cw_wait(wa, need); } } } while (0)
#define PACE(LW, k) do { vmw<LW>(); if ((k) == ((LW / 4) & 3) && it - LW / 16 > pub) { pub = it - LW / 16; cw_full(pub); } } while (0)
    asm volatile("" : "+s"(it));
    if (s < 3) {
      const rsrc_t rk = mkrs(a.in[1], 1u << 26), rv = mkrs(a.in[2], 1u << 26), rt = mkrs((const char*)a.ws + WS_TK + ah * 128, 4u);
      u32 so = ((u32)(ab * 16 + ah) * 128u + (s == 0 ? 0 : s == 1 ? n0 : n0 + n1)) * 8192u;
      asm volatile("v_mov_b32 v254, 1" ::: "v254");
#pragma unroll 1
      for (;; ++it) {
        const u32 sb = (it & 7) * SLOT;
        if (it == 5) cw_wait(W_WI, 1);
        SLOT_WAIT();
        PACE(LW_A, 0);
        if (it >= ITU) {
          u32 t;
          asm volatile("v_readlane_b32 %0, v254, %1\n\ts_cmp_lg_u32 %0, 1\n\ts_cbranch_scc1 1f\n\ts_waitcnt vmcnt(0)\n\tv_readlane_b32 %0, v254, %1\n1:\n\t"
                       "v_writelane_b32 v254, 1, %1\n\ts_nop 4" : "=&s"(t) : "s"(it & 3) : "memory", "scc", "v254");
          t -= 2u;
          if (t >= (u32)NTICK) break;
          cw_st(W_TK + (it & 15) * 4, (it << 8) | t);
          so = (((t & 3u) * 16u + ah) * 128u + NSUM + (t >> 2)) * 8192u;
        }
        if (it >= ITU - 2) {
          unsigned long long ex;
          asm volatile("s_mov_b64 %0, exec\n\ts_lshl_b64 exec, 1, %2\n\tbuffer_atomic_add v254, off, %1, 0 sc0\n\ts_mov_b64 exec, %0"
                       : "=&s"(ex) : "s"(rt), "s"((it + 2) & 3) : "memory", "scc", "v254");
        }
#pragma unroll
        for (int k = 0; k < 2; ++k) {
          if (k) PACE(LW_A, 1);
          DMA(rk, sb + k * 4096, vq[0], so + k * 4096, 0, NT); DMA(rk, sb + k * 4096, vq[1], so + k * 4096, 1024, NT);
          DMA(rk, sb + k * 4096, vq[2], so + k * 4096, 2048, NT); DMA(rk, sb + k * 4096, vq[3], so + k * 4096, 3072, NT);
        }
#pragma unroll
        for (int k = 0; k < 2; ++k) {
          PACE(LW_A, k + 2);
          DMA(rv, sb + 8192 + k * 4096, vq[0], so + k * 4096, 0, NT); DMA(rv, sb + 8192 + k * 4096, vq[1], so + k * 4096, 1024, NT);
          DMA(rv, sb + 8192 + k * 4096, vq[2], so + k * 4096, 2048, NT); DMA(rv, sb + 8192 + k * 4096, vq[3], so + k * 4096, 3072, NT);
        }
        so += 8192u;
      }
      cw_st(W_TK + ((it + (lane & 3)) & 15) * 4, ((it + (lane & 3)) << 8) | 255u);
    } else {
#pragma unroll 1
      for (; it < IT_W; ++it) {
        const u32 sb = (it & 7) * SLOT;
        if (it == 5) cw_wait(W_WI, 1);
        if (it == GATE1 || it == GATE2) {
          VMW(16); if (it - 1 > pub) cw_full(it - 1);
          VMW(0); pub = it; cw_full(it);
          if (it == GATE1) cw_wait(W_G1, 1);
          if (it == GATE2) cw_wait(W_GI, 4);
        }
        SLOT_WAIT();
        if (it < IT_UV) {
          const rsrc_t r = mkrs(a.in[6], 1u << 24);
          const u32 so = ((u32)bq * 4096u + sp * 256u + (it - IT_C) * 16u) * 1024u;
#pragma unroll
          for (int k = 0; k < 4; ++k) {
            PACE(LW_B, k);
            DMA(r, sb + k * 4096, (lane ^ swz(4 * k + 0)) << 4, so + k * 4096, 0, NT);
            DMA(r, sb + k * 4096, (lane ^ swz(4 * k + 1)) << 4, so + k * 4096, 1024, NT);
            DMA(r, sb + k * 4096, (lane ^ swz(4 * k + 2)) << 4, so + k * 4096, 2048, NT);
            DMA(r, sb + k * 4096, (lane ^ swz(4 * k + 3)) << 4, so + k * 4096, 3072, NT);
          }
        } else {
          const rsrc_t r = mkrs(a.in[9], 512u * 8192u);
          const u32 so = ((u32)b * 128u + (it & 3) * 32u) * 8192u + hkv * 512u;
#pragma unroll
          for (int i = 0; i < 16; ++i) { if ((i & 3) == 0) PACE(LW_B, i >> 2); DMA(r, sb + i * 1024, vu ^ (swz((2 * i) & 15) << 4), so + i * (2 * 8192), 0, NT); }
        }
      }
      VMW(0); pub = it; cw_full(it);
      cw_wait(W_GI, 8);
      LOAD_W(4, LW_W);
      vmw<32>(); cw_st(W_WI, 2u);
    }
    VMW(16); if (it - 1 > pub) cw_full(it - 1);
    VMW(0); cw_full(ALL);
    return;
  }

  const int c = wave - 1, g4 = lane >> 4, n = lane & 15, q = n >> 2, p = lane & 3;
  const rsrc_t rws = mkrs(a.ws, WS_END);
  const char* wsp = (const char*)a.ws; asm volatile("" : "+s"(wsp));
  const u32 tag = *(const __attribute__((address_space(4))) u32*)(wsp + WS_EPOCH) + 1u;
  asm volatile("" ::"s"(tag));
  VMW(0);
  const u32 yso = WS_YG + (s == 1 ? 288 * 2048 : 0) + (b >> 1) * (36 * 2048) + (hkv * 2 + (b & 1)) * 64;
  if (c == 4) {
    u32x4 wf[16];
    if (s == 1) {
      read_w(L, wf, 4, lane);
      cw_st(W_MF, 1u);
    }
    cw_wait(W_FULL, s == 3 ? GATE1 : s == 0 ? POLL_A0 : POLL_A);
    fetch<4>(L, rws, lane * 16 + WS_QG + (s < 3 ? ah * 128 : 8192 + hkv * 128) * 8, 16384, tag, (s < 3 ? QA_OFF : OB_OFF) + lane * 8, s < 3 ? 512 : OB_STR, W_QA, bud);
    if (s == 3) {
      cw_wait(W_QS, 5); cw_wait(W_FULL, GATE2);
      fetch<1>(L, rws, lane * 16 + WS_HINT, 0, tag, MG_OFF + lane * 8, 0, W_HINT, bud);
      cw_wait(W_BP, 4);
      fetch<4>(L, rws, n * 32 + (g4 & 1) * 16 + WS_SG + hkv * 2048, 512, tag, MG_OFF + lane * 8, 512, W_SGF, bud);
      cw_wait(W_FULL, ALL);
      read_w(L, wf, 4, lane);
    } else if (s == 1) {
      cw_wait(W_FULL, POLL_C);
      const u32 so = WS_CG + hkv * 32768 + (b & 1) * 1024;
      for (;;) {
        const u32x4 t = gld(rws, q * 8192 + (p & 1) * 512, so);
        if (allz(tagx(t, tag)) || !glb_retry()) break;
        __builtin_amdgcn_s_sleep(16);
      }
      u32x4 cv[16];
      for (;;) {
        u32 bad = 0;
#pragma unroll
        for (int i = 0; i < 16; ++i) cv[i] = gld(rws, lane * 16 + (i & 1) * 2048, so + (i >> 1) * 4096);
#pragma unroll
        for (int i = 0; i < 16; ++i) bad |= tagx(cv[i], tag);
        if (allz(bad) || !glb_retry()) break;
      }
#pragma unroll
      for (int i = 0; i < 4; ++i)
        *(LAS u32*)(L + OB_OFF + i * OB_STR + lane * 4) = pk((asf(cv[i][0]) + asf(cv[4 + i][0])) + (asf(cv[8 + i][0]) + asf(cv[12 + i][0])), (asf(cv[i][2]) + asf(cv[4 + i][2])) + (asf(cv[8 + i][2]) + asf(cv[12 + i][2])));
      cw_st(W_PJ, 4u);
    }
    if (s & 1) {
      project(L, wf, rws, yso, tag, 4, lane, bud);
    } else {
      cw_wait(W_O2, 4);
      __builtin_amdgcn_s_sleep(16);
      final_sum(rws, a.out, s == 0, 256 + hkv * 4 + b, tag, lane, bud);
      if (g == 16 && lane == 0) __builtin_amdgcn_raw_buffer_store_b32(tag, rws, 0, WS_EPOCH, SC1);
    }
    return;
  }

  if (s < 3) {
    u32x4 wf[16];
    if (s == 1) {
      cw_wait(W_WF, 4);
      read_w(L, wf, c, lane);
      cw_wait(W_MF, 1);
      if (lane < 4) asm volatile("ds_add_u32 %0, %1" ::"v"(W_FREE + lane * 4), "v"(1u) : "memory");
    } else {
      const u32 aw = g4 * 1024 + (c >> 1) * 512 + q * 64 + (c & 1) * 32 + 8 * p, ax = SB_OFF + p * 1152 + g4 * 16;
      const u32 vo = (n * 2048 + 2 * g4) * 8, so = WS_QG + ((s ? 0 : 8192) + hkv * 128 + b * 32 + 8 * c) * 8;
      if (j == 2 && c == 0) { __builtin_amdgcn_raw_buffer_store_b32(2u, rws, WS_TK + hkv * 128, 0, SC1); VMW(0); }
      cw_wait(W_WF, 2);
      u32x4 xr[18], wq[9];
#pragma unroll
      for (int ks = 0; ks < 18; ++ks) xr[ks] = ld128(L, ax + ks * 64);
      f32x4 a0 = ZERO4, a1 = ZERO4;
#pragma unroll
      for (int hf = 0; hf < 2; ++hf) {
        if (hf) { cw_wait(W_WF, 4); }
#pragma unroll
        for (int ks = 0; ks < 9; ++ks) wq[ks] = ldtr2(L, aw + (hf * 9 + ks) * 4096, aw + (hf * 9 + ks) * 4096 + 256);
        if (lane < 2) asm volatile("ds_add_u32 %0, %1" ::"v"(W_FREE + hf * 8 + lane * 4), "v"(1u) : "memory");
        __builtin_amdgcn_sched_barrier(0);
#pragma unroll
        for (int ks = 0; ks < 9; ++ks) { if (ks & 1) a1 = mfma(wq[ks], xr[hf * 9 + ks], a1); else a0 = mfma(wq[ks], xr[hf * 9 + ks], a0); }
        __builtin_amdgcn_sched_barrier(0);
      }
      a0 += a1;
      if (n < 4) gst(rws, u32x4{pk(a0[0], a0[1]), tag, pk(a0[2], a0[3]), tag}, vo, so);
    }
    float m = -1e30f, l = 0.f;
    f32x4 acc[8];
#pragma unroll
    for (int i = 0; i < 8; ++i) acc[i] = ZERO4;
    u32x4 qf[4], kf[8], vf[8];
    const u32 lpK = n * 256 + ((g4 ^ swz(n)) << 4), lpQ = QA_OFF + (n & 1) * 256 + g4 * 16;
    const u32 lpV = (4 * g4 + q) * 256 + (((p >> 1) ^ swz(4 * g4 + q)) << 4) + 8 * (p & 1);
#define READ_K() _Pragma("unroll") for (int kk = 0; kk < 4; ++kk) { const u32 ak = (sb + lpK) ^ (kk << 6); kf[kk] = ld128(L, ak); kf[4 + kk] = ld128(L, ak + 4096); }
#define READ_V() do { _Pragma("unroll") for (int ct = 0; ct < 8; ++ct) { const u32 av = (sb + lpV) ^ (ct << 5); vf[ct] = ldtr2(L, av + 8192, av + 12288); } \
      cw_add(W_FREE + (it & 7) * 4, 4, lane); } while (0)
#define SCORES() f32x4 s0 = ZERO4, s1 = ZERO4; \
      _Pragma("unroll") for (int kk = 0; kk < 4; ++kk) { s0 = mfma(kf[kk], qf[kk], s0); s1 = mfma(kf[4 + kk], qf[kk], s1); }
#define PICK8(e) const f32x4 e##0 = (n & 2) ? acc[1] : acc[0], e##1 = (n & 2) ? acc[3] : acc[2], e##2 = (n & 2) ? acc[5] : acc[4], e##3 = (n & 2) ? acc[7] : acc[6], \
      e##4 = (n & 4) ? e##1 : e##0, e##5 = (n & 4) ? e##3 : e##2, e = (n & 8) ? e##5 : e##4
    const u32 vrec = (n & 1) * 512 + (n >> 1) * 64 + g4 * 16;
    bool first = true;
    int it = 4 + c;
#pragma unroll 1
    for (; it < ITU; it += 4) {
      const u32 sb = (it & 7) * SLOT;
      cw_wait(W_FULL, it + 1);
      READ_K(); READ_V();
      if (first) {
        first = false;
        cw_wait(W_QA, 1);
#pragma unroll
        for (int kk = 0; kk < 4; ++kk) qf[kk] = ld128(L, lpQ + ab * 512 + kk * 64);
      }
      SCORES();
      const u32x4 pf = soft<8>(s0, s1, m, l, acc);
#pragma unroll
      for (int ct = 0; ct < 8; ++ct) acc[ct] = mfma(vf[ct], pf, acc[ct]);
    }
    {
      l = xg_sum(l);
      PICK8(e);
      *(LAS f32x4*)(L + MG_OFF + c * 1024 + lane * 16) = e;
      if (lane < 2) *(LAS f32x2*)(L + MS_OFF + (c * 2 + lane) * 8) = f32x2{m, l};
      if (cw_add_rtn(W_MRG, 1, lane) == 3u) {
        f32x2 sv[4]; f32x4 ov[4];
#pragma unroll
        for (int i = 0; i < 4; ++i) { sv[i] = *(LAS f32x2*)(L + MS_OFF + (i * 2 + (n & 1)) * 8); ov[i] = *(LAS f32x4*)(L + MG_OFF + i * 1024 + lane * 16); }
        const float M = fmaxf(fmaxf(sv[0][0], sv[1][0]), fmaxf(sv[2][0], sv[3][0]));
        float Ls = 0.f; f32x4 o = ZERO4;
#pragma unroll
        for (int i = 0; i < 4; ++i) { const float w = ex2((sv[i][0] - M) * CS); Ls = fmaf(w, sv[i][1], Ls); o += w * ov[i]; }
        o *= __builtin_amdgcn_rcpf(Ls);
        gst(rws, u32x4{pk(o[0], o[1]), tag, pk(o[2], o[3]), tag}, vrec, WS_AG + ((ah * 4 + ab) * 16 + as) * 1024);
        if (lane < 2) gst(rws, u32x4{asu(M), tag, asu(Ls), tag}, lane * 16, WS_AS + (ah * 4 + ab) * 2048 + as * 128);
      }
    }
#pragma unroll 1
    for (;; it += 4) {
      const u32 t = cw_wait(W_TK + (it & 15) * 4, it << 8) & 255u, sb = (it & 7) * SLOT;
      if (t >= (u32)NTICK || bud == 0u) break;
#pragma unroll
      for (int kk = 0; kk < 4; ++kk) qf[kk] = ld128(L, lpQ + (t & 3u) * 512 + kk * 64);
      const u32 pr = ah * 4 + (t & 3u), rec = 3 + (t >> 2), va = vrec + WS_AG + (pr * 16 + rec) * 1024, vs = lane * 16 + WS_AS + pr * 2048 + rec * 128;
      cw_wait(W_FULL, it + 1);
      READ_K(); READ_V();
      SCORES();
      m = xg_max(MAX8(s0, s1));
      const float mc = -m * CS;
#pragma unroll
      for (int i = 0; i < 4; ++i) { s0[i] = ex2(fmaf(s0[i], CS, mc)); s1[i] = ex2(fmaf(s1[i], CS, mc)); }
      const u32x4 pf = {pk(s0[0], s0[1]), pk(s0[2], s0[3]), pk(s1[0], s1[1]), pk(s1[2], s1[3])};
      f32x4 e0 = ZERO4, e1 = ZERO4;
#pragma unroll
      for (int ct = 0; ct < 8; ct += 2) { e0 = mfma(vf[ct], (n >> 1) == ct ? pf : u32x4{0u, 0u, 0u, 0u}, e0); e1 = mfma(vf[ct + 1], (n >> 1) == ct + 1 ? pf : u32x4{0u, 0u, 0u, 0u}, e1); }
      s0 += s1;
      l = xg_sum((s0[0] + s0[1]) + (s0[2] + s0[3]));
      e0 = (e0 + e1) * __builtin_amdgcn_rcpf(l);
      gst(rws, u32x4{pk(e0[0], e0[1]), tag, pk(e0[2], e0[3]), tag}, va, 0);
      if (lane < 2) gst(rws, u32x4{asu(m), tag, asu(l), tag}, vs, 0);
    }
    if (s == 1) {
      project(L, wf, rws, yso, tag, c, lane, bud);
    } else {
      cw_add(W_O2, 1, lane);
      cw_wait(W_O2, 4);
      __builtin_amdgcn_s_sleep(16);
      final_sum(rws, a.out, s == 0, (hkv * 4 + b) * 4 + c, tag, lane, bud);
    }
    return;
  }

  {
    cw_wait(W_WF, c + 1);
    u32x4 wu[16];
    {
      const u32 lp = (IT_UK + c) * SLOT + n * 512 + ((g4 ^ swz(n)) << 4);
#pragma unroll
      for (int i = 0; i < 16; ++i) wu[i] = ld128(L, (lp ^ ((i & 3) << 6)) + ((i >> 2) & 1) * 8192 + (i >> 3) * 256);
    }
    cw_add(W_FREE + (IT_UK + c) * 4, 4, lane);
    cw_wait(W_QA, 1);
    f32x4 d[4] = {ZERO4, ZERO4, ZERO4, ZERO4};
#pragma unroll
    for (int i = 0; i < 8; ++i) {
      const u32x4 qv = ld128(L, OB_OFF + p * OB_STR + i * 64 + g4 * 16);
      d[(i >> 2) * 2] = mfma(wu[(i >> 2) * 8 + (i & 3)], qv, d[(i >> 2) * 2]); d[(i >> 2) * 2 + 1] = mfma(wu[(i >> 2) * 8 + 4 + (i & 3)], qv, d[(i >> 2) * 2 + 1]);
    }
    const f32x4 e0 = (q & 1) ? d[1] : d[0], e1 = (q & 1) ? d[3] : d[2], e = (q & 2) ? e1 : e0;
    __builtin_amdgcn_raw_buffer_store_b64(u32x2{pk(e[0], e[1]), pk(e[2], e[3])}, rws, p * 32768 + q * 32 + g4 * 8 + WS_QAG + hkv * 2048 + (b * 4 + c) * 128, 0, SC1);
    if (cw_add_rtn(W_QS, 1, lane) == 3u) cw_st(W_G1, 1u);
    VMW(0);
    if (cw_add_rtn(W_MF, 1, lane) == 3u) { if (lane == 0) gst(rws, u32x4{tag, tag, tag, tag}, WS_HINT + (hkv * 4 + b) * 16, 0); cw_add(W_QS, 1, lane); }
  }
  {
    const int ch = c & 1;
    const u32 lpR = n * 1024 + ((g4 ^ swz(n)) << 4) + ch * 512;
    const u32 lpC = (4 * g4 + q) * 1024 + (((p >> 1) ^ swz(4 * g4 + q)) << 4) + 8 * (p & 1) + ch * 512;
    u32x4 qa[8], rf[16], cf[16];
#define READ_C(it) do { const u32 sb = ((it) & 7) * SLOT; cw_wait(W_FULL, (it) + 2); \
      _Pragma("unroll") for (int kk = 0; kk < 8; ++kk) { const u32 ak = ((sb + lpR) ^ ((kk & 3) << 6)) + (kk >> 2) * 256; rf[2 * kk] = ld128(L, ak); rf[2 * kk + 1] = ld128(L, ak + SLOT); } \
      _Pragma("unroll") for (int kk = 0; kk < 8; ++kk) { const u32 av = (sb + lpC) ^ (kk << 5); cf[kk] = ldtr2(L, av, av + SLOT); cf[8 + kk] = ldtr2(L, av + 256, av + 256 + SLOT); } \
      if (lane < 2) asm volatile("ds_add_u32 %0, %1" ::"v"(W_FREE + ((it) & 7) * 4 + lane * 4), "v"(1u) : "memory"); __builtin_amdgcn_sched_barrier(0); } while (0)
    READ_C(IT_C);
    {
      const u32 so = WS_QAG + bq * 32768 + (c >> 1) * 16384 + ch * 1024;
      cw_wait(W_HINT, 1);
#pragma unroll
      for (int i = 0; i < 8; ++i) qa[i] = gld(rws, (n >> 1) * 2048 + (n & 1) * 64 + g4 * 16 + i * 128, so);
      cw_add(W_GI, 1, lane);
    }
    float mb = -1e30f, lb = 0.f;
    f32x4 ob[16];
#pragma unroll
    for (int i = 0; i < 16; ++i) ob[i] = ZERO4;
#pragma unroll 1
    for (int st = 0;; ++st) {
      const u32 xw = ((st & 1) ? (4 + c) * SLOT + 16384 : SB_OFF + c * 2048) + lane * 16, xr = ((st & 1) ? (4 + (c ^ 1)) * SLOT + 16384 : SB_OFF + (c ^ 1) * 2048) + lane * 16;
      f32x4 s0 = ZERO4, s1 = ZERO4;
#pragma unroll
      for (int kk = 0; kk < 8; ++kk) { s0 = mfma(rf[2 * kk], qa[kk], s0); s1 = mfma(rf[2 * kk + 1], qa[kk], s1); }
      *(LAS f32x4*)(L + xw) = s0;
      *(LAS f32x4*)(L + xw + 1024) = s1;
      cw_st(W_S + c * 4, st + 1);
      cw_wait(W_S + (c ^ 1) * 4, st + 1);
      s0 += *(LAS f32x4*)(L + xr); s1 += *(LAS f32x4*)(L + xr + 1024);
      const u32x4 pf = soft<16>(s0, s1, mb, lb, ob);
#pragma unroll
      for (int ct = 0; ct < 16; ++ct) ob[ct] = mfma(cf[ct], pf, ob[ct]);
      if (st == 7) break;
      __builtin_amdgcn_sched_barrier(0);
      READ_C(IT_C + 2 * st + 2);
    }
#pragma unroll
    for (int i = 0; i < 8; ++i) {
      auto x = __builtin_amdgcn_permlane16_swap(pk(ob[2 * i][0], ob[2 * i][1]), pk(ob[2 * i + 1][0], ob[2 * i + 1][1]), false, false);
      auto y = __builtin_amdgcn_permlane16_swap(pk(ob[2 * i][2], ob[2 * i][3]), pk(ob[2 * i + 1][2], ob[2 * i + 1][3]), false, false);
      gst(rws, u32x4{x[0], y[0], x[1], y[1]}, (n >> 1) * 512 + (n & 1) * 64 + (g4 & 1) * 32 + (g4 >> 1) * 16 + (i & 3) * 128,
          WS_PB + ((bq * 16 + sp) * 4 + ch * 2 + (i >> 2)) * 8192 + (c >> 1) * 4096);
    }
    lb = xg_sum(lb);
    if (g4 == 0 && ch == 0) *(LAS f32x2*)(L + ST_OFF + ((c >> 1) * 16 + n) * 8) = f32x2{mb, lb};
    VMW(0);
    if (cw_add_rtn(W_BP, 1, lane) == 3u) {
      const f32x2 ml = *(LAS f32x2*)(L + ST_OFF + (lane & 31) * 8);
      if (lane < 32) gst(rws, u32x4{asu(ml[0]), tag, asu(ml[1]), tag}, (lane >> 1) * 2048 + (lane & 1) * 16, WS_SG + (bq * 16 + sp) * 32);
    }
  }
  {
    cw_wait(W_SGF, 1);
    u32x4 pv[8];
#pragma unroll
    for (int i = 0; i < 8; ++i) pv[i] = gld(rws, (lane >> 5) * 32768 + (lane & 31) * 16, WS_PB + ((c * 16 + 2 * i) * 4 + b) * 8192 + hkv * 512);
    cw_add(W_GI, 1, lane);
    const f32x2 st = *(LAS f32x2*)(L + MG_OFF + c * 512 + lane * 8);
    const float w = ex2((st[0] - row_max(st[0])) * CS), cf = w / row_sum(w * st[1]);
    f32x4 o0 = ZERO4, o1 = ZERO4;
#pragma unroll
    for (int i = 0; i < 8; ++i) {
      const float f = asf(__builtin_amdgcn_ds_bpermute((((lane >> 2) & 1) * 16 + 2 * i + (lane >> 5)) * 4, asu(cf)));
      o0 += f * f32x4{bflo(pv[i][0]), bfhi(pv[i][0]), bflo(pv[i][1]), bfhi(pv[i][1])};
      o1 += f * f32x4{bflo(pv[i][2]), bfhi(pv[i][2]), bflo(pv[i][3]), bfhi(pv[i][3])};
    }
#pragma unroll
    for (int i = 0; i < 4; ++i) {
      auto t0 = __builtin_amdgcn_permlane32_swap(asu(o0[i]), asu(o0[i]), false, false); o0[i] = asf(t0[0]) + asf(t0[1]);
      auto t1 = __builtin_amdgcn_permlane32_swap(asu(o1[i]), asu(o1[i]), false, false); o1[i] = asf(t1[0]) + asf(t1[1]);
    }
    if (lane < 32) *(LAS u32x4*)(L + OB_OFF + c * OB_STR + ((lane >> 2) & 1) * 256 + (lane >> 3) * 64 + p * 16) = u32x4{pk(o0[0], o0[1]), pk(o0[2], o0[3]), pk(o1[0], o1[1]), pk(o1[2], o1[3])};
    cw_add(W_OB, 1, lane);
    cw_wait(W_FULL, IT_UV + 4);
    u32x4 wf[16];
    {
      const u32 r0 = 8 * (g4 & 1) + q;
      const u32 lp0 = (8 * g4 + q) * 512 + ((((p >> 1) + 8 * c) ^ swz(r0)) << 4) + 8 * (p & 1);
      const u32 lp1 = (8 * g4 + q + 4) * 512 + ((((p >> 1) + 8 * c) ^ swz(r0 + 4)) << 4) + 8 * (p & 1);
#pragma unroll
      for (int i = 0; i < 16; ++i) wf[i] = ldtr2(L, (lp0 ^ ((i & 3) << 5)) + ((IT_UV + (i >> 2)) & 7) * SLOT, (lp1 ^ ((i & 3) << 5)) + ((IT_UV + (i >> 2)) & 7) * SLOT);
    }
    cw_wait(W_OB, 4);
    f32x4 d[4] = {ZERO4, ZERO4, ZERO4, ZERO4};
#pragma unroll
    for (int ks = 0; ks < 4; ++ks) {
      const u32x4 of = ld128(L, OB_OFF + p * OB_STR + (c >> 1) * 256 + ks * 64 + g4 * 16);
#pragma unroll
      for (int t = 0; t < 4; ++t) d[t] = mfma(wf[ks * 4 + t], of, d[t]);
    }
    {
      const u32 vo = p * 2048 + (q >> 1) * 128 + g4 * 32 + (q & 1) * 16, so = WS_CG + ((hkv * 4 + b) * 1024 + c * 64) * 8;
      gst(rws, pick2(d[0], d[1], q, tag), vo, so); gst(rws, pick2(d[2], d[3], q, tag), vo + 256, so);
    }
  }
  {
    cw_add(W_O2, 1, lane);
    cw_wait(W_O2, 4);
    const int rn = n ^ (lane >> 5);
    const u32 so = WS_AG + (hkv * 4 + c) * 16384 + (b & 1) * 512, ss = WS_AS + (hkv * 4 + c) * 2048 + (b & 1) * 16 + (rn < NREC ? rn : 0) * 128;
    const u32 lr = (4 + c) * SLOT, v0 = (lane & 31) * 16, v1 = v0 + (lane >> 5) * 1024;
    u32x4 wf[16], sv, ov[NPR];
    u32 todo = 0x5555u & ((1u << NREC) - 1u);
    {
      u32 ask = todo;
      while (cw_ld(W_WI) < 2u && bud != 0u) {
        sv = gld(rws, ss, 0);
        const unsigned long long sb = __builtin_amdgcn_ballot_w64(tagx(sv, tag) != 0u);
        const u32 nw = ask & ~((u32)sb | (u32)(sb >> 32));
        for (u32 k = nw; k; k &= k - 1u) { const u32 i = __builtin_ctz(k); dma_sc1(rws, lr + i * 512, i + 1 < NREC ? v1 : v0, so + i * 1024); }
        ask &= ~nw;
        bud = bud < 16u ? 0u : bud - 16u;
        __builtin_amdgcn_s_sleep(2);
      }
      todo = ask | 0x10000u;
    }
    for (;;) {
      sv = gld(rws, ss, 0);
      for (u32 k = todo & 0xffffu; k; k &= k - 1u) { const u32 i = __builtin_ctz(k); dma_sc1(rws, lr + i * 512, i + 1 < NREC ? v1 : v0, so + i * 1024); }
      if (todo >> 16) { cw_wait(W_FULL, ALL); read_w(L, wf, c, lane); }
      VMW(0);
      const unsigned long long sb = __builtin_amdgcn_ballot_w64(tagx(sv, tag) != 0u);
      if (todo & ~(((u32)sb | (u32)(sb >> 32)) & 0xffffu)) {
#pragma unroll
        for (int i = 0; i < NPR; ++i) ov[i] = ld128(L, lr + lane * 16 + i * 1024);
        u32 bad = 0;
#pragma unroll
        for (int i = 0; i < NPR; ++i) bad |= (tagx(ov[i], tag) != 0u) << (2 * i);
        if (allz(bad)) todo = 0u;
        else { bad |= __builtin_amdgcn_mov_dpp(bad, 0x128, 0xf, 0xf, true); bad |= __builtin_amdgcn_mov_dpp(bad, 0x124, 0xf, 0xf, true); bad |= __builtin_amdgcn_mov_dpp(bad, 0x122, 0xf, 0xf, true); bad |= __builtin_amdgcn_mov_dpp(bad, 0x121, 0xf, 0xf, true);
          todo = __builtin_amdgcn_readlane(bad, 0) | __builtin_amdgcn_readlane(bad, 16) | __builtin_amdgcn_readlane(bad, 32) | __builtin_amdgcn_readlane(bad, 48); }
      }
      if (todo == 0u && sb == 0ull) break;
      bud = bud < 16u ? 0u : bud - 16u;
      if (bud == 0u) break;
      asm volatile("" ::: "memory");
    }
    const float ms = asf(sv[0]), w0 = ex2((ms - row_max(ms)) * CS) * asf(sv[2]), w = rn < NREC ? w0 : 0.f;
    f32x4 o[2] = {ZERO4, ZERO4};
    merge_rec<0>(o, ov, w);
    o[0] += o[1];
    const float rs = __builtin_amdgcn_rcpf(asf(__builtin_amdgcn_readfirstlane(asu(row_sum(w)))));
#pragma unroll
    for (int i = 0; i < 4; ++i) { auto t = __builtin_amdgcn_permlane32_swap(asu(o[0][i]), asu(o[0][i]), false, false); o[0][i] = (asf(t[0]) + asf(t[1])) * rs; }
    *(LAS u32x2*)(L + OB_OFF + c * OB_STR + (lane & 31) * 8) = u32x2{pk(o[0][0], o[0][1]), pk(o[0][2], o[0][3])};
    cw_add(W_PJ, 1, lane);
    project(L, wf, rws, yso, tag, c, lane, bud);
  }
}

extern "C" void kernel_launch(void* const* d_in, const int* in_sizes, int n_in, void* d_out, int out_size, void* d_ws, size_t ws_size, hipStream_t stream) {
  Args a;
  for (int i = 0; i < 11; ++i) a.in[i] = d_in[i];
  a.out = d_out;
  a.ws = d_ws;
  concat_heads_decode_kernel<<<256, 384, 0, stream>>>(a);
}
```

```cpp
#include <hip/hip_runtime.h>
#include <stdint.h>

#define LAS __attribute__((address_space(3)))
typedef unsigned u32;
typedef u32 u32x2 __attribute__((ext_vector_type(2)));
typedef u32 u32x4 __attribute__((ext_vector_type(4)));
typedef float f32x2 __attribute__((ext_vector_type(2)));
typedef float f32x4 __attribute__((ext_vector_type(4)));
typedef short s16x4 __attribute__((ext_vector_type(4)));
typedef __bf16 bf16x2 __attribute__((ext_vector_type(2)));
typedef __bf16 bf16x8 __attribute__((ext_vector_type(8)));
typedef __amdgpu_buffer_rsrc_t rsrc_t;

constexpr int N0 = 38, N1 = 38, N2 = 39, SLOWK = 2;
constexpr int NSUM = N0 + N1 + N2, NU = 128 - NSUM, NREC = 3 + NU, NTICK = 4 * NU;
static_assert(NU >= 1 && NREC <= 16, "split");
constexpr int LW_A = 32, LW_B = 32, LW_W = 44;
constexpr int POLL_A = 9, POLL_A0 = 7, POLL_C = N1 + 2;
constexpr int GATE1 = 12, GATE2 = 14;
constexpr int NT = 2;
static_assert(LW_A <= 32, "a ticket is read two items after it was drawn");

constexpr int SLOT = 18432, NS = 8, RING = SLOT * NS;
constexpr int CTL = RING;
constexpr int W_FULL = CTL, W_WF = CTL + 4, W_QA = CTL + 8, W_MRG = CTL + 12, W_HINT = CTL + 16, W_SGF = CTL + 20, W_OB = CTL + 24, W_BP = CTL + 28,
              W_FREE = CTL + 32, W_S = CTL + 64, W_GI = CTL + 80, W_QS = CTL + 84, W_PJ = CTL + 88, W_WI = CTL + 92, W_MF = CTL + 96, W_O2 = CTL + 100, W_G1 = CTL + 104, W_TK = CTL + 128;
constexpr int ST_OFF = CTL + 256;
constexpr int OB_OFF = ST_OFF + 512, OB_STR = 576;
constexpr int SB_OFF = OB_OFF + 4 * OB_STR;
constexpr int QA_OFF = SB_OFF + 5120;
constexpr int MG_OFF = SB_OFF + 8192;
constexpr int MS_OFF = MG_OFF + 4096;
constexpr int LDS_BYTES = MS_OFF + 64;
static_assert(LDS_BYTES <= 163840 && (SB_OFF & 15) == 0 && (MG_OFF & 15) == 0, "lds");

constexpr u32 WS_EPOCH = 0, WS_HINT = 1024;
constexpr u32 WS_TK = 2048;
constexpr u32 WS_QG = 4096;
constexpr u32 WS_QAG = WS_QG + 2 * 4 * 2048 * 8;
constexpr u32 WS_AG = WS_QAG + 4 * 32 * 512 * 2;
constexpr u32 WS_AS = WS_AG + 64 * 16 * 1024;
constexpr u32 WS_SG = WS_AS + 64 * 2048;
constexpr u32 WS_CG = WS_SG + 4 * 16 * 32 * 16;
constexpr u32 WS_YG = WS_CG + 16 * 4 * 4 * 256 * 8;
constexpr u32 WS_PB = WS_YG + 2 * 32 * 18432;
constexpr u32 WS_END = WS_PB + 4 * 16 * 4 * 16 * 512;
static_assert((WS_PB & 127) == 0, "ws");

constexpr float CS = 0.08838834764831845f * 1.4426950408889634f;
constexpr int SC1 = 16;
constexpr u32 CODE_BYTES = 24576;
constexpr u32 SPIN_BUDGET = 1u << 19;

struct Args { const void* in[11]; void* out; void* ws; };

__device__ __forceinline__ rsrc_t mkrs(const void* p, u32 n) { return __builtin_amdgcn_make_buffer_rsrc((void*)p, 0, n, 0x00020000); }
constexpr __device__ __forceinline__ u32 swz(u32 r) { return (((r & 7u) ^ ((r >> 1) & 4u)) << 1) | ((r >> 3) & 1u); }
__device__ __forceinline__ f32x4 mfma(u32x4 a, u32x4 b, f32x4 c) {
  return __builtin_amdgcn_mfma_f32_16x16x32_bf16(__builtin_bit_cast(bf16x8, a), __builtin_bit_cast(bf16x8, b), c, 0, 0, 0);
}
__device__ __forceinline__ u32 pk(float lo, float hi) { f32x2 v = {lo, hi}; return __builtin_bit_cast(u32, __builtin_convertvector(v, bf16x2)); }
__device__ __forceinline__ float bflo(u32 v) { return __builtin_bit_cast(float, v << 16); }
__device__ __forceinline__ float bfhi(u32 v) { return __builtin_bit_cast(float, v & 0xffff0000u); }
__device__ __forceinline__ float asf(u32 v) { return __builtin_bit_cast(float, v); }
__device__ __forceinline__ u32 asu(float v) { return __builtin_bit_cast(u32, v); }
__device__ __forceinline__ float ex2(float x) { return __builtin_amdgcn_exp2f(x); }
__device__ __forceinline__ u32x4 ld128(LAS unsigned char* L, u32 a) { return *(LAS u32x4*)(L + a); }
__device__ __forceinline__ u32x4 ldtr2(LAS unsigned char* L, u32 a0, u32 a1) {
  const u32x2 x = __builtin_bit_cast(u32x2, __builtin_amdgcn_ds_read_tr16_b64_v4i16((LAS s16x4*)(L + a0)));
  const u32x2 y = __builtin_bit_cast(u32x2, __builtin_amdgcn_ds_read_tr16_b64_v4i16((LAS s16x4*)(L + a1)));
  return u32x4{x[0], x[1], y[0], y[1]};
}
__device__ __forceinline__ float xg_max(float x) {
  auto s = __builtin_amdgcn_permlane16_swap(asu(x), asu(x), false, false);
  x = fmaxf(asf(s[0]), asf(s[1]));
  auto t = __builtin_amdgcn_permlane32_swap(asu(x), asu(x), false, false);
  return fmaxf(asf(t[0]), asf(t[1]));
}
__device__ __forceinline__ float xg_sum(float x) {
  auto s = __builtin_amdgcn_permlane16_swap(asu(x), asu(x), false, false);
  x = asf(s[0]) + asf(s[1]);
  auto t = __builtin_amdgcn_permlane32_swap(asu(x), asu(x), false, false);
  return asf(t[0]) + asf(t[1]);
}
template <int CTRL> __device__ __forceinline__ float dpp(float x) { return asf(__builtin_amdgcn_mov_dpp(asu(x), CTRL, 0xf, 0xf, true)); }
__device__ __forceinline__ float row_sum(float x) { x += dpp<0x128>(x); x += dpp<0x124>(x); x += dpp<0x122>(x); x += dpp<0x121>(x); return x; }
__device__ __forceinline__ float row_max(float x) { x = fmaxf(x, dpp<0x128>(x)); x = fmaxf(x, dpp<0x124>(x)); x = fmaxf(x, dpp<0x122>(x)); x = fmaxf(x, dpp<0x121>(x)); return x; }

__device__ __forceinline__ u32 cw_ld(u32 a) { u32 v; asm volatile("ds_read_b32 %0, %1\n\ts_waitcnt lgkmcnt(0)" : "=v"(v) : "v"(a) : "memory"); return __builtin_amdgcn_readfirstlane(v); }
__device__ __forceinline__ void cw_st(u32 a, u32 v) { asm volatile("ds_write_b32 %0, %1" ::"v"(a), "v"(v) : "memory"); }
#define cw_full(v) cw_st(W_FULL, (v))
__device__ __forceinline__ void cw_add(u32 a, u32 v, int lane) { if (lane == 0) asm volatile("ds_add_u32 %0, %1" ::"v"(a), "v"(v) : "memory"); asm volatile("" ::: "memory"); }
__device__ __forceinline__ u32 cw_add_rtn(u32 a, u32 v, int lane) {
  u32 r = 0;
  if (lane == 0) asm volatile("ds_add_rtn_u32 %0, %1, %2\n\ts_waitcnt lgkmcnt(0)" : "=v"(r) : "v"(a), "v"(v) : "memory");
  asm volatile("" ::: "memory");
  return __builtin_amdgcn_readfirstlane(r);
}
__device__ __forceinline__ u32 cw_wait_(u32 a, u32 want, u32& bud) {
  u32 t;
  bud = __builtin_amdgcn_readfirstlane(bud); want = __builtin_amdgcn_readfirstlane(want);
  asm volatile("1:\n\tds_read_b32 v255, %2\n\ts_waitcnt lgkmcnt(0)\n\tv_readfirstlane_b32 %0, v255\n\ts_cmp_ge_u32 %0, %3\n\ts_cbranch_scc1 2f\n\t"
               "s_sleep 1\n\ts_sub_u32 %1, %1, 1\n\ts_cbranch_scc0 1b\n\ts_mov_b32 %1, 0\n2:\n\ts_nop 1"
               : "=&s"(t), "+s"(bud) : "v"(a), "s"(want) : "memory", "scc", "v255");
  return t;
}
#define cw_wait(a, want) cw_wait_((a), (want), bud)
__device__ __forceinline__ bool glb_retry_(u32& bud) {
  bud = __builtin_amdgcn_readfirstlane(bud);
  asm volatile("s_sleep 1\n\ts_sub_u32 %0, %0, 16\n\ts_cselect_b32 %0, 0, %0" : "+s"(bud) :: "memory", "scc");
  return bud != 0u;
}
#define glb_retry() glb_retry_(bud)
__device__ __forceinline__ u32x4 gld(rsrc_t r, u32 voff, u32 soff) { return __builtin_amdgcn_raw_buffer_load_b128(r, voff, soff, SC1); }
__device__ __forceinline__ void gst(rsrc_t r, u32x4 v, u32 voff, u32 soff) { __builtin_amdgcn_raw_buffer_store_b128(v, r, voff + soff, 0, SC1); }
__device__ __forceinline__ u32 tagx(u32x4 v, u32 tag) { return (v[1] ^ tag) | (v[3] ^ tag); }
__device__ __forceinline__ bool allz(u32 x) { return __builtin_amdgcn_ballot_w64(x != 0u) == 0ull; }
template <int N> __device__ __forceinline__ void fetch(LAS unsigned char* L, rsrc_t r, u32 voff, u32 ss, u32 tag, u32 dst, u32 ds, u32 flag, u32& bud) {
  u32x4 v[N];
  for (;;) {
    u32 bad = 0;
#pragma unroll
    for (int i = 0; i < N; ++i) v[i] = gld(r, voff + i * ss, 0);
#pragma unroll
    for (int i = 0; i < N; ++i) bad |= tagx(v[i], tag);
    if (allz(bad) || !glb_retry()) break;
    if (flag == W_QA) cw_st(W_G1, 1u);
  }
#pragma unroll
  for (int i = 0; i < N; ++i) *(LAS u32x2*)(L + dst + i * ds) = u32x2{v[i][0], v[i][2]};
  cw_st(flag, 1u);
}
__device__ __forceinline__ void dma_sc1(rsrc_t r, u32 lds, u32 voff, u32 soff) {
  u32 keep;
  asm volatile("s_mov_b32 %0, m0\n\ts_mov_b32 m0, %1\n\ts_nop 4\n\tbuffer_load_dwordx4 %2, %3, %4 offen sc1 lds\n\ts_mov_b32 m0, %0"
               : "=&s"(keep) : "s"(__builtin_amdgcn_readfirstlane(lds)), "v"(voff), "s"(r), "s"(__builtin_amdgcn_readfirstlane(soff)) : "memory");
}
#define DMA(r, ldsoff, voff, soff, imm, aux) __builtin_amdgcn_raw_ptr_buffer_load_lds((r), (LAS void*)(L + (ldsoff)), 16, (voff), (soff), (imm), (aux))

#define VMW(n) asm volatile("s_waitcnt vmcnt(" #n ")" ::: "memory")
#define ZERO4 f32x4{0.f, 0.f, 0.f, 0.f}
#define MAX8(a, b) fmaxf(fmaxf(fmaxf(a[0], a[1]), fmaxf(a[2], a[3])), fmaxf(fmaxf(b[0], b[1]), fmaxf(b[2], b[3])))
constexpr float LAZY = 192.f;
template <int NA> __device__ __forceinline__ u32x4 soft(const f32x4 s0, const f32x4 s1, float& m, float& l, f32x4* acc) {
  const float mx = MAX8(s0, s1);
  if (__builtin_amdgcn_ballot_w64(mx > m + LAZY) != 0ull) {
    const float mn = fmaxf(m, xg_max(mx)), al = ex2((m - mn) * CS);
    m = mn; l *= al;
#pragma unroll
    for (int i = 0; i < NA; ++i) acc[i] *= al;
  }
  const float mc = -m * CS;
  float pr[8];
#pragma unroll
  for (int i = 0; i < 4; ++i) { pr[i] = ex2(fmaf(s0[i], CS, mc)); pr[4 + i] = ex2(fmaf(s1[i], CS, mc)); }
  l += ((pr[0] + pr[1]) + (pr[2] + pr[3])) + ((pr[4] + pr[5]) + (pr[6] + pr[7]));
  return u32x4{pk(pr[0], pr[1]), pk(pr[2], pr[3]), pk(pr[4], pr[5]), pk(pr[6], pr[7])};
}
__device__ __forceinline__ u32x4 pick2(const f32x4 d0, const f32x4 d1, int q, u32 tag) {
  const float x0 = (q & 2) ? d1[0] : d0[0], x1 = (q & 2) ? d1[1] : d0[1], x2 = (q & 2) ? d1[2] : d0[2], x3 = (q & 2) ? d1[3] : d0[3];
  return u32x4{asu((q & 1) ? x2 : x0), tag, asu((q & 1) ? x3 : x1), tag};
}

constexpr int NPR = (NREC + 1) / 2;
template <int I> __device__ __forceinline__ void merge_rec(f32x4* o, const u32x4* t, float w) {
  if constexpr (I < NPR) { o[I & 1] += dpp<0x150 + 2 * I>(w) * f32x4{bflo(t[I][0]), bfhi(t[I][0]), bflo(t[I][2]), bfhi(t[I][2])}; merge_rec<I + 1>(o, t, w); }
}

constexpr int IT_UK = 0, IT_C = 4, IT_UV = 20, IT_W = 24;
constexpr u32 ALL = 1u << 20;
template <int N> __device__ __forceinline__ void vmw() { asm volatile("s_waitcnt vmcnt(%0)" ::"i"(N) : "memory"); }

__device__ __forceinline__ void read_w(LAS unsigned char* L, u32x4* wf, int c, int lane) {
  const u32 lp = (8 * (lane >> 4) + ((lane >> 2) & 3)) * 576 + 8 * (lane & 3) + 128 * c, t2 = c < 4 ? 64 : 0;
#pragma unroll
  for (int i = 0; i < 16; ++i) { const u32 ad = (i >> 2) * SLOT + lp + (i & 1) * 32 + ((i & 2) ? t2 : 0); wf[i] = ldtr2(L, ad, ad + 2304); }
}
__device__ __forceinline__ void project(LAS unsigned char* L, const u32x4* wf, rsrc_t rws, u32 so, u32 tag, int c, int lane, u32& bud) {
  const int g4 = lane >> 4, q = (lane >> 2) & 3, p = lane & 3;
  u32 ao = OB_OFF + p * OB_STR + g4 * 16, vo = so + c * 16384 + p * (72 * 2048) + ((q >> 1) * 2 + (g4 >> 1)) * 2048 + (g4 & 1) * 32 + (q & 1) * 16;
  asm volatile("" : "+v"(ao), "+v"(vo));
  cw_wait(W_PJ, 4);
  f32x4 d[4] = {ZERO4, ZERO4, ZERO4, ZERO4};
  u32x4 of[4];
#pragma unroll
  for (int ks = 0; ks < 4; ++ks) of[ks] = ld128(L, ao + ks * 64);
  __builtin_amdgcn_sched_barrier(0);
#pragma unroll
  for (int ks = 0; ks < 4; ++ks) { d[0] = mfma(wf[ks * 4], of[ks], d[0]); d[1] = mfma(wf[ks * 4 + 1], of[ks], d[1]); }
#pragma unroll
  for (int ks = 0; ks < 4; ++ks) { d[2] = mfma(wf[ks * 4 + 2], of[ks], d[2]); d[3] = mfma(wf[ks * 4 + 3], of[ks], d[3]); }
  gst(rws, pick2(d[0], d[1], q, tag), vo, 0);
  if (c < 4) gst(rws, pick2(d[2], d[3], q, tag), vo + 8192, 0);
}
__device__ __forceinline__ void final_sum(rsrc_t rws, void* out, int br, int t, u32 tag, int lane, u32& bud) {
  if (t >= 288) return;
  u32 vo = WS_YG + (br * 288 + t) * 2048 + lane * 16, oo = ((t / 72) * 1152 + br * 576 + (t % 72) * 8) * 2 + lane * 4;
  const rsrc_t ro = mkrs(out, 4u * 1152u * 2u);
  asm volatile("" : "+v"(vo), "+v"(oo));
  u32x4 y, z;
  for (;;) { y = gld(rws, vo, 0); z = gld(rws, vo + 1024, 0); if (allz(tagx(y, tag) | tagx(z, tag)) || !glb_retry()) break; }
  float r0 = asf(y[0]) + asf(z[0]), r1 = asf(y[2]) + asf(z[2]);
  r0 += dpp<0x128>(r0); r1 += dpp<0x128>(r1); r0 += dpp<0x124>(r0); r1 += dpp<0x124>(r1);
  const u32 r = pk(xg_sum(r0), xg_sum(r1));
  if (lane < 4) __builtin_amdgcn_raw_buffer_store_b32(r, ro, oo, 0, SC1);
}

__global__ void __launch_bounds__(384, 2) concat_heads_decode_kernel(Args a) {
  __shared__ __attribute__((aligned(16))) unsigned char smem[LDS_BYTES];
  LAS unsigned char* L = (LAS unsigned char*)smem;
  const int lane = threadIdx.x & 63, wave = __builtin_amdgcn_readfirstlane(threadIdx.x >> 6);
  const int g = blockIdx.x, gi = g >> 3, hkv = 2 * (g & 7) + (gi >> 4), j = gi & 15, b = j >> 2, s = j & 3;
  if (wave == 1) {
    cw_st(CTL + lane * 4, 0u);
    unsigned long long pc; asm volatile("s_getpc_b64 %0" : "=s"(pc));
    DMA(mkrs((const void*)(pc & ~255ull), CODE_BYTES), MG_OFF, lane * 16, gi * 1024, 0, 0);
    asm volatile("s_waitcnt lgkmcnt(0)" ::: "memory");
  }
  __builtin_amdgcn_s_barrier();
  const u32 vu = (lane >> 5) * 8192 + (((lane & 31) ^ ((lane >> 5) << 1)) << 4);
#define LOAD_W(in_, LW) do { const rsrc_t r = mkrs(a.in[in_], 4096u * 1152u); u32 wo[9]; int ln = lane; asm volatile("" : "+v"(ln)); \
      _Pragma("unroll") for (int i = 0; i < 9; ++i) { const u32 ci = i * 64 + ln; wo[i] = ci * 16 + ((ci * 3641u) >> 17) * 576u; } \
      u32 sw = ((u32)hkv * 256u + (b & 1) * 128u) * 1152u + (b >> 1) * 576u; \
      _Pragma("unroll 1") for (int t = 0; t < 8; ++t, sw += 16u * 1152u) { _Pragma("unroll") for (int i = 0; i < 9; ++i) { if (i % 3 == 0) vmw<LW>(); DMA(r, t * 9216 + i * 1024, wo[i], sw, 0, NT); } } } while (0)
  if (wave == 5) {
    if (s != 1) {
      const rsrc_t rw = mkrs(a.in[s == 0 ? 7 : s == 2 ? 3 : 8], s < 3 ? 576u * 8192u : 512u * 8192u), rx = mkrs(a.in[s ? 0 : 5], s < 3 ? 4u * 576u * 2u : 0u);
      const u32 vo = s < 3 ? ((lane >> 2) & 7) * 8192 + (lane & 3) * 16 + (lane >> 5) * 64 : vu, st = s < 3 ? 8 * 8192 : 2 * 8192, xm = s == 3 ? 0xf0u : 0u;
      u32 so = hkv * 512 + (s < 3 ? b * 128 : b * (128u * 8192u)), lo = 0;
#pragma unroll 1
      for (int t = 0; t < (s < 3 ? 9 : 8); ++t) {
        vmw<40>();
        if (t == 7) cw_st(W_WF, 1u);
#pragma unroll
        for (int i = 0; i < 8; ++i, so += st) DMA(rw, lo + i * 1024, vo ^ ((swz(2 * i) << 4) & xm), so, 0, NT);
        lo += s == 3 && (t & 1) ? SLOT - 8192 : 8192;
        if (t == 1 && s < 3) {
#pragma unroll
          for (int i = 0; i < 5; ++i) DMA(rx, SB_OFF + i * 1024, lane * 16, i * 1024, 0, 0);
        }
      }
    } else {
      LOAD_W(10, 40);
    }
    VMW(32); cw_st(W_WF, 2u); cw_st(W_WI, 1u);
    VMW(16); cw_st(W_WF, 3u);
    VMW(0); cw_st(W_WF, 4u);
  }
  const int ab = gi >> 3, ah = ((g & 7) + 2 * (gi & 4) + 3 * (ab + 4 * s)) & 15, as = s;
  const int z0 = ab == 1 && ((ah - 3) & 8) ? SLOWK : 0, z2 = ab == 1 && ((ah - 27) & 8) ? SLOWK : 0;
  const int n0 = N0 - 2 * z0 + z2, n1 = N1 + z0 + z2;
  const int ITU = 4 + (s == 0 ? n0 : s == 1 ? n1 : NSUM - n0 - n1);
  const int bq = (g & 7) >> 1, sp = (g & 1) * 8 + (gi >> 2);
  u32 bud = SPIN_BUDGET;

  if (wave == 0) {
    __builtin_amdgcn_s_setprio(3);
    u32 vq[4];
#pragma unroll
    for (int q = 0; q < 4; ++q) vq[q] = (lane >> 4) * 256 + (((lane & 15) ^ swz(4 * q + (lane >> 4))) << 4);
    int pub = 0, it = 4;
#define SLOT_WAIT() do { if (it >= NS) { const u32 need = 4u * (it >> 3), wa = W_FREE + (it & 7) * 4; \
      if (cw_ld(wa) < need) { VMW(16); if (it - 1 > pub) cw_full(it - 1); VMW(0); pub = it; cw_full(pub); cw_wait(wa, need); } } } while (0)
#define PACE(LW, k) do { vmw<LW>(); if ((k) == ((LW / 4) & 3) && it - LW / 16 > pub) { pub = it - LW / 16; cw_full(pub); } } while (0)
    asm volatile("" : "+s"(it));
    if (s < 3) {
      const rsrc_t rk = mkrs(a.in[1], 1u << 26), rv = mkrs(a.in[2], 1u << 26), rt = mkrs((const char*)a.ws + WS_TK + ah * 128, 4u);
      u32 so = ((u32)(ab * 16 + ah) * 128u + (s == 0 ? 0 : s == 1 ? n0 : n0 + n1)) * 8192u;
      asm volatile("v_mov_b32 v254, 1" ::: "v254");
#pragma unroll 1
      for (;; ++it) {
        const u32 sb = (it & 7) * SLOT;
        if (it == 5) cw_wait(W_WI, 1);
        SLOT_WAIT();
        PACE(LW_A, 0);
        if (it >= ITU) {
          u32 t;
          asm volatile("v_readlane_b32 %0, v254, %1\n\ts_cmp_lg_u32 %0, 1\n\ts_cbranch_scc1 1f\n\ts_waitcnt vmcnt(0)\n\tv_readlane_b32 %0, v254, %1\n1:\n\t"
                       "v_writelane_b32 v254, 1, %1\n\ts_nop 4" : "=&s"(t) : "s"(it & 3) : "memory", "scc", "v254");
          t -= 2u;
          if (t >= (u32)NTICK) break;
          cw_st(W_TK + (it & 15) * 4, (it << 8) | t);
          so = (((t & 3u) * 16u + ah) * 128u + NSUM + (t >> 2)) * 8192u;
        }
        if (it >= ITU - 2) {
          unsigned long long ex;
          asm volatile("s_mov_b64 %0, exec\n\ts_lshl_b64 exec, 1, %2\n\tbuffer_atomic_add v254, off, %1, 0 sc0\n\ts_mov_b64 exec, %0"
                       : "=&s"(ex) : "s"(rt), "s"((it + 2) & 3) : "memory", "scc", "v254");
        }
#pragma unroll
        for (int k = 0; k < 2; ++k) {
          if (k) PACE(LW_A, 1);
          DMA(rk, sb + k * 4096, vq[0], so + k * 4096, 0, NT); DMA(rk, sb + k * 4096, vq[1], so + k * 4096, 1024, NT);
          DMA(rk, sb + k * 4096, vq[2], so + k * 4096, 2048, NT); DMA(rk, sb + k * 4096, vq[3], so + k * 4096, 3072, NT);
        }
#pragma unroll
        for (int k = 0; k < 2; ++k) {
          PACE(LW_A, k + 2);
          DMA(rv, sb + 8192 + k * 4096, vq[0], so + k * 4096, 0, NT); DMA(rv, sb + 8192 + k * 4096, vq[1], so + k * 4096, 1024, NT);
          DMA(rv, sb + 8192 + k * 4096, vq[2], so + k * 4096, 2048, NT); DMA(rv, sb + 8192 + k * 4096, vq[3], so + k * 4096, 3072, NT);
        }
        so += 8192u;
      }
      cw_st(W_TK + ((it + (lane & 3)) & 15) * 4, ((it + (lane & 3)) << 8) | 255u);
    } else {
#pragma unroll 1
      for (; it < IT_W; ++it) {
        const u32 sb = (it & 7) * SLOT;
        if (it == 5) cw_wait(W_WI, 1);
        if (it == GATE1 || it == GATE2) {
          VMW(16); if (it - 1 > pub) cw_full(it - 1);
          VMW(0); pub = it; cw_full(it);
          if (it == GATE1) cw_wait(W_G1, 1);
          if (it == GATE2) cw_wait(W_GI, 4);
        }
        SLOT_WAIT();
        if (it < IT_UV) {
          const rsrc_t r = mkrs(a.in[6], 1u << 24);
          const u32 so = ((u32)bq * 4096u + sp * 256u + (it - IT_C) * 16u) * 1024u;
#pragma unroll
          for (int k = 0; k < 4; ++k) {
            PACE(LW_B, k);
            DMA(r, sb + k * 4096, (lane ^ swz(4 * k + 0)) << 4, so + k * 4096, 0, NT);
            DMA(r, sb + k * 4096, (lane ^ swz(4 * k + 1)) << 4, so + k * 4096, 1024, NT);
            DMA(r, sb + k * 4096, (lane ^ swz(4 * k + 2)) << 4, so + k * 4096, 2048, NT);
            DMA(r, sb + k * 4096, (lane ^ swz(4 * k + 3)) << 4, so + k * 4096, 3072, NT);
          }
        } else {
          const rsrc_t r = mkrs(a.in[9], 512u * 8192u);
          const u32 so = ((u32)b * 128u + (it & 3) * 32u) * 8192u + hkv * 512u;
#pragma unroll
          for (int i = 0; i < 16; ++i) { if ((i & 3) == 0) PACE(LW_B, i >> 2); DMA(r, sb + i * 1024, vu ^ (swz((2 * i) & 15) << 4), so + i * (2 * 8192), 0, NT); }
        }
      }
      VMW(0); pub = it; cw_full(it);
      cw_wait(W_GI, 8);
      LOAD_W(4, LW_W);
      vmw<32>(); cw_st(W_WI, 2u);
    }
    VMW(16); if (it - 1 > pub) cw_full(it - 1);
    VMW(0); cw_full(ALL);
    return;
  }

  const int c = wave - 1, g4 = lane >> 4, n = lane & 15, q = n >> 2, p = lane & 3;
  const rsrc_t rws = mkrs(a.ws, WS_END);
  const char* wsp = (const char*)a.ws; asm volatile("" : "+s"(wsp));
  const u32 tag = *(const __attribute__((address_space(4))) u32*)(wsp + WS_EPOCH) + 1u;
  asm volatile("" ::"s"(tag));
  VMW(0);
  const u32 yso = WS_YG + (s == 1 ? 288 * 2048 : 0) + (b >> 1) * (36 * 2048) + (hkv * 2 + (b & 1)) * 64;
  if (c == 4) {
    u32x4 wf[16];
    if (s == 1) {
      read_w(L, wf, 4, lane);
      cw_st(W_MF, 1u);
    }
    cw_wait(W_FULL, s == 3 ? GATE1 : s == 0 ? POLL_A0 : POLL_A);
    fetch<4>(L, rws, lane * 16 + WS_QG + (s < 3 ? ah * 128 : 8192 + hkv * 128) * 8, 16384, tag, (s < 3 ? QA_OFF : OB_OFF) + lane * 8, s < 3 ? 512 : OB_STR, W_QA, bud);
    if (s == 3) {
      cw_wait(W_QS, 5); cw_wait(W_FULL, GATE2);
      fetch<1>(L, rws, lane * 16 + WS_HINT, 0, tag, MG_OFF + lane * 8, 0, W_HINT, bud);
      cw_wait(W_BP, 4);
      fetch<4>(L, rws, n * 32 + (g4 & 1) * 16 + WS_SG + hkv * 2048, 512, tag, MG_OFF + lane * 8, 512, W_SGF, bud);
      cw_wait(W_FULL, ALL);
      read_w(L, wf, 4, lane);
    } else if (s == 1) {
      cw_wait(W_FULL, POLL_C);
      const u32 so = WS_CG + hkv * 32768 + (b & 1) * 1024;
      for (;;) {
        const u32x4 t = gld(rws, q * 8192 + (p & 1) * 512, so);
        if (allz(tagx(t, tag)) || !glb_retry()) break;
        __builtin_amdgcn_s_sleep(16);
      }
      u32x4 cv[16];
      for (;;) {
        u32 bad = 0;
#pragma unroll
        for (int i = 0; i < 16; ++i) cv[i] = gld(rws, lane * 16 + (i & 1) * 2048, so + (i >> 1) * 4096);
#pragma unroll
        for (int i = 0; i < 16; ++i) bad |= tagx(cv[i], tag);
        if (allz(bad) || !glb_retry()) break;
      }
#pragma unroll
      for (int i = 0; i < 4; ++i)
        *(LAS u32*)(L + OB_OFF + i * OB_STR + lane * 4) = pk((asf(cv[i][0]) + asf(cv[4 + i][0])) + (asf(cv[8 + i][0]) + asf(cv[12 + i][0])), (asf(cv[i][2]) + asf(cv[4 + i][2])) + (asf(cv[8 + i][2]) + asf(cv[12 + i][2])));
      cw_st(W_PJ, 4u);
    }
    if (s & 1) {
      project(L, wf, rws, yso, tag, 4, lane, bud);
    } else {
      cw_wait(W_O2, 4);
      __builtin_amdgcn_s_sleep(16);
      final_sum(rws, a.out, s == 0, 256 + hkv * 4 + b, tag, lane, bud);
      if (g == 16 && lane == 0) __builtin_amdgcn_raw_buffer_store_b32(tag, rws, 0, WS_EPOCH, SC1);
    }
    return;
  }

  if (s < 3) {
    u32x4 wf[16];
    if (s == 1) {
      cw_wait(W_WF, 4);
      read_w(L, wf, c, lane);
      cw_wait(W_MF, 1);
      if (lane < 4) asm volatile("ds_add_u32 %0, %1" ::"v"(W_FREE + lane * 4), "v"(1u) : "memory");
    } else {
      const u32 aw = g4 * 1024 + (c >> 1) * 512 + q * 64 + (c & 1) * 32 + 8 * p, ax = SB_OFF + p * 1152 + g4 * 16;
      const u32 vo = (n * 2048 + 2 * g4) * 8, so = WS_QG + ((s ? 0 : 8192) + hkv * 128 + b * 32 + 8 * c) * 8;
      if (j == 2 && c == 0) { __builtin_amdgcn_raw_buffer_store_b32(2u, rws, WS_TK + hkv * 128, 0, SC1); VMW(0); }
      cw_wait(W_WF, 2);
      u32x4 xr[18], wq[9];
#pragma unroll
      for (int ks = 0; ks < 18; ++ks) xr[ks] = ld128(L, ax + ks * 64);
      f32x4 a0 = ZERO4, a1 = ZERO4;
#pragma unroll
      for (int hf = 0; hf < 2; ++hf) {
        if (hf) { cw_wait(W_WF, 4); }
#pragma unroll
        for (int ks = 0; ks < 9; ++ks) wq[ks] = ldtr2(L, aw + (hf * 9 + ks) * 4096, aw + (hf * 9 + ks) * 4096 + 256);
        if (lane < 2) asm volatile("ds_add_u32 %0, %1" ::"v"(W_FREE + hf * 8 + lane * 4), "v"(1u) : "memory");
        __builtin_amdgcn_sched_barrier(0);
#pragma unroll
        for (int ks = 0; ks < 9; ++ks) { if (ks & 1) a1 = mfma(wq[ks], xr[hf * 9 + ks], a1); else a0 = mfma(wq[ks], xr[hf * 9 + ks], a0); }
        __builtin_amdgcn_sched_barrier(0);
      }
      a0 += a1;
      if (n < 4) gst(rws, u32x4{pk(a0[0], a0[1]), tag, pk(a0[2], a0[3]), tag}, vo, so);
    }
    float m = -1e30f, l = 0.f;
    f32x4 acc[8];
#pragma unroll
    for (int i = 0; i < 8; ++i) acc[i] = ZERO4;
    u32x4 qf[4], kf[8], vf[8];
    const u32 lpK = n * 256 + ((g4 ^ swz(n)) << 4), lpQ = QA_OFF + (n & 1) * 256 + g4 * 16;
    const u32 lpV = (4 * g4 + q) * 256 + (((p >> 1) ^ swz(4 * g4 + q)) << 4) + 8 * (p & 1);
#define READ_K() _Pragma("unroll") for (int kk = 0; kk < 4; ++kk) { const u32 ak = (sb + lpK) ^ (kk << 6); kf[kk] = ld128(L, ak); kf[4 + kk] = ld128(L, ak + 4096); }
#define READ_V() do { _Pragma("unroll") for (int ct = 0; ct < 8; ++ct) { const u32 av = (sb + lpV) ^ (ct << 5); vf[ct] = ldtr2(L, av + 8192, av + 12288); } \
      cw_add(W_FREE + (it & 7) * 4, 4, lane); } while (0)
#define SCORES() f32x4 s0 = ZERO4, s1 = ZERO4; \
      _Pragma("unroll") for (int kk = 0; kk < 4; ++kk) { s0 = mfma(kf[kk], qf[kk], s0); s1 = mfma(kf[4 + kk], qf[kk], s1); }
#define PICK8(e) const f32x4 e##0 = (n & 2) ? acc[1] : acc[0], e##1 = (n & 2) ? acc[3] : acc[2], e##2 = (n & 2) ? acc[5] : acc[4], e##3 = (n & 2) ? acc[7] : acc[6], \
      e##4 = (n & 4) ? e##1 : e##0, e##5 = (n & 4) ? e##3 : e##2, e = (n & 8) ? e##5 : e##4
    const u32 vrec = (n & 1) * 512 + (n >> 1) * 64 + g4 * 16;
    bool first = true;
    int it = 4 + c;
#pragma unroll 1
    for (; it < ITU; it += 4) {
      const u32 sb = (it & 7) * SLOT;
      cw_wait(W_FULL, it + 1);
      READ_K(); READ_V();
      if (first) {
        first = false;
        cw_wait(W_QA, 1);
#pragma unroll
        for (int kk = 0; kk < 4; ++kk) qf[kk] = ld128(L, lpQ + ab * 512 + kk * 64);
      }
      SCORES();
      const u32x4 pf = soft<8>(s0, s1, m, l, acc);
#pragma unroll
      for (int ct = 0; ct < 8; ++ct) acc[ct] = mfma(vf[ct], pf, acc[ct]);
    }
    {
      l = xg_sum(l);
      PICK8(e);
      *(LAS f32x4*)(L + MG_OFF + c * 1024 + lane * 16) = e;
      if (lane < 2) *(LAS f32x2*)(L + MS_OFF + (c * 2 + lane) * 8) = f32x2{m, l};
      if (cw_add_rtn(W_MRG, 1, lane) == 3u) {
        f32x2 sv[4]; f32x4 ov[4];
#pragma unroll
        for (int i = 0; i < 4; ++i) { sv[i] = *(LAS f32x2*)(L + MS_OFF + (i * 2 + (n & 1)) * 8); ov[i] = *(LAS f32x4*)(L + MG_OFF + i * 1024 + lane * 16); }
        const float M = fmaxf(fmaxf(sv[0][0], sv[1][0]), fmaxf(sv[2][0], sv[3][0]));
        float Ls = 0.f; f32x4 o = ZERO4;
#pragma unroll
        for (int i = 0; i < 4; ++i) { const float w = ex2((sv[i][0] - M) * CS); Ls = fmaf(w, sv[i][1], Ls); o += w * ov[i]; }
        o *= __builtin_amdgcn_rcpf(Ls);
        gst(rws, u32x4{pk(o[0], o[1]), tag, pk(o[2], o[3]), tag}, vrec, WS_AG + ((ah * 4 + ab) * 16 + as) * 1024);
        if (lane < 2) gst(rws, u32x4{asu(M), tag, asu(Ls), tag}, lane * 16, WS_AS + (ah * 4 + ab) * 2048 + as * 128);
      }
    }
#pragma unroll 1
    for (;; it += 4) {
      const u32 t = cw_wait(W_TK + (it & 15) * 4, it << 8) & 255u, sb = (it & 7) * SLOT;
      if (t >= (u32)NTICK || bud == 0u) break;
#pragma unroll
      for (int kk = 0; kk < 4; ++kk) qf[kk] = ld128(L, lpQ + (t & 3u) * 512 + kk * 64);
      const u32 pr = ah * 4 + (t & 3u), rec = 3 + (t >> 2), va = vrec + WS_AG + (pr * 16 + rec) * 1024, vs = lane * 16 + WS_AS + pr * 2048 + rec * 128;
      cw_wait(W_FULL, it + 1);
      READ_K(); READ_V();
      SCORES();
      m = xg_max(MAX8(s0, s1));
      const float mc = -m * CS;
#pragma unroll
      for (int i = 0; i < 4; ++i) { s0[i] = ex2(fmaf(s0[i], CS, mc)); s1[i] = ex2(fmaf(s1[i], CS, mc)); }
      const u32x4 pf = {pk(s0[0], s0[1]), pk(s0[2], s0[3]), pk(s1[0], s1[1]), pk(s1[2], s1[3])};
      f32x4 e0 = ZERO4, e1 = ZERO4;
#pragma unroll
      for (int ct = 0; ct < 8; ct += 2) { e0 = mfma(vf[ct], (n >> 1) == ct ? pf : u32x4{0u, 0u, 0u, 0u}, e0); e1 = mfma(vf[ct + 1], (n >> 1) == ct + 1 ? pf : u32x4{0u, 0u, 0u, 0u}, e1); }
      s0 += s1;
      l = xg_sum((s0[0] + s0[1]) + (s0[2] + s0[3]));
      e0 = (e0 + e1) * __builtin_amdgcn_rcpf(l);
      gst(rws, u32x4{pk(e0[0], e0[1]), tag, pk(e0[2], e0[3]), tag}, va, 0);
      if (lane < 2) gst(rws, u32x4{asu(m), tag, asu(l), tag}, vs, 0);
    }
    if (s == 1) {
      project(L, wf, rws, yso, tag, c, lane, bud);
    } else {
      cw_add(W_O2, 1, lane);
      cw_wait(W_O2, 4);
      __builtin_amdgcn_s_sleep(16);
      final_sum(rws, a.out, s == 0, (hkv * 4 + b) * 4 + c, tag, lane, bud);
    }
    return;
  }

  const int ch = c & 1;
  const u32 lpR = n * 1024 + ((g4 ^ swz(n)) << 4) + ch * 512;
  const u32 lpC = (4 * g4 + q) * 1024 + (((p >> 1) ^ swz(4 * g4 + q)) << 4) + 8 * (p & 1) + ch * 512;
  u32x4 qa[8], rf[16], cf[16];
#define READ_C(it) do { const u32 sb = ((it) & 7) * SLOT; cw_wait(W_FULL, (it) + 2); \
    _Pragma("unroll") for (int kk = 0; kk < 8; ++kk) { const u32 ak = ((sb + lpR) ^ ((kk & 3) << 6)) + (kk >> 2) * 256; rf[2 * kk] = ld128(L, ak); rf[2 * kk + 1] = ld128(L, ak + SLOT); } \
    _Pragma("unroll") for (int kk = 0; kk < 8; ++kk) { const u32 av = (sb + lpC) ^ (kk << 5); cf[kk] = ldtr2(L, av, av + SLOT); cf[8 + kk] = ldtr2(L, av + 256, av + 256 + SLOT); } \
    if (lane < 2) asm volatile("ds_add_u32 %0, %1" ::"v"(W_FREE + ((it) & 7) * 4 + lane * 4), "v"(1u) : "memory"); __builtin_amdgcn_sched_barrier(0); } while (0)
  {
    cw_wait(W_WF, c + 1);
    u32x4 wu[16];
    {
      const u32 lp = (IT_UK + c) * SLOT + n * 512 + ((g4 ^ swz(n)) << 4);
#pragma unroll
      for (int i = 0; i < 16; ++i) wu[i] = ld128(L, (lp ^ ((i & 3) << 6)) + ((i >> 2) & 1) * 8192 + (i >> 3) * 256);
    }
    cw_add(W_FREE + (IT_UK + c) * 4, 4, lane);
    READ_C(IT_C);
    cw_wait(W_QA, 1);
    f32x4 d[4] = {ZERO4, ZERO4, ZERO4, ZERO4};
#pragma unroll
    for (int i = 0; i < 8; ++i) {
      const u32x4 qv = ld128(L, OB_OFF + p * OB_STR + i * 64 + g4 * 16);
      d[(i >> 2) * 2] = mfma(wu[(i >> 2) * 8 + (i & 3)], qv, d[(i >> 2) * 2]); d[(i >> 2) * 2 + 1] = mfma(wu[(i >> 2) * 8 + 4 + (i & 3)], qv, d[(i >> 2) * 2 + 1]);
    }
    const f32x4 e0 = (q & 1) ? d[1] : d[0], e1 = (q & 1) ? d[3] : d[2], e = (q & 2) ? e1 : e0;
    __builtin_amdgcn_raw_buffer_store_b64(u32x2{pk(e[0], e[1]), pk(e[2], e[3])}, rws, p * 32768 + q * 32 + g4 * 8 + WS_QAG + hkv * 2048 + (b * 4 + c) * 128, 0, SC1);
    if (cw_add_rtn(W_QS, 1, lane) == 3u) cw_st(W_G1, 1u);
    VMW(0);
    if (cw_add_rtn(W_MF, 1, lane) == 3u) { if (lane == 0) gst(rws, u32x4{tag, tag, tag, tag}, WS_HINT + (hkv * 4 + b) * 16, 0); cw_add(W_QS, 1, lane); }
  }
  {
    {
      const u32 so = WS_QAG + bq * 32768 + (c >> 1) * 16384 + ch * 1024;
      cw_wait(W_HINT, 1);
#pragma unroll
      for (int i = 0; i < 8; ++i) qa[i] = gld(rws, (n >> 1) * 2048 + (n & 1) * 64 + g4 * 16 + i * 128, so);
      cw_add(W_GI, 1, lane);
    }
    float mb = -1e30f, lb = 0.f;
    f32x4 ob[16];
#pragma unroll
    for (int i = 0; i < 16; ++i) ob[i] = ZERO4;
#pragma unroll 1
    for (int st = 0;; ++st) {
      const u32 xw = ((st & 1) ? (4 + c) * SLOT + 16384 : SB_OFF + c * 2048) + lane * 16, xr = ((st & 1) ? (4 + (c ^ 1)) * SLOT + 16384 : SB_OFF + (c ^ 1) * 2048) + lane * 16;
      f32x4 s0 = ZERO4, s1 = ZERO4;
#pragma unroll
      for (int kk = 0; kk < 8; ++kk) { s0 = mfma(rf[2 * kk], qa[kk], s0); s1 = mfma(rf[2 * kk + 1], qa[kk], s1); }
      *(LAS f32x4*)(L + xw) = s0;
      *(LAS f32x4*)(L + xw + 1024) = s1;
      cw_st(W_S + c * 4, st + 1);
      cw_wait(W_S + (c ^ 1) * 4, st + 1);
      s0 += *(LAS f32x4*)(L + xr); s1 += *(LAS f32x4*)(L + xr + 1024);
      const u32x4 pf = soft<16>(s0, s1, mb, lb, ob);
#pragma unroll
      for (int ct = 0; ct < 16; ++ct) ob[ct] = mfma(cf[ct], pf, ob[ct]);
      if (st == 7) break;
      __builtin_amdgcn_sched_barrier(0);
      READ_C(IT_C + 2 * st + 2);
    }
#pragma unroll
    for (int i = 0; i < 8; ++i) {
      auto x = __builtin_amdgcn_permlane16_swap(pk(ob[2 * i][0], ob[2 * i][1]), pk(ob[2 * i + 1][0], ob[2 * i + 1][1]), false, false);
      auto y = __builtin_amdgcn_permlane16_swap(pk(ob[2 * i][2], ob[2 * i][3]), pk(ob[2 * i + 1][2], ob[2 * i + 1][3]), false, false);
      gst(rws, u32x4{x[0], y[0], x[1], y[1]}, (n >> 1) * 512 + (n & 1) * 64 + (g4 & 1) * 32 + (g4 >> 1) * 16 + (i & 3) * 128,
          WS_PB + ((bq * 16 + sp) * 4 + ch * 2 + (i >> 2)) * 8192 + (c >> 1) * 4096);
    }
    lb = xg_sum(lb);
    if (g4 == 0 && ch == 0) *(LAS f32x2*)(L + ST_OFF + ((c >> 1) * 16 + n) * 8) = f32x2{mb, lb};
    VMW(0);
    if (cw_add_rtn(W_BP, 1, lane) == 3u) {
      const f32x2 ml = *(LAS f32x2*)(L + ST_OFF + (lane & 31) * 8);
      if (lane < 32) gst(rws, u32x4{asu(ml[0]), tag, asu(ml[1]), tag}, (lane >> 1) * 2048 + (lane & 1) * 16, WS_SG + (bq * 16 + sp) * 32);
    }
  }
  {
    cw_wait(W_SGF, 1);
    u32x4 pv[8];
#pragma unroll
    for (int i = 0; i < 8; ++i) pv[i] = gld(rws, (lane >> 5) * 32768 + (lane & 31) * 16, WS_PB + ((c * 16 + 2 * i) * 4 + b) * 8192 + hkv * 512);
    cw_add(W_GI, 1, lane);
    const f32x2 st = *(LAS f32x2*)(L + MG_OFF + c * 512 + lane * 8);
    const float w = ex2((st[0] - row_max(st[0])) * CS), cf = w / row_sum(w * st[1]);
    f32x4 o0 = ZERO4, o1 = ZERO4;
#pragma unroll
    for (int i = 0; i < 8; ++i) {
      const float f = asf(__builtin_amdgcn_ds_bpermute((((lane >> 2) & 1) * 16 + 2 * i + (lane >> 5)) * 4, asu(cf)));
      o0 += f * f32x4{bflo(pv[i][0]), bfhi(pv[i][0]), bflo(pv[i][1]), bfhi(pv[i][1])};
      o1 += f * f32x4{bflo(pv[i][2]), bfhi(pv[i][2]), bflo(pv[i][3]), bfhi(pv[i][3])};
    }
#pragma unroll
    for (int i = 0; i < 4; ++i) {
      auto t0 = __builtin_amdgcn_permlane32_swap(asu(o0[i]), asu(o0[i]), false, false); o0[i] = asf(t0[0]) + asf(t0[1]);
      auto t1 = __builtin_amdgcn_permlane32_swap(asu(o1[i]), asu(o1[i]), false, false); o1[i] = asf(t1[0]) + asf(t1[1]);
    }
    if (lane < 32) *(LAS u32x4*)(L + OB_OFF + c * OB_STR + ((lane >> 2) & 1) * 256 + (lane >> 3) * 64 + p * 16) = u32x4{pk(o0[0], o0[1]), pk(o0[2], o0[3]), pk(o1[0], o1[1]), pk(o1[2], o1[3])};
    cw_add(W_OB, 1, lane);
    cw_wait(W_FULL, IT_UV + 4);
    u32x4 wf[16];
    {
      const u32 r0 = 8 * (g4 & 1) + q;
      const u32 lp0 = (8 * g4 + q) * 512 + ((((p >> 1) + 8 * c) ^ swz(r0)) << 4) + 8 * (p & 1);
      const u32 lp1 = (8 * g4 + q + 4) * 512 + ((((p >> 1) + 8 * c) ^ swz(r0 + 4)) << 4) + 8 * (p & 1);
#pragma unroll
      for (int i = 0; i < 16; ++i) wf[i] = ldtr2(L, (lp0 ^ ((i & 3) << 5)) + ((IT_UV + (i >> 2)) & 7) * SLOT, (lp1 ^ ((i & 3) << 5)) + ((IT_UV + (i >> 2)) & 7) * SLOT);
    }
    cw_wait(W_OB, 4);
    f32x4 d[4] = {ZERO4, ZERO4, ZERO4, ZERO4};
#pragma unroll
    for (int ks = 0; ks < 4; ++ks) {
      const u32x4 of = ld128(L, OB_OFF + p * OB_STR + (c >> 1) * 256 + ks * 64 + g4 * 16);
#pragma unroll
      for (int t = 0; t < 4; ++t) d[t] = mfma(wf[ks * 4 + t], of, d[t]);
    }
    {
      const u32 vo = p * 2048 + (q >> 1) * 128 + g4 * 32 + (q & 1) * 16, so = WS_CG + ((hkv * 4 + b) * 1024 + c * 64) * 8;
      gst(rws, pick2(d[0], d[1], q, tag), vo, so); gst(rws, pick2(d[2], d[3], q, tag), vo + 256, so);
    }
  }
  {
    cw_add(W_O2, 1, lane);
    cw_wait(W_O2, 4);
    const int rn = n ^ (lane >> 5);
    const u32 so = WS_AG + (hkv * 4 + c) * 16384 + (b & 1) * 512, ss = WS_AS + (hkv * 4 + c) * 2048 + (b & 1) * 16 + (rn < NREC ? rn : 0) * 128;
    const u32 lr = (4 + c) * SLOT, v0 = (lane & 31) * 16, v1 = v0 + (lane >> 5) * 1024;
    u32x4 wf[16], sv, ov[NPR];
    u32 todo = 0x5555u & ((1u << NREC) - 1u);
    {
      u32 ask = todo;
      while (cw_ld(W_WI) < 2u && bud != 0u) {
        sv = gld(rws, ss, 0);
        const unsigned long long sb = __builtin_amdgcn_ballot_w64(tagx(sv, tag) != 0u);
        const u32 nw = ask & ~((u32)sb | (u32)(sb >> 32));
        for (u32 k = nw; k; k &= k - 1u) { const u32 i = __builtin_ctz(k); dma_sc1(rws, lr + i * 512, i + 1 < NREC ? v1 : v0, so + i * 1024); }
        ask &= ~nw;
        bud = bud < 16u ? 0u : bud - 16u;
        __builtin_amdgcn_s_sleep(2);
      }
      todo = ask | 0x10000u;
    }
    for (;;) {
      sv = gld(rws, ss, 0);
      for (u32 k = todo & 0xffffu; k; k &= k - 1u) { const u32 i = __builtin_ctz(k); dma_sc1(rws, lr + i * 512, i + 1 < NREC ? v1 : v0, so + i * 1024); }
      if (todo >> 16) { cw_wait(W_FULL, ALL); read_w(L, wf, c, lane); }
      VMW(0);
      const unsigned long long sb = __builtin_amdgcn_ballot_w64(tagx(sv, tag) != 0u);
      if (todo & ~(((u32)sb | (u32)(sb >> 32)) & 0xffffu)) {
#pragma unroll
        for (int i = 0; i < NPR; ++i) ov[i] = ld128(L, lr + lane * 16 + i * 1024);
        u32 bad = 0;
#pragma unroll
        for (int i = 0; i < NPR; ++i) bad |= (tagx(ov[i], tag) != 0u) << (2 * i);
        if (allz(bad)) todo = 0u;
        else { bad |= __builtin_amdgcn_mov_dpp(bad, 0x128, 0xf, 0xf, true); bad |= __builtin_amdgcn_mov_dpp(bad, 0x124, 0xf, 0xf, true); bad |= __builtin_amdgcn_mov_dpp(bad, 0x122, 0xf, 0xf, true); bad |= __builtin_amdgcn_mov_dpp(bad, 0x121, 0xf, 0xf, true);
          todo = __builtin_amdgcn_readlane(bad, 0) | __builtin_amdgcn_readlane(bad, 16) | __builtin_amdgcn_readlane(bad, 32) | __builtin_amdgcn_readlane(bad, 48); }
      }
      if (todo == 0u && sb == 0ull) break;
      bud = bud < 16u ? 0u : bud - 16u;
      if (bud == 0u) break;
      asm volatile("" ::: "memory");
    }
    const float ms = asf(sv[0]), w0 = ex2((ms - row_max(ms)) * CS) * asf(sv[2]), w = rn < NREC ? w0 : 0.f;
    f32x4 o[2] = {ZERO4, ZERO4};
    merge_rec<0>(o, ov, w);
    o[0] += o[1];
    const float rs = __builtin_amdgcn_rcpf(asf(__builtin_amdgcn_readfirstlane(asu(row_sum(w)))));
#pragma unroll
    for (int i = 0; i < 4; ++i) { auto t = __builtin_amdgcn_permlane32_swap(asu(o[0][i]), asu(o[0][i]), false, false); o[0][i] = (asf(t[0]) + asf(t[1])) * rs; }
    *(LAS u32x2*)(L + OB_OFF + c * OB_STR + (lane & 31) * 8) = u32x2{pk(o[0][0], o[0][1]), pk(o[0][2], o[0][3])};
    cw_add(W_PJ, 1, lane);
    project(L, wf, rws, yso, tag, c, lane, bud);
  }
}

extern "C" void kernel_launch(void* const* d_in, const int* in_sizes, int n_in, void* d_out, int out_size, void* d_ws, size_t ws_size, hipStream_t stream) {
  Args a;
  for (int i = 0; i < 11; ++i) a.in[i] = d_in[i];
  a.out = d_out;
  a.ws = d_ws;
  concat_heads_decode_kernel<<<256, 384, 0, stream>>>(a);
}
```
